```python
import torch

def _softmax(x, axis=-1):
    return torch.softmax(x, dim=axis)
def _gelu(x):
    return torch.nn.functional.gelu(x, approximate="tanh")
def _silu(x):
    return torch.nn.functional.silu(x)
def _sigmoid(x):
    return torch.sigmoid(x)
def _layernorm(x, axis=-1, eps=1e-5):
    m = x.mean(dim=axis, keepdim=True)
    v = x.var(dim=axis, keepdim=True, unbiased=False)
    return (x - m) / torch.sqrt(v + eps)
def _scatter_add(tgt, idx, src, axis=0):
    return tgt.clone().index_add_(axis, idx, src)
def _segment_sum(src, seg, n):
    out = torch.zeros((n,) + src.shape[1:], dtype=src.dtype)
    return out.index_add_(0, seg, src)
def _pad(x, pw):
    flat = tuple(v for pair in reversed(pw) for v in pair)
    return torch.nn.functional.pad(x, flat)

kernel_name = 'standalone__modern_conv_block_slak__vision'

def setup_inputs():

    _w = (lambda a: (lambda t: t.to(torch.bfloat16) if t.is_floating_point() else t)(torch.as_tensor(a)))
    H = W = 384
    C = 384
    K = 1
    return {
        "x": _w((lambda *s: torch.randn(*s, dtype=torch.bfloat16) * 0.05)(1, H, W, C)),
        "dw": _w((lambda *s: torch.randn(*s, dtype=torch.bfloat16) * 0.05)(K, K, C)),
        "w1": _w((lambda *s: torch.randn(*s, dtype=torch.bfloat16) * 0.05)(C, 4 * C)),
        "w2": _w((lambda *s: torch.randn(*s, dtype=torch.bfloat16) * 0.05)(4 * C, C)),
        "grn_g": _w((lambda *s: torch.randn(*s, dtype=torch.bfloat16) * 0.05)(4 * C)),
        "grn_b": _w((lambda *s: torch.randn(*s, dtype=torch.bfloat16) * 0.05)(4 * C)),
    }

def reference(x, dw, w1, w2, grn_g, grn_b):
    B, H, W, C = x.shape
    K = dw.shape[0]
    pad = K // 2
    xp = _pad(x, ((0,0),(pad,pad),(pad,pad),(0,0)))
    out = torch.zeros_like(x)
    for i in range(K):
        for j in range(K):
            out = out + xp[:, i:i+H, j:j+W, :] * dw[i, j][None, None, None, :]
    h = _layernorm(out, axis=-1)
    h = _gelu(h @ w1)
    gx = torch.sqrt((h * h).sum(axis=(1, 2), keepdims=True) + 1e-6)
    nx = gx / (gx.mean(axis=-1, keepdims=True) + 1e-6)
    h = grn_g[None, None, None, :] * (h * nx) + grn_b[None, None, None, :] + h
    return x + h @ w2
```

```cpp
#include <hip/hip_runtime.h>
#include <hip/hip_bf16.h>
#include <stdint.h>
#include <stdio.h>

typedef short bf16x8 __attribute__((ext_vector_type(8)));
typedef short s16x4 __attribute__((ext_vector_type(4)));
typedef short v4i16_t __attribute__((ext_vector_type(4)));
typedef float f32x16 __attribute__((ext_vector_type(16)));
typedef float f32x4 __attribute__((ext_vector_type(4)));
typedef float f32x2 __attribute__((ext_vector_type(2)));
typedef unsigned u32x4 __attribute__((ext_vector_type(4)));
typedef __bf16 bf16x2_t __attribute__((ext_vector_type(2)));
typedef unsigned short u16;

#define DI __device__ __forceinline__
#define LAS __attribute__((address_space(3)))

constexpr int NPIX = 147456;
constexpr int CH = 384;
constexpr int HID = 1536;
constexpr int NTILE = NPIX / 32;
constexpr int NWG = 256;
constexpr int TPW = NTILE / NWG;

constexpr size_t H_BYTES = (size_t)NPIX * HID * 2;
constexpr unsigned K1_LDS = 163840, K1_PF_OFF = 65536, K1_SMAX = 10, K1_NREP = 4, K1_G_TAB = 1179648, K1_G_CNT = 1189888, K1_G_LOC = 1187840, K1_G_DUM = 1206272,
  K1_G_ACC = 1271808, K1_G_LUT = 1333248, K1_G_BYTES = 3288064;
constexpr bool K1_LUT = true;
constexpr float K1_ZS = 64.0f, K1_AIN = 4096.0f;
constexpr unsigned K1_LE0 = 100, K1_LNB = 46, K1_RHO = 0, K1_TAB_OFF = 61952, K1_W_GD = 452853760, K1_GDS = 32, K1_XRING = 139264, K1_WS3 = 102912, K1_G_PT = 1333504;
constexpr unsigned K1_G_W1P = 2108416;
constexpr int K2_NU = 18, K2_IM0 = 73728, K2_BIAS_OFF = 159744, K2_PF_OFF = 162816, K2_S16_OFF = 110592;

constexpr unsigned G_W2R = K1_G_BYTES, G_W2F = G_W2R + 1179648, G_PRM = G_W2F + 1179648, G_BIAS = G_PRM + 262144;
__device__ __attribute__((aligned(256))) char g_buf[G_BIAS + 8448];
#define g_w2r (g_buf + G_W2R)
#define g_prm ((unsigned*)(g_buf + G_PRM))
#define g_bias ((float*)(g_buf + G_BIAS))
extern "C" __device__ __attribute__((aligned(256), used)) const unsigned c_lut[6144] = {
0,0,0,0,0,0,0,0,0,0,0,0,0,0,0,0,0,0,0,0,0,0,0,0,0,0,0,0,0,0,0,0,
0,0,0,0,0,0,0,0,0,0,0,0,0,0,0,0,0,0,0,0,0,0,0,0,0,0,0,0,0,0,0,0,
0,0,0,0,0,0,0,0,0,0,0,0,0,0,0,0,0,0,0,0,0,0,0,0,0,0,0,0,0,0,0,0,
0,0,0,0,0,0,0,0,0,0,0,0,0,0,0,0,0,0,0,0,0,0,0,0,0,0,0,0,0,0,0,0,
0,0,0,0,0,0,0,0,0,0,0,0,0,0,0,0,0,0,0,0,0,0,0,0,0,0,0,0,0,0,0,0,
0,0,0,0,0,0,0,0,0,0,0,0,0,0,0,0,0,0,0,0,0,0,0,0,0,0,0,0,0,0,0,0,
0,0,0,0,0,0,0,0,0,0,0,0,0,0,0,0,0,0,0,0,0,0,0,0,0,0,0,0,0,0,0,0,
0,0,0,0,0,0,0,0,0,0,0,0,0,0,0,0,0,0,0,0,0,0,0,0,0,0,0,0,0,0,0,0,
0,0,0,0,0,0,0,0,0,0,0,0,0,0,0,0,0,0,0,0,0,0,0,0,0,0,0,0,0,0,0,0,
0,0,0,0,0,0,0,0,0,0,0,0,0,0,0,0,0,0,0,0,0,0,0,0,0,0,0,0,0,0,0,0,
0,0,0,0,0,0,0,0,0,0,0,0,0,0,0,0,0,0,0,0,0,0,0,0,0,0,0,0,0,0,0,0,
0,0,0,0,0,0,0,0,0,0,0,0,0,0,0,0,0,0,0,0,0,0,0,0,0,0,0,0,0,0,0,0,
0,0,0,0,0,0,0,0,0,0,0,0,0,0,0,0,0,0,0,0,0,0,0,0,0,0,0,0,0,0,0,0,
0,0,0,0,0,0,0,0,0,0,0,0,0,0,0,0,0,0,0,0,0,0,0,0,0,0,0,0,0,0,0,0,
0,0,0,0,0,0,0,0,0,0,0,0,0,0,0,0,0,0,0,0,0,0,0,0,0,0,0,0,0,0,0,0,
0,0,0,0,0,0,0,0,0,0,0,0,0,0,0,0,0,0,0,0,0,0,0,0,0,0,0,0,0,0,0,0,
0,0,0,0,0,0,0,0,0,0,0,0,0,0,0,0,0,0,0,0,0,0,0,0,0,0,0,0,0,0,0,0,
0,0,0,0,0,0,0,0,0,0,0,0,0,0,0,0,0,0,0,0,0,0,0,0,0,0,0,0,0,0,0,0,
0,0,0,0,0,0,0,0,0,0,0,0,0,0,0,0,0,0,0,0,0,0,0,0,0,0,0,0,0,0,0,0,
0,0,0,0,0,0,0,0,0,0,0,0,0,0,0,0,0,0,0,0,0,0,0,0,0,0,0,0,0,0,0,0,
0,0,0,0,0,0,0,0,0,0,0,0,0,0,0,0,0,0,0,0,0,0,0,0,0,0,0,0,0,0,0,0,
0,0,0,0,0,0,0,0,0,0,0,0,0,0,0,0,0,0,0,0,0,0,0,0,0,0,0,0,0,0,0,0,
0,0,0,0,0,0,0,0,0,0,0,0,0,0,0,0,0,0,0,0,0,0,0,0,0,0,0,0,0,0,0,0,
0,0,0,0,0,0,0,0,0,0,0,0,0,0,0,0,0,0,0,0,0,0,0,0,0,0,0,0,0,0,0,0,
0,0,0,0,0,0,0,0,0,0,0,0,0,0,0,0,0,0,0,0,0,0,0,0,0,0,0,0,0,0,0,0,
0,0,0,0,0,0,0,0,0,0,0,0,0,0,0,0,0,0,0,0,0,0,0,0,0,0,0,0,0,0,0,0,
0,0,0,0,0,0,0,0,0,0,0,0,0,0,0,0,0,0,0,0,0,0,0,0,0,0,0,0,0,0,0,0,
0,0,0,0,0,0,0,0,0,0,0,0,0,0,0,0,0,0,0,0,0,0,0,0,0,0,0,0,0,0,0,0,
0,0,0,0,0,0,0,0,0,0,0,0,0,0,0,0,0,0,0,0,0,0,0,0,0,0,0,0,0,0,0,0,
0,0,0,0,0,0,0,0,0,0,0,0,0,0,0,0,0,0,0,0,0,0,0,0,0,0,0,0,0,0,0,0,
0,0,0,0,0,0,0,0,0,0,0,0,0,0,0,0,0,0,0,0,0,0,0,0,0,0,0,0,0,0,0,0,
0,0,0,0,0,0,0,0,0,0,0,0,0,0,0,0,0,0,0,0,0,0,0,0,0,0,0,0,0,0,0,0,
0,0,0,0,0x10000,0x10001,0x10001,0x10001,0x10001,0x10001,0x10001,0x10001,0x10001,0x10001,0x10001,0x10001,0x10001,0x10001,0x10001,0x10001,0x10001,0x10001,0x10001,0x10001,0x10001,0x10001,0x10001,0x10001,0x10001,0x10001,0x10001,0x10001,
0x10001,0x10001,0x10001,0x10001,0x10001,0x10001,0x10001,0x10001,0x10001,0x10001,0x10001,0x10001,0x10001,0x10001,0x10001,0x10001,0x10001,0x10001,0x10001,0x10001,0x10001,0x10001,0x10001,0x10001,0x10001,0x10001,0x10001,0x10001,0x10001,0x10001,0x10001,0x10001,
0x10001,0x10001,0x10001,0x10001,0x10001,0x10001,0x10001,0x10001,0x10001,0x10001,0x10001,0x10001,0x10001,0x10001,0x10001,0x10001,0x10001,0x10001,0x10001,0x10001,0x10001,0x10001,0x10001,0x10001,0x10001,0x10001,0x10001,0x10001,0x10001,0x10001,0x10001,0x10001,
0x10001,0x10001,0x10001,0x10001,0x10001,0x10001,0x20002,0x20002,0x20002,0x20002,0x20002,0x20002,0x20002,0x20002,0x20002,0x20002,0x20002,0x20002,0x20002,0x20002,0x20002,0x20002,0x20002,0x20002,0x20002,0x20002,0x20002,0x20002,0x20002,0x20002,0x20002,0x20002,
0x20002,0x20002,0x20002,0x20002,0x20002,0x20002,0x20002,0x20002,0x20002,0x20002,0x20002,0x20002,0x20002,0x20002,0x20002,0x20002,0x20002,0x20002,0x20002,0x20002,0x20002,0x30002,0x30003,0x30003,0x30003,0x30003,0x30003,0x30003,0x30003,0x30003,0x30003,0x30003,
0x30003,0x30003,0x30003,0x30003,0x30003,0x30003,0x30003,0x30003,0x30003,0x30003,0x30003,0x30003,0x30003,0x30003,0x30003,0x30003,0x30003,0x30003,0x30003,0x30003,0x30003,0x30003,0x30003,0x40003,0x40004,0x40004,0x40004,0x40004,0x40004,0x40004,0x40004,0x40004,
0x40004,0x40004,0x40004,0x40004,0x40004,0x40004,0x40004,0x40004,0x40004,0x40004,0x40004,0x40004,0x40004,0x50005,0x50005,0x50005,0x50005,0x50005,0x50005,0x50005,0x50005,0x50005,0x50005,0x50005,0x50005,0x50005,0x50005,0x50005,0x50005,0x60005,0x60006,0x60006,
0x60006,0x60006,0x60006,0x60006,0x60006,0x60006,0x60006,0x60006,0x60006,0x60006,0x60006,0x60006,0x60006,0x60006,0x60006,0x70006,0x70007,0x70007,0x70007,0x70007,0x70007,0x70007,0x70007,0x70007,0x70007,0x70007,0x70007,0x70007,0x70007,0x70007,0x70007,0x70007,
0x80008,0x80008,0x80008,0x80008,0x80008,0x80008,0x80008,0x80008,0x80008,0x90009,0x90009,0x90009,0x90009,0x90009,0x90009,0x90009,0x90009,0xa000a,0xa000a,0xa000a,0xa000a,0xa000a,0xa000a,0xa000a,0xa000a,0xa000a,0xb000b,0xb000b,0xb000b,0xb000b,0xb000b,0xb000b,
0xb000b,0xb000b,0xc000c,0xc000c,0xc000c,0xc000c,0xc000c,0xc000c,0xc000c,0xc000c,0xc000c,0xd000d,0xd000d,0xd000d,0xd000d,0xd000d,0xd000d,0xd000d,0xd000d,0xe000e,0xe000e,0xe000e,0xe000e,0xe000e,0xe000e,0xe000e,0xe000e,0xe000e,0xf000f,0xf000f,0xf000f,0xf000f,
0xf000f,0xf000f,0x100010,0x100010,0x100010,0x100010,0x110010,0x110011,0x110011,0x110011,0x120011,0x120012,0x120012,0x120012,0x120012,0x130013,0x130013,0x130013,0x130013,0x140014,0x140014,0x140014,0x140014,0x140014,0x150015,0x150015,0x150015,0x160015,0x160016,0x160016,0x160016,0x160016,
0x170016,0x170017,0x170017,0x170017,0x180018,0x180018,0x180018,0x180018,0x180018,0x190019,0x190019,0x190019,0x1a0019,0x1a001a,0x1a001a,0x1a001a,0x1a001a,0x1b001a,0x1b001b,0x1b001b,0x1b001b,0x1c001c,0x1c001c,0x1c001c,0x1c001c,0x1c001c,0x1d001d,0x1d001d,0x1d001d,0x1e001d,0x1e001e,0x1e001e,
0x1e001e,0x1f001f,0x1f001f,0x200020,0x200020,0x210020,0x210021,0x220022,0x220022,0x220022,0x230023,0x240023,0x240024,0x240024,0x250025,0x250025,0x260026,0x260026,0x270026,0x270027,0x280028,0x280028,0x290028,0x290029,0x2a0029,0x2a002a,0x2a002a,0x2b002b,0x2c002b,0x2c002c,0x2c002c,0x2d002d,
0x2d002d,0x2e002e,0x2e002e,0x2f002e,0x2f002f,0x300030,0x300030,0x300030,0x310031,0x320031,0x320032,0x320032,0x330033,0x340033,0x340034,0x340034,0x350034,0x350035,0x360036,0x360036,0x370036,0x370037,0x380038,0x380038,0x380038,0x390039,0x3a0039,0x3a003a,0x3a003a,0x3b003b,0x3c003b,0x3c003c,
0x3d003c,0x3e003d,0x3e003e,0x40003f,0x400040,0x420041,0x420042,0x430043,0x440044,0x450044,0x460046,0x470046,0x480048,0x490048,0x4a004a,0x4a004a,0x4c004b,0x4c004c,0x4e004d,0x4e004e,0x50004f,0x500050,0x520051,0x520052,0x530052,0x540054,0x550054,0x560056,0x570056,0x580058,0x590058,0x5a005a,
0x5a005a,0x5c005b,0x5c005c,0x5e005d,0x5e005e,0x60005f,0x600060,0x610061,0x620062,0x630062,0x640064,0x650064,0x660066,0x670066,0x680068,0x680068,0x6a0069,0x6a006a,0x6c006b,0x6c006c,0x6e006d,0x6e006e,0x70006f,0x700070,0x710070,0x720072,0x730072,0x740074,0x750074,0x760076,0x770076,0x780077,
0x7a0078,0x7b007a,0x7d007c,0x7f007e,0x810080,0x830082,0x850084,0x860086,0x880087,0x8a0089,0x8c008b,0x8e008d,0x90008f,0x920091,0x940093,0x950095,0x970096,0x990098,0x9b009a,0x9d009c,0x9f009e,0xa100a0,0xa300a2,0xa400a4,0xa600a5,0xa800a7,0xaa00a9,0xac00ab,0xae00ad,0xb000af,0xb200b1,0xb300b3,
0xb500b4,0xb700b6,0xb900b8,0xbb00ba,0xbd00bc,0xbf00be,0xc100c0,0xc200c2,0xc400c3,0xc600c5,0xc800c7,0xca00c9,0xcc00cb,0xce00cd,0xd000cf,0xd100d1,0xd300d2,0xd500d4,0xd700d6,0xd900d8,0xdb00da,0xdd00dc,0xdf00de,0xe000e0,0xe200e1,0xe400e3,0xe600e5,0xe800e7,0xea00e9,0xec00eb,0xee00ed,0xef00ee,
0xf300f1,0xf600f5,0xfa00f8,0xfe00fc,0x1020100,0x1060104,0x10a0108,0x10c010c,0x110010e,0x1140112,0x1180116,0x11c011a,0x120011e,0x1240122,0x1280126,0x12a012a,0x12e012c,0x1320130,0x1360134,0x13a0138,0x13e013c,0x1420140,0x1460144,0x1480148,0x14c014a,0x150014e,0x1540152,0x1580156,0x15c015a,0x160015e,0x1640162,0x1660166,
0x16a0168,0x16e016c,0x1720170,0x1760174,0x17a0178,0x17e017c,0x1820180,0x1840184,0x1880186,0x18c018a,0x190018e,0x1940192,0x1980196,0x19c019a,0x1a0019e,0x1a201a2,0x1a601a4,0x1aa01a8,0x1ae01ac,0x1b201b0,0x1b601b4,0x1ba01b8,0x1be01bc,0x1c001c0,0x1c401c2,0x1c801c6,0x1cc01ca,0x1d001ce,0x1d401d2,0x1d801d6,0x1dc01da,0x1de01de,
0x1e601e2,0x1ec01ea,0x1f401f0,0x1fc01f8,0x2040200,0x20c0208,0x2140210,0x2180218,0x220021c,0x2280224,0x230022c,0x2380234,0x240023c,0x2480244,0x250024c,0x2540254,0x25c0258,0x2640260,0x26c0268,0x2740270,0x27c0278,0x2840280,0x28c0288,0x2900290,0x2980294,0x2a0029c,0x2a802a4,0x2b002ac,0x2b802b4,0x2c002bc,0x2c802c4,0x2cc02cc,
0x2d402d0,0x2dc02d8,0x2e402e0,0x2ec02e8,0x2f402f0,0x2fc02f8,0x3040300,0x3080308,0x310030c,0x3180314,0x320031c,0x3280324,0x330032c,0x3380334,0x340033c,0x3440344,0x34c0348,0x3540350,0x35c0358,0x3640360,0x36c0368,0x3740370,0x37c0378,0x3800380,0x3880384,0x390038c,0x3980394,0x3a0039c,0x3a803a4,0x3b003ac,0x3b803b4,0x3bc03bc,
0x3cc03c4,0x3d803d4,0x3e803e0,0x3f803f0,0x4080400,0x4180410,0x4280420,0x4300430,0x4400438,0x4500448,0x4600458,0x4700468,0x4800478,0x4900488,0x4a00498,0x4a804a8,0x4b804b0,0x4c804c0,0x4d804d0,0x4e804e0,0x4f804f0,0x5080500,0x5180510,0x5200520,0x5300528,0x5400538,0x5500548,0x5600558,0x5700568,0x5800578,0x5900588,0x5a00598,
0x5a805a0,0x5b805b0,0x5c805c0,0x5d805d0,0x5e805e0,0x5f805f0,0x6080600,0x6180610,0x6200618,0x6300628,0x6400638,0x6500648,0x6600658,0x6700668,0x6800678,0x6900688,0x6980690,0x6a806a0,0x6b806b0,0x6c806c0,0x6d806d0,0x6e806e0,0x6f806f0,0x7080700,0x7100708,0x7200718,0x7300728,0x7400738,0x7500748,0x7600758,0x7700768,0x7800778,
0x7980788,0x7b807a8,0x7d007c0,0x7f007e0,0x8080800,0x8180810,0x8280820,0x8380830,0x8400838,0x8500848,0x8600858,0x8700868,0x8800878,0x8900888,0x8a00898,0x8b008a8,0x8b808b0,0x8c808c0,0x8d808d0,0x8e808e0,0x8f808f0,0x9080900,0x9180910,0x9280920,0x9300928,0x9400938,0x9500948,0x9600958,0x9700968,0x9800978,0x9900988,0x9a00998,
0x9a809a0,0x9b809b0,0x9c809c0,0x9d809d0,0x9e809e0,0x9f809f0,0xa080a00,0xa180a10,0xa200a20,0xa300a28,0xa400a38,0xa500a48,0xa600a58,0xa700a68,0xa800a78,0xa900a88,0xa980a98,0xaa80aa0,0xab80ab0,0xac80ac0,0xad80ad0,0xae80ae0,0xaf80af0,0xb080b00,0xb100b10,0xb200b18,0xb300b28,0xb400b38,0xb500b48,0xb600b58,0xb700b68,0xb800b78,
0xb980b88,0xbb80ba8,0xbd00bc8,0xbf00be0,0xc080c00,0xc180c10,0xc280c20,0xc380c30,0xc400c40,0xc500c48,0xc600c58,0xc700c68,0xc800c78,0xc900c88,0xca00c98,0xcb00ca8,0xcb80cb8,0xcc80cc0,0xcd80cd0,0xce80ce0,0xcf80cf0,0xd080d00,0xd180d10,0xd280d20,0xd380d30,0xd400d38,0xd500d48,0xd600d58,0xd700d68,0xd800d78,0xd900d88,0xda00d98,
0xdb00da8,0xdb80db0,0xdc80dc0,0xdd80dd0,0xde80de0,0xdf80df0,0xe080e00,0xe180e10,0xe280e20,0xe300e30,0xe400e38,0xe500e48,0xe600e58,0xe700e68,0xe800e78,0xe900e88,0xea00e98,0xea80ea8,0xeb80eb0,0xec80ec0,0xed80ed0,0xee80ee0,0xef80ef0,0xf080f00,0xf180f10,0xf280f20,0xf300f28,0xf400f38,0xf500f48,0xf600f58,0xf700f68,0xf800f78,
0xf980f88,0xfb80fa8,0xfd80fc8,0xff00fe0,0x10081000,0x10181010,0x10281020,0x10381030,0x10481040,0x10501050,0x10601058,0x10701068,0x10801078,0x10901088,0x10a01098,0x10b010a8,0x10c010b8,0x10d010c8,0x10d810d0,0x10e810e0,0x10f810f0,0x11081100,0x11181110,0x11281120,0x11381130,0x11481140,0x11501150,0x11601158,0x11701168,0x11801178,0x11901188,0x11a01198,
0x11b011a8,0x11c011b8,0x11d011c8,0x11d811d0,0x11e811e0,0x11f811f0,0x12081200,0x12181210,0x12281220,0x12381230,0x12481240,0x12501250,0x12601258,0x12701268,0x12801278,0x12901288,0x12a01298,0x12b012a8,0x12c012b8,0x12d012c8,0x12d812d0,0x12e812e0,0x12f812f0,0x13081300,0x13181310,0x13281320,0x13381330,0x13481340,0x13581350,0x13601358,0x13701368,0x13801378,
0x13981390,0x13b813a8,0x13d813c8,0x13f813e8,0x14081400,0x14181410,0x14281420,0x14381430,0x14481440,0x14581450,0x14681460,0x14701470,0x14801478,0x14901488,0x14a01498,0x14b014a8,0x14c014b8,0x14d014c8,0x14e014d8,0x14f014e8,0x14f814f8,0x15081500,0x15181510,0x15281520,0x15381530,0x15481540,0x15581550,0x15681560,0x15781570,0x15881580,0x15901588,0x15a01598,
0x15b015a8,0x15c015b8,0x15d015c8,0x15e015d8,0x15f015e8,0x160015f8,0x16101608,0x16181610,0x16281620,0x16381630,0x16481640,0x16581650,0x16681660,0x16781670,0x16881680,0x16981690,0x16a016a0,0x16b016a8,0x16c016b8,0x16d016c8,0x16e016d8,0x16f016e8,0x170016f8,0x17101708,0x17201718,0x17301728,0x17381730,0x17481740,0x17581750,0x17681760,0x17781770,0x17881780,
0x17a01790,0x17c017b0,0x17e017d0,0x180017f0,0x18101808,0x18201818,0x18301828,0x18381830,0x18481840,0x18581850,0x18681860,0x18781870,0x18881880,0x18981890,0x18a818a0,0x18b818b0,0x18c818c0,0x18d018c8,0x18e018d8,0x18f018e8,0x190018f8,0x19101908,0x19201918,0x19301928,0x19401938,0x19501948,0x19601958,0x19681968,0x19781970,0x19881980,0x19981990,0x19a819a0,
0x19b819b0,0x19c819c0,0x19d819d0,0x19e819e0,0x19f819f0,0x1a081a00,0x1a101a10,0x1a201a18,0x1a301a28,0x1a401a38,0x1a501a48,0x1a601a58,0x1a701a68,0x1a801a78,0x1a901a88,0x1aa01a98,0x1ab01aa8,0x1ac01ab8,0x1ac81ac8,0x1ad81ad0,0x1ae81ae0,0x1af81af0,0x1b081b00,0x1b181b10,0x1b281b20,0x1b381b30,0x1b481b40,0x1b581b50,0x1b681b60,0x1b781b70,0x1b801b80,0x1b901b88,
0x1bb01ba0,0x1bd01bc0,0x1be81be0,0x1c081bf8,0x1c181c10,0x1c201c20,0x1c301c28,0x1c401c38,0x1c501c48,0x1c601c58,0x1c701c68,0x1c801c78,0x1c901c88,0x1ca01c98,0x1cb01ca8,0x1cc01cb8,0x1cd01cc8,0x1ce01cd8,0x1cf01ce8,0x1d001cf8,0x1d081d00,0x1d181d10,0x1d281d20,0x1d381d30,0x1d481d40,0x1d581d50,0x1d681d60,0x1d781d70,0x1d881d80,0x1d981d90,0x1da81da0,0x1db81db0,
0x1dc81dc0,0x1dd81dd0,0x1de81de0,0x1df81df0,0x1e081e00,0x1e101e08,0x1e201e18,0x1e301e28,0x1e401e38,0x1e501e48,0x1e601e58,0x1e701e68,0x1e801e78,0x1e901e88,0x1ea01e98,0x1eb01ea8,0x1ec01eb8,0x1ed01ec8,0x1ee01ed8,0x1ef01ee8,0x1f001ef8,0x1f101f08,0x1f201f18,0x1f301f28,0x1f401f38,0x1f501f48,0x1f601f58,0x1f701f68,0x1f781f70,0x1f881f80,0x1f981f90,0x1fa81fa0,
0x1fc81fb8,0x1fe81fd8,0x20001ff8,0x20102008,0x20202018,0x20302028,0x20402038,0x20502048,0x20602058,0x20702068,0x20802078,0x20902088,0x20a02098,0x20b020a8,0x20c020b8,0x20d020c8,0x20e020d8,0x20f020e8,0x210020f8,0x21102108,0x21202118,0x21302128,0x21402138,0x21502148,0x21602158,0x21702168,0x21802178,0x21902188,0x21a02198,0x21b021a8,0x21c021b8,0x21d021c8,
0x21e021d8,0x21f021e8,0x220021f8,0x22102208,0x22202218,0x22302228,0x22402238,0x22502248,0x22602258,0x22702268,0x22802278,0x22902288,0x22a02298,0x22b022a8,0x22c822b8,0x22d822d0,0x22e822e0,0x22f822f0,0x23082300,0x23182310,0x23282320,0x23382330,0x23482340,0x23582350,0x23682360,0x23782370,0x23882380,0x23982390,0x23a823a0,0x23b823b0,0x23c823c0,0x23d823d0,
0x23f823e8,0x24102408,0x24202418,0x24302428,0x24402438,0x24502448,0x24602458,0x24702468,0x24802478,0x24902488,0x24a02498,0x24b024a8,0x24c024b8,0x24d824c8,0x24e824e0,0x24f824f0,0x25082500,0x25182510,0x25282520,0x25382530,0x25482540,0x25582550,0x25702560,0x25802578,0x25902588,0x25a02598,0x25b025a8,0x25c025b8,0x25d025c8,0x25e825d8,0x25f825f0,0x26082600,
0x26182610,0x26282620,0x26382630,0x26482640,0x26602658,0x26702668,0x26802678,0x26902688,0x26a02698,0x26b026a8,0x26c826c0,0x26d826d0,0x26e826e0,0x26f826f0,0x27082700,0x27202718,0x27302728,0x27402738,0x27502748,0x27602758,0x27782770,0x27882780,0x27982790,0x27a827a0,0x27c027b0,0x27d027c8,0x27e027d8,0x27f027e8,0x280027f8,0x28082808,0x28102810,0x28202818,
0x28302820,0x28402838,0x28502848,0x28602858,0x28782868,0x28882880,0x28982890,0x28a828a0,0x28c028b8,0x28d028c8,0x28e028d8,0x28f828f0,0x29082900,0x29182910,0x29302920,0x29402938,0x29502948,0x29682958,0x29782970,0x29882980,0x29a02998,0x29b029a8,0x29c029b8,0x29d829d0,0x29e829e0,0x2a0029f0,0x2a102a08,0x2a202a18,0x2a382a30,0x2a482a40,0x2a602a50,0x2a702a68,
0x2a802a78,0x2a982a90,0x2aa82aa0,0x2ac02ab8,0x2ad02ac8,0x2ae82ae0,0x2af82af0,0x2b102b00,0x2b202b18,0x2b382b28,0x2b482b40,0x2b602b50,0x2b702b68,0x2b882b78,0x2b982b90,0x2bb02ba0,0x2bc02bb8,0x2bd82bc8,0x2be82be0,0x2c002bf0,0x2c082c00,0x2c102c10,0x2c202c18,0x2c282c20,0x2c302c30,0x2c402c38,0x2c482c40,0x2c502c48,0x2c582c58,0x2c682c60,0x2c702c68,0x2c782c78,
0x2c902c80,0x2ca02c98,0x2cb82cb0,0x2cc82cc0,0x2ce02cd8,0x2cf82ce8,0x2d082d00,0x2d202d18,0x2d382d28,0x2d502d40,0x2d602d58,0x2d782d70,0x2d902d80,0x2da02d98,0x2db82db0,0x2dd02dc8,0x2de82dd8,0x2e002df0,0x2e102e08,0x2e282e20,0x2e402e38,0x2e582e48,0x2e702e60,0x2e882e78,0x2e982e90,0x2eb02ea8,0x2ec82ec0,0x2ee02ed8,0x2ef82ef0,0x2f102f00,0x2f282f18,0x2f402f30,
0x2f582f48,0x2f702f60,0x2f882f78,0x2fa02f90,0x2fb82fa8,0x2fd02fc0,0x2fe82fd8,0x30002ff0,0x30083008,0x30183010,0x30203020,0x30303028,0x30383038,0x30483040,0x30583050,0x30603058,0x30703068,0x30783070,0x30883080,0x30903090,0x30a03098,0x30a830a8,0x30b830b0,0x30c830c0,0x30d030c8,0x30e030d8,0x30e830e8,0x30f830f0,0x31083100,0x31103108,0x31203118,0x31283128,
0x31403138,0x31603150,0x31783168,0x31903188,0x31b031a0,0x31c831b8,0x31e031d8,0x320031f0,0x32183208,0x32303228,0x32503240,0x32683260,0x32883278,0x32a03290,0x32c032b0,0x32d832c8,0x32f832e8,0x33103300,0x33303320,0x33483340,0x33683358,0x33883378,0x33a03390,0x33c033b0,0x33d833d0,0x33f833e8,0x34083400,0x34183410,0x34283420,0x34383430,0x34483440,0x34583450,
0x34683460,0x34703468,0x34803478,0x34903488,0x34a03498,0x34b034a8,0x34c034b8,0x34d034c8,0x34e034d8,0x34f034e8,0x350034f8,0x35103508,0x35183518,0x35283520,0x35383530,0x35483540,0x35583550,0x35683560,0x35783570,0x35883580,0x35983590,0x35a835a0,0x35b835b0,0x35c835c0,0x35d835d0,0x35e835e0,0x35f835f0,0x36083600,0x36183610,0x36283620,0x36383630,0x36483640,
0x36683658,0x36883678,0x36a83698,0x36c836b8,0x36e836d8,0x370836f8,0x37283718,0x37503740,0x37703760,0x37903780,0x37b037a0,0x37d037c0,0x37f037e0,0x38083800,0x38183810,0x38303820,0x38403838,0x38503848,0x38603858,0x38703868,0x38803878,0x38903888,0x38a03898,0x38b038a8,0x38c838b8,0x38d838d0,0x38e838e0,0x38f838f0,0x39083900,0x39183910,0x39283920,0x39383930,
0x39483940,0x39603958,0x39703968,0x39803978,0x39903988,0x39a03998,0x39b039a8,0x39c039b8,0x39d039c8,0x39e039d8,0x39f839e8,0x3a083a00,0x3a183a10,0x3a283a20,0x3a383a30,0x3a483a40,0x3a583a50,0x3a683a60,0x3a783a70,0x3a883a80,0x3a983a90,0x3aa83aa0,0x3ab83ab0,0x3ad03ac8,0x3ae03ad8,0x3af03ae8,0x3b003af8,0x3b103b08,0x3b203b18,0x3b303b28,0x3b403b38,0x3b503b48,
0x3b683b58,0x3b903b80,0x3bb03ba0,0x3bd03bc0,0x3bf03be0,0x3c083c00,0x3c183c10,0x3c283c20,0x3c383c30,0x3c483c40,0x3c583c50,0x3c683c60,0x3c783c70,0x3c883c80,0x3c983c90,0x3ca83ca0,0x3cb03cb0,0x3cc03cb8,0x3cd03cc8,0x3ce03cd8,0x3cf03ce8,0x3d003cf8,0x3d103d08,0x3d203d18,0x3d303d28,0x3d403d38,0x3d503d48,0x3d603d58,0x3d703d68,0x3d783d70,0x3d883d80,0x3d983d90,
0x3da83da0,0x3db83db0,0x3dc83dc0,0x3dd83dd0,0x3de83de0,0x3df83df0,0x3e003e00,0x3e103e08,0x3e203e18,0x3e303e28,0x3e403e38,0x3e503e48,0x3e603e58,0x3e703e68,0x3e803e78,0x3e883e80,0x3e983e90,0x3ea83ea0,0x3eb83eb0,0x3ec83ec0,0x3ed83ed0,0x3ee83ee0,0x3ef83ef0,0x3f003ef8,0x3f103f08,0x3f203f18,0x3f303f28,0x3f403f38,0x3f503f48,0x3f603f58,0x3f703f68,0x3f783f70,
0x3f983f88,0x3fb03fa8,0x3fd03fc0,0x3ff03fe0,0x40084000,0x40184010,0x40284020,0x40304030,0x40404038,0x40504048,0x40604058,0x40704068,0x40804078,0x40904088,0x40a04098,0x40a840a8,0x40b840b0,0x40c840c0,0x40d840d0,0x40e840e0,0x40f840f0,0x41084100,0x41184110,0x41204120,0x41304128,0x41404138,0x41504148,0x41604158,0x41704168,0x41804178,0x41904188,0x41984198,
0x41a841a0,0x41b841b0,0x41c841c0,0x41d841d0,0x41e841e0,0x41f841f0,0x42084200,0x42104210,0x42204218,0x42304228,0x42404238,0x42504248,0x42604258,0x42704268,0x42804278,0x42884280,0x42984290,0x42a842a0,0x42b842b0,0x42c842c0,0x42d842d0,0x42e842e0,0x42f842f0,0x430042f8,0x43104308,0x43204318,0x43304328,0x43404338,0x43504348,0x43604358,0x43704368,0x43784370,
0x43984388,0x43b043a8,0x43d043c0,0x43f043e0,0x44084400,0x44184410,0x44284420,0x44304430,0x44404438,0x44504448,0x44604458,0x44704468,0x44804478,0x44904488,0x44a04498,0x44a844a8,0x44b844b0,0x44c844c0,0x44d844d0,0x44e844e0,0x44f844f0,0x45084500,0x45184510,0x45204520,0x45304528,0x45404538,0x45504548,0x45604558,0x45704568,0x45804578,0x45904588,0x45984598,
0x45a845a0,0x45b845b0,0x45c845c0,0x45d845d0,0x45e845e0,0x45f845f0,0x46084600,0x46104610,0x46204618,0x46304628,0x46404638,0x46504648,0x46604658,0x46704668,0x46804678,0x46884680,0x46984690,0x46a846a0,0x46b846b0,0x46c846c0,0x46d846d0,0x46e846e0,0x46f846f0,0x470046f8,0x47104708,0x47204718,0x47304728,0x47404738,0x47504748,0x47604758,0x47704768,0x47784770,
0x47984788,0x47b047a8,0x47d047c0,0x47f047e0,0x48084800,0x48184810,0x48284820,0x48304830,0x48404838,0x48504848,0x48604858,0x48704868,0x48804878,0x48904888,0x48a04898,0x48a848a8,0x48b848b0,0x48c848c0,0x48d848d0,0x48e848e0,0x48f848f0,0x49084900,0x49184910,0x49204920,0x49304928,0x49404938,0x49504948,0x49604958,0x49704968,0x49804978,0x49904988,0x49984998,
0x49a849a0,0x49b849b0,0x49c849c0,0x49d849d0,0x49e849e0,0x49f849f0,0x4a084a00,0x4a104a10,0x4a204a18,0x4a304a28,0x4a404a38,0x4a504a48,0x4a604a58,0x4a704a68,0x4a804a78,0x4a884a80,0x4a984a90,0x4aa84aa0,0x4ab84ab0,0x4ac84ac0,0x4ad84ad0,0x4ae84ae0,0x4af84af0,0x4b004af8,0x4b104b08,0x4b204b18,0x4b304b28,0x4b404b38,0x4b504b48,0x4b604b58,0x4b704b68,0x4b784b70,
0x4b984b88,0x4bb04ba8,0x4bd04bc0,0x4bf04be0,0x4c084c00,0x4c184c10,0x4c284c20,0x4c304c30,0x4c404c38,0x4c504c48,0x4c604c58,0x4c704c68,0x4c804c78,0x4c904c88,0x4ca04c98,0x4ca84ca8,0x4cb84cb0,0x4cc84cc0,0x4cd84cd0,0x4ce84ce0,0x4cf84cf0,0x4d084d00,0x4d184d10,0x4d204d20,0x4d304d28,0x4d404d38,0x4d504d48,0x4d604d58,0x4d704d68,0x4d804d78,0x4d904d88,0x4d984d98,
0x4da84da0,0x4db84db0,0x4dc84dc0,0x4dd84dd0,0x4de84de0,0x4df84df0,0x4e084e00,0x4e104e10,0x4e204e18,0x4e304e28,0x4e404e38,0x4e504e48,0x4e604e58,0x4e704e68,0x4e804e78,0x4e884e80,0x4e984e90,0x4ea84ea0,0x4eb84eb0,0x4ec84ec0,0x4ed84ed0,0x4ee84ee0,0x4ef84ef0,0x4f004ef8,0x4f104f08,0x4f204f18,0x4f304f28,0x4f404f38,0x4f504f48,0x4f604f58,0x4f704f68,0x4f784f70,
0x4f984f88,0x4fb04fa8,0x4fd04fc0,0x4ff04fe0,0x50085000,0x50185010,0x50285020,0x50305030,0x50405038,0x50505048,0x50605058,0x50705068,0x50805078,0x50905088,0x50a05098,0x50a850a8,0x50b850b0,0x50c850c0,0x50d850d0,0x50e850e0,0x50f850f0,0x51085100,0x51185110,0x51205120,0x51305128,0x51405138,0x51505148,0x51605158,0x51705168,0x51805178,0x51905188,0x51985198,
0x51a851a0,0x51b851b0,0x51c851c0,0x51d851d0,0x51e851e0,0x51f851f0,0x52085200,0x52105210,0x52205218,0x52305228,0x52405238,0x52505248,0x52605258,0x52705268,0x52805278,0x52885280,0x52985290,0x52a852a0,0x52b852b0,0x52c852c0,0x52d852d0,0x52e852e0,0x52f852f0,0x530052f8,0x53105308,0x53205318,0x53305328,0x53405338,0x53505348,0x53605358,0x53705368,0x53785370,
0,0,0,0,0,0,0,0,0,0,0,0,0,0,0,0,0,0,0,0,0,0,0,0,0,0,0,0,0,0,0,0,
0,0,0,0,0,0,0,0,0,0,0,0,0,0,0,0,0,0,0,0,0,0,0,0,0,0,0,0,0,0,0,0,
0,0,0,0,0,0,0,0,0,0,0,0,0,0,0,0,0,0,0,0,0,0,0,0,0,0,0,0,0,0,0,0,
0,0,0,0,0,0,0,0,0,0,0,0,0,0,0,0,0,0,0,0,0,0,0,0,0,0,0,0,0,0,0,0,
0x80008000,0x80008000,0x80008000,0x80008000,0x80008000,0x80008000,0x80008000,0x80008000,0x80008000,0x80008000,0x80008000,0x80008000,0x80008000,0x80008000,0x80008000,0x80008000,0x80008000,0x80008000,0x80008000,0x80008000,0x80008000,0x80008000,0x80008000,0x80008000,0x80008000,0x80008000,0x80008000,0x80008000,0x80008000,0x80008000,0x80008000,0x80008000,
0x80008000,0x80008000,0x80008000,0x80008000,0x80008000,0x80008000,0x80008000,0x80008000,0x80008000,0x80008000,0x80008000,0x80008000,0x80008000,0x80008000,0x80008000,0x80008000,0x80008000,0x80008000,0x80008000,0x80008000,0x80008000,0x80008000,0x80008000,0x80008000,0x80008000,0x80008000,0x80008000,0x80008000,0x80008000,0x80008000,0x80008000,0x80008000,
0x80008000,0x80008000,0x80008000,0x80008000,0x80008000,0x80008000,0x80008000,0x80008000,0x80008000,0x80008000,0x80008000,0x80008000,0x80008000,0x80008000,0x80008000,0x80008000,0x80008000,0x80008000,0x80008000,0x80008000,0x80008000,0x80008000,0x80008000,0x80008000,0x80008000,0x80008000,0x80008000,0x80008000,0x80008000,0x80008000,0x80008000,0x80008000,
0x80008000,0x80008000,0x80008000,0x80008000,0x80008000,0x80008000,0x80008000,0x80008000,0x80008000,0x80008000,0x80008000,0x80008000,0x80008000,0x80008000,0x80008000,0x80008000,0x80008000,0x80008000,0x80008000,0x80008000,0x80008000,0x80008000,0x80008000,0x80008000,0x80008000,0x80008000,0x80008000,0x80008000,0x80008000,0x80008000,0x80008000,0x80008000,
0x80008000,0x80008000,0x80008000,0x80008000,0x80008000,0x80008000,0x80008000,0x80008000,0x80008000,0x80008000,0x80008000,0x80008000,0x80008000,0x80008000,0x80008000,0x80008000,0x80008000,0x80008000,0x80008000,0x80008000,0x80008000,0x80008000,0x80008000,0x80008000,0x80008000,0x80008000,0x80008000,0x80008000,0x80008000,0x80008000,0x80008000,0x80008000,
0x80008000,0x80008000,0x80008000,0x80008000,0x80008000,0x80008000,0x80008000,0x80008000,0x80008000,0x80008000,0x80008000,0x80008000,0x80008000,0x80008000,0x80008000,0x80008000,0x80008000,0x80008000,0x80008000,0x80008000,0x80008000,0x80008000,0x80008000,0x80008000,0x80008000,0x80008000,0x80008000,0x80008000,0x80008000,0x80008000,0x80008000,0x80008000,
0x80008000,0x80008000,0x80008000,0x80008000,0x80008000,0x80008000,0x80008000,0x80008000,0x80008000,0x80008000,0x80008000,0x80008000,0x80008000,0x80008000,0x80008000,0x80008000,0x80008000,0x80008000,0x80008000,0x80008000,0x80008000,0x80008000,0x80008000,0x80008000,0x80008000,0x80008000,0x80008000,0x80008000,0x80008000,0x80008000,0x80008000,0x80008000,
0x80008000,0x80008000,0x80008000,0x80008000,0x80008000,0x80008000,0x80008000,0x80008000,0x80008000,0x80008000,0x80008000,0x80008000,0x80008000,0x80008000,0x80008000,0x80008000,0x80008000,0x80008000,0x80008000,0x80008000,0x80008000,0x80008000,0x80008000,0x80008000,0x80008000,0x80008000,0x80008000,0x80008000,0x80008000,0x80008000,0x80008000,0x80008000,
0x80008000,0x80008000,0x80008000,0x80008000,0x80008000,0x80008000,0x80008000,0x80008000,0x80008000,0x80008000,0x80008000,0x80008000,0x80008000,0x80008000,0x80008000,0x80008000,0x80008000,0x80008000,0x80008000,0x80008000,0x80008000,0x80008000,0x80008000,0x80008000,0x80008000,0x80008000,0x80008000,0x80008000,0x80008000,0x80008000,0x80008000,0x80008000,
0x80008000,0x80008000,0x80008000,0x80008000,0x80008000,0x80008000,0x80008000,0x80008000,0x80008000,0x80008000,0x80008000,0x80008000,0x80008000,0x80008000,0x80008000,0x80008000,0x80008000,0x80008000,0x80008000,0x80008000,0x80008000,0x80008000,0x80008000,0x80008000,0x80008000,0x80008000,0x80008000,0x80008000,0x80008000,0x80008000,0x80008000,0x80008000,
0x80008000,0x80008000,0x80008000,0x80008000,0x80008000,0x80008000,0x80008000,0x80008000,0x80008000,0x80008000,0x80008000,0x80008000,0x80008000,0x80008000,0x80008000,0x80008000,0x80008000,0x80008000,0x80008000,0x80008000,0x80008000,0x80008000,0x80008000,0x80008000,0x80008000,0x80008000,0x80008000,0x80008000,0x80008000,0x80008000,0x80008000,0x80008000,
0x80008000,0x80008000,0x80008000,0x80008000,0x80008000,0x80008000,0x80008000,0x80008000,0x80008000,0x80008000,0x80008000,0x80008000,0x80008000,0x80008000,0x80008000,0x80008000,0x80008000,0x80008000,0x80008000,0x80008000,0x80008000,0x80008000,0x80008000,0x80008000,0x80008000,0x80008000,0x80008000,0x80008000,0x80008000,0x80008000,0x80008000,0x80008000,
0x80008000,0x80008000,0x80008000,0x80008000,0x80008000,0x80008000,0x80008000,0x80008000,0x80008000,0x80008000,0x80008000,0x80008000,0x80008000,0x80008000,0x80008000,0x80008000,0x80008000,0x80008000,0x80008000,0x80008000,0x80008000,0x80008000,0x80008000,0x80008000,0x80008000,0x80008000,0x80008000,0x80008000,0x80008000,0x80008000,0x80008000,0x80008000,
0x80008000,0x80008000,0x80008000,0x80008000,0x80008000,0x80008000,0x80008000,0x80008000,0x80008000,0x80008000,0x80008000,0x80008000,0x80008000,0x80008000,0x80008000,0x80008000,0x80008000,0x80008000,0x80008000,0x80008000,0x80008000,0x80008000,0x80008000,0x80008000,0x80008000,0x80008000,0x80008000,0x80008000,0x80008000,0x80008000,0x80008000,0x80008000,
0x80008000,0x80008000,0x80008000,0x80008000,0x80008000,0x80008000,0x80008000,0x80008000,0x80008000,0x80008000,0x80008000,0x80008000,0x80008000,0x80008000,0x80008000,0x80008000,0x80008000,0x80008000,0x80008000,0x80008000,0x80008000,0x80008000,0x80008000,0x80008000,0x80008000,0x80008000,0x80008000,0x80008000,0x80008000,0x80008000,0x80008000,0x80008000,
0x80008000,0x80008000,0x80008000,0x80008000,0x80008000,0x80008000,0x80008000,0x80008000,0x80008000,0x80008000,0x80008000,0x80008000,0x80008000,0x80008000,0x80008000,0x80008000,0x80008000,0x80008000,0x80008000,0x80008000,0x80008000,0x80008000,0x80008000,0x80008000,0x80008000,0x80008000,0x80008000,0x80008000,0x80008000,0x80008000,0x80008000,0x80008000,
0x80008000,0x80008000,0x80008000,0x80008000,0x80008000,0x80008000,0x80008000,0x80008000,0x80008000,0x80008000,0x80008000,0x80008000,0x80008000,0x80008000,0x80008000,0x80008000,0x80008000,0x80008000,0x80008000,0x80008000,0x80008000,0x80008000,0x80008000,0x80008000,0x80008000,0x80008000,0x80008000,0x80008000,0x80008000,0x80008000,0x80008000,0x80008000,
0x80008000,0x80008000,0x80008000,0x80008000,0x80008000,0x80008000,0x80008000,0x80008000,0x80008000,0x80008000,0x80008000,0x80008000,0x80008000,0x80008000,0x80008000,0x80008000,0x80008000,0x80008000,0x80008000,0x80008000,0x80008000,0x80008000,0x80008000,0x80008000,0x80008000,0x80008000,0x80008000,0x80008000,0x80008000,0x80008000,0x80008000,0x80008000,
0x80008000,0x80008000,0x80008000,0x80008000,0x80008000,0x80008000,0x80008000,0x80008000,0x80008000,0x80008000,0x80008000,0x80008000,0x80008000,0x80008000,0x80008000,0x80008000,0x80008000,0x80008000,0x80008000,0x80008000,0x80008000,0x80008000,0x80008000,0x80008000,0x80008000,0x80008000,0x80008000,0x80008000,0x80008000,0x80008000,0x80008000,0x80008000,
0x80008000,0x80008000,0x80008000,0x80008000,0x80008000,0x80008000,0x80008000,0x80008000,0x80008000,0x80008000,0x80008000,0x80008000,0x80008000,0x80008000,0x80008000,0x80008000,0x80008000,0x80008000,0x80008000,0x80008000,0x80008000,0x80008000,0x80008000,0x80008000,0x80008000,0x80008000,0x80008000,0x80008000,0x80008000,0x80008000,0x80008000,0x80008000,
0x80008000,0x80008000,0x80008000,0x80008000,0x80008000,0x80008000,0x80008000,0x80008000,0x80008000,0x80008000,0x80008000,0x80008000,0x80008000,0x80008000,0x80008000,0x80008000,0x80008000,0x80008000,0x80008000,0x80008000,0x80008000,0x80008000,0x80008000,0x80008000,0x80008000,0x80008000,0x80008000,0x80008000,0x80008000,0x80008000,0x80008000,0x80008000,
0x80008000,0x80008000,0x80008000,0x80008000,0x80008000,0x80008000,0x80008000,0x80008000,0x80008000,0x80008000,0x80008000,0x80008000,0x80008000,0x80008000,0x80008000,0x80008000,0x80008000,0x80008000,0x80008000,0x80008000,0x80008000,0x80008000,0x80008000,0x80008000,0x80008000,0x80008000,0x80008000,0x80008000,0x80008000,0x80008000,0x80008000,0x80008000,
0x80008000,0x80008000,0x80008000,0x80008000,0x80008000,0x80008000,0x80008000,0x80008000,0x80008000,0x80008000,0x80008000,0x80008000,0x80008000,0x80008000,0x80008000,0x80008000,0x80008000,0x80008000,0x80008000,0x80008000,0x80008000,0x80008000,0x80008000,0x80008000,0x80008000,0x80008000,0x80008000,0x80008000,0x80008000,0x80008000,0x80008000,0x80008000,
0x80008000,0x80008000,0x80008000,0x80008000,0x80008000,0x80008000,0x80008000,0x80008000,0x80008000,0x80008000,0x80008000,0x80008000,0x80008000,0x80008000,0x80008000,0x80008000,0x80008000,0x80008000,0x80008000,0x80008000,0x80008000,0x80008000,0x80008000,0x80008000,0x80008000,0x80008000,0x80008000,0x80008000,0x80008000,0x80008000,0x80008000,0x80008000,
0x80008000,0x80008000,0x80008000,0x80008000,0x80008000,0x80008000,0x80008000,0x80008000,0x80008000,0x80008000,0x80008000,0x80008000,0x80008000,0x80008000,0x80008000,0x80008000,0x80008000,0x80008000,0x80008000,0x80008000,0x80008000,0x80008000,0x80008000,0x80008000,0x80008000,0x80008000,0x80008000,0x80008000,0x80008000,0x80008000,0x80008000,0x80008000,
0x80008000,0x80008000,0x80008000,0x80008000,0x80008000,0x80008000,0x80008000,0x80008000,0x80008000,0x80008000,0x80008000,0x80008000,0x80008000,0x80008000,0x80008000,0x80008000,0x80008000,0x80008000,0x80008000,0x80008000,0x80008000,0x80008000,0x80008000,0x80008000,0x80008000,0x80008000,0x80008000,0x80008000,0x80008000,0x80008000,0x80008000,0x80008000,
0x80008000,0x80008000,0x80008000,0x80008000,0x80008000,0x80008000,0x80008000,0x80008000,0x80008000,0x80008000,0x80008000,0x80008000,0x80008000,0x80008000,0x80008000,0x80008000,0x80008000,0x80008000,0x80008000,0x80008000,0x80008000,0x80008000,0x80008000,0x80008000,0x80008000,0x80008000,0x80008000,0x80008000,0x80008000,0x80008000,0x80008000,0x80008000,
0x80008000,0x80008000,0x80008000,0x80008000,0x80008000,0x80008000,0x80008000,0x80008000,0x80008000,0x80008000,0x80008000,0x80008000,0x80008000,0x80008000,0x80008000,0x80008000,0x80008000,0x80008000,0x80008000,0x80008000,0x80008000,0x80008000,0x80008000,0x80008000,0x80008000,0x80008000,0x80008000,0x80008000,0x80008000,0x80008000,0x80008000,0x80008000,
0x80008000,0x80008000,0x80008000,0x80008000,0x80008000,0x80008000,0x80008000,0x80008000,0x80008000,0x80008000,0x80008000,0x80008000,0x80008000,0x80008000,0x80008000,0x80008000,0x80008000,0x80008000,0x80008000,0x80008000,0x80008000,0x80008000,0x80008000,0x80008000,0x80008000,0x80008000,0x80008000,0x80008000,0x80008000,0x80008000,0x80008000,0x80008000,
0x80008000,0x80008000,0x80008000,0x80008000,0x80008000,0x80008000,0x80008000,0x80008000,0x80008000,0x80008000,0x80008000,0x80008000,0x80008000,0x80008000,0x80008000,0x80008000,0x80008000,0x80008000,0x80008000,0x80008000,0x80008000,0x80008000,0x80008000,0x80008000,0x80008000,0x80008000,0x80008000,0x80008000,0x80008000,0x80008000,0x80008000,0x80008000,
0x80008000,0x80008000,0x80008000,0x80008000,0x80008000,0x80008000,0x80008000,0x80008000,0x80008000,0x80008000,0x80008000,0x80008000,0x80008000,0x80008000,0x80008000,0x80008000,0x80008000,0x80008000,0x80008000,0x80008000,0x80008000,0x80008000,0x80008000,0x80008000,0x80008000,0x80008000,0x80008000,0x80008000,0x80008000,0x80008000,0x80008000,0x80008000,
0x80008000,0x80008000,0x80008000,0x80008000,0x80008000,0x80008000,0x80008000,0x80008000,0x80008000,0x80008000,0x80008000,0x80008000,0x80008000,0x80008000,0x80008000,0x80008000,0x80008000,0x80008000,0x80008000,0x80008000,0x80008000,0x80008000,0x80008000,0x80008000,0x80008000,0x80008000,0x80008000,0x80008000,0x80008000,0x80008000,0x80008000,0x80008000,
0x80008000,0x80008000,0x80008000,0x80008000,0x80018000,0x80018001,0x80018001,0x80018001,0x80018001,0x80018001,0x80018001,0x80018001,0x80018001,0x80018001,0x80018001,0x80018001,0x80018001,0x80018001,0x80018001,0x80018001,0x80018001,0x80018001,0x80018001,0x80018001,0x80018001,0x80018001,0x80018001,0x80018001,0x80018001,0x80018001,0x80018001,0x80018001,
0x80018001,0x80018001,0x80018001,0x80018001,0x80018001,0x80018001,0x80018001,0x80018001,0x80018001,0x80018001,0x80018001,0x80018001,0x80018001,0x80018001,0x80018001,0x80018001,0x80018001,0x80018001,0x80018001,0x80018001,0x80018001,0x80018001,0x80018001,0x80018001,0x80018001,0x80018001,0x80018001,0x80018001,0x80018001,0x80018001,0x80018001,0x80018001,
0x80018001,0x80018001,0x80018001,0x80018001,0x80018001,0x80018001,0x80018001,0x80018001,0x80018001,0x80018001,0x80018001,0x80018001,0x80018001,0x80018001,0x80018001,0x80018001,0x80018001,0x80018001,0x80018001,0x80018001,0x80018001,0x80018001,0x80018001,0x80018001,0x80018001,0x80018001,0x80018001,0x80018001,0x80018001,0x80018001,0x80018001,0x80018001,
0x80018001,0x80018001,0x80018001,0x80018001,0x80018001,0x80018001,0x80028002,0x80028002,0x80028002,0x80028002,0x80028002,0x80028002,0x80028002,0x80028002,0x80028002,0x80028002,0x80028002,0x80028002,0x80028002,0x80028002,0x80028002,0x80028002,0x80028002,0x80028002,0x80028002,0x80028002,0x80028002,0x80028002,0x80028002,0x80028002,0x80028002,0x80028002,
0x80028002,0x80028002,0x80028002,0x80028002,0x80028002,0x80028002,0x80028002,0x80028002,0x80028002,0x80028002,0x80028002,0x80028002,0x80028002,0x80028002,0x80028002,0x80028002,0x80028002,0x80028002,0x80028002,0x80028002,0x80028002,0x80038002,0x80038003,0x80038003,0x80038003,0x80038003,0x80038003,0x80038003,0x80038003,0x80038003,0x80038003,0x80038003,
0x80038003,0x80038003,0x80038003,0x80038003,0x80038003,0x80038003,0x80038003,0x80038003,0x80038003,0x80038003,0x80038003,0x80038003,0x80038003,0x80038003,0x80038003,0x80038003,0x80038003,0x80038003,0x80038003,0x80038003,0x80038003,0x80038003,0x80038003,0x80048003,0x80048004,0x80048004,0x80048004,0x80048004,0x80048004,0x80048004,0x80048004,0x80048004,
0x80048004,0x80048004,0x80048004,0x80048004,0x80048004,0x80048004,0x80048004,0x80048004,0x80048004,0x80048004,0x80048004,0x80048004,0x80048004,0x80058005,0x80058005,0x80058005,0x80058005,0x80058005,0x80058005,0x80058005,0x80058005,0x80058005,0x80058005,0x80058005,0x80058005,0x80058005,0x80058005,0x80058005,0x80058005,0x80068005,0x80068006,0x80068006,
0x80068006,0x80068006,0x80068006,0x80068006,0x80068006,0x80068006,0x80068006,0x80068006,0x80068006,0x80068006,0x80068006,0x80068006,0x80068006,0x80068006,0x80068006,0x80078006,0x80078007,0x80078007,0x80078007,0x80078007,0x80078007,0x80078007,0x80078007,0x80078007,0x80078007,0x80078007,0x80078007,0x80078007,0x80078007,0x80078007,0x80078007,0x80078007,
0x80088008,0x80088008,0x80088008,0x80088008,0x80088008,0x80088008,0x80088008,0x80088008,0x80088008,0x80098009,0x80098009,0x80098009,0x80098009,0x80098009,0x80098009,0x80098009,0x80098009,0x800a800a,0x800a800a,0x800a800a,0x800a800a,0x800a800a,0x800a800a,0x800a800a,0x800a800a,0x800a800a,0x800b800b,0x800b800b,0x800b800b,0x800b800b,0x800b800b,0x800b800b,
0x800b800b,0x800b800b,0x800c800c,0x800c800c,0x800c800c,0x800c800c,0x800c800c,0x800c800c,0x800c800c,0x800c800c,0x800c800c,0x800d800d,0x800d800d,0x800d800d,0x800d800d,0x800d800d,0x800d800d,0x800d800d,0x800d800d,0x800e800e,0x800e800e,0x800e800e,0x800e800e,0x800e800e,0x800e800e,0x800e800e,0x800e800e,0x800e800e,0x800f800f,0x800f800f,0x800f800f,0x800f800f,
0x800f800f,0x800f800f,0x80108010,0x80108010,0x80108010,0x80108010,0x80118010,0x80118011,0x80118011,0x80118011,0x80128011,0x80128012,0x80128012,0x80128012,0x80128012,0x80138013,0x80138013,0x80138013,0x80138013,0x80148014,0x80148014,0x80148014,0x80148014,0x80148014,0x80158015,0x80158015,0x80158015,0x80168015,0x80168016,0x80168016,0x80168016,0x80168016,
0x80178016,0x80178017,0x80178017,0x80178017,0x80188018,0x80188018,0x80188018,0x80188018,0x80188018,0x80198019,0x80198019,0x80198019,0x801a8019,0x801a801a,0x801a801a,0x801a801a,0x801a801a,0x801b801a,0x801b801b,0x801b801b,0x801b801b,0x801c801c,0x801c801c,0x801c801c,0x801c801c,0x801c801c,0x801d801d,0x801d801d,0x801d801d,0x801e801d,0x801e801e,0x801e801e,
0x801e801e,0x801f801f,0x801f801f,0x80208020,0x80208020,0x80218020,0x80218021,0x80228022,0x80228022,0x80228022,0x80238023,0x80248023,0x80248024,0x80248024,0x80258025,0x80258025,0x80268026,0x80268026,0x80278026,0x80278027,0x80288028,0x80288028,0x80298028,0x80298029,0x802a8029,0x802a802a,0x802a802a,0x802b802b,0x802c802b,0x802c802c,0x802c802c,0x802d802d,
0x802d802d,0x802e802e,0x802e802e,0x802f802e,0x802f802f,0x80308030,0x80308030,0x80308030,0x80318031,0x80328031,0x80328032,0x80328032,0x80338033,0x80348033,0x80348034,0x80348034,0x80358034,0x80358035,0x80368036,0x80368036,0x80378036,0x80378037,0x80388038,0x80388038,0x80388038,0x80398039,0x803a8039,0x803a803a,0x803a803a,0x803b803b,0x803c803b,0x803c803c,
0x803d803c,0x803e803d,0x803e803e,0x8040803f,0x80408040,0x80428041,0x80428042,0x80438043,0x80448044,0x80458044,0x80468046,0x80478046,0x80488048,0x80498048,0x804a804a,0x804a804a,0x804c804b,0x804c804c,0x804e804d,0x804e804e,0x8050804f,0x80508050,0x80528051,0x80528052,0x80538052,0x80548054,0x80558054,0x80568056,0x80578056,0x80588058,0x80598058,0x805a805a,
0x805a805a,0x805c805b,0x805c805c,0x805e805d,0x805e805e,0x8060805f,0x80608060,0x80618061,0x80628062,0x80638062,0x80648064,0x80658064,0x80668066,0x80678066,0x80688068,0x80688068,0x806a8069,0x806a806a,0x806c806b,0x806c806c,0x806e806d,0x806e806e,0x8070806f,0x80708070,0x80718070,0x80728072,0x80738072,0x80748074,0x80758074,0x80768076,0x80778076,0x80788077,
0x807a8078,0x807b807a,0x807d807c,0x807f807e,0x80818080,0x80838082,0x80858084,0x80868086,0x80888087,0x808a8089,0x808c808b,0x808e808d,0x8090808f,0x80928091,0x80948093,0x80958095,0x80978096,0x80998098,0x809b809a,0x809d809c,0x809f809e,0x80a180a0,0x80a380a2,0x80a480a4,0x80a680a5,0x80a880a7,0x80aa80a9,0x80ac80ab,0x80ae80ad,0x80b080af,0x80b280b1,0x80b380b3,
0x80b580b4,0x80b780b6,0x80b980b8,0x80bb80ba,0x80bd80bc,0x80bf80be,0x80c180c0,0x80c280c1,0x80c480c3,0x80c680c5,0x80c880c7,0x80ca80c9,0x80cc80cb,0x80ce80cd,0x80d080cf,0x80d180d0,0x80d380d2,0x80d580d4,0x80d780d6,0x80d980d8,0x80db80da,0x80dd80dc,0x80df80de,0x80e080df,0x80e280e1,0x80e480e3,0x80e680e5,0x80e880e7,0x80ea80e9,0x80ec80eb,0x80ee80ed,0x80ef80ee,
0x80f380f1,0x80f680f5,0x80fa80f8,0x80fe80fc,0x81028100,0x81068104,0x810a8108,0x810c810c,0x8110810e,0x81148112,0x81188116,0x811c811a,0x8120811e,0x81248122,0x81288126,0x812a812a,0x812e812c,0x81328130,0x81368134,0x813a8138,0x813e813c,0x81428140,0x81468144,0x81488148,0x814c814a,0x8150814e,0x81548152,0x81588156,0x815c815a,0x8160815e,0x81648162,0x81668164,
0x816a8168,0x816e816c,0x81728170,0x81768174,0x817a8178,0x817e817c,0x81828180,0x81848182,0x81888186,0x818c818a,0x8190818e,0x81948192,0x81988196,0x819c819a,0x81a0819e,0x81a281a0,0x81a681a4,0x81aa81a8,0x81ae81ac,0x81b281b0,0x81b681b4,0x81ba81b8,0x81be81bc,0x81c081be,0x81c481c2,0x81c881c6,0x81cc81ca,0x81d081ce,0x81d481d2,0x81d881d6,0x81dc81da,0x81de81dc,
0x81e681e2,0x81ec81ea,0x81f481f0,0x81fc81f8,0x82048200,0x820c8208,0x82148210,0x82188218,0x8220821c,0x82288224,0x8230822c,0x82388234,0x8240823c,0x82488244,0x8250824c,0x82548254,0x825c8258,0x82648260,0x826c8268,0x82748270,0x827c8278,0x82848280,0x828c8288,0x8290828c,0x82988294,0x82a0829c,0x82a882a4,0x82b082ac,0x82b882b4,0x82c082bc,0x82c882c4,0x82cc82c8,
0x82d482d0,0x82dc82d8,0x82e482e0,0x82ec82e8,0x82f482f0,0x82fc82f8,0x83048300,0x83088304,0x8310830c,0x83188314,0x8320831c,0x83288324,0x8330832c,0x83388334,0x8340833c,0x83448340,0x834c8348,0x83548350,0x835c8358,0x83648360,0x836c8368,0x83748370,0x837c8378,0x8380837c,0x83888384,0x8390838c,0x83988394,0x83a0839c,0x83a883a4,0x83b083ac,0x83b883b4,0x83bc83b8,
0x83cc83c4,0x83d883d4,0x83e883e0,0x83f883f0,0x84088400,0x84188410,0x84288420,0x84308430,0x84408438,0x84508448,0x84608458,0x84708468,0x84808478,0x84908488,0x84a08498,0x84a884a0,0x84b884b0,0x84c884c0,0x84d884d0,0x84e884e0,0x84f884f0,0x85088500,0x85188510,0x85208518,0x85308528,0x85408538,0x85508548,0x85608558,0x85708568,0x85808578,0x85908588,0x85988590,
0x85a885a0,0x85b885b0,0x85c885c0,0x85d885d0,0x85e885e0,0x85f885f0,0x86088600,0x86108608,0x86208618,0x86308628,0x86408638,0x86508648,0x86608658,0x86708668,0x86788678,0x86888680,0x86988690,0x86a886a0,0x86b886b0,0x86c886c0,0x86d886d0,0x86e886e0,0x86f086f0,0x870086f8,0x87108708,0x87208718,0x87308728,0x87408738,0x87508748,0x87608758,0x87688768,0x87788770,
0x87988788,0x87b087a0,0x87d087c0,0x87f087e0,0x88088800,0x88188810,0x88288820,0x88308828,0x88408838,0x88508848,0x88608858,0x88708868,0x88808878,0x88908888,0x88a08898,0x88a888a0,0x88b888b0,0x88c888c0,0x88d888d0,0x88e888e0,0x88f888f0,0x89088900,0x89108910,0x89208918,0x89308928,0x89408938,0x89508948,0x89608958,0x89708968,0x89808978,0x89888988,0x89988990,
0x89a889a0,0x89b889b0,0x89c889c0,0x89d889d0,0x89e889e0,0x89f889f0,0x8a008a00,0x8a108a08,0x8a208a18,0x8a308a28,0x8a408a38,0x8a508a48,0x8a608a58,0x8a708a68,0x8a788a70,0x8a888a80,0x8a988a90,0x8aa88aa0,0x8ab88ab0,0x8ac88ac0,0x8ad88ad0,0x8ae88ae0,0x8af08ae8,0x8b008af8,0x8b108b08,0x8b208b18,0x8b308b28,0x8b408b38,0x8b508b48,0x8b588b58,0x8b688b60,0x8b788b70,
0x8b988b88,0x8bb08ba0,0x8bd08bc0,0x8bf08be0,0x8c088c00,0x8c188c10,0x8c208c20,0x8c308c28,0x8c408c38,0x8c508c48,0x8c608c58,0x8c708c68,0x8c808c78,0x8c908c88,0x8c988c98,0x8ca88ca0,0x8cb88cb0,0x8cc88cc0,0x8cd88cd0,0x8ce88ce0,0x8cf88cf0,0x8d088d00,0x8d108d08,0x8d208d18,0x8d308d28,0x8d408d38,0x8d508d48,0x8d608d58,0x8d708d68,0x8d788d78,0x8d888d80,0x8d988d90,
0x8da88da0,0x8db88db0,0x8dc88dc0,0x8dd88dd0,0x8de88de0,0x8df08df0,0x8e008df8,0x8e108e08,0x8e208e18,0x8e308e28,0x8e408e38,0x8e508e48,0x8e608e58,0x8e688e60,0x8e788e70,0x8e888e80,0x8e988e90,0x8ea88ea0,0x8eb88eb0,0x8ec88ec0,0x8ed08ed0,0x8ee08ed8,0x8ef08ee8,0x8f008ef8,0x8f108f08,0x8f208f18,0x8f308f28,0x8f408f38,0x8f488f48,0x8f588f50,0x8f688f60,0x8f788f70,
0x8f908f80,0x8fb08fa0,0x8fd08fc0,0x8ff08fe0,0x90088ff8,0x90109010,0x90209018,0x90309028,0x90409038,0x90509048,0x90609058,0x90709068,0x90809078,0x90889088,0x90989090,0x90a890a0,0x90b890b0,0x90c890c0,0x90d890d0,0x90e890e0,0x90f890f0,0x910090f8,0x91109108,0x91209118,0x91309128,0x91409138,0x91509148,0x91609158,0x91689168,0x91789170,0x91889180,0x91989190,
0x91a891a0,0x91b891b0,0x91c891c0,0x91d091d0,0x91e091d8,0x91f091e8,0x920091f8,0x92109208,0x92209218,0x92309228,0x92409238,0x92489240,0x92589250,0x92689260,0x92789270,0x92889280,0x92989290,0x92a892a0,0x92b092b0,0x92c092b8,0x92d092c8,0x92e092d8,0x92f092e8,0x930092f8,0x93109308,0x93189318,0x93289320,0x93389330,0x93489340,0x93589350,0x93689360,0x93789370,
0x93909380,0x93b093a0,0x93c893c0,0x93e893d8,0x940093f8,0x94109408,0x94209418,0x94309428,0x94409438,0x94509448,0x94609458,0x94689468,0x94789470,0x94889480,0x94989490,0x94a894a0,0x94b894b0,0x94c894c0,0x94d094d0,0x94e094d8,0x94f094e8,0x950094f8,0x95109508,0x95209518,0x95309528,0x95389538,0x95489540,0x95589550,0x95689560,0x95789570,0x95889580,0x95989590,
0x95a095a0,0x95b095a8,0x95c095b8,0x95d095c8,0x95e095d8,0x95f095e8,0x960095f8,0x96089608,0x96189610,0x96289620,0x96389630,0x96489640,0x96589650,0x96689660,0x96709670,0x96809678,0x96909688,0x96a09698,0x96b096a8,0x96c096b8,0x96d096c8,0x96d896d0,0x96e896e0,0x96f896f0,0x97089700,0x97189710,0x97289720,0x97389730,0x97409738,0x97509748,0x97609758,0x97709768,
0x97889778,0x97a89798,0x97c897b8,0x97e097d0,0x980097f0,0x98109808,0x98209818,0x98309828,0x98389830,0x98489840,0x98589850,0x98689860,0x98789870,0x98889880,0x98909890,0x98a09898,0x98b098a8,0x98c098b8,0x98d098c8,0x98e098d8,0x98f098e8,0x98f898f0,0x99089900,0x99189910,0x99289920,0x99389930,0x99489940,0x99509948,0x99609958,0x99709968,0x99809978,0x99909988,
0x99a09998,0x99a899a8,0x99b899b0,0x99c899c0,0x99d899d0,0x99e899e0,0x99f899f0,0x9a009a00,0x9a109a08,0x9a209a18,0x9a309a28,0x9a409a38,0x9a509a48,0x9a589a58,0x9a689a60,0x9a789a70,0x9a889a80,0x9a989a90,0x9aa89aa0,0x9ab09ab0,0x9ac09ab8,0x9ad09ac8,0x9ae09ad8,0x9af09ae8,0x9b009af8,0x9b089b08,0x9b189b10,0x9b289b20,0x9b389b30,0x9b489b40,0x9b589b50,0x9b609b60,
0x9b809b70,0x9b989b88,0x9bb89ba8,0x9bd89bc8,0x9bf09be0,0x9c089c00,0x9c189c10,0x9c289c20,0x9c309c30,0x9c409c38,0x9c509c48,0x9c609c58,0x9c709c68,0x9c809c78,0x9c889c80,0x9c989c90,0x9ca89ca0,0x9cb89cb0,0x9cc89cc0,0x9cd09cc8,0x9ce09cd8,0x9cf09ce8,0x9d009cf8,0x9d109d08,0x9d189d18,0x9d289d20,0x9d389d30,0x9d489d40,0x9d589d50,0x9d609d60,0x9d709d68,0x9d809d78,
0x9d909d88,0x9da09d98,0x9db09da8,0x9db89db0,0x9dc89dc0,0x9dd89dd0,0x9de89de0,0x9df89df0,0x9e009df8,0x9e109e08,0x9e209e18,0x9e309e28,0x9e389e38,0x9e489e40,0x9e589e50,0x9e689e60,0x9e789e70,0x9e809e80,0x9e909e88,0x9ea09e98,0x9eb09ea8,0x9ec09eb8,0x9ec89ec8,0x9ed89ed0,0x9ee89ee0,0x9ef89ef0,0x9f089f00,0x9f109f08,0x9f209f18,0x9f309f28,0x9f409f38,0x9f489f48,
0x9f689f58,0x9f809f70,0x9fa09f90,0x9fb89fa8,0x9fd89fc8,0x9ff09fe8,0xa008a000,0xa018a010,0xa020a020,0xa030a028,0xa040a038,0xa050a048,0xa060a058,0xa068a060,0xa078a070,0xa088a080,0xa098a090,0xa0a0a098,0xa0b0a0a8,0xa0c0a0b8,0xa0d0a0c8,0xa0d8a0d0,0xa0e8a0e0,0xa0f8a0f0,0xa108a100,0xa110a108,0xa120a118,0xa130a128,0xa140a138,0xa148a140,0xa158a150,0xa168a160,
0xa170a170,0xa180a178,0xa190a188,0xa1a0a198,0xa1a8a1a8,0xa1b8a1b0,0xa1c8a1c0,0xa1d8a1d0,0xa1e0a1d8,0xa1f0a1e8,0xa200a1f8,0xa208a208,0xa218a210,0xa228a220,0xa238a230,0xa240a238,0xa250a248,0xa260a258,0xa268a268,0xa278a270,0xa288a280,0xa298a290,0xa2a0a298,0xa2b0a2a8,0xa2c0a2b8,0xa2c8a2c8,0xa2d8a2d0,0xa2e8a2e0,0xa2f0a2f0,0xa300a2f8,0xa310a308,0xa320a318,
0xa330a328,0xa350a340,0xa368a360,0xa388a378,0xa3a0a390,0xa3b8a3b0,0xa3d8a3c8,0xa3f0a3e0,0xa408a400,0xa410a408,0xa420a418,0xa430a428,0xa438a430,0xa448a440,0xa458a450,0xa460a458,0xa470a468,0xa478a478,0xa488a480,0xa498a490,0xa4a0a4a0,0xa4b0a4a8,0xa4c0a4b8,0xa4c8a4c0,0xa4d8a4d0,0xa4e0a4e0,0xa4f0a4e8,0xa500a4f8,0xa508a500,0xa518a510,0xa520a520,0xa530a528,
0xa540a538,0xa548a540,0xa558a550,0xa560a560,0xa570a568,0xa580a578,0xa588a580,0xa598a590,0xa5a0a5a0,0xa5b0a5a8,0xa5b8a5b8,0xa5c8a5c0,0xa5d8a5d0,0xa5e0a5d8,0xa5f0a5e8,0xa5f8a5f0,0xa608a600,0xa610a610,0xa620a618,0xa628a628,0xa638a630,0xa640a640,0xa650a648,0xa660a658,0xa668a660,0xa678a670,0xa680a678,0xa690a688,0xa698a690,0xa6a8a6a0,0xa6b0a6a8,0xa6c0a6b8,
0xa6d0a6c8,0xa6e8a6e0,0xa700a6f8,0xa718a710,0xa730a728,0xa748a740,0xa760a758,0xa778a770,0xa790a780,0xa7a8a798,0xa7c0a7b0,0xa7d8a7c8,0xa7e8a7e0,0xa800a7f8,0xa810a808,0xa818a810,0xa820a820,0xa830a828,0xa838a830,0xa848a840,0xa850a848,0xa858a858,0xa868a860,0xa870a868,0xa878a878,0xa888a880,0xa890a888,0xa898a898,0xa8a8a8a0,0xa8b0a8b0,0xa8c0a8b8,0xa8c8a8c0,
0xa8d0a8c8,0xa8d8a8d8,0xa8e8a8e0,0xa8f0a8e8,0xa8f8a8f8,0xa908a900,0xa910a908,0xa918a918,0xa920a920,0xa930a928,0xa938a930,0xa940a940,0xa950a948,0xa958a950,0xa960a958,0xa968a968,0xa970a970,0xa980a978,0xa988a980,0xa990a990,0xa998a998,0xa9a8a9a0,0xa9b0a9a8,0xa9b8a9b0,0xa9c0a9c0,0xa9c8a9c8,0xa9d0a9d0,0xa9e0a9d8,0xa9e8a9e0,0xa9f0a9e8,0xa9f8a9f8,0xaa00aa00,
0xaa10aa08,0xaa20aa18,0xaa38aa30,0xaa48aa40,0xaa58aa50,0xaa68aa60,0xaa78aa70,0xaa90aa88,0xaaa0aa98,0xaab0aaa8,0xaac0aab8,0xaad0aac8,0xaae0aad8,0xaaf0aae8,0xab00aaf8,0xab10ab08,0xab20ab18,0xab30ab28,0xab40ab38,0xab48ab40,0xab58ab50,0xab68ab60,0xab78ab70,0xab88ab80,0xab90ab90,0xaba0ab98,0xabb0aba8,0xabc0abb8,0xabc8abc0,0xabd8abd0,0xabe8abe0,0xabf0abe8,
0xac00abf8,0xac08ac00,0xac08ac08,0xac10ac10,0xac18ac18,0xac20ac18,0xac28ac20,0xac28ac28,0xac30ac30,0xac38ac30,0xac38ac38,0xac40ac40,0xac48ac40,0xac48ac48,0xac50ac50,0xac58ac50,0xac58ac58,0xac60ac60,0xac68ac60,0xac68ac68,0xac70ac70,0xac70ac70,0xac78ac78,0xac80ac78,0xac80ac80,0xac88ac80,0xac88ac88,0xac90ac90,0xac90ac90,0xac98ac98,0xac98ac98,0xaca0aca0,
0xaca8aca0,0xacb0aca8,0xacb8acb0,0xacc0acb8,0xacc0acc0,0xacc8acc8,0xacd0acd0,0xacd8acd0,0xacd8acd8,0xace0ace0,0xace8ace0,0xace8ace8,0xacf0acf0,0xacf0acf0,0xacf8acf8,0xacf8acf8,0xad00ad00,0xad00ad00,0xad08ad08,0xad08ad08,0xad08ad08,0xad10ad10,0xad10ad10,0xad10ad10,0xad10ad10,0xad18ad18,0xad18ad18,0xad18ad18,0xad18ad18,0xad18ad18,0xad18ad18,0xad18ad18,
0xad18ad18,0xad18ad18,0xad18ad18,0xad18ad18,0xad18ad18,0xad18ad18,0xad18ad18,0xad10ad18,0xad10ad10,0xad10ad10,0xad10ad10,0xad10ad10,0xad08ad08,0xad08ad08,0xad08ad08,0xad00ad08,0xad00ad00,0xad00ad00,0xacf8acf8,0xacf8acf8,0xacf0acf8,0xacf0acf0,0xace8acf0,0xace8ace8,0xace8ace8,0xace0ace0,0xacd8ace0,0xacd8acd8,0xacd0acd8,0xacd0acd0,0xacc8acc8,0xacc8acc8,
0xacb8acc0,0xacb0acb8,0xaca8aca8,0xac98aca0,0xac90ac98,0xac80ac88,0xac78ac80,0xac68ac70,0xac60ac60,0xac50ac58,0xac40ac48,0xac38ac38,0xac28ac30,0xac18ac20,0xac08ac10,0xabf8ac00,0xabd8abe8,0xabb8abc8,0xaba0abb0,0xab80ab90,0xab60ab70,0xab40ab50,0xab20ab30,0xab08ab10,0xaae8aaf8,0xaac8aad8,0xaaa8aab8,0xaa88aa98,0xaa68aa78,0xaa48aa58,0xaa30aa38,0xaa10aa20,
0xa9f0aa00,0xa9d0a9e0,0xa9b0a9c0,0xa990a9a0,0xa978a988,0xa958a968,0xa938a948,0xa920a928,0xa900a910,0xa8e0a8f0,0xa8c8a8d8,0xa8a8a8b8,0xa890a8a0,0xa870a880,0xa858a868,0xa840a848,0xa820a830,0xa808a818,0xa7d8a7f8,0xa7a8a7c0,0xa778a790,0xa740a760,0xa710a728,0xa6e0a6f8,0xa6b0a6c8,0xa688a698,0xa658a670,0xa628a640,0xa600a610,0xa5d0a5e8,0xa5a8a5b8,0xa580a590,
0xa538a560,0xa4e8a510,0xa498a4c0,0xa450a478,0xa410a430,0xa398a3d8,0xa318a358,0xa2a0a2e0,0xa230a268,0xa1c8a1f8,0xa160a190,0xa100a130,0xa0a8a0d0,0xa050a078,0x9ff8a028,0x9f609fb0,0x9ed09f18,0x9e489e90,0x9dd09e08,0x9d589d90,0x9ce89d20,0x9c809cb8,0x9c209c50,0x9b909be8,0x9af09b40,0x9a589aa0,0x99c89a10,0x99409980,0x98c89908,0x98589890,0x97e89828,0x97289788,
0x967896d0,0x95d89628,0x95489590,0x94c09500,0x94489488,0x93b09410,0x92e89348,0x92309288,0x918891d8,0x90f09140,0x906890b0,0x8fe09028,0x8f008f68,0x8e388e98,0x8d808dd8,0x8ce08d30,0x8c508c98,0x8ba08c10,0x8ab88b28,0x89e88a48,0x89308988,0x889088d8,0x87f88840,0x86f88770,0x86188680,0x854885b0,0x849884f0,0x84008448,0x837883bc,0x8300833c,0x829882cc,0x823c8268,
0x81b88200,0x8144817a,0x80ed8116,0x80ac80ca,0x807c8092,0x80588068,0x803f804a,0x802c8035,0x801f8025,0x8015801a,0x800f8012,0x800a800c,0x80078008,0x80058006,0x80038004,0x80028002,0x80018002,0x80018001,0x80018001,0x80008000,0x80008000,0x80008000,0x80008000,0x80008000,0x80008000,0x80008000,0x80008000,0x80008000,0x80008000,0x80008000,0x80008000,0x80008000,
0x80008000,0x80008000,0x80008000,0x80008000,0x80008000,0x80008000,0x80008000,0x80008000,0x80008000,0x80008000,0x80008000,0x80008000,0x80008000,0x80008000,0x80008000,0x80008000,0x80008000,0x80008000,0x80008000,0x80008000,0x80008000,0x80008000,0x80008000,0x80008000,0x80008000,0x80008000,0x80008000,0x80008000,0x80008000,0x80008000,0x80008000,0x80008000,
0x80008000,0x80008000,0x80008000,0x80008000,0x80008000,0x80008000,0x80008000,0x80008000,0x80008000,0x80008000,0x80008000,0x80008000,0x80008000,0x80008000,0x80008000,0x80008000,0x80008000,0x80008000,0x80008000,0x80008000,0x80008000,0x80008000,0x80008000,0x80008000,0x80008000,0x80008000,0x80008000,0x80008000,0x80008000,0x80008000,0x80008000,0x80008000,
0x80008000,0x80008000,0x80008000,0x80008000,0x80008000,0x80008000,0x80008000,0x80008000,0x80008000,0x80008000,0x80008000,0x80008000,0x80008000,0x80008000,0x80008000,0x80008000,0x80008000,0x80008000,0x80008000,0x80008000,0x80008000,0x80008000,0x80008000,0x80008000,0x80008000,0x80008000,0x80008000,0x80008000,0x80008000,0x80008000,0x80008000,0x80008000,
0x80008000,0x80008000,0x80008000,0x80008000,0x80008000,0x80008000,0x80008000,0x80008000,0x80008000,0x80008000,0x80008000,0x80008000,0x80008000,0x80008000,0x80008000,0x80008000,0x80008000,0x80008000,0x80008000,0x80008000,0x80008000,0x80008000,0x80008000,0x80008000,0x80008000,0x80008000,0x80008000,0x80008000,0x80008000,0x80008000,0x80008000,0x80008000,
0x80008000,0x80008000,0x80008000,0x80008000,0x80008000,0x80008000,0x80008000,0x80008000,0x80008000,0x80008000,0x80008000,0x80008000,0x80008000,0x80008000,0x80008000,0x80008000,0x80008000,0x80008000,0x80008000,0x80008000,0x80008000,0x80008000,0x80008000,0x80008000,0x80008000,0x80008000,0x80008000,0x80008000,0x80008000,0x80008000,0x80008000,0x80008000,
0x80008000,0x80008000,0x80008000,0x80008000,0x80008000,0x80008000,0x80008000,0x80008000,0x80008000,0x80008000,0x80008000,0x80008000,0x80008000,0x80008000,0x80008000,0x80008000,0x80008000,0x80008000,0x80008000,0x80008000,0x80008000,0x80008000,0x80008000,0x80008000,0x80008000,0x80008000,0x80008000,0x80008000,0x80008000,0x80008000,0x80008000,0x80008000,
0x80008000,0x80008000,0x80008000,0x80008000,0x80008000,0x80008000,0x80008000,0x80008000,0x80008000,0x80008000,0x80008000,0x80008000,0x80008000,0x80008000,0x80008000,0x80008000,0x80008000,0x80008000,0x80008000,0x80008000,0x80008000,0x80008000,0x80008000,0x80008000,0x80008000,0x80008000,0x80008000,0x80008000,0x80008000,0x80008000,0x80008000,0x80008000,
0x80008000,0x80008000,0x80008000,0x80008000,0x80008000,0x80008000,0x80008000,0x80008000,0x80008000,0x80008000,0x80008000,0x80008000,0x80008000,0x80008000,0x80008000,0x80008000,0x80008000,0x80008000,0x80008000,0x80008000,0x80008000,0x80008000,0x80008000,0x80008000,0x80008000,0x80008000,0x80008000,0x80008000,0x80008000,0x80008000,0x80008000,0x80008000,
0x80008000,0x80008000,0x80008000,0x80008000,0x80008000,0x80008000,0x80008000,0x80008000,0x80008000,0x80008000,0x80008000,0x80008000,0x80008000,0x80008000,0x80008000,0x80008000,0x80008000,0x80008000,0x80008000,0x80008000,0x80008000,0x80008000,0x80008000,0x80008000,0x80008000,0x80008000,0x80008000,0x80008000,0x80008000,0x80008000,0x80008000,0x80008000,
0,0,0,0,0,0,0,0,0,0,0,0,0,0,0,0,0,0,0,0,0,0,0,0,0,0,0,0,0,0,0,0,
0,0,0,0,0,0,0,0,0,0,0,0,0,0,0,0,0,0,0,0,0,0,0,0,0,0,0,0,0,0,0,0,
0,0,0,0,0,0,0,0,0,0,0,0,0,0,0,0,0,0,0,0,0,0,0,0,0,0,0,0,0,0,0,0,
0,0,0,0,0,0,0,0,0,0,0,0,0,0,0,0,0,0,0,0,0,0,0,0,0,0,0,0,0,0,0,0,};
constexpr unsigned G_FLAG = K1_G_CNT + 14336;
constexpr unsigned G_PUB = K1_G_DUM;
constexpr unsigned G_BAR = K1_G_CNT + 13312;
constexpr unsigned H_SB = 1u << 18, H_RS = 9 * H_SB, NPAGE = 217, NPAGX = 55;
#define TS(i) do { } while (0)
#define HAS_PT
#define K2_END "s_cmp_lg_u64 %[flg], 0\ns_cbranch_scc1 1f\ns_endpgm\n1:"
#define MK_START(i) do { } while (0)
#define MK_START2(i) do { } while (0)
#define MK_END(i) do { } while (0)

constexpr float GELU_A = 0.46853395f;
constexpr float H_TRUNC = K1_LUT ? 1.0f : 1.0024f;
DI unsigned pack_bf16(float a, float b) {
  f32x2 v = {a, b};
  bf16x2_t r = __builtin_convertvector(v, bf16x2_t);
  return __builtin_bit_cast(unsigned, r);
}
typedef _Float16 f16x2_t __attribute__((ext_vector_type(2)));
DI unsigned pack_f16(float a, float b) {
  f32x2 v = {a, b};
  f16x2_t r = __builtin_convertvector(v, f16x2_t);
  return __builtin_bit_cast(unsigned, r);
}
DI float bf_lo(unsigned u) { return __uint_as_float(u << 16); }
DI float bf_hi(unsigned u) { return __uint_as_float(u & 0xffff0000u); }
DI float round_bf16(float a) { return bf_lo(pack_bf16(a, 0.f)); }
DI float dot2acc(unsigned a, unsigned b, float c) {
  return __builtin_amdgcn_fdot2_f32_bf16(__builtin_bit_cast(bf16x2_t, a), __builtin_bit_cast(bf16x2_t, b), c, false);
}
DI float ld_bf16(const u16* p) { return __uint_as_float((unsigned)(*p) << 16); }

DI s16x4 tr_read(const char* p) {
  return __builtin_bit_cast(s16x4, __builtin_amdgcn_ds_read_tr16_b64_v4i16((LAS v4i16_t*)p));
}

#define MFMA32(a, b, c) __builtin_amdgcn_mfma_f32_32x32x16_bf16((a), (b), (c), 0, 0, 0)
#define BAR() __builtin_amdgcn_s_barrier()

DI void dma16(unsigned voff, const char* sbase, unsigned lds_dst) {
  unsigned keep;
  asm volatile("s_mov_b32 %0, m0\n\ts_mov_b32 m0, %3\n\ts_nop 0\n\tglobal_load_lds_dwordx4 %1, %2\n\ts_mov_b32 m0, %0"
               : "=&s"(keep) : "v"(voff), "s"(sbase), "s"(lds_dst) : "memory");
}
DI unsigned lds_addr(const char* p) { return (unsigned)(size_t)(const LAS char*)p; }

DI float gelu_tanh(float x) {
  const float c0 = -2.0f * 0.7978845608028654f * 1.4426950408889634f;
  const float c1 = c0 * 0.044715f;
  float x2 = x * x;
  float t = __builtin_fmaf(x2, c1, c0);
  float e = __builtin_amdgcn_exp2f(x * t);
  float r = __builtin_amdgcn_rcpf(1.0f + e);
  return x * r;
}

DI void gload_b128_asm(u32x4& dst, const char* sbase, unsigned voff) {
  asm volatile("global_load_dwordx4 %0, %1, %2" : "=v"(dst) : "v"(voff), "s"(sbase) : "memory");
}
DI void lds_rd128(u32x4& dst, unsigned addr, int off) {
  asm volatile("ds_read_b128 %0, %1 offset:%2" : "=v"(dst) : "v"(addr), "i"(off));
}
DI void lds_rd128_far(u32x4& dst, unsigned voff, unsigned sbase, int imm) {
  unsigned t;
  asm volatile("v_add_u32 %1, %3, %2\n\tv_add_u32 %1, %4, %1\n\tds_read_b128 %0, %1" : "=v"(dst), "=&v"(t) : "v"(voff), "s"(sbase), "i"(imm));
}
DI void gload16i(u32x4& dst, const char* sbase, unsigned voff, int imm) {
  unsigned t;
  asm volatile("v_add_u32 %1, %4, %2\n\tglobal_load_dwordx4 %0, %1, %3" : "=v"(dst), "=&v"(t) : "v"(voff), "s"(sbase), "i"(imm) : "memory");
}
DI void dma16i(const char* sbase, unsigned voff, int immv, unsigned lbase, int imml) {
  unsigned t, keep;
  asm volatile("s_mov_b32 %1, m0\n\ts_add_u32 m0, %4, %6\n\tv_add_u32 %0, %5, %2\n\tglobal_load_lds_dwordx4 %0, %3\n\ts_mov_b32 m0, %1"
               : "=&v"(t), "=&s"(keep) : "v"(voff), "s"(sbase), "s"(lbase), "i"(immv), "i"(imml) : "memory", "scc");
}

DI void st16_wt(void* p, u32x4 v) { asm volatile("global_store_dwordx4 %0, %1, off sc1\n\ts_nop 1" :: "v"(p), "v"(v) : "memory"); }
template <int I> struct IC { static constexpr int value = I; };
template <int N, int I = 0, typename F> DI void static_for(F&& f) {
  if constexpr (I < N) { f(IC<I>{}); static_for<N, I + 1>(f); }
}
template <int C, int M> DI float dpp_add(float v) { return v + __int_as_float(__builtin_amdgcn_update_dpp(0, __float_as_int(v), C, M, 0xf, false)); }
DI float wave_sum63(float v) {
  v = dpp_add<0x111, 0xf>(v); v = dpp_add<0x112, 0xf>(v); v = dpp_add<0x114, 0xf>(v); v = dpp_add<0x118, 0xf>(v);
  v = dpp_add<0x142, 0xa>(v); return dpp_add<0x143, 0xc>(v);
}
DI uint64_t uni64(uint64_t v) { return (uint64_t)__builtin_amdgcn_readfirstlane(v >> 32) << 32 | (unsigned)__builtin_amdgcn_readfirstlane(v); }
DI unsigned wg_side(unsigned b) { return (((const unsigned*)(g_buf + K1_G_LOC))[b & 3] ^ (b >> 2)) & 1; }
DI unsigned h_flip(const char* ws, unsigned region, unsigned sb) { return __popc(((const unsigned*)(g_buf + K1_G_PT))[32 * ((((unsigned)(size_t)ws & 0x1fffffu) + region * H_RS + sb * H_SB) >> 21)]) > 2; }
DI unsigned h_near(const char* ws, unsigned o) { o += (unsigned)(size_t)ws & 0x1fffffu; return (__popc(((const unsigned*)(g_buf + K1_G_PT + 4))[32 * (o >> 21)]) > 16) ^ ((o >> 19) & 1); }
DI unsigned x_flip(const u16* x, unsigned region) { return __popc(((const unsigned*)(g_buf + K1_G_PT))[32 * (NPAGE + ((((unsigned)(size_t)x & 0x1fffffu) + (12 * region + 6) * 49152u) >> 21))]) > 2; }
DI unsigned sub_pixel(const u16* x, unsigned sub, unsigned m) {
  const unsigned blk = sub / 24, u = sub % 24;
  const unsigned region = blk < 256 ? (blk >> 3) * 4 + (blk & 3) : 128 + ((blk - 256) >> 1), side = blk < 256 ? wg_side(blk) : blk & 1;
  return (12 * region + (u >> 1)) * 64 + ((0xda43cb52fe987610ull >> (32 * (u & 1) + 16 * (side ^ x_flip(x, region)) + (m & 12))) & 15) * 4 + (m & 3);
}
DI unsigned h_byte(unsigned t, unsigned b) { return ((b & 255) + (b >> 7) * 0x8100) ^ ((t & 15) << 7); }
DI unsigned h_piece(const char* ws, unsigned sub, unsigned t) {
  const unsigned blk = sub / 24, u = sub % 24;
  const unsigned region = blk < 256 ? (blk >> 3) * 4 + (blk & 2) + ((u >> 2) & 1) : 128 + ((blk - 256) >> 1), side = blk < 256 ? wg_side(blk) : blk & 1;
  const unsigned sb = 3 * (t >> 4) + (u >> 3), o = region * H_RS + sb * H_SB;
  return o + (blk < 256 ? ((blk & 1) ^ h_near(ws, o)) << 14 : (u & 4) * 4096) + (u & 3) * 2048 + ((side ^ h_flip(ws, region, sb)) << 13);
}
constexpr unsigned G_ACC2 = K1_G_ACC + K1_NREP * 12288;

#define VA_CLOB \
  "v0", "v1", "v2", "v3", "v4", "v5", "v6", "v7", "v8", "v9", "v10", "v11", "v12", "v13", "v14", "v15", "v16", "v17", "v18", "v19", "v20", "v21", "v22", "v23", "v24", "v25", "v26", "v27", "v28", "v29", "v30", "v31", \
  "v32", "v33", "v34", "v35", "v36", "v37", "v38", "v39", "v40", "v41", "v42", "v43", "v44", "v45", "v46", "v47", "v48", "v49", "v50", "v51", "v52", "v53", "v54", "v55", "v56", "v57", "v58", "v59", "v60", "v61", "v62", "v63", \
  "v64", "v65", "v66", "v67", "v68", "v69", "v70", "v71", "v72", "v73", "v74", "v75", "v76", "v77", "v78", "v79", "v80", "v81", "v82", "v83", "v84", "v85", "v86", "v87", "v88", "v89", "v90", "v91", "v92", "v93", "v94", "v95", \
  "v96", "v97", "v98", "v99", "v100", "v101", "v102", "v103", "v104", "v105", "v106", "v107", "v108", "v109", "v110", "v111", "v112", "v113", "v114", "v115", "v116", "v117", "v118", "v119", "v120", "v121", "v122", "v123", "v124", "v125", "v126", "v127", \
  "v128", "v129", "v130", "v131", "v132", "v133", "v134", "v135", "v136", "v137", "v138", "v139", "v140", "v141", "v142", "v143", "v144", "v145", "v146", "v147", "v148", "v149", "v150", "v151", "v152", "v153", "v154", "v155", "v156", "v157", "v158", "v159", \
  "v160", "v161", "v162", "v163", "v164", "v165", "v166", "v167", "v168", "v169", "v170", "v171", "v172", "v173", "v174", "v175", "v176", "v177", "v178", "v179", "v180", "v181", "v182", "v183", "v184", "v185", "v186", "v187", "v188", "v189", "v190", "v191", \
  "v192", "v193", "v194", "v195", "v196", "v197", "v198", "v199", "v200", "v201", "v202", "v203", "v204", "v205", "v206", "v207", "v208", "v209", "v210", "v211", "v212", "v213", "v214", "v215", "v216", "v217", "v218", "v219", "v220", "v221", "v222", "v223", \
  "v224", "v225", "v226", "v227", "v228", "v229", "v230", "v231", "v232", "v233", "v234", "v235", "v236", "v237", "v238", "v239", "v240", "v241", "v242", "v243", "v244", "v245", "v246", "v247", "v248", "v249", "v250", "v251", "v252", "v253", "v254", "v255", \
  "a0", "a1", "a2", "a3", "a4", "a5", "a6", "a7", "a8", "a9", "a10", "a11", "a12", "a13", "a14", "a15", "a16", "a17", "a18", "a19", "a20", "a21", "a22", "a23", "a24", "a25", "a26", "a27", "a28", "a29", "a30", "a31", \
  "a32", "a33", "a34", "a35", "a36", "a37", "a38", "a39", "a40", "a41", "a42", "a43", "a44", "a45", "a46", "a47", "a48", "a49", "a50", "a51", "a52", "a53", "a54", "a55", "a56", "a57", "a58", "a59", "a60", "a61", "a62", "a63", \
  "a64", "a65", "a66", "a67", "a68", "a69", "a70", "a71", "a72", "a73", "a74", "a75", "a76", "a77", "a78", "a79", "a80", "a81", "a82", "a83", "a84", "a85", "a86", "a87", "a88", "a89", "a90", "a91", "a92", "a93", "a94", "a95", \
  "a96", "a97", "a98", "a99", "a100", "a101", "a102", "a103", "a104", "a105", "a106", "a107", "a108", "a109", "a110", "a111", "a112", "a113", "a114", "a115", "a116", "a117", "a118", "a119", "a120", "a121", "a122", "a123", "a124", "a125", "a126", "a127", \
  "a128", "a129", "a130", "a131", "a132", "a133", "a134", "a135", "a136", "a137", "a138", "a139", "a140", "a141", "a142", "a143", "a144", "a145", "a146", "a147", "a148", "a149", "a150", "a151", "a152", "a153", "a154", "a155", "a156", "a157", "a158", "a159", \
  "a160", "a161", "a162", "a163", "a164", "a165", "a166", "a167", "a168", "a169", "a170", "a171", "a172", "a173", "a174", "a175", "a176", "a177", "a178", "a179", "a180", "a181", "a182", "a183", "a184", "a185", "a186", "a187", "a188", "a189", "a190", "a191", \
  "a192", "a193", "a194", "a195", "a196", "a197", "a198", "a199", "a200", "a201", "a202", "a203", "a204", "a205", "a206", "a207", "a208", "a209", "a210", "a211", "a212", "a213", "a214", "a215", "a216", "a217", "a218", "a219", "a220", "a221", "a222", "a223", \
  "a224", "a225", "a226", "a227", "a228", "a229", "a230", "a231", "a232", "a233", "a234", "a235", "a236", "a237", "a238", "a239", "a240", "a241", "a242", "a243", "a244", "a245", "a246", "a247", "a248", "a249", "a250", "a251", "a252", "a253", "a254", "a255"
#define K1_CLOB "memory", "vcc", "scc", "s36", "s37", "s38", "s39", "s40", "s41", "s42", "s43", "s44", "s45", "s46", "s47", "s48", "s49", "s50", "s51", "s52", "s53", "s54", "s55", "s56", "s57", "s58", "s59", "s60", "s61", "s62", "s63", "s64", "s65", "s66", "s67", "s68", "s69", "s70", "s71", "s72", "s73", "s74", "s75", "s76", "s77", "s78", "s79", "s80", "s81", "s82", "s83", "s84", "s85", "s86", "s87", "s88", "s89", "s90", "s91", "s92", "s93", "s94", "s95", "s96", "s97", VA_CLOB
#define PX0 {0, 12 * 49152}
#define PX1 {1536 * 49152, 2 * 49152, 8 * 49152}
extern "C" __device__ __attribute__((aligned(32), used)) const unsigned c_prm[24][8] = {{}, {}, {}, {}, {0, 0, 2 * H_RS, H_RS}, {8192, 0, 2 * H_RS, H_RS}, {128 * H_RS, 16384, H_RS - 32768, 4096}, {128 * H_RS + 8192, 16384, H_RS - 32768, 4096},
    {}, {}, PX0, PX1, PX0, PX1, {}, {G_FLAG}, {G_W2F, 0, 0, 12288, 24576}, {G_W2R, 0, 0, 12288, 24576}, {G_W2R, 0, 0, 1152, 2304, 4608}, {G_W2F, 0, 0, 1152, 2304, 4608},
    {0, 0, 0, 256, 512}, {G_BIAS, 0, 0, 1536, 3072}, {G_PUB, 0, 0, 0, 0, 256}, {G_PUB, 0, 0, 16384, 32768}};
typedef __bf16 bf16x8_t __attribute__((ext_vector_type(8)));
DI f32x4 mfma16(u32x4 a, u32x4 b, f32x4 c) {
  return __builtin_amdgcn_mfma_f32_16x16x32_bf16(__builtin_bit_cast(bf16x8_t, a), __builtin_bit_cast(bf16x8_t, b), c, 0, 0, 0);
}
DI float grn_gx(double s, float scale) { return round_bf16(__builtin_amdgcn_sqrtf(round_bf16(round_bf16((float)s * scale) + 1e-6f))); }
DI void slow_fc1(const u16* __restrict__ x, const u16* __restrict__ dw, char* __restrict__ hws, float* sacc, int w) {
  const int lane = __builtin_amdgcn_mbcnt_hi(~0u, __builtin_amdgcn_mbcnt_lo(~0u, 0u)), m = lane & 15, g = lane >> 4;
  for (int i = lane; i < HID; i += 64) sacc[i] = 0.f;
#pragma unroll 1
  for (int it = 0; it < 9; ++it) {
    const int sub = blockIdx.x * 36 + it * 4 + w;
    const unsigned px = sub_pixel(x, sub, m);
    u32x4 A[12];
    float sum = 0.f;
    const u16* dq = dw;
    asm volatile("" : "+s"(dq));
#pragma unroll
    for (int ks = 0; ks < 12; ++ks) {
      const u32x4 v = *(const u32x4*)(x + (size_t)px * CH + 32 * ks + 8 * g);
      const u32x4 d = *(const u32x4*)(dq + 32 * ks + 8 * g);
#pragma unroll
      for (int j = 0; j < 4; ++j) {
        A[ks][j] = pack_bf16(bf_lo(v[j]) * bf_lo(d[j]), bf_hi(v[j]) * bf_hi(d[j]));
        sum += bf_lo(A[ks][j]) + bf_hi(A[ks][j]);
      }
      asm volatile("" : "+v"(A[ks]));
    }
    sum += __shfl_xor(sum, 16); sum += __shfl_xor(sum, 32);
    const float mean = sum * (1.0f / CH);
    float var = 0.f;
#pragma unroll
    for (int ks = 0; ks < 12; ++ks) {
#pragma unroll
      for (int j = 0; j < 4; ++j) {
        const float a = bf_lo(A[ks][j]) - mean, b = bf_hi(A[ks][j]) - mean;
        var += a * a + b * b;
      }
      asm volatile("" : "+v"(A[ks]));
    }
    var += __shfl_xor(var, 16); var += __shfl_xor(var, 32);
    const float mb = round_bf16(mean);
    const float rd = 1.0f / round_bf16(sqrtf(round_bf16(round_bf16(var * (1.0f / CH)) + 1e-5f)));
#pragma unroll
    for (int ks = 0; ks < 12; ++ks)
#pragma unroll
      for (int j = 0; j < 4; ++j) A[ks][j] = pack_bf16((bf_lo(A[ks][j]) - mb) * rd, (bf_hi(A[ks][j]) - mb) * rd);
#pragma unroll 1
    for (int tc = 0; tc < 96; ++tc) {
      f32x4 acc = {0.f, 0.f, 0.f, 0.f};
#pragma unroll
      for (int ks = 0; ks < 12; ++ks) acc = mfma16(A[ks], *(const u32x4*)(g_buf + K1_G_W1P + (size_t)(tc * 12 + ks) * 1024 + lane * 16), acc);
      float q = 0.f;
#pragma unroll
      for (int r = 0; r < 4; ++r) { acc[r] = gelu_tanh(acc[r]); q = __builtin_fmaf(acc[r], acc[r], q); }
      typedef unsigned u32x2_ __attribute__((ext_vector_type(2)));
      *(u32x2_*)(hws + h_piece(hws, sub, tc >> 1) + h_byte(tc >> 1, g * 256 + ((m + 4 * g) & 15) * 16 + (tc & 1) * 8)) = (u32x2_){pack_bf16(acc[0], acc[1]), pack_bf16(acc[2], acc[3])};
      q += __shfl_xor(q, 16); q += __shfl_xor(q, 32);
      if (g == 0) sacc[tc * 16 + m] += q;
    }
  }
  __builtin_amdgcn_wave_barrier();
  __builtin_amdgcn_fence(__ATOMIC_ACQ_REL, "workgroup");
  for (int i = lane; i < HID; i += 64) atomicAdd((double*)(g_buf + G_ACC2) + i, (double)sacc[i]);
}
DI void slow_bar(unsigned* c, int w) {
  const int lane = __builtin_amdgcn_mbcnt_hi(~0u, __builtin_amdgcn_mbcnt_lo(~0u, 0u));
  asm volatile("s_waitcnt vmcnt(0)" ::: "memory");
  __syncthreads();
  if (w == 0 && lane == 0) {
    __threadfence();
    asm volatile("s_waitcnt vmcnt(0)" ::: "memory");
    __hip_atomic_fetch_add(c, 1u, __ATOMIC_RELAXED, __HIP_MEMORY_SCOPE_AGENT);
    for (int i = 0; i < (1 << 22) && __hip_atomic_load(c, __ATOMIC_RELAXED, __HIP_MEMORY_SCOPE_AGENT) < (unsigned)NWG; ++i) __builtin_amdgcn_s_sleep(32);
    __threadfence();
    asm volatile("s_waitcnt vmcnt(0)" ::: "memory");
  }
  __syncthreads();
}

#define K2_CLOB "memory", "vcc", "scc", "s38", "s39", "s40", "s41", "s42", "s43", "s44", "s45", "s46", "s47", "s48", "s49", "s50", "s51", "s52", "s53", "s54", "s55", "s56", "s57", "s58", "s59", "s60", "s61", "s62", "s63", "s64", "s65", "s66", "s67", "s68", "s69", "s70", "s71", "s72", "s73", "s74", "s75", "s76", "s77", "s78", "s79", "s80", "s81", "s82", "s83", "s84", "s85", "s86", "s87", "s88", "s89", "s90", "s91", "s92", "s93", "s94", "s95", "s96", "s97", "s98", "s99", "s100", "s101", VA_CLOB
DI void slow_fc2(const u16* __restrict__ x, const char* __restrict__ hws, u16* __restrict__ out, const u16* __restrict__ grn_g, const u16* __restrict__ w2,
                 char* smem, bool hbf, int w) {
  float* sc = (float*)smem;
  float* red = sc + HID;
  u16* wf = (u16*)(smem + 8192);
  const int lane = __builtin_amdgcn_mbcnt_hi(~0u, __builtin_amdgcn_mbcnt_lo(~0u, 0u)), tid = w * 64 + lane, m = lane & 15, g = lane >> 4;
  __syncthreads();
  float part = 0.f;
  for (int c = tid; c < HID; c += 256) {
    double s = 0.0;
    if (hbf) s = ((const double*)(g_buf + G_ACC2))[c];
    else for (int r = 0; r < (int)K1_NREP; ++r) s += ((const double*)(g_buf + K1_G_ACC + r * 12288))[c];
    const float gx = grn_gx(s, hbf ? 1.0f : 1.0f / (GELU_A * GELU_A));
    sc[c] = gx; part += gx;
  }
#pragma unroll
  for (int o = 32; o > 0; o >>= 1) part += __shfl_xor(part, o);
  if (lane == 0) red[tid >> 6] = part;
  __syncthreads();
  const float gmean = round_bf16(round_bf16(((red[0] + red[1]) + (red[2] + red[3])) * (1.0f / HID)) + 1e-6f);
  for (int c = tid; c < HID; c += 256) sc[c] = (1.0f + ld_bf16(grn_g + c) * round_bf16(sc[c] / gmean)) * (hbf ? 1.0f : H_TRUNC / GELU_A);
#pragma unroll 1
  for (int it = 0; it < 9; ++it) {
    const int sub = blockIdx.x * 36 + it * 4 + (tid >> 6);
    f32x4 acc[24];
#pragma unroll
    for (int i = 0; i < 24; ++i) acc[i] = (f32x4){0.f, 0.f, 0.f, 0.f};
#pragma unroll 1
    for (int t = 0; t < 48; ++t) {
      __syncthreads();
      for (int e = tid; e < 32 * CH; e += 256) {
        const int k = e / CH, n = e % CH;
        wf[((n >> 4) * 64 + (k >> 3) * 16 + (n & 15)) * 8 + (k & 7)] = (u16)pack_bf16(sc[32 * t + k] * ld_bf16(w2 + (size_t)(32 * t + k) * CH + n), 0.f);
      }
      __syncthreads();
      const char* hp = hws + h_piece(hws, sub, t) + (g >> 1) * 8 + 2 * (m & 3);
      u32x4 A;
#pragma unroll
      for (int j = 0; j < 4; ++j) {
        unsigned v[2];
#pragma unroll
        for (int k = 0; k < 2; ++k) {
          v[k] = *(const u16*)(hp + h_byte(t, (m >> 2) * 256 + ((8 * (g & 1) + 2 * j + k + (m & 12)) & 15) * 16));
          if (!hbf) v[k] = pack_bf16((float)__builtin_bit_cast(_Float16, (u16)v[k]), 0.f) & 0xffffu;
        }
        A[j] = v[0] | (v[1] << 16);
      }
#pragma unroll
      for (int i = 0; i < 24; ++i) acc[i] = mfma16(A, *(const u32x4*)(wf + (i * 64 + lane) * 8), acc[i]);
    }
#pragma unroll
    for (int i = 0; i < 24; ++i) {
      const int n = 16 * i + m;
      const f32x4 b4 = *(const f32x4*)(g_bias + (n < 256 ? ((n >> 6) * 6 + (n & 3)) * 16 + ((n & 63) >> 2) : (((n - 256) >> 5) * 6 + 4 + (n & 1)) * 16 + ((n & 31) >> 1)) * 4);
      const float bias = (b4[0] + b4[1]) + (b4[2] + b4[3]);
#pragma unroll
      for (int r = 0; r < 4; ++r) {
        const size_t o = (size_t)(sub_pixel(x, sub, 4 * g) + r) * CH + n;
        out[o] = (u16)pack_bf16(acc[i][r] + bias + ld_bf16(x + o), 0.f);
      }
    }
  }
}
__global__ __launch_bounds__(256, 1) __attribute__((aligned(4096))) void k_block(const u16* __restrict__ x, char* __restrict__ hws, const u16* __restrict__ w1, const u16* __restrict__ dw,
                                                const u16* __restrict__ grn_b, const u16* __restrict__ w2, const u16* __restrict__ out, const u16* __restrict__ grn_g) {
  extern __shared__ __attribute__((aligned(16))) char smem[];
  const unsigned w = __builtin_amdgcn_readfirstlane(threadIdx.x >> 6), b = blockIdx.x, lds0 = lds_addr(smem), pm = lds0 + K1_PF_OFF;
  TS(0); MK_START(1);
  unsigned nb, t[22];
  uint64_t xp, wp;
  asm volatile(
R"(.macro M t,f,w
.if \f < 30
v_mfma_f32_16x16x32_bf16 v[56+4*\t:56+4*\t+3], a[216+4*\w:216+4*\w+3], v[128+4*\f:128+4*\f+3], v[56+4*\t:56+4*\t+3]
.else
v_mfma_f32_16x16x32_bf16 v[56+4*\t:56+4*\t+3], a[216+4*\w:216+4*\w+3], a[4*\f-4*30:4*\f-4*30+3], v[56+4*\t:56+4*\t+3]
.endif
.endm
.macro Z t,f,w
.if \f < 30
v_mfma_f32_16x16x32_bf16 v[56+4*\t:56+4*\t+3], a[216+4*\w:216+4*\w+3], v[128+4*\f:128+4*\f+3], 1.0
.else
v_mfma_f32_16x16x32_bf16 v[56+4*\t:56+4*\t+3], a[216+4*\w:216+4*\w+3], a[4*\f-4*30:4*\f-4*30+3], 1.0
.endif
.endm
.macro LZ a,f
.if \f < 30
v_mfma_f32_16x16x32_bf16 v[36:36+3], v[\a:\a+3], v[128+4*\f:128+4*\f+3], 0
.else
v_mfma_f32_16x16x32_bf16 v[36:36+3], v[\a:\a+3], a[4*\f-4*30:4*\f-4*30+3], 0
.endif
.endm
.macro LS a,f
.if \f < 30
v_mfma_f32_16x16x32_bf16 v[36:36+3], v[\a:\a+3], v[128+4*\f:128+4*\f+3], v[36:36+3]
.else
v_mfma_f32_16x16x32_bf16 v[36:36+3], v[\a:\a+3], a[4*\f-4*30:4*\f-4*30+3], v[36:36+3]
.endif
.endm
.macro LD d,a,f
.if \f < 30
v_mfma_f32_16x16x32_bf16 v[\d:\d+3], a[\a:\a+3], v[128+4*\f:128+4*\f+3], v[36:36+3]
.else
v_mfma_f32_16x16x32_bf16 v[\d:\d+3], a[\a:\a+3], a[4*\f-4*30:4*\f-4*30+3], v[36:36+3]
.endif
.endm
.macro GZ f
.if \f < 30
v_mfma_f32_16x16x32_bf16 v[52:55], v[128+4*\f:128+4*\f+3], v[128+4*\f:128+4*\f+3], 0
.else
v_mfma_f32_16x16x32_bf16 v[52:55], a[4*\f-4*30:4*\f-4*30+3], a[4*\f-4*30:4*\f-4*30+3], 0
.endif
.endm
.macro GS f
.if \f < 30
v_mfma_f32_16x16x32_bf16 v[52:55], v[128+4*\f:128+4*\f+3], v[128+4*\f:128+4*\f+3], v[52:55]
.else
v_mfma_f32_16x16x32_bf16 v[52:55], a[4*\f-4*30:4*\f-4*30+3], a[4*\f-4*30:4*\f-4*30+3], v[52:55]
.endif
.endm
.macro S0 q,t
v_mul_f32 v[\q], v[\t], v[\t]
.endm
.macro S q,t
v_fmac_f32 v[\q], v[\t], v[\t]
.endm
.macro LS_ t
v_lshlrev_b32_sdwa v[\t], 1, v[\t] dst_sel:DWORD dst_unused:UNUSED_PAD src0_sel:DWORD src1_sel:WORD_1
.endm
.macro FL d,z,s
v_fma_mix_f32 v[\d], v[\z], v[\s], -v[\s] op_sel_hi:[0,1,1]
.endm
.macro FH d,z,s
v_fma_mix_f32 v[\d], v[\z], v[\s], -v[\s] op_sel:[0,1,1] op_sel_hi:[0,1,1]
.endm
.macro LU o
s_getpc_b64 s[40:41]
s_add_u32 s40, s40, c_lut@rel32@lo+4+\o
s_addc_u32 s41, s41, c_lut@rel32@hi+12+\o
s_add_u32 s40, s40, s53
s_addc_u32 s41, s41, 0
.endm
.macro VL_
global_load_dwordx2 v[66:67], v64, s[40:41] sc1
.endm
.macro VP
s_branch 28f
26:
s_sleep 2
VL_
28:
s_sub_u32 s72, s72, 1
s_waitcnt vmcnt(0)
v_lshrrev_b64 v[68:69], 16, v[66:67]
v_cmp_ne_u64 vcc, s[42:43], v[68:69]
v_xor_b32 v68, v66, v60
v_readlane_b32 s53, v66, s52
v_bfe_u32 v68, v68, 2, 1
v_and_b32 v69, 3, v66
s_cbranch_scc1 27f
s_cbranch_vccnz 26b
27:
v_mov_b32_dpp v70, v68 row_shr:1 row_mask:0xf bank_mask:0xf bound_ctrl:1
v_mov_b32_dpp v71, v68 row_shl:1 row_mask:0xf bank_mask:0xf bound_ctrl:1
s_lshr_b32 s53, s53, 3
v_add3_u32 v69, v69, v70, v71
s_xor_b32 s53, s53, %[s2]
v_cmp_lt_u32 vcc, 2, v69
s_and_b32 s96, s53, 1
s_sub_u32 s53, 0, s96
s_xor_b32 s48, vcc_lo, s53
s_lshl_b32 s91, s48, 7
.endm
.macro VQ
v_bfe_u32 v68, v66, 8, 4
v_lshrrev_b32 v62, v62, s48
s_bfe_i32 s53, s48, 0x10009
v_add_u32_dpp v69, v68, v68 row_shr:1 row_mask:0xf bank_mask:0xf bound_ctrl:1
v_and_b32 v62, 1, v62
v_mov_b32_dpp v70, v68 row_shl:1 row_mask:0xf bank_mask:0xf bound_ctrl:1
v_cmp_ne_u32 s[92:93], 0, v62
v_mov_b32_dpp v71, v68 row_shl:2 row_mask:0xf bank_mask:0xf bound_ctrl:1
v_add3_u32 v69, v69, v70, v71
s_bfe_i32 s90, s48, 0x10019
v_cmp_lt_u32 vcc, 16, v69
s_and_b32 s53, s53, ~199728
s_and_b32 s90, s90, 199728
v_mov_b32 v69, s92
s_mov_b32 s49, vcc_lo
s_or_b32 s53, s53, s90
v_lshrrev_b64 v[66:67], v61, s[48:49]
v_lshrrev_b32 v61, 6, v61
v_mov_b32 v70, s53
v_xor_b32 v66, v66, v61
v_and_b32 v66, 1, v66
v_cmp_ne_u32 vcc, 0, v66
s_mov_b32 s48, %[glo]
s_mov_b32 s49, %[ghi]
s_mov_b32 s90, vcc_lo
v_mov_b32 v68, vcc_hi
.endm
.macro VS
s_mov_b64 exec, 1
global_store_dwordx3 v63, v[68:70], s[48:49]
s_mov_b64 exec, -1
.endm
.macro FL_
global_load_dwordx2 v[56:57], v58, s[40:41] sc1
.endm
.macro FB
v_lshrrev_b64 v[56:57], 16, v[56:57]
v_cmp_ne_u64 vcc, s[42:43], v[56:57]
s_nop 4
s_cbranch_vccnz 21b
22:
.endm
.macro E2
s_mov_b64 exec, 0
s_cmp_eq_u32 %[w], 0
s_cbranch_scc1 .Lk2p1
s_cmp_eq_u32 %[w], 1
s_cbranch_scc1 .Lk2p2
s_cmp_eq_u32 %[w], 2
s_cbranch_scc1 .Lk2p3
s_branch .Lk2p0
.Lk2r0:
s_mov_b64 exec, -1
.endm
.macro DN
s_waitcnt lgkmcnt(0)
s_barrier
s_and_b32 exec_lo, s57, 2
s_mov_b32 exec_hi, 0
v_mov_b32 v88, s69
global_store_dwordx2 v87, v[88:89], s[40:41] sc1
s_mov_b64 exec, -1
.endm
.macro RF
s_cmp_lg_u32 s95, 0
s_cbranch_scc1 30b
31:
.endm
.macro SN
s_cbranch_vccnz 29b
24:
.endm
.macro FX
s_branch 25f
21:
s_sleep 8
FL_
s_waitcnt vmcnt(0)
v_lshrrev_b64 v[56:57], 16, v[56:57]
v_cmp_ne_u64 vcc, s[42:43], v[56:57]
s_sub_u32 s72, s72, 1
s_cbranch_scc1 22f
s_nop 2
s_cbranch_vccnz 21b
s_branch 22f
30:
v_mov_b32 v56, s95
s_mov_b64 exec, 1
global_store_dword v56, v65, s[68:69] sc1
s_mov_b64 exec, -1
s_waitcnt vmcnt(0)
s_branch 31f
29:
s_mov_b32 s53, 0x40000
23:
s_sleep 8
v_and_b32 v254, 0xf0, v0
v_lshlrev_b32 v254, 3, v254
v_add_u32 v254, 9216, v254
s_sub_u32 s53, s53, 1
s_cbranch_scc1 24f
global_load_dword v254, v254, s[68:69] sc1
s_waitcnt vmcnt(0)
v_cmp_ne_u32 vcc, 16, v254
s_nop 4
s_cbranch_vccnz 23b
s_branch 24f
25:
.endm
.macro C d,a,b
v_cvt_pk_bf16_f32 v[\d], v[\a], v[\b]
.endm
.macro LA t
ds_read_u16 v[\t], v[\t]
.endm
.macro LC_ t
ds_read_u16_d16_hi v[\t], v[\t]
.endm
.macro LO p,a,b
v_bfi_b32 v[\p], s36, v[\a], v[\b]
.endm
.macro HZ q,p
v_pk_mul_f16 v[\q], v[\p], v[\p]
.endm
.macro HS q,p
v_pk_fma_f16 v[\q], v[\p], v[\p], v[\q]
.endm
.macro RA d,v,o
ds_read_b128 a[\d:\d+3], v[\v] offset:\o
.endm
.macro RV d,v,o
ds_read_b128 v[\d:\d+3], v[\v] offset:\o
.endm
.macro DW o
global_load_lds_dwordx4 v0, s[40:41] offset:\o sc1
.endm
.macro DF
global_load_lds_dwordx4 v0, s[70:71]
.endm
.macro PFN
s_getpc_b64 s[70:71]
s_add_u32 s70, s70, k2_code@rel32@lo-1020
s_addc_u32 s71, s71, k2_code@rel32@hi-1012
s_add_u32 s70, s70, %[pf]
s_addc_u32 s71, s71, 0
s_andn2_b32 s70, s70, 0x7f
.endm
.macro PFW
s_add_u32 s70, %[glo], 3288064
s_addc_u32 s71, %[ghi], 0
s_add_u32 s70, s70, %[pf]
s_addc_u32 s71, s71, 0
.endm
.macro WL n
s_waitcnt lgkmcnt(\n)
.endm
.macro WV n
s_waitcnt vmcnt(\n)
.endm
.macro ST d,o,s
global_store_dwordx4 v[\o], v[\d:\d+3], s[\s:\s+1] nt sc1
.endm
.macro SU d,o,s
global_store_dwordx4 v[\o], v[\d:\d+3], s[\s:\s+1] sc1
.endm
.macro DX a,o
global_load_lds_dwordx4 v[\a], s[40:41] offset:\o
.endm
.macro DR
global_load_lds_dword v31, s[50:51] sc1
.endm
.macro EL_
global_load_dwordx2 v[22:23], v12, s[48:49] sc1
.endm
.macro RS
s_mov_b64 exec, s[54:55]
global_store_dwordx4 v13, v[40:43], s[86:87] sc1
s_mov_b64 exec, -1
.endm
.macro Y3 a0,x0,a1,x1,a2,x2
v_mfma_f32_4x4x4_16b_bf16 v[40:43], a[\a0:\a0+1], v[\x0:\x0+1], 0
v_mfma_f32_4x4x4_16b_bf16 v[44:47], a[\a1:\a1+1], v[\x1:\x1+1], 0
v_mfma_f32_4x4x4_16b_bf16 v[48:51], a[\a2:\a2+1], v[\x2:\x2+1], 0
.endm
.macro LZA a,f
v_mfma_f32_16x16x32_bf16 v[36:39], a[\a:\a+3], v[128+4*\f:128+4*\f+3], 0
.endm
.macro LSA a,f
v_mfma_f32_16x16x32_bf16 v[36:39], a[\a:\a+3], v[128+4*\f:128+4*\f+3], v[36:39]
.endm
.macro CK
v_min_u32 v9, v22, v23
v_cmp_eq_u32 vcc, 0, v9
.endm
.macro CB k,n
s_cbranch_vccnz .Lcs\n
.Lcr\n:
.endm
.macro CS k,n
.Lcs\n:
s_call_b64 s[92:93], .Lcc\k
s_branch .Lcr\n
.endm
.macro XW r,h
v_xor_b32_sdwa v21, v[\r+2], v[\r+3] dst_sel:DWORD dst_unused:UNUSED_PAD src0_sel:DWORD src1_sel:WORD_\h
.endm
.macro HY d,q,h
v_fma_mix_f32 v[\d], v[\q], 1.0, v[\d] op_sel:[\h,0,0] op_sel_hi:[1,0,0]
.endm
.macro HW d,q
v_fma_mix_f32 v[\d], v[\q], 1.0, v[\q] op_sel:[0,0,1] op_sel_hi:[1,0,1]
.endm
.macro AT_
s_mov_b64 exec, 1
global_atomic_add v38, v36, v37, s[68:69] sc0
s_mov_b64 exec, -1
.endm
.macro CW
s_mov_b64 exec, 1
ds_write_b32 v41, v38
s_mov_b64 exec, -1
.endm
.macro DC
s_add_u32 %[nb], %[nb], 1
s_cmp_ge_u32 s72, 96
s_addc_u32 s39, s39, 0
s_min_u32 s72, s72, 95
s_bfe_u32 s53, s72, 0x20003
s_bitcmp1_b32 s90, s53
s_cselect_b32 s73, 2304, 0
s_and_b32 s53, s72, 31
s_mul_i32 s53, s53, 72
s_lshr_b32 s72, s72, 5
s_add_u32 s53, s53, s72
s_add_u32 s53, s53, s73
s_add_u32 s57, s53, 69
s_cmp_eq_u32 s39, 0
s_cselect_b32 s57, s57, s61
.endm
.macro SE
PFN
s_mov_b32 m0, %[pm]
v_mov_b32 v37, 1
DF
PFW
DF
v_mov_b32 v41, 135168
s_lshl2_add_u32 s53, %[w], 135168
v_mov_b32 v36, %[wrap]
v_writelane_b32 v41, s53, 0
.endm
.macro SG
v_and_b32 v254, 0xf0, v0
v_lshlrev_b32 v254, 3, v254
v_add_u32 v254, 9216, v254
global_load_dword v254, v254, s[68:69] sc1
.endm
.macro SF
s_waitcnt vmcnt(12)
v_cmp_ne_u32 vcc, 16, v254
s_nop 4
SN
.endm
.macro PL
s_lshl_b32 s53, s63, 7
s_add_u32 s50, s84, s53
s_addc_u32 s51, s85, 0
s_mov_b32 s53, 0x40000
41:
global_load_dword v254, v0, s[50:51] sc1
s_sub_u32 s53, s53, 1
s_waitcnt vmcnt(0)
v_cmp_eq_u32 vcc, 0, v254
s_cbranch_scc1 42f
s_nop 3
s_cbranch_vccnz 41b
42:
.endm
s_load_dwordx16 s[80:95], %[ka], 0
v_mbcnt_lo_u32_b32 v16, -1, 0
v_mbcnt_hi_u32_b32 v16, -1, v16
s_movk_i32 s53, 0x2211
v_lshlrev_b32 v100, 2, v16
s_lshr_b32 s72, %[b], 3
v_bfe_u32 v100, s53, v100, 2
s_sub_u32 s96, %[b], s72
s_lshr_b32 s36, %[w], 1
v_xad_u32 v101, v100, -1, s96
v_add_u32 v101, -48, v101
s_sub_u32 s96, s96, 1
v_add_u32 v102, 176, v101
s_add_u32 s72, s72, 224
s_and_b32 s37, %[b], 7
s_cselect_b32 s96, s96, s72
v_min_u32 v101, v101, v102
s_and_b32 s37, %[w], 1
s_sub_u32 s53, s36, 80
)"
R"(s_lshl_b32 s72, s37, 2
v_lshl_add_u32 v102, v101, 1, s53
s_add_u32 s72, s72, s36
v_max_i32 v101, v101, v102
s_lshl_b32 s72, s72, 2
v_cmp_gt_u32 vcc, 217, v101
v_min_u32 v100, 1, v100
v_add_lshl_u32 v100, v100, s72, 10
v_mov_b32 v102, 9
v_mov_b32 v103, 29128
v_mov_b32 v104, 1727
v_add_u32 v105, -217, v101
v_lshlrev_b32 v0, 4, v16
s_mov_b32 m0, 4096
s_waitcnt lgkmcnt(0)
v_mov_b64 v[106:107], s[80:81]
s_mov_b64 exec, vcc
v_mov_b64 v[106:107], s[82:83]
v_mov_b32 v102, 12
v_mov_b32 v103, 21846
v_mov_b32 v104, 6911
v_mov_b32 v105, v101
s_mov_b64 exec, -1
v_lshl_add_u32 v105, v105, 5, v102
v_and_b32 v108, 0x1fffff, v106
v_lshlrev_b32 v105, 16, v105
v_xad_u32 v105, v108, -1, v105
v_max_i32 v105, 0, v105
v_lshrrev_b32 v105, 16, v105
v_mul_u32_u24 v105, v105, v103
s_add_u32 s74, s82, 452784128
v_lshrrev_b32 v105, 18, v105
s_addc_u32 s75, s83, 0
v_mul_u32_u24 v105, v105, v102
v_min_u32 v105, v105, v104
v_lshl_add_u32 v105, v105, 16, v100
v_add_co_u32 v106, vcc, v106, v105
s_getpc_b64 s[76:77]
31:
s_cmp_eq_u32 %[w], 1
v_addc_co_u32 v107, vcc, 0, v107, vcc
v_readlane_b32 s44, v106, 4
v_readlane_b32 s62, v106, 0
v_readlane_b32 s64, v106, 2
v_readlane_b32 s45, v107, 4
v_readlane_b32 s63, v107, 0
v_readlane_b32 s65, v107, 2
v_readlane_b32 s46, v101, 4
v_mov_b32 v100, 0
s_mov_b64 exec, 1
global_load_lds_dword v100, s[74:75] sc1
s_mov_b64 exec, -1
s_cselect_b32 s53, 1, 0
s_cmp_lt_u32 s96, 48
s_cbranch_scc1 49f
s_load_dword s47, s[44:45], 0
s_load_dword s47, s[62:63], 0
s_load_dword s47, s[64:65], 0
s_load_dword s47, s[62:63], 0x2000
s_load_dword s47, s[64:65], 0x2000
49:
s_cmp_eq_u32 s53, 1
s_cselect_b32 s53, k1_code-31b, 0
s_mul_i32 s73, s36, 8192
s_cselect_b32 s72, 56, 0
v_lshlrev_b32 v100, 7, v16
s_and_b32 s72, s72, %[b]
s_lshl_b32 s72, s72, 10
s_add_u32 s53, s53, s72
s_add_u32 s53, s53, s73
s_add_u32 s74, s76, s53
s_addc_u32 s75, s77, 0
global_load_lds_dword v100, s[74:75]
s_mov_b64 %[xp], s[80:81]
s_mul_i32 s48, %[w], 3
s_movk_i32 s97, 3072
s_mul_i32 s56, %[w], 6144
s_cmp_lt_u32 s96, 48
s_cselect_b64 s[50:51], -1, 0
s_and_b32 s53, %[b], 7
s_max_u32 s53, s53, 1
s_mul_i32 s53, s53, 7
s_sub_u32 s53, s53, 7
s_lshr_b32 s72, %[b], 3
s_add_u32 s71, s53, s72
s_lshl2_add_u32 s72, s72, %[w]
s_mul_i32 s72, s72, 9216
s_lshr_b32 s53, s53, 1
v_and_b32 v100, 7, v16
v_lshrrev_b32 v101, 3, v16
v_add_u32 v100, s53, v100
v_mul_u32_u24 v101, 0xc00, v101
v_min_u32 v100, 23, v100
v_lshl_add_u32 v100, v100, 7, v101
v_add_u32 v100, s72, v100
s_mov_b64 exec, 0xf0f0f
global_load_lds_dword v100, s[84:85]
s_add_u32 %[glo], s76, g_buf@rel32@lo+(.-31b+4)
s_mov_b64 exec, -1
s_addc_u32 %[ghi], s77, g_buf@rel32@hi+(.-31b+4)
v_lshl_add_u32 v75, %[w], 6, v16
s_add_u32 s74, s76, c_prm@rel32@lo+(.-31b+4)
v_min_u32 v100, 191, v75
s_addc_u32 s75, s77, c_prm@rel32@hi+(.-31b+4)
v_and_b32 v101, 3, v100
v_and_b32 v100, -4, v100
v_max_u32 v102, 64, v75
v_lshl_add_u32 v100, v100, 1, v101
v_lshlrev_b32 v102, 2, v102
v_lshlrev_b32 v100, 1, v100
v_min_u32 v103, 23, v16
global_load_ushort v72, v100, s[86:87]
global_load_ushort v73, v100, s[86:87] offset:8
global_load_dword v74, v102, s[86:87] offset:-256
v_lshlrev_b32 v103, 5, v103
s_mov_b32 s78, %[glo]
s_mov_b32 s79, %[ghi]
global_load_dwordx4 v[76:79], v103, s[74:75]
global_load_dwordx2 v[80:81], v103, s[74:75] offset:16
s_lshl1_add_u32 s53, s96, s37
s_mul_i32 s53, s53, 50
v_add_u32 v100, s53, v16
v_mov_b32 v101, 1189888
v_mov_b32 v104, 0
v_cmp_le_u32 vcc, 896, v100
v_mov_b32 v102, 1257472
v_mov_b32 v105, 0
v_cndmask_b32 v101, v101, v102, vcc
v_cmp_le_u32 vcc, 4736, v100
v_mov_b32 v102, 1128832
s_lshl_b32 s53, %[b], 8
v_cndmask_b32 v101, v101, v102, vcc
s_add_u32 s53, s53, 1206272
v_cmp_ge_u32 vcc, 4736, v100
v_lshl_add_u32 v100, v100, 4, v101
v_mov_b64 v[106:107], 0
s_and_b32 s72, %[b], 7
v_writelane_b32 v100, s53, 50
s_lshl_b32 s72, s72, 10
s_add_u32 s72, s72, 1179648
s_cmp_lt_u32 %[b], 8
v_writelane_b32 v100, s72, 51
s_cselect_b32 s53, 3, 1
s_lshl_b32 s53, s53, 18
s_and_b32 vcc_hi, vcc_hi, 262143
s_or_b32 vcc_hi, vcc_hi, s53
s_cmp_eq_u32 s36, 0
s_cselect_b64 exec, vcc, 0
global_store_dwordx4 v100, v[104:107], s[78:79] sc1
s_mov_b64 exec, -1
s_and_b32 s53, %[b], 7
s_lshr_b32 s72, %[b], 5
s_lshl3_add_u32 s53, s53, s72
s_mul_i32 %[pos0], s53, 72
s_bfe_u32 s72, %[b], 0x20003
s_lshl2_add_u32 %[rst], s72, %[w]
s_lshl1_add_u32 s53, %[pos0], %[rst]
v_lshrrev_b32 v100, 2, v16
v_and_b32 v101, 48, v0
v_lshl_add_u32 v100, v100, 10, v101
s_lshl_b32 s72, s53, 6
s_add_u32 s72, s72, 1517568
v_add_u32 v100, s72, v100
s_lshl_b32 s72, s53, 4
s_add_u32 s72, s72, 1360896
s_mov_b32 exec_lo, 0
s_mov_b32 exec_hi, 0x1ff0
v_lshl_add_u32 v100, v16, 8, s72
s_mov_b32 exec_lo, -1
s_mov_b32 exec_hi, 0x1fff
global_store_dwordx4 v100, v[104:107], s[78:79] sc1
s_mov_b64 exec, -1
v_mov_b32 v232, 0
v_mov_b32 v233, 0
s_cmp_lt_u32 s96, 48
s_cbranch_scc1 47f
50:
s_lshr_b32 s53, %[b], 3
s_and_b32 s72, %[b], 3
s_lshl2_add_u32 s38, s53, s72
s_lshr_b32 s39, s38, 1
s_and_b32 s52, s53, 1
s_lshl2_add_u32 %[pf], s53, %[w]
s_lshl_b32 %[pf], %[pf], 10
s_bfe_u32 %[s2], %[b], 0x10002
s_and_b32 s72, s72, 3
s_mul_i32 s72, s72, 12288
s_add_u32 s72, s72, 1271808
s_add_u32 %[alo], %[glo], s72
s_addc_u32 %[ahi], %[ghi], 0
s_mov_b32 %[xlo], s80
s_mov_b32 %[xhi], s81
s_mov_b32 %[hlo], s82
s_mov_b32 %[hhi], s83
s_mul_i32 s53, s38, 12
s_add_u32 s53, s53, %[w]
s_lshl3_add_u32 s53, s52, %[w]
s_add_u32 s53, s53, 1536
s_lshl1_add_u32 s53, s39, s36
s_cmp_eq_u32 s52, 0
s_cselect_b32 s53, 0, 526336
s_lshl2_add_u32 s54, %[b], %[w]
s_lshl_b32 s53, s54, 6
s_add_u32 s53, s53, 16384
s_lshl_b32 s72, s52, 6
s_cmp_eq_u32 %[w], 0
s_cselect_b32 %[ca0], s72, s53
s_and_b32 s60, s82, 0x1fffff
s_and_b32 s61, s80, 0x1fffff
s_mul_i32 s62, s39, 4718592
s_add_u32 s62, s62, s60
s_mul_i32 s63, s39, 2359296
s_add_u32 s63, s63, s60
s_add_u32 s63, s63, 301989888
s_mul_i32 s64, s38, 589824
s_add_u32 s64, s64, s61
s_add_u32 s64, s64, 294912
s_mul_i32 s65, s39, 589824
s_add_u32 s65, s65, s61
s_add_u32 s65, s65, 75792384
s_lshr_b32 s60, s62, 21
s_lshr_b32 s61, s63, 21
s_lshr_b32 s64, s64, 21
s_lshr_b32 s65, s65, 21
s_pack_ll_b32_b16 s60, s60, s61
s_pack_ll_b32_b16 s64, s64, s65
v_and_b32 v100, 24, v16
s_lshl_b32 s64, s64, 8
s_mov_b32 s53, 0x2040206
s_or_b32 s60, s60, s64
v_and_b32 v101, 7, v16
v_bfe_u32 v102, s53, v100, 3
v_bfe_u32 v100, s60, v100, 8
v_min_u32 v101, v101, v102
v_bfe_i32 v102, v16, 3, 1
v_add3_u32 v100, v100, v101, -1
v_and_b32 v103, 162, v102
v_and_b32 v102, 217, v102
v_sub_u32 v103, 216, v103
s_and_b32 s62, s62, 0x1fffff
v_med3_i32 v101, v100, 0, v103
s_and_b32 s63, s63, 0x1fffff
v_cmp_eq_u32 vcc, v100, v101
v_add_u32 v101, v101, v102
s_and_b32 s72, %[b], 3
s_add_u32 s72, s72, 4
v_cndmask_b32 v100, 0, 4, vcc
v_lshrrev_b32 v102, 3, v16
v_bfe_u32 v103, v16, 1, 2
v_mad_u32_u24 v102, v102, 3, v103
v_and_b32 v104, 1, v16
s_mul_i32 s53, s36, 9
v_and_b32 v105, 15, v16
v_add_u32 v102, s53, v102
s_mul_i32 s53, s37, 9
s_mov_b32 exec_lo, 0
v_and_or_b32 v101, v16, 3, 4
v_add_u32 v102, s53, v105
v_bfe_u32 v104, v16, 4, 1
s_mov_b32 exec_lo, -1
v_mul_u32_u24 v101, 128, v101
v_lshl_add_u32 v102, v102, 18, s62
s_mov_b32 s53, 452788224
v_lshl_add_u32 v105, v105, 18, s63
v_add3_u32 v60, v101, v100, s53
v_lshrrev_b32 v100, 19, v102
v_lshrrev_b32 v102, 21, v102
v_xor_b32 v100, %[b], v100
v_lshrrev_b32 v105, 21, v105
v_and_b32 v100, v100, v104
v_add_u32 v62, 17, v105
v_lshl_add_u32 v100, v100, 1, v104
s_lshl_b32 s53, s54, 8
v_lshl_add_u32 v100, v100, 5, v102
s_add_u32 s55, s53, 5647360
v_add_u32 v61, 1, v100
v_mov_b32 v63, s55
s_getreg_b32 s59, hwreg(HW_REG_XCC_ID)
s_mov_b32 s66, %[b]
s_cmp_lt_u32 s46, 160
s_cselect_b32 s57, 0xff, 15
s_lshr_b32 s53, %[b], 2
s_cmp_eq_u32 s53, 15
s_cselect_b32 s57, 0x1ff, s57
s_sub_u32 s49, s96, 48
s_cmp_lt_u32 s49, 176
s_cselect_b32 s57, s57, 0
s_cmp_lt_u32 s49, 80
s_cselect_b32 s53, 8, 10
s_cselect_b32 s67, 7, 5
s_cselect_b32 s68, 0, 2
s_bitcmp1_b32 s53, %[w]
s_cselect_b32 s57, s57, 0
s_xor_b32 s53, s59, s66
s_bitcmp1_b32 s53, 0
s_cselect_b32 s58, 0, 0xf0
s_bitcmp1_b32 s66, 0
s_cselect_b32 s53, 0xf0f, 0
s_or_b32 s58, s58, s53
s_bitcmp1_b32 s44, 19
s_cselect_b32 s53, -1, 0
s_xor_b32 s58, s58, s53
s_bfe_u32 s59, s59, 0x10002
s_and_b32 s68, s68, %[w]
v_xor_b32 v100, %[w], v16
v_and_b32 v83, s67, v16
v_and_b32 v100, 1, v100
v_add_lshl_u32 v83, v83, s68, 2
v_lshl_add_u32 v82, v100, 2, %[w]
v_bfe_u32 v100, v16, 2, 2
v_lshrrev_b32 v101, 4, v16
v_lshlrev_b32 v82, 2, v82
v_lshl_add_u32 v100, v101, 3, v100
v_and_b32 v102, 3, v16
s_add_u32 s53, s56, 102912
v_lshl_add_u32 v100, v100, 3, v102
s_mul_i32 s72, s49, 24
v_lshl_add_u32 v84, v100, 3, s53
s_add_u32 s72, s72, s48
v_lshl_add_u32 v85, s72, 10, v0
s_mul_i32 s53, s46, 128
s_mul_i32 s72, s96, 32
v_add_u32 v86, 0x3000, v85
s_add_u32 s53, s53, 452788224
s_add_u32 s72, s72, 452853760
v_mov_b32 v87, s72
v_mov_b32 v89, 0x5a17c3e9
v_writelane_b32 v87, s53, 0
s_lshl_b32 s53, s37, 13
s_add_u32 s60, s53, 1024
s_sub_u32 s61, 9216, s53
s_xor_b32 s70, s59, 1
s_lshl_b32 s69, s70, 3
s_or_b32 s69, s69, 0xb26d0400
s_cmp_lt_u32 s49, 176
s_waitcnt lgkmcnt(0)
s_cbranch_scc0 38f
s_sleep 3
.p2align 6
s_memtime s[62:63]
s_waitcnt lgkmcnt(0)
s_load_dword s47, s[44:45], s60
s_waitcnt lgkmcnt(0)
s_memtime s[64:65]
s_waitcnt lgkmcnt(0)
s_load_dword s47, s[44:45], s61
s_waitcnt lgkmcnt(0)
s_memtime s[66:67]
s_sub_u32 s62, s64, s62
s_waitcnt lgkmcnt(0)
s_sub_u32 s64, s66, s64
v_mov_b32 v100, s62
v_mov_b32 v102, 9999
v_writelane_b32 v100, s64, 1
s_mov_b64 exec, 3
ds_write_b32 v82, v100 offset:2048
s_mov_b64 exec, -1
s_waitcnt vmcnt(0) lgkmcnt(0)
s_barrier
ds_read_b32 v100, v83 offset:2048
s_movk_i32 s53, 140
s_waitcnt lgkmcnt(0)
v_cmp_le_u32 vcc, 235, v100
v_mov_b32_dpp v101, v100 quad_perm:[1,0,3,2] row_mask:0xf bank_mask:0xf
s_nop 0
v_cndmask_b32 v102, v102, v100, vcc
v_cmp_gt_u32 s[62:63], v100, v101
v_sad_u32 v101, v100, v101, -10
v_min_u32_dpp v102, v102, v102 quad_perm:[1,0,3,2] row_mask:0xf bank_mask:0xf
s_lshr_b32 s66, vcc_lo, 1
v_cmp_ge_u32 s[64:65], s53, v101
v_min_u32_dpp v102, v102, v102 quad_perm:[2,3,0,1] row_mask:0xf bank_mask:0xf
s_and_b32 s66, s66, vcc_lo
s_and_b32 s66, s66, 0x55
v_readlane_b32 s67, v102, 0
v_readlane_b32 s68, v102, 4
s_and_b32 s66, s66, s64
s_bcnt1_i32_b32 s64, s66
s_cmp_gt_u32 s67, s68
s_cselect_b32 s53, 4, 0
s_cselect_b32 s60, 1, 0
s_lshr_b32 s58, s58, s53
s_xor_b32 s60, s60, s59
s_xor_b32 s58, s58, s62
s_absdiff_i32 s67, s67, s68
s_and_b32 s58, s58, s66
s_bcnt1_i32_b32 s58, s58
s_cmp_gt_u32 s67, 80
s_cselect_b32 s53, 56, 49
s_mul_i32 s61, s60, 3
s_lshr_b32 s53, s53, s61
s_lshl_b32 s58, s58, 1
s_and_b32 s61, s53, 7
s_sub_u32 s58, s58, s64
s_add_u32 s69, s69, s61
s_lshl_b32 s53, s58, 8
s_add_u32 s69, s69, s53
s_and_b32 exec_lo, s57, 3
s_mov_b32 exec_hi, 0
v_mov_b32 v88, s69
)"
R"(global_store_dwordx2 v87, v[88:89], s[82:83] sc1
s_mov_b64 exec, -1
38:
s_waitcnt vmcnt(1)
s_cmp_lg_u32 s57, 0
s_cbranch_scc1 39f
s_waitcnt vmcnt(0)
39:
s_cmp_lt_u32 s96, 224
s_cbranch_scc1 45f
s_mov_b64 s[40:41], s[82:83]
s_lshr_b32 s57, 16, %[w]
s_waitcnt vmcnt(0)
DN
s_mov_b32 s57, 0
45:
s_sub_u32 s73, s96, 144
s_xor_b32 s59, s59, 1
s_cmp_lt_u32 s73, 96
s_cbranch_scc1 32f
33:
s_mul_i32 s53, %[w], 3072
s_min_u32 s53, s53, 8704
LU 0
s_add_u32 m0, s53, 25600
s_nop 0
DW 0
DW 1024
DW 2048
s_add_u32 s40, s40, 12288
s_addc_u32 s41, s41, 0
s_add_u32 m0, s53, 91136
s_nop 0
DW 0
DW 1024
DW 2048
s_mov_b32 s62, 0x18091312
s_mov_b32 s63, 0x070e151c
s_cmp_eq_u32 s46, 0
s_cselect_b32 s63, 0x140e151c, s63
s_cselect_b32 s53, 217, s46
s_cmp_eq_u32 s53, 217
s_cselect_b32 s62, 0x18901312, s62
s_cmp_eq_u32 s46, 216
s_cselect_b32 s62, 0x91091312, s62
s_cmp_eq_u32 s46, 271
s_cselect_b32 s62, 0x91091312, s62
v_lshlrev_b32 v100, 3, v16
s_lshl_b32 s53, s46, 7
v_lshrrev_b64 v[100:101], v100, s[62:63]
s_add_u32 s53, s53, 1333248
v_and_b32 v101, 7, v100
v_bfe_u32 v102, v100, 3, 2
v_bfe_u32 v100, v100, 7, 1
v_lshlrev_b32 v102, 7, v102
s_xor_b32 s60, s60, 1
v_add3_u32 v101, v101, v102, s53
s_lshl2_add_u32 s53, s46, 1187824
v_xor_b32 v100, s60, v100
s_add_u32 s58, s58, 4
s_and_b32 s61, s61, 3
s_bfm_b32 s58, s58, 0
s_bfm_b32 s61, s61, 0
v_writelane_b32 v101, s53, 8
s_mov_b64 exec, 0xf0
v_mov_b32 v100, s58
s_mov_b32 exec_lo, s57
v_writelane_b32 v100, s61, 0
v_writelane_b32 v100, 0, 1
v_writelane_b32 v100, s59, 8
global_store_byte v101, v100, s[78:79] sc1
s_mov_b64 exec, -1
v_lshlrev_b32 v72, 16, v72
v_lshlrev_b32 v73, 16, v73
v_cmp_le_u32 vcc, 208, v75
v_mul_f32 v72, 0x42800000, v72
v_mov_b32 v104, 0x7e007e00
v_mul_f32 v73, 0x42800000, v73
v_cndmask_b32 v104, 0, v104, vcc
v_and_b32 v100, 16, v0
v_cvt_pk_bf16_f32 v72, v72, 0
v_cvt_pk_bf16_f32 v73, v73, 0
v_mov_b32 v105, v104
v_min_u32 v101, 223, v75
v_mov_b32 v106, v104
v_lshlrev_b32 v101, 4, v101
v_mov_b32 v107, v104
v_add_u32 v101, 0x10000, v101
v_lshlrev_b32 v72, v100, v72
v_lshlrev_b32 v73, v100, v73
ds_write_b128 v101, v[104:107] offset:61952
s_cmp_lt_u32 %[w], 3
v_mov_b64 v[108:109], v[104:105]
v_mov_b64 v[110:111], v[104:105]
s_cselect_b32 s62, 0x33333333, 0
s_mov_b32 s63, s62
v_lshlrev_b32 v101, 4, v75
v_lshlrev_b32 v102, 2, v75
s_mov_b64 exec, s[62:63]
v_mov_b32 v108, v73
v_mov_b32 v110, v72
s_lshl_b64 exec, s[62:63], 2
v_mov_b32 v109, v73
v_mov_b32 v111, v72
s_mov_b64 exec, -1
s_cmp_eq_u32 %[w], 0
s_cselect_b32 s53, 0, -1
ds_write_b128 v101, v[108:111] offset:61952
v_and_b32 v74, s53, v74
ds_write_b32 v102, v74
v_mov_b64 v[100:101], s[78:79]
v_mad_u32_u24 v76, v77, s38, v76
s_mov_b64 exec, 0xff
v_mov_b64 v[100:101], s[82:83]
s_mov_b64 exec, 0xf00
v_mov_b64 v[100:101], s[80:81]
s_mov_b64 exec, 0x3000
v_mov_b64 v[100:101], s[92:93]
s_mov_b64 exec, 0x100000
v_mov_b64 v[100:101], s[94:95]
s_mov_b64 exec, -1
v_mad_u32_u24 v76, v78, s39, v76
v_mad_u32_u24 v76, v79, s37, v76
v_mad_u32_u24 v76, v80, s36, v76
v_mad_u32_u24 v76, v81, %[b], v76
v_add_co_u32 v100, vcc, v100, v76
s_mul_i32 s53, s54, 72
s_and_b32 s72, %[pf], 0x1c00
v_addc_co_u32 v101, vcc, 0, v101, vcc
s_mov_b32 s60, 0x4019999a
v_cmp_nge_f32 vcc, s60, v232
v_cmp_nge_f32 s[62:63], s60, v233
v_writelane_b32 v100, s72, 2
v_writelane_b32 v101, s53, 1
s_mov_b32 s53, 0x1000000
v_writelane_b32 v101, 0, 14
v_writelane_b32 v100, s53, 14
v_lshl_add_u32 v102, v16, 3, s55
s_or_b64 vcc, vcc, s[62:63]
s_and_b64 vcc, vcc, s[50:51]
s_cselect_b32 s95, 14720, 0
s_mov_b32 exec_hi, 0
global_store_dwordx2 v102, v[100:101], s[78:79]
s_mov_b64 exec, -1
s_mov_b64 exec, 0
s_cmp_eq_u32 %[w], 0
s_cbranch_scc1 .Lk1p1
s_cmp_eq_u32 %[w], 1
s_cbranch_scc1 .Lk1p2
s_cmp_eq_u32 %[w], 2
s_cbranch_scc1 .Lk1p3
s_branch .Lk1p0
.Lk1r0:
s_mov_b64 exec, -1
s_cmp_lt_u32 s73, 96
s_cbranch_scc1 34f
35:
s_branch .Lk1s
47:
v_lshl_add_u32 v212, s71, 8, v75
s_mov_b32 s53, 0x9e3779b1
v_mul_lo_u32 v212, v212, s53
v_alignbit_b32 v100, v212, v212, 15
v_xor_b32 v100, 0x1234567, v100
v_mul_lo_u32 v212, v100, s53
v_alignbit_b32 v100, v212, v212, 15
v_xor_b32 v100, 0x2468ace, v100
v_mul_lo_u32 v213, v100, s53
v_alignbit_b32 v100, v213, v213, 15
v_xor_b32 v100, 0x369d035, v100
v_mul_lo_u32 v214, v100, s53
v_alignbit_b32 v100, v214, v214, 15
v_xor_b32 v100, 0x48d159c, v100
v_mul_lo_u32 v215, v100, s53
v_alignbit_b32 v100, v215, v215, 15
v_xor_b32 v100, 0x5b05b03, v100
v_mul_lo_u32 v216, v100, s53
v_alignbit_b32 v100, v216, v216, 15
v_xor_b32 v100, 0x6d3a06a, v100
v_mul_lo_u32 v217, v100, s53
v_alignbit_b32 v100, v217, v217, 15
v_xor_b32 v100, 0x7f6e5d1, v100
v_mul_lo_u32 v218, v100, s53
v_alignbit_b32 v100, v218, v218, 15
v_xor_b32 v100, 0x91a2b38, v100
v_mul_lo_u32 v219, v100, s53
v_alignbit_b32 v100, v219, v219, 15
v_xor_b32 v100, 0xa3d709f, v100
v_mul_lo_u32 v220, v100, s53
v_alignbit_b32 v100, v220, v220, 15
v_xor_b32 v100, 0xb60b606, v100
v_mul_lo_u32 v221, v100, s53
v_alignbit_b32 v100, v221, v221, 15
v_xor_b32 v100, 0xc83fb6d, v100
v_mul_lo_u32 v222, v100, s53
v_alignbit_b32 v100, v222, v222, 15
v_xor_b32 v100, 0xda740d4, v100
v_mul_lo_u32 v223, v100, s53
v_bfe_u32 v100, v16, 2, 2
v_lshrrev_b32 v101, 4, v16
v_lshl_add_u32 v100, v101, 3, v100
v_and_b32 v101, 3, v16
v_lshl_add_u32 v100, v100, 3, v101
s_add_u32 s53, s56, 102912
v_lshl_add_u32 v84, v100, 3, s53
s_mul_i32 s72, s96, 32
s_add_u32 s72, s72, 452853760
v_mov_b32 v87, s72
v_mov_b32 v89, 0x5a17c3e9
s_mov_b32 s69, 0xb26d0000
s_sleep 2
v_lshrrev_b32 v100, 2, v16
v_and_b32 v101, 48, v0
v_lshl_add_u32 v100, s48, 5, v100
v_lshl_add_u32 v101, s71, 6, v101
v_mad_u32_u24 v100, v100, s97, v101
v_and_b32 v103, 48, v16
v_lshl_add_u32 v103, s48, 6, v103
s_add_u32 m0, s56, 102912
v_add_u32 v104, 0, v100
global_load_lds_dwordx4 v104, s[84:85]
s_add_u32 m0, s56, 103936
v_add_u32 v105, 49152, v100
global_load_lds_dwordx4 v105, s[84:85]
s_add_u32 m0, s56, 104960
v_add_u32 v106, 98304, v100
global_load_lds_dwordx4 v106, s[84:85]
s_add_u32 m0, s56, 105984
v_add_u32 v107, 147456, v100
global_load_lds_dwordx4 v107, s[84:85]
s_add_u32 m0, s56, 107008
v_add_u32 v108, 196608, v100
global_load_lds_dwordx4 v108, s[84:85]
s_add_u32 m0, s56, 108032
v_add_u32 v109, 245760, v100
global_load_lds_dwordx4 v109, s[84:85]
global_load_dwordx4 v[236:239], v103, s[86:87] offset:0
global_load_dwordx4 v[240:243], v103, s[86:87] offset:64
global_load_dwordx4 v[244:247], v103, s[86:87] offset:128
s_waitcnt vmcnt(0)
ds_read_b64_tr_b16 v[188:189], v84 offset:0
ds_read_b64_tr_b16 v[190:191], v84 offset:256
ds_read_b64_tr_b16 v[192:193], v84 offset:2048
ds_read_b64_tr_b16 v[194:195], v84 offset:2304
ds_read_b64_tr_b16 v[196:197], v84 offset:4096
ds_read_b64_tr_b16 v[198:199], v84 offset:4352
ds_read_b64_tr_b16 v[200:201], v84 offset:32
ds_read_b64_tr_b16 v[202:203], v84 offset:288
ds_read_b64_tr_b16 v[204:205], v84 offset:2080
ds_read_b64_tr_b16 v[206:207], v84 offset:2336
ds_read_b64_tr_b16 v[208:209], v84 offset:4128
ds_read_b64_tr_b16 v[210:211], v84 offset:4384
s_mul_i32 s72, s71, 24
s_add_u32 s72, s72, s48
v_lshl_add_u32 v85, s72, 10, v0
v_add_u32 v86, 0x3000, v85
s_mov_b32 s53, 0x3f803f80
v_and_b32 v101, 15, v16
v_lshrrev_b32 v102, 4, v75
v_lshlrev_b32 v101, 7, v101
v_lshl_add_u32 v102, v102, 3, v101
s_mov_b32 s72, 0xffff0000
s_waitcnt lgkmcnt(0)
v_dot2_f32_bf16 v90, v188, s53, 0
v_dot2_f32_bf16 v91, v200, s53, 0
v_dot2_f32_bf16 v90, v189, s53, v90
v_dot2_f32_bf16 v91, v201, s53, v91
v_dot2_f32_bf16 v90, v190, s53, v90
v_dot2_f32_bf16 v91, v202, s53, v91
v_dot2_f32_bf16 v90, v191, s53, v90
v_dot2_f32_bf16 v91, v203, s53, v91
v_dot2_f32_bf16 v90, v192, s53, v90
v_dot2_f32_bf16 v91, v204, s53, v91
v_dot2_f32_bf16 v90, v193, s53, v90
v_dot2_f32_bf16 v91, v205, s53, v91
v_dot2_f32_bf16 v90, v194, s53, v90
v_dot2_f32_bf16 v91, v206, s53, v91
v_dot2_f32_bf16 v90, v195, s53, v90
v_dot2_f32_bf16 v91, v207, s53, v91
v_dot2_f32_bf16 v90, v196, s53, v90
v_dot2_f32_bf16 v91, v208, s53, v91
v_dot2_f32_bf16 v90, v197, s53, v90
v_dot2_f32_bf16 v91, v209, s53, v91
v_dot2_f32_bf16 v90, v198, s53, v90
v_dot2_f32_bf16 v91, v210, s53, v91
v_dot2_f32_bf16 v90, v199, s53, v90
v_dot2_f32_bf16 v91, v211, s53, v91
v_lshlrev_b32 v164, 16, v236
v_and_b32 v165, s72, v236
v_lshlrev_b32 v166, 16, v237
v_and_b32 v167, s72, v237
v_lshlrev_b32 v168, 16, v238
v_and_b32 v169, s72, v238
ds_write_b64 v102, v[90:91] offset:8192
v_lshlrev_b32 v170, 16, v239
v_and_b32 v171, s72, v239
v_lshlrev_b32 v172, 16, v240
v_and_b32 v173, s72, v240
v_lshlrev_b32 v174, 16, v241
v_and_b32 v175, s72, v241
v_lshlrev_b32 v176, 16, v242
v_and_b32 v177, s72, v242
v_lshlrev_b32 v178, 16, v243
v_and_b32 v179, s72, v243
v_lshlrev_b32 v180, 16, v244
v_and_b32 v181, s72, v244
v_lshlrev_b32 v182, 16, v245
v_and_b32 v183, s72, v245
v_lshlrev_b32 v184, 16, v246
v_and_b32 v185, s72, v246
v_lshlrev_b32 v186, 16, v247
v_and_b32 v187, s72, v247
s_waitcnt lgkmcnt(0)
s_barrier
ds_read_b128 v[108:111], v101 offset:8192
ds_read_b128 v[112:115], v101 offset:8208
ds_read_b128 v[116:119], v101 offset:8224
ds_read_b128 v[120:123], v101 offset:8240
ds_read_b128 v[124:127], v101 offset:8256
ds_read_b128 v[128:131], v101 offset:8272
ds_read_b128 v[132:135], v101 offset:8288
ds_read_b128 v[136:139], v101 offset:8304
v_lshlrev_b32 v140, 16, v188
v_and_b32 v141, s72, v188
v_lshlrev_b32 v142, 16, v189
v_and_b32 v143, s72, v189
v_lshlrev_b32 v144, 16, v190
v_and_b32 v145, s72, v190
v_lshlrev_b32 v146, 16, v191
v_and_b32 v147, s72, v191
v_lshlrev_b32 v148, 16, v192
v_and_b32 v149, s72, v192
v_lshlrev_b32 v150, 16, v193
v_and_b32 v151, s72, v193
v_lshlrev_b32 v152, 16, v194
v_and_b32 v153, s72, v194
v_lshlrev_b32 v154, 16, v195
v_and_b32 v155, s72, v195
v_lshlrev_b32 v156, 16, v196
v_and_b32 v157, s72, v196
v_lshlrev_b32 v158, 16, v197
v_and_b32 v159, s72, v197
v_lshlrev_b32 v160, 16, v198
v_and_b32 v161, s72, v198
v_lshlrev_b32 v162, 16, v199
v_and_b32 v163, s72, v199
v_mov_b32 v92, 0x42800000
s_waitcnt lgkmcnt(0)
v_pk_add_f32 v[108:109], v[108:109], v[124:125]
v_pk_add_f32 v[110:111], v[110:111], v[126:127]
v_pk_add_f32 v[112:113], v[112:113], v[128:129]
v_pk_add_f32 v[114:115], v[114:115], v[130:131]
v_pk_add_f32 v[116:117], v[116:117], v[132:133]
v_pk_add_f32 v[118:119], v[118:119], v[134:135]
v_pk_add_f32 v[120:121], v[120:121], v[136:137]
v_pk_add_f32 v[122:123], v[122:123], v[138:139]
v_pk_add_f32 v[108:109], v[108:109], v[116:117]
v_pk_add_f32 v[110:111], v[110:111], v[118:119]
v_pk_add_f32 v[112:113], v[112:113], v[120:121]
v_pk_add_f32 v[114:115], v[114:115], v[122:123]
v_pk_add_f32 v[108:109], v[108:109], v[112:113]
v_pk_add_f32 v[110:111], v[110:111], v[114:115]
v_pk_add_f32 v[108:109], v[108:109], v[110:111]
v_mul_f32 v108, 0xbe2aaaab, v108
v_mul_f32 v109, 0xbe2aaaab, v109
s_mov_b32 s53, 0x07060302
v_pk_fma_f32 v[140:141], v[140:141], v[92:93], v[108:109] op_sel:[0,0,0] op_sel_hi:[1,0,0]
v_pk_fma_f32 v[142:143], v[142:143], v[92:93], v[108:109] op_sel:[0,0,0] op_sel_hi:[1,0,0]
v_pk_fma_f32 v[144:145], v[144:145], v[92:93], v[108:109] op_sel:[0,0,0] op_sel_hi:[1,0,0]
v_pk_fma_f32 v[146:147], v[146:147], v[92:93], v[108:109] op_sel:[0,0,0] op_sel_hi:[1,0,0]
v_pk_fma_f32 v[148:149], v[148:149], v[92:93], v[108:109] op_sel:[0,0,0] op_sel_hi:[1,0,0]
v_pk_fma_f32 v[150:151], v[150:151], v[92:93], v[108:109] op_sel:[0,0,0] op_sel_hi:[1,0,0]
v_pk_fma_f32 v[152:153], v[152:153], v[92:93], v[108:109] op_sel:[0,0,0] op_sel_hi:[1,0,0]
v_pk_fma_f32 v[154:155], v[154:155], v[92:93], v[108:109] op_sel:[0,0,0] op_sel_hi:[1,0,0]
v_pk_fma_f32 v[156:157], v[156:157], v[92:93], v[108:109] op_sel:[0,0,0] op_sel_hi:[1,0,0]
v_pk_fma_f32 v[158:159], v[158:159], v[92:93], v[108:109] op_sel:[0,0,0] op_sel_hi:[1,0,0]
v_pk_fma_f32 v[160:161], v[160:161], v[92:93], v[108:109] op_sel:[0,0,0] op_sel_hi:[1,0,0]
v_pk_fma_f32 v[162:163], v[162:163], v[92:93], v[108:109] op_sel:[0,0,0] op_sel_hi:[1,0,0]
v_pk_mul_f32 v[140:141], v[140:141], v[164:165]
v_pk_mul_f32 v[142:143], v[142:143], v[166:167]
v_pk_mul_f32 v[144:145], v[144:145], v[168:169]
v_pk_mul_f32 v[146:147], v[146:147], v[170:171]
v_pk_mul_f32 v[148:149], v[148:149], v[172:173]
v_pk_mul_f32 v[150:151], v[150:151], v[174:175]
v_pk_mul_f32 v[152:153], v[152:153], v[176:177]
v_pk_mul_f32 v[154:155], v[154:155], v[178:179]
v_pk_mul_f32 v[156:157], v[156:157], v[180:181]
v_pk_mul_f32 v[158:159], v[158:159], v[182:183]
v_pk_mul_f32 v[160:161], v[160:161], v[184:185]
v_pk_mul_f32 v[162:163], v[162:163], v[186:187]
v_add_u32_sdwa v140, v140, v212 dst_sel:DWORD dst_unused:UNUSED_PAD src0_sel:DWORD src1_sel:WORD_0
v_add_u32_sdwa v141, v141, v212 dst_sel:DWORD dst_unused:UNUSED_PAD src0_sel:DWORD src1_sel:WORD_1
v_perm_b32 v112, v141, v140, s53
v_add_u32_sdwa v142, v142, v213 dst_sel:DWORD dst_unused:UNUSED_PAD src0_sel:DWORD src1_sel:WORD_0
v_add_u32_sdwa v143, v143, v213 dst_sel:DWORD dst_unused:UNUSED_PAD src0_sel:DWORD src1_sel:WORD_1
v_perm_b32 v113, v143, v142, s53
v_add_u32_sdwa v144, v144, v214 dst_sel:DWORD dst_unused:UNUSED_PAD src0_sel:DWORD src1_sel:WORD_0
v_add_u32_sdwa v145, v145, v214 dst_sel:DWORD dst_unused:UNUSED_PAD src0_sel:DWORD src1_sel:WORD_1
v_perm_b32 v114, v145, v144, s53
v_add_u32_sdwa v146, v146, v215 dst_sel:DWORD dst_unused:UNUSED_PAD src0_sel:DWORD src1_sel:WORD_0
)"
R"(v_add_u32_sdwa v147, v147, v215 dst_sel:DWORD dst_unused:UNUSED_PAD src0_sel:DWORD src1_sel:WORD_1
v_perm_b32 v115, v147, v146, s53
v_add_u32_sdwa v148, v148, v216 dst_sel:DWORD dst_unused:UNUSED_PAD src0_sel:DWORD src1_sel:WORD_0
v_add_u32_sdwa v149, v149, v216 dst_sel:DWORD dst_unused:UNUSED_PAD src0_sel:DWORD src1_sel:WORD_1
v_perm_b32 v116, v149, v148, s53
v_add_u32_sdwa v150, v150, v217 dst_sel:DWORD dst_unused:UNUSED_PAD src0_sel:DWORD src1_sel:WORD_0
v_add_u32_sdwa v151, v151, v217 dst_sel:DWORD dst_unused:UNUSED_PAD src0_sel:DWORD src1_sel:WORD_1
v_perm_b32 v117, v151, v150, s53
v_add_u32_sdwa v152, v152, v218 dst_sel:DWORD dst_unused:UNUSED_PAD src0_sel:DWORD src1_sel:WORD_0
v_add_u32_sdwa v153, v153, v218 dst_sel:DWORD dst_unused:UNUSED_PAD src0_sel:DWORD src1_sel:WORD_1
v_perm_b32 v118, v153, v152, s53
v_add_u32_sdwa v154, v154, v219 dst_sel:DWORD dst_unused:UNUSED_PAD src0_sel:DWORD src1_sel:WORD_0
v_add_u32_sdwa v155, v155, v219 dst_sel:DWORD dst_unused:UNUSED_PAD src0_sel:DWORD src1_sel:WORD_1
v_perm_b32 v119, v155, v154, s53
v_add_u32_sdwa v156, v156, v220 dst_sel:DWORD dst_unused:UNUSED_PAD src0_sel:DWORD src1_sel:WORD_0
v_add_u32_sdwa v157, v157, v220 dst_sel:DWORD dst_unused:UNUSED_PAD src0_sel:DWORD src1_sel:WORD_1
v_perm_b32 v120, v157, v156, s53
v_add_u32_sdwa v158, v158, v221 dst_sel:DWORD dst_unused:UNUSED_PAD src0_sel:DWORD src1_sel:WORD_0
v_add_u32_sdwa v159, v159, v221 dst_sel:DWORD dst_unused:UNUSED_PAD src0_sel:DWORD src1_sel:WORD_1
v_perm_b32 v121, v159, v158, s53
v_add_u32_sdwa v160, v160, v222 dst_sel:DWORD dst_unused:UNUSED_PAD src0_sel:DWORD src1_sel:WORD_0
v_add_u32_sdwa v161, v161, v222 dst_sel:DWORD dst_unused:UNUSED_PAD src0_sel:DWORD src1_sel:WORD_1
v_perm_b32 v122, v161, v160, s53
v_add_u32_sdwa v162, v162, v223 dst_sel:DWORD dst_unused:UNUSED_PAD src0_sel:DWORD src1_sel:WORD_0
v_add_u32_sdwa v163, v163, v223 dst_sel:DWORD dst_unused:UNUSED_PAD src0_sel:DWORD src1_sel:WORD_1
v_perm_b32 v123, v163, v162, s53
global_store_dwordx4 v85, v[112:115], s[78:79] offset:0 sc1
global_store_dwordx4 v85, v[116:119], s[78:79] offset:1024 sc1
global_store_dwordx4 v85, v[120:123], s[78:79] offset:2048 sc1
v_lshlrev_b32 v140, 16, v200
v_and_b32 v141, s72, v200
v_lshlrev_b32 v142, 16, v201
v_and_b32 v143, s72, v201
v_lshlrev_b32 v144, 16, v202
v_and_b32 v145, s72, v202
v_lshlrev_b32 v146, 16, v203
v_and_b32 v147, s72, v203
v_lshlrev_b32 v148, 16, v204
v_and_b32 v149, s72, v204
v_lshlrev_b32 v150, 16, v205
v_and_b32 v151, s72, v205
v_lshlrev_b32 v152, 16, v206
v_and_b32 v153, s72, v206
v_lshlrev_b32 v154, 16, v207
v_and_b32 v155, s72, v207
v_lshlrev_b32 v156, 16, v208
v_and_b32 v157, s72, v208
v_lshlrev_b32 v158, 16, v209
v_and_b32 v159, s72, v209
v_lshlrev_b32 v160, 16, v210
v_and_b32 v161, s72, v210
v_lshlrev_b32 v162, 16, v211
v_and_b32 v163, s72, v211
v_pk_fma_f32 v[140:141], v[140:141], v[92:93], v[108:109] op_sel:[0,0,1] op_sel_hi:[1,0,1]
v_pk_fma_f32 v[142:143], v[142:143], v[92:93], v[108:109] op_sel:[0,0,1] op_sel_hi:[1,0,1]
v_pk_fma_f32 v[144:145], v[144:145], v[92:93], v[108:109] op_sel:[0,0,1] op_sel_hi:[1,0,1]
v_pk_fma_f32 v[146:147], v[146:147], v[92:93], v[108:109] op_sel:[0,0,1] op_sel_hi:[1,0,1]
v_pk_fma_f32 v[148:149], v[148:149], v[92:93], v[108:109] op_sel:[0,0,1] op_sel_hi:[1,0,1]
v_pk_fma_f32 v[150:151], v[150:151], v[92:93], v[108:109] op_sel:[0,0,1] op_sel_hi:[1,0,1]
v_pk_fma_f32 v[152:153], v[152:153], v[92:93], v[108:109] op_sel:[0,0,1] op_sel_hi:[1,0,1]
v_pk_fma_f32 v[154:155], v[154:155], v[92:93], v[108:109] op_sel:[0,0,1] op_sel_hi:[1,0,1]
v_pk_fma_f32 v[156:157], v[156:157], v[92:93], v[108:109] op_sel:[0,0,1] op_sel_hi:[1,0,1]
v_pk_fma_f32 v[158:159], v[158:159], v[92:93], v[108:109] op_sel:[0,0,1] op_sel_hi:[1,0,1]
v_pk_fma_f32 v[160:161], v[160:161], v[92:93], v[108:109] op_sel:[0,0,1] op_sel_hi:[1,0,1]
v_pk_fma_f32 v[162:163], v[162:163], v[92:93], v[108:109] op_sel:[0,0,1] op_sel_hi:[1,0,1]
v_pk_mul_f32 v[140:141], v[140:141], v[164:165]
v_pk_mul_f32 v[142:143], v[142:143], v[166:167]
v_pk_mul_f32 v[144:145], v[144:145], v[168:169]
v_pk_mul_f32 v[146:147], v[146:147], v[170:171]
v_pk_mul_f32 v[148:149], v[148:149], v[172:173]
v_pk_mul_f32 v[150:151], v[150:151], v[174:175]
v_pk_mul_f32 v[152:153], v[152:153], v[176:177]
v_pk_mul_f32 v[154:155], v[154:155], v[178:179]
v_pk_mul_f32 v[156:157], v[156:157], v[180:181]
v_pk_mul_f32 v[158:159], v[158:159], v[182:183]
v_pk_mul_f32 v[160:161], v[160:161], v[184:185]
v_pk_mul_f32 v[162:163], v[162:163], v[186:187]
v_add_u32_sdwa v140, v140, v212 dst_sel:DWORD dst_unused:UNUSED_PAD src0_sel:DWORD src1_sel:WORD_1
v_add_u32_sdwa v141, v141, v212 dst_sel:DWORD dst_unused:UNUSED_PAD src0_sel:DWORD src1_sel:WORD_0
v_perm_b32 v124, v141, v140, s53
v_add_u32_sdwa v142, v142, v213 dst_sel:DWORD dst_unused:UNUSED_PAD src0_sel:DWORD src1_sel:WORD_1
v_add_u32_sdwa v143, v143, v213 dst_sel:DWORD dst_unused:UNUSED_PAD src0_sel:DWORD src1_sel:WORD_0
v_perm_b32 v125, v143, v142, s53
v_add_u32_sdwa v144, v144, v214 dst_sel:DWORD dst_unused:UNUSED_PAD src0_sel:DWORD src1_sel:WORD_1
v_add_u32_sdwa v145, v145, v214 dst_sel:DWORD dst_unused:UNUSED_PAD src0_sel:DWORD src1_sel:WORD_0
v_perm_b32 v126, v145, v144, s53
v_add_u32_sdwa v146, v146, v215 dst_sel:DWORD dst_unused:UNUSED_PAD src0_sel:DWORD src1_sel:WORD_1
v_add_u32_sdwa v147, v147, v215 dst_sel:DWORD dst_unused:UNUSED_PAD src0_sel:DWORD src1_sel:WORD_0
v_perm_b32 v127, v147, v146, s53
v_add_u32_sdwa v148, v148, v216 dst_sel:DWORD dst_unused:UNUSED_PAD src0_sel:DWORD src1_sel:WORD_1
v_add_u32_sdwa v149, v149, v216 dst_sel:DWORD dst_unused:UNUSED_PAD src0_sel:DWORD src1_sel:WORD_0
v_perm_b32 v128, v149, v148, s53
v_add_u32_sdwa v150, v150, v217 dst_sel:DWORD dst_unused:UNUSED_PAD src0_sel:DWORD src1_sel:WORD_1
v_add_u32_sdwa v151, v151, v217 dst_sel:DWORD dst_unused:UNUSED_PAD src0_sel:DWORD src1_sel:WORD_0
v_perm_b32 v129, v151, v150, s53
v_add_u32_sdwa v152, v152, v218 dst_sel:DWORD dst_unused:UNUSED_PAD src0_sel:DWORD src1_sel:WORD_1
v_add_u32_sdwa v153, v153, v218 dst_sel:DWORD dst_unused:UNUSED_PAD src0_sel:DWORD src1_sel:WORD_0
v_perm_b32 v130, v153, v152, s53
v_add_u32_sdwa v154, v154, v219 dst_sel:DWORD dst_unused:UNUSED_PAD src0_sel:DWORD src1_sel:WORD_1
v_add_u32_sdwa v155, v155, v219 dst_sel:DWORD dst_unused:UNUSED_PAD src0_sel:DWORD src1_sel:WORD_0
v_perm_b32 v131, v155, v154, s53
v_add_u32_sdwa v156, v156, v220 dst_sel:DWORD dst_unused:UNUSED_PAD src0_sel:DWORD src1_sel:WORD_1
v_add_u32_sdwa v157, v157, v220 dst_sel:DWORD dst_unused:UNUSED_PAD src0_sel:DWORD src1_sel:WORD_0
v_perm_b32 v132, v157, v156, s53
v_add_u32_sdwa v158, v158, v221 dst_sel:DWORD dst_unused:UNUSED_PAD src0_sel:DWORD src1_sel:WORD_1
v_add_u32_sdwa v159, v159, v221 dst_sel:DWORD dst_unused:UNUSED_PAD src0_sel:DWORD src1_sel:WORD_0
v_perm_b32 v133, v159, v158, s53
v_add_u32_sdwa v160, v160, v222 dst_sel:DWORD dst_unused:UNUSED_PAD src0_sel:DWORD src1_sel:WORD_1
v_add_u32_sdwa v161, v161, v222 dst_sel:DWORD dst_unused:UNUSED_PAD src0_sel:DWORD src1_sel:WORD_0
v_perm_b32 v134, v161, v160, s53
v_add_u32_sdwa v162, v162, v223 dst_sel:DWORD dst_unused:UNUSED_PAD src0_sel:DWORD src1_sel:WORD_1
v_add_u32_sdwa v163, v163, v223 dst_sel:DWORD dst_unused:UNUSED_PAD src0_sel:DWORD src1_sel:WORD_0
v_perm_b32 v135, v163, v162, s53
global_store_dwordx4 v86, v[124:127], s[78:79] offset:0 sc1
global_store_dwordx4 v86, v[128:131], s[78:79] offset:1024 sc1
global_store_dwordx4 v86, v[132:135], s[78:79] offset:2048 sc1
v_dot2_f32_bf16 v232, v188, v188, v232
v_dot2_f32_bf16 v233, v200, v200, v233
v_dot2_f32_bf16 v232, v189, v189, v232
v_dot2_f32_bf16 v233, v201, v201, v233
v_dot2_f32_bf16 v232, v190, v190, v232
v_dot2_f32_bf16 v233, v202, v202, v233
v_dot2_f32_bf16 v232, v191, v191, v232
v_dot2_f32_bf16 v233, v203, v203, v233
v_dot2_f32_bf16 v232, v192, v192, v232
v_dot2_f32_bf16 v233, v204, v204, v233
v_dot2_f32_bf16 v232, v193, v193, v232
v_dot2_f32_bf16 v233, v205, v205, v233
v_dot2_f32_bf16 v232, v194, v194, v232
v_dot2_f32_bf16 v233, v206, v206, v233
v_dot2_f32_bf16 v232, v195, v195, v232
v_dot2_f32_bf16 v233, v207, v207, v233
v_dot2_f32_bf16 v232, v196, v196, v232
v_dot2_f32_bf16 v233, v208, v208, v233
v_dot2_f32_bf16 v232, v197, v197, v232
v_dot2_f32_bf16 v233, v209, v209, v233
v_dot2_f32_bf16 v232, v198, v198, v232
v_dot2_f32_bf16 v233, v210, v210, v233
v_dot2_f32_bf16 v232, v199, v199, v232
v_dot2_f32_bf16 v233, v211, v211, v233
v_add_u32 v85, 2108416, v85
v_add_u32 v86, 2108416, v86
s_mov_b64 s[40:41], s[82:83]
s_lshr_b32 s57, 16, %[w]
s_waitcnt vmcnt(0)
DN
s_mov_b32 s57, 0
s_mov_b64 exec, s[50:51]
global_store_dwordx4 v85, v[188:191], s[78:79] offset:0 sc1
global_store_dwordx4 v85, v[192:195], s[78:79] offset:1024 sc1
global_store_dwordx4 v85, v[196:199], s[78:79] offset:2048 sc1
global_store_dwordx4 v86, v[200:203], s[78:79] offset:0 sc1
global_store_dwordx4 v86, v[204:207], s[78:79] offset:1024 sc1
global_store_dwordx4 v86, v[208:211], s[78:79] offset:2048 sc1
s_mov_b64 exec, -1
s_branch 50b
32:
s_cmp_lt_u32 s73, 72
s_cbranch_scc0 36f
s_mul_i32 s53, s73, 43
s_lshr_b32 s53, s53, 8
s_mul_i32 s72, s53, 6
s_sub_u32 s70, s73, s72
s_lshl2_add_u32 s71, s53, %[w]
v_lshrrev_b32 v100, 4, v16
v_and_b32 v101, 1, v100
v_lshl_add_u32 v100, v101, 2, v100
v_lshl_add_u32 v100, s71, 3, v100
v_and_b32 v102, 15, v16
s_cmp_lt_u32 s70, 4
s_cselect_b32 s53, 3, 2
s_cselect_b32 s72, 4, 64
v_lshlrev_b32 v102, s53, v102
v_lshl_add_u32 v102, s70, 7, v102
v_mad_u32_u24 v101, v100, s97, v102
v_xor_b32 v100, 4, v100
v_mad_u32_u24 v100, v100, s97, v102
v_add_u32 v103, s72, v101
v_add_u32 v102, s72, v100
global_load_dword v128, v101, s[90:91] offset:0
global_load_dword v129, v103, s[90:91] offset:0
global_load_dword v130, v101, s[90:91] offset:768
global_load_dword v131, v103, s[90:91] offset:768
global_load_dword v132, v101, s[90:91] offset:1536
global_load_dword v133, v103, s[90:91] offset:1536
global_load_dword v134, v101, s[90:91] offset:2304
global_load_dword v135, v103, s[90:91] offset:2304
global_load_dword v136, v100, s[90:91] offset:0
global_load_dword v137, v102, s[90:91] offset:0
global_load_dword v138, v100, s[90:91] offset:768
global_load_dword v139, v102, s[90:91] offset:768
global_load_dword v140, v100, s[90:91] offset:1536
global_load_dword v141, v102, s[90:91] offset:1536
global_load_dword v142, v100, s[90:91] offset:2304
global_load_dword v143, v102, s[90:91] offset:2304
s_branch 33b
36:
s_sub_u32 s70, s73, 72
s_lshr_b32 s71, s70, 2
s_and_b32 s53, s70, 3
s_mul_i32 s53, s53, 384
v_lshrrev_b32 v100, 3, v75
v_and_b32 v101, 0x70, v0
v_add_u32 v100, s53, v100
v_lshl_add_u32 v101, s71, 7, v101
s_movk_i32 s53, 768
v_mad_u32_u24 v101, v100, s53, v101
v_lshlrev_b32 v100, 1, v100
global_load_dwordx4 v[128:131], v101, s[90:91]
global_load_ushort v176, v100, s[88:89] offset:0
v_add_u32 v101, 0x6000, v101
global_load_dwordx4 v[132:135], v101, s[90:91]
global_load_ushort v177, v100, s[88:89] offset:64
v_add_u32 v101, 0x6000, v101
global_load_dwordx4 v[136:139], v101, s[90:91]
global_load_ushort v178, v100, s[88:89] offset:128
v_add_u32 v101, 0x6000, v101
global_load_dwordx4 v[140:143], v101, s[90:91]
global_load_ushort v179, v100, s[88:89] offset:192
v_add_u32 v101, 0x6000, v101
global_load_dwordx4 v[144:147], v101, s[90:91]
global_load_ushort v180, v100, s[88:89] offset:256
v_add_u32 v101, 0x6000, v101
global_load_dwordx4 v[148:151], v101, s[90:91]
global_load_ushort v181, v100, s[88:89] offset:320
v_add_u32 v101, 0x6000, v101
global_load_dwordx4 v[152:155], v101, s[90:91]
global_load_ushort v182, v100, s[88:89] offset:384
v_add_u32 v101, 0x6000, v101
global_load_dwordx4 v[156:159], v101, s[90:91]
global_load_ushort v183, v100, s[88:89] offset:448
v_add_u32 v101, 0x6000, v101
global_load_dwordx4 v[160:163], v101, s[90:91]
global_load_ushort v184, v100, s[88:89] offset:512
v_add_u32 v101, 0x6000, v101
global_load_dwordx4 v[164:167], v101, s[90:91]
global_load_ushort v185, v100, s[88:89] offset:576
v_add_u32 v101, 0x6000, v101
global_load_dwordx4 v[168:171], v101, s[90:91]
global_load_ushort v186, v100, s[88:89] offset:640
v_add_u32 v101, 0x6000, v101
global_load_dwordx4 v[172:175], v101, s[90:91]
global_load_ushort v187, v100, s[88:89] offset:704
v_add_u32 v101, 0x6000, v101
s_branch 33b
34:
s_waitcnt vmcnt(0)
s_mov_b32 s60, 0xffff0000
s_cmp_lt_u32 s73, 72
s_cbranch_scc0 37f
v_mov_b32 v100, 0
v_mov_b32 v101, 0
s_mov_b32 s61, 0x39800000
v_lshlrev_b32 v144, 16, v128
v_and_b32 v145, s60, v128
v_lshlrev_b32 v146, 16, v129
v_and_b32 v147, s60, v129
v_max3_f32 v102, |v144|, |v145|, |v146|
v_max_f32 v102, v102, |v147|
v_cmp_le_f32 vcc, s61, v102
v_cmp_neq_f32 s[62:63], 0, v102
v_max_f32 v101, v101, v102
v_addc_co_u32 v100, vcc, v100, v100, vcc
v_addc_co_u32 v100, vcc, v100, v100, s[62:63]
v_lshlrev_b32 v148, 16, v130
v_and_b32 v149, s60, v130
v_lshlrev_b32 v150, 16, v131
v_and_b32 v151, s60, v131
v_max3_f32 v102, |v148|, |v149|, |v150|
v_max_f32 v102, v102, |v151|
v_cmp_le_f32 vcc, s61, v102
v_cmp_neq_f32 s[62:63], 0, v102
v_max_f32 v101, v101, v102
v_addc_co_u32 v100, vcc, v100, v100, vcc
v_addc_co_u32 v100, vcc, v100, v100, s[62:63]
v_lshlrev_b32 v152, 16, v132
v_and_b32 v153, s60, v132
v_lshlrev_b32 v154, 16, v133
v_and_b32 v155, s60, v133
v_max3_f32 v102, |v152|, |v153|, |v154|
v_max_f32 v102, v102, |v155|
v_cmp_le_f32 vcc, s61, v102
v_cmp_neq_f32 s[62:63], 0, v102
v_max_f32 v101, v101, v102
v_addc_co_u32 v100, vcc, v100, v100, vcc
v_addc_co_u32 v100, vcc, v100, v100, s[62:63]
v_lshlrev_b32 v156, 16, v134
v_and_b32 v157, s60, v134
v_lshlrev_b32 v158, 16, v135
v_and_b32 v159, s60, v135
v_max3_f32 v102, |v156|, |v157|, |v158|
v_max_f32 v102, v102, |v159|
v_cmp_le_f32 vcc, s61, v102
v_cmp_neq_f32 s[62:63], 0, v102
v_max_f32 v101, v101, v102
v_addc_co_u32 v100, vcc, v100, v100, vcc
v_addc_co_u32 v100, vcc, v100, v100, s[62:63]
v_lshlrev_b32 v160, 16, v136
v_and_b32 v161, s60, v136
v_lshlrev_b32 v162, 16, v137
v_and_b32 v163, s60, v137
v_max3_f32 v102, |v160|, |v161|, |v162|
v_max_f32 v102, v102, |v163|
v_cmp_le_f32 vcc, s61, v102
v_cmp_neq_f32 s[62:63], 0, v102
v_max_f32 v101, v101, v102
v_addc_co_u32 v100, vcc, v100, v100, vcc
v_addc_co_u32 v100, vcc, v100, v100, s[62:63]
v_lshlrev_b32 v164, 16, v138
v_and_b32 v165, s60, v138
v_lshlrev_b32 v166, 16, v139
v_and_b32 v167, s60, v139
v_max3_f32 v102, |v164|, |v165|, |v166|
v_max_f32 v102, v102, |v167|
v_cmp_le_f32 vcc, s61, v102
v_cmp_neq_f32 s[62:63], 0, v102
v_max_f32 v101, v101, v102
v_addc_co_u32 v100, vcc, v100, v100, vcc
v_addc_co_u32 v100, vcc, v100, v100, s[62:63]
v_lshlrev_b32 v168, 16, v140
v_and_b32 v169, s60, v140
v_lshlrev_b32 v170, 16, v141
v_and_b32 v171, s60, v141
v_max3_f32 v102, |v168|, |v169|, |v170|
v_max_f32 v102, v102, |v171|
v_cmp_le_f32 vcc, s61, v102
v_cmp_neq_f32 s[62:63], 0, v102
v_max_f32 v101, v101, v102
v_addc_co_u32 v100, vcc, v100, v100, vcc
v_addc_co_u32 v100, vcc, v100, v100, s[62:63]
v_lshlrev_b32 v172, 16, v142
v_and_b32 v173, s60, v142
v_lshlrev_b32 v174, 16, v143
v_and_b32 v175, s60, v143
v_max3_f32 v102, |v172|, |v173|, |v174|
v_max_f32 v102, v102, |v175|
v_cmp_le_f32 vcc, s61, v102
v_cmp_neq_f32 s[62:63], 0, v102
v_max_f32 v101, v101, v102
v_addc_co_u32 v100, vcc, v100, v100, vcc
v_addc_co_u32 v100, vcc, v100, v100, s[62:63]
s_and_b32 s53, s71, 1
s_sub_u32 s72, s71, s53
s_mul_i32 s72, s72, 24
s_add_u32 s72, s72, s53
s_mul_i32 s53, s70, 12
s_lshl1_add_u32 s62, s53, -88
s_cmp_lt_u32 s70, 4
s_cselect_b32 s53, s53, s62
s_cselect_b32 s63, 4, 12
s_add_u32 s72, s72, s53
s_lshl_b32 s63, s63, 10
s_lshl_b32 s72, s72, 10
s_add_u32 s72, s72, 3288064
s_add_u32 s62, s72, 0
v_add_u32 v104, s62, v0
v_cvt_pk_f16_f32 v108, v144, v148
v_cvt_pk_f16_f32 v109, v152, v156
v_cvt_pk_f16_f32 v110, v160, v164
v_cvt_pk_f16_f32 v111, v168, v172
v_or_b32_dpp v100, v100, v100 row_ror:1 row_mask:0xf bank_mask:0xf
global_store_dwordx4 v104, v[108:111], s[78:79] sc1
s_add_u32 s62, s72, 2048
v_add_u32 v105, s62, v0
v_cvt_pk_f16_f32 v112, v145, v149
v_cvt_pk_f16_f32 v113, v153, v157
v_cvt_pk_f16_f32 v114, v161, v165
v_cvt_pk_f16_f32 v115, v169, v173
v_or_b32_dpp v100, v100, v100 row_ror:2 row_mask:0xf bank_mask:0xf
global_store_dwordx4 v105, v[112:115], s[78:79] sc1
s_add_u32 s72, s72, s63
s_add_u32 s62, s72, 0
v_add_u32 v106, s62, v0
v_cvt_pk_f16_f32 v116, v146, v150
v_cvt_pk_f16_f32 v117, v154, v158
v_cvt_pk_f16_f32 v118, v162, v166
v_cvt_pk_f16_f32 v119, v170, v174
v_or_b32_dpp v100, v100, v100 row_ror:4 row_mask:0xf bank_mask:0xf
global_store_dwordx4 v106, v[116:119], s[78:79] sc1
s_add_u32 s62, s72, 2048
v_add_u32 v107, s62, v0
v_cvt_pk_f16_f32 v120, v147, v151
v_cvt_pk_f16_f32 v121, v155, v159
v_cvt_pk_f16_f32 v122, v163, v167
v_cvt_pk_f16_f32 v123, v171, v175
v_or_b32_dpp v100, v100, v100 row_ror:8 row_mask:0xf bank_mask:0xf
global_store_dwordx4 v107, v[120:123], s[78:79] sc1
v_lshrrev_b32 v102, 1, v100
v_cmp_lt_f32 vcc, 0x43fa0000, v101
v_bfi_b32 v100, v102, 0, v100
v_and_b32 v100, 0x5555, v100
v_cmp_ne_u32 s[62:63], 0, v100
s_nop 1
s_or_b64 vcc, vcc, s[62:63]
s_cselect_b32 s95, 14724, 0
s_branch 35b
37:
v_lshlrev_b32 v100, 16, v176
v_lshlrev_b32 v101, 16, v128
v_and_b32 v102, s60, v128
)"
R"(v_mul_f32 v104, v100, v101
v_mul_f32 v105, v100, v102
v_mul_f32 v112, v101, v101
v_mul_f32 v113, v102, v102
v_lshlrev_b32 v101, 16, v129
v_and_b32 v102, s60, v129
v_mul_f32 v106, v100, v101
v_mul_f32 v107, v100, v102
v_mul_f32 v114, v101, v101
v_mul_f32 v115, v102, v102
v_lshlrev_b32 v101, 16, v130
v_and_b32 v102, s60, v130
v_mul_f32 v108, v100, v101
v_mul_f32 v109, v100, v102
v_mul_f32 v116, v101, v101
v_mul_f32 v117, v102, v102
v_lshlrev_b32 v101, 16, v131
v_and_b32 v102, s60, v131
v_mul_f32 v110, v100, v101
v_mul_f32 v111, v100, v102
v_mul_f32 v118, v101, v101
v_mul_f32 v119, v102, v102
v_lshlrev_b32 v100, 16, v177
v_lshlrev_b32 v101, 16, v132
v_and_b32 v102, s60, v132
v_fmac_f32 v104, v100, v101
v_fmac_f32 v105, v100, v102
v_fmac_f32 v112, v101, v101
v_fmac_f32 v113, v102, v102
v_lshlrev_b32 v101, 16, v133
v_and_b32 v102, s60, v133
v_fmac_f32 v106, v100, v101
v_fmac_f32 v107, v100, v102
v_fmac_f32 v114, v101, v101
v_fmac_f32 v115, v102, v102
v_lshlrev_b32 v101, 16, v134
v_and_b32 v102, s60, v134
v_fmac_f32 v108, v100, v101
v_fmac_f32 v109, v100, v102
v_fmac_f32 v116, v101, v101
v_fmac_f32 v117, v102, v102
v_lshlrev_b32 v101, 16, v135
v_and_b32 v102, s60, v135
v_fmac_f32 v110, v100, v101
v_fmac_f32 v111, v100, v102
v_fmac_f32 v118, v101, v101
v_fmac_f32 v119, v102, v102
v_lshlrev_b32 v100, 16, v178
v_lshlrev_b32 v101, 16, v136
v_and_b32 v102, s60, v136
v_fmac_f32 v104, v100, v101
v_fmac_f32 v105, v100, v102
v_fmac_f32 v112, v101, v101
v_fmac_f32 v113, v102, v102
v_lshlrev_b32 v101, 16, v137
v_and_b32 v102, s60, v137
v_fmac_f32 v106, v100, v101
v_fmac_f32 v107, v100, v102
v_fmac_f32 v114, v101, v101
v_fmac_f32 v115, v102, v102
v_lshlrev_b32 v101, 16, v138
v_and_b32 v102, s60, v138
v_fmac_f32 v108, v100, v101
v_fmac_f32 v109, v100, v102
v_fmac_f32 v116, v101, v101
v_fmac_f32 v117, v102, v102
v_lshlrev_b32 v101, 16, v139
v_and_b32 v102, s60, v139
v_fmac_f32 v110, v100, v101
v_fmac_f32 v111, v100, v102
v_fmac_f32 v118, v101, v101
v_fmac_f32 v119, v102, v102
v_lshlrev_b32 v100, 16, v179
v_lshlrev_b32 v101, 16, v140
v_and_b32 v102, s60, v140
v_fmac_f32 v104, v100, v101
v_fmac_f32 v105, v100, v102
v_fmac_f32 v112, v101, v101
v_fmac_f32 v113, v102, v102
v_lshlrev_b32 v101, 16, v141
v_and_b32 v102, s60, v141
v_fmac_f32 v106, v100, v101
v_fmac_f32 v107, v100, v102
v_fmac_f32 v114, v101, v101
v_fmac_f32 v115, v102, v102
v_lshlrev_b32 v101, 16, v142
v_and_b32 v102, s60, v142
v_fmac_f32 v108, v100, v101
v_fmac_f32 v109, v100, v102
v_fmac_f32 v116, v101, v101
v_fmac_f32 v117, v102, v102
v_lshlrev_b32 v101, 16, v143
v_and_b32 v102, s60, v143
v_fmac_f32 v110, v100, v101
v_fmac_f32 v111, v100, v102
v_fmac_f32 v118, v101, v101
v_fmac_f32 v119, v102, v102
v_lshlrev_b32 v100, 16, v180
v_lshlrev_b32 v101, 16, v144
v_and_b32 v102, s60, v144
v_fmac_f32 v104, v100, v101
v_fmac_f32 v105, v100, v102
v_fmac_f32 v112, v101, v101
v_fmac_f32 v113, v102, v102
v_lshlrev_b32 v101, 16, v145
v_and_b32 v102, s60, v145
v_fmac_f32 v106, v100, v101
v_fmac_f32 v107, v100, v102
v_fmac_f32 v114, v101, v101
v_fmac_f32 v115, v102, v102
v_lshlrev_b32 v101, 16, v146
v_and_b32 v102, s60, v146
v_fmac_f32 v108, v100, v101
v_fmac_f32 v109, v100, v102
v_fmac_f32 v116, v101, v101
v_fmac_f32 v117, v102, v102
v_lshlrev_b32 v101, 16, v147
v_and_b32 v102, s60, v147
v_fmac_f32 v110, v100, v101
v_fmac_f32 v111, v100, v102
v_fmac_f32 v118, v101, v101
v_fmac_f32 v119, v102, v102
v_lshlrev_b32 v100, 16, v181
v_lshlrev_b32 v101, 16, v148
v_and_b32 v102, s60, v148
v_fmac_f32 v104, v100, v101
v_fmac_f32 v105, v100, v102
v_fmac_f32 v112, v101, v101
v_fmac_f32 v113, v102, v102
v_lshlrev_b32 v101, 16, v149
v_and_b32 v102, s60, v149
v_fmac_f32 v106, v100, v101
v_fmac_f32 v107, v100, v102
v_fmac_f32 v114, v101, v101
v_fmac_f32 v115, v102, v102
v_lshlrev_b32 v101, 16, v150
v_and_b32 v102, s60, v150
v_fmac_f32 v108, v100, v101
v_fmac_f32 v109, v100, v102
v_fmac_f32 v116, v101, v101
v_fmac_f32 v117, v102, v102
v_lshlrev_b32 v101, 16, v151
v_and_b32 v102, s60, v151
v_fmac_f32 v110, v100, v101
v_fmac_f32 v111, v100, v102
v_fmac_f32 v118, v101, v101
v_fmac_f32 v119, v102, v102
v_lshlrev_b32 v100, 16, v182
v_lshlrev_b32 v101, 16, v152
v_and_b32 v102, s60, v152
v_fmac_f32 v104, v100, v101
v_fmac_f32 v105, v100, v102
v_fmac_f32 v112, v101, v101
v_fmac_f32 v113, v102, v102
v_lshlrev_b32 v101, 16, v153
v_and_b32 v102, s60, v153
v_fmac_f32 v106, v100, v101
v_fmac_f32 v107, v100, v102
v_fmac_f32 v114, v101, v101
v_fmac_f32 v115, v102, v102
v_lshlrev_b32 v101, 16, v154
v_and_b32 v102, s60, v154
v_fmac_f32 v108, v100, v101
v_fmac_f32 v109, v100, v102
v_fmac_f32 v116, v101, v101
v_fmac_f32 v117, v102, v102
v_lshlrev_b32 v101, 16, v155
v_and_b32 v102, s60, v155
v_fmac_f32 v110, v100, v101
v_fmac_f32 v111, v100, v102
v_fmac_f32 v118, v101, v101
v_fmac_f32 v119, v102, v102
v_lshlrev_b32 v100, 16, v183
v_lshlrev_b32 v101, 16, v156
v_and_b32 v102, s60, v156
v_fmac_f32 v104, v100, v101
v_fmac_f32 v105, v100, v102
v_fmac_f32 v112, v101, v101
v_fmac_f32 v113, v102, v102
v_lshlrev_b32 v101, 16, v157
v_and_b32 v102, s60, v157
v_fmac_f32 v106, v100, v101
v_fmac_f32 v107, v100, v102
v_fmac_f32 v114, v101, v101
v_fmac_f32 v115, v102, v102
v_lshlrev_b32 v101, 16, v158
v_and_b32 v102, s60, v158
v_fmac_f32 v108, v100, v101
v_fmac_f32 v109, v100, v102
v_fmac_f32 v116, v101, v101
v_fmac_f32 v117, v102, v102
v_lshlrev_b32 v101, 16, v159
v_and_b32 v102, s60, v159
v_fmac_f32 v110, v100, v101
v_fmac_f32 v111, v100, v102
v_fmac_f32 v118, v101, v101
v_fmac_f32 v119, v102, v102
v_lshlrev_b32 v100, 16, v184
v_lshlrev_b32 v101, 16, v160
v_and_b32 v102, s60, v160
v_fmac_f32 v104, v100, v101
v_fmac_f32 v105, v100, v102
v_fmac_f32 v112, v101, v101
v_fmac_f32 v113, v102, v102
v_lshlrev_b32 v101, 16, v161
v_and_b32 v102, s60, v161
v_fmac_f32 v106, v100, v101
v_fmac_f32 v107, v100, v102
v_fmac_f32 v114, v101, v101
v_fmac_f32 v115, v102, v102
v_lshlrev_b32 v101, 16, v162
v_and_b32 v102, s60, v162
v_fmac_f32 v108, v100, v101
v_fmac_f32 v109, v100, v102
v_fmac_f32 v116, v101, v101
v_fmac_f32 v117, v102, v102
v_lshlrev_b32 v101, 16, v163
v_and_b32 v102, s60, v163
v_fmac_f32 v110, v100, v101
v_fmac_f32 v111, v100, v102
v_fmac_f32 v118, v101, v101
v_fmac_f32 v119, v102, v102
v_lshlrev_b32 v100, 16, v185
v_lshlrev_b32 v101, 16, v164
v_and_b32 v102, s60, v164
v_fmac_f32 v104, v100, v101
v_fmac_f32 v105, v100, v102
v_fmac_f32 v112, v101, v101
v_fmac_f32 v113, v102, v102
v_lshlrev_b32 v101, 16, v165
v_and_b32 v102, s60, v165
v_fmac_f32 v106, v100, v101
v_fmac_f32 v107, v100, v102
v_fmac_f32 v114, v101, v101
v_fmac_f32 v115, v102, v102
v_lshlrev_b32 v101, 16, v166
v_and_b32 v102, s60, v166
v_fmac_f32 v108, v100, v101
v_fmac_f32 v109, v100, v102
v_fmac_f32 v116, v101, v101
v_fmac_f32 v117, v102, v102
v_lshlrev_b32 v101, 16, v167
v_and_b32 v102, s60, v167
v_fmac_f32 v110, v100, v101
v_fmac_f32 v111, v100, v102
v_fmac_f32 v118, v101, v101
v_fmac_f32 v119, v102, v102
v_lshlrev_b32 v100, 16, v186
v_lshlrev_b32 v101, 16, v168
v_and_b32 v102, s60, v168
v_fmac_f32 v104, v100, v101
v_fmac_f32 v105, v100, v102
v_fmac_f32 v112, v101, v101
v_fmac_f32 v113, v102, v102
v_lshlrev_b32 v101, 16, v169
v_and_b32 v102, s60, v169
v_fmac_f32 v106, v100, v101
v_fmac_f32 v107, v100, v102
v_fmac_f32 v114, v101, v101
v_fmac_f32 v115, v102, v102
v_lshlrev_b32 v101, 16, v170
v_and_b32 v102, s60, v170
v_fmac_f32 v108, v100, v101
v_fmac_f32 v109, v100, v102
v_fmac_f32 v116, v101, v101
v_fmac_f32 v117, v102, v102
v_lshlrev_b32 v101, 16, v171
v_and_b32 v102, s60, v171
v_fmac_f32 v110, v100, v101
v_fmac_f32 v111, v100, v102
v_fmac_f32 v118, v101, v101
v_fmac_f32 v119, v102, v102
v_lshlrev_b32 v100, 16, v187
v_lshlrev_b32 v101, 16, v172
v_and_b32 v102, s60, v172
v_fmac_f32 v104, v100, v101
v_fmac_f32 v105, v100, v102
v_fmac_f32 v112, v101, v101
v_fmac_f32 v113, v102, v102
v_lshlrev_b32 v101, 16, v173
v_and_b32 v102, s60, v173
v_fmac_f32 v106, v100, v101
v_fmac_f32 v107, v100, v102
v_fmac_f32 v114, v101, v101
v_fmac_f32 v115, v102, v102
v_lshlrev_b32 v101, 16, v174
v_and_b32 v102, s60, v174
v_fmac_f32 v108, v100, v101
v_fmac_f32 v109, v100, v102
v_fmac_f32 v116, v101, v101
v_fmac_f32 v117, v102, v102
v_lshlrev_b32 v101, 16, v175
v_and_b32 v102, s60, v175
v_fmac_f32 v110, v100, v101
v_fmac_f32 v111, v100, v102
v_fmac_f32 v118, v101, v101
v_fmac_f32 v119, v102, v102
v_lshrrev_b32 v100, 4, v75
v_and_b32 v101, 7, v16
v_lshlrev_b32 v101, 5, v101
v_lshl_add_u32 v100, v100, 8, v101
v_add_u32 v100, 0x20000, v100
v_add_f32_dpp v104, v104, v104 row_shr:8 row_mask:0xf bank_mask:0xf
v_add_f32_dpp v105, v105, v105 row_shr:8 row_mask:0xf bank_mask:0xf
v_add_f32_dpp v106, v106, v106 row_shr:8 row_mask:0xf bank_mask:0xf
v_add_f32_dpp v107, v107, v107 row_shr:8 row_mask:0xf bank_mask:0xf
v_add_f32_dpp v108, v108, v108 row_shr:8 row_mask:0xf bank_mask:0xf
v_add_f32_dpp v109, v109, v109 row_shr:8 row_mask:0xf bank_mask:0xf
v_add_f32_dpp v110, v110, v110 row_shr:8 row_mask:0xf bank_mask:0xf
v_add_f32_dpp v111, v111, v111 row_shr:8 row_mask:0xf bank_mask:0xf
v_add_f32_dpp v112, v112, v112 row_shr:8 row_mask:0xf bank_mask:0xf
v_add_f32_dpp v113, v113, v113 row_shr:8 row_mask:0xf bank_mask:0xf
v_add_f32_dpp v114, v114, v114 row_shr:8 row_mask:0xf bank_mask:0xf
v_add_f32_dpp v115, v115, v115 row_shr:8 row_mask:0xf bank_mask:0xf
v_add_f32_dpp v116, v116, v116 row_shr:8 row_mask:0xf bank_mask:0xf
v_add_f32_dpp v117, v117, v117 row_shr:8 row_mask:0xf bank_mask:0xf
v_add_f32_dpp v118, v118, v118 row_shr:8 row_mask:0xf bank_mask:0xf
v_add_f32_dpp v119, v119, v119 row_shr:8 row_mask:0xf bank_mask:0xf
s_mov_b32 exec_lo, 0xff00ff00
s_mov_b32 exec_hi, 0xff00ff00
ds_write_b128 v100, v[104:107] offset:0
ds_write_b128 v100, v[108:111] offset:16
ds_write_b128 v100, v[112:115] offset:4096
ds_write_b128 v100, v[116:119] offset:4112
s_mov_b64 exec, -1
s_lshl_b32 s53, s37, 12
s_add_u32 s53, s53, 0x20000
v_lshl_add_u32 v100, v16, 2, s53
s_waitcnt lgkmcnt(0)
s_barrier
ds_read2st64_b32 v[104:105], v100 offset0:0 offset1:1
ds_read2st64_b32 v[106:107], v100 offset0:2 offset1:3
ds_read2st64_b32 v[108:109], v100 offset0:4 offset1:5
ds_read2st64_b32 v[110:111], v100 offset0:6 offset1:7
ds_read2st64_b32 v[112:113], v100 offset0:8 offset1:9
ds_read2st64_b32 v[114:115], v100 offset0:10 offset1:11
ds_read2st64_b32 v[116:117], v100 offset0:12 offset1:13
ds_read2st64_b32 v[118:119], v100 offset0:14 offset1:15
s_mul_i32 s53, s71, 6
v_and_b32 v100, 3, v16
v_and_b32 v101, 0x3c, v16
v_add_u32 v100, s53, v100
s_lshl1_add_u32 s53, s53, -44
s_cmp_lt_u32 s71, 4
s_cselect_b64 exec, 0, -1
v_lshrrev_b32 v100, 5, v16
v_and_b32 v101, 1, v16
v_mad_u32_u24 v100, v100, 6, v101
v_and_b32 v101, 30, v16
v_add_u32 v100, s53, v100
v_lshlrev_b32 v101, 1, v101
s_mov_b64 exec, -1
v_lshl_add_u32 v100, v100, 6, v101
s_and_b32 s53, s70, 3
v_add_lshl_u32 v100, v100, s53, 2
v_add_u32 v100, 5909504, v100
s_waitcnt lgkmcnt(0)
v_add_f32 v104, v104, v105
v_add_f32 v104, v104, v106
v_add_f32 v104, v104, v107
v_add_f32 v104, v104, v108
v_add_f32 v104, v104, v109
v_add_f32 v104, v104, v110
v_add_f32 v104, v104, v111
v_add_f32 v104, v104, v112
v_add_f32 v104, v104, v113
v_add_f32 v104, v104, v114
v_add_f32 v104, v104, v115
v_add_f32 v104, v104, v116
v_add_f32 v104, v104, v117
v_add_f32 v104, v104, v118
v_add_f32 v104, v104, v119
s_cmp_eq_u32 %[w], 0
s_cselect_b64 exec, -1, 0
global_store_dword v100, v104, s[78:79] sc1
s_mov_b64 exec, -1
v_mov_b32 v105, v104
s_nop 1
v_min_f32_dpp v105, v105, v105 row_shr:1 row_mask:0xf bank_mask:0xf
s_nop 1
v_min_f32_dpp v105, v105, v105 row_shr:2 row_mask:0xf bank_mask:0xf
s_nop 1
v_min_f32_dpp v105, v105, v105 row_shr:4 row_mask:0xf bank_mask:0xf
s_nop 1
v_min_f32_dpp v105, v105, v105 row_shr:8 row_mask:0xf bank_mask:0xf
s_nop 1
v_min_f32_dpp v105, v105, v105 row_bcast:15 row_mask:0xa bank_mask:0xf
s_nop 1
v_min_f32_dpp v105, v105, v105 row_bcast:31 row_mask:0xc bank_mask:0xf
s_lshl2_add_u32 s72, s70, 5917696
v_sqrt_f32 v105, v105
v_mov_b32 v100, s72
v_mul_f32 v105, 0x41000000, v105
v_and_b32 v105, 0x7f800000, v105
v_or_b32 v105, 0x2aaaab, v105
s_cmp_eq_u32 %[w], 1
s_cselect_b32 exec_hi, 0x80000000, 0
s_mov_b32 exec_lo, 0
global_store_dword v100, v105, s[78:79] sc1
s_mov_b64 exec, -1
)"
R"(s_branch 35b
CS 0,0
CS 0,1
CS 0,2
CS 0,3
CS 1,4
CS 2,5
CS 0,6
CS 1,7
CS 2,8
CS 0,9
CS 1,10
CS 2,11
CS 0,12
CS 1,13
CS 2,14
CS 0,15
CS 1,16
CS 2,17
CS 0,18
CS 1,19
CS 2,20
CS 0,21
CS 1,22
CS 2,23
CS 0,24
CS 1,25
CS 2,26
CS 0,27
CS 1,28
CS 2,29
CS 0,30
CS 1,31
CS 2,32
CS 0,33
CS 1,34
CS 2,35
CS 0,36
CS 1,37
CS 2,38
CS 0,39
CS 1,40
CS 2,41
CS 0,42
CS 1,43
CS 2,44
.Lcc0:
s_cselect_b32 s65, 1, 0
s_mov_b32 s91, 0x40000
43:
s_sleep 2
EL_
s_sub_u32 s91, s91, 1
s_waitcnt vmcnt(0)
CK
s_cbranch_scc1 44f
s_nop 3
s_cbranch_vccnz 43b
44:
s_cmp_lg_u32 s65, 0
s_setpc_b64 s[92:93]
.Lcc1:
s_cselect_b32 s65, 1, 0
s_mov_b32 s91, 0x40000
s_lshl_b32 s94, s64, 7
s_add_u32 s94, s84, s94
s_addc_u32 s95, s85, 0
43:
s_sleep 2
global_load_dwordx4 v[116:119], v13, s[94:95] offset:0 sc1
s_sub_u32 s91, s91, 1
s_waitcnt vmcnt(0)
v_cmp_eq_u32 vcc, 0, v116
s_cbranch_scc1 44f
s_nop 3
s_cbranch_vccnz 43b
44:
s_cmp_lg_u32 s65, 0
s_setpc_b64 s[92:93]
.Lcc2:
s_cselect_b32 s65, 1, 0
s_mov_b32 s91, 0x40000
s_lshl_b32 s94, s64, 7
s_add_u32 s94, s84, s94
s_addc_u32 s95, s85, 0
43:
s_sleep 2
global_load_dwordx4 v[120:123], v13, s[94:95] offset:64 sc1
s_sub_u32 s91, s91, 1
s_waitcnt vmcnt(0)
v_cmp_eq_u32 vcc, 0, v120
s_cbranch_scc1 44f
s_nop 3
s_cbranch_vccnz 43b
44:
s_cmp_lg_u32 s65, 0
s_setpc_b64 s[92:93]
.p2align 12
.Lk1s:
k1_code:
.Lk1p0:
s_cbranch_execz .Lk1p4
s_mov_b32 s40, %[hlo]
s_mov_b32 s41, %[hhi]
s_mov_b32 s72, 0x40000
v_and_b32 v64, -5, v60
VL_
s_mov_b32 s42, 0xc3e9b26d
s_mov_b32 s43, 0x5a17
s_and_b32 s60, %[b], 0xe7
v_and_b32 v58, 3, v16
v_lshl_add_u32 v58, v58, 3, s60
v_lshrrev_b32 v59, 3, v58
v_xad_u32 v58, v59, -1, v58
s_and_b32 s60, %[b], 7
s_cselect_b64 exec, 0, -1
v_add_u32 v58, 224, v59
s_mov_b32 exec_lo, -1
s_mov_b32 exec_hi, 0xffff
v_mov_b32 v58, v16
s_mov_b64 exec, -1
s_and_b32 s52, %[b], 3
s_add_u32 s52, s52, 32
v_mul_u32_u24 v58, 32, v58
v_add_u32 v58, 452853760, v58
v_and_b32 v17, 15, v16
v_lshrrev_b32 v18, 4, v16
v_bfe_u32 v100, v16, 3, 2
v_mul_u32_u24 v100, 0x300, v100
v_and_b32 v101, 7, v16
v_xor_b32 v101, v101, v18
v_lshl_add_u32 v102, v101, 4, v100
v_lshrrev_b32 v12, 5, v16
v_lshlrev_b32 v12, 3, v12
v_lshrrev_b32 v100, 1, v17
v_xor_b32 v101, v100, v18
v_lshlrev_b32 v101, 4, v101
v_lshl_add_u32 v14, v17, 7, v101
v_add_u32 v6, -1, v102
s_mul_i32 s56, %[w], 6144
s_add_u32 s56, s56, 102912
v_add_u32 v14, s56, v14
v_xor_b32 v15, 64, v14
s_mul_i32 s64, %[rst], 24576
s_add_u32 s64, s64, 4096
s_add_u32 s64, %[glo], s64
s_addc_u32 s65, %[ghi], 0
s_mov_b64 %[wp], s[64:65]
v_add_u32 v106, 0, v0
v_add_u32 v107, 8192, v0
v_add_u32 v108, 16384, v0
v_add_u32 v109, 393216, v0
v_add_u32 v110, 401408, v0
v_add_u32 v111, 409600, v0
v_add_u32 v112, 786432, v0
v_add_u32 v113, 794624, v0
v_add_u32 v114, 802816, v0
FX
VP
v_and_b32 v100, 8, v66
v_cmp_ne_u32 s[64:65], 0, v100
VQ
VS
s_mov_b32 s93, vcc_hi
v_lshrrev_b32 v100, 2, v16
v_and_b32 v101, 3, v16
s_bitcmp1_b32 %[w], 1
s_cselect_b32 s60, 0x24dbbd42, 0
s_xor_b32 s60, s60, 0xfe987610
s_lshr_b32 s61, s60, 16
s_bitcmp1_b32 s53, 0
s_cselect_b32 s62, s61, s60
s_bitcmp1_b32 s53, 4
s_cselect_b32 s63, s61, s60
s_mul_i32 s60, s38, 12
s_lshl1_add_u32 s60, s37, s60
s_lshl3_add_u32 s61, s39, s37
s_lshl1_add_u32 s61, s38, s61
s_add_u32 s61, s61, 1512
v_lshlrev_b32 v101, 2, v101
v_and_b32 v102, 1, v100
v_lshlrev_b32 v103, 1, v100
v_sub_u32 v103, v103, v102
v_add_u32 v103, s60, v103
v_bfe_u32 v104, s62, v101, 4
s_mov_b32 exec_lo, 0xff000000
v_lshl_add_u32 v103, v100, 2, s61
v_bfe_u32 v104, s63, v101, 4
s_mov_b32 exec_lo, -1
v_lshl_add_u32 v103, v103, 4, v104
v_mul_u32_u24 v103, 0xc00, v103
v_add_u32 v103, 1, v103
v_lshrrev_b32 v104, 1, v12
ds_bpermute_b32 v34, v104, v103
ds_bpermute_b32 v35, v104, v103 offset:8
s_sub_u32 s60, 0, s96
s_xor_b32 s90, s65, s60
s_mov_b32 s57, %[pos0]
s_bfe_u32 s61, %[b], 0x20003
s_mov_b32 s62, %[rst]
s_lshl1_add_u32 s63, s57, s62
s_waitcnt lgkmcnt(0)
v_add_u32 v34, v34, v6
v_add_u32 v35, v35, v6
v_xor_b32 v35, 64, v35
s_add_u32 m0, s56, 36352
s_nop 0
global_load_lds_dwordx4 v34, %[xp] offset:0
s_add_u32 m0, s56, 37376
s_nop 0
global_load_lds_dwordx4 v35, %[xp] offset:0
s_add_u32 m0, s56, 38272
s_nop 0
global_load_lds_dwordx4 v34, %[xp] offset:128
s_add_u32 m0, s56, 39296
s_nop 0
global_load_lds_dwordx4 v35, %[xp] offset:128
FL_
s_add_u32 m0, s56, 40192
s_nop 0
global_load_lds_dwordx4 v34, %[xp] offset:256
s_add_u32 m0, s56, 41216
s_nop 0
global_load_lds_dwordx4 v35, %[xp] offset:256
s_add_u32 m0, s56, -384
s_nop 0
global_load_lds_dwordx4 v34, %[xp] offset:384
s_add_u32 m0, s56, 640
s_nop 0
global_load_lds_dwordx4 v35, %[xp] offset:384
s_add_u32 m0, s56, 1536
s_nop 0
global_load_lds_dwordx4 v34, %[xp] offset:512
s_add_u32 m0, s56, 2560
s_nop 0
global_load_lds_dwordx4 v35, %[xp] offset:512
s_add_u32 m0, s56, 3456
s_nop 0
global_load_lds_dwordx4 v34, %[xp] offset:640
s_add_u32 m0, s56, 4480
s_nop 0
global_load_lds_dwordx4 v35, %[xp] offset:640
s_lshl_b32 s64, s63, 6
s_add_u32 s64, s64, 1517568
s_add_u32 s86, %[glo], s64
s_addc_u32 s87, %[ghi], 0
s_add_u32 s84, %[glo], 1517568
s_addc_u32 s85, %[ghi], 0
s_lshr_b32 s64, s63, 1
s_lshl3_add_u32 s64, s64, s37
s_lshl2_add_u32 s64, s64, 1370112
s_add_u32 s88, %[glo], s64
s_addc_u32 s89, %[ghi], 0
s_lshl4_add_u32 s64, s37, 1370112
s_add_u32 s82, %[glo], s64
s_addc_u32 s83, %[ghi], 0
v_lshlrev_b32 v101, 1, v101
v_lshl_add_u32 v105, v100, 8, v101
s_lshl1_add_u32 s64, s39, s37
s_mul_i32 s64, s64, 2359296
s_lshl_b32 s65, s36, 11
s_add_u32 s64, s64, s65
s_mul_i32 s73, s39, 2326528
s_add_u32 s65, s65, s73
s_lshl2_add_u32 s73, s38, s37
s_lshl_b32 s73, s73, 12
s_add_u32 s65, s65, s73
s_add_u32 s65, s65, 301989888
s_and_b32 s92, s92, 0xffff
v_lshrrev_b32 v100, 1, v16
v_and_b32 v101, 1, v16
v_lshl_add_u32 v101, v101, 12, s64
v_lshrrev_b32 v102, v100, s93
s_mov_b32 exec_lo, 0x1c0
v_add_u32 v100, -6, v16
v_mov_b32 v101, s65
v_lshrrev_b32 v102, v100, s92
s_mov_b32 exec_lo, -1
v_lshl_add_u32 v101, v100, 18, v101
v_lshrrev_b32 v100, 15, v102
v_and_b32 v102, 0x49, v102
v_and_b32 v100, 0x92, v100
v_or_b32 v100, v100, v102
v_and_b32 v102, 3, v100
v_lshl_add_u32 v32, v102, 13, v101
v_mul_u32_u24 v102, 0x48, v102
v_xor_b32 v100, v100, v102
v_bfe_u32 v101, v100, 3, 2
v_bfe_u32 v100, v100, 6, 2
v_lshl_or_b32 v100, v100, 16, v101
v_lshlrev_b32 v33, 13, v100
WV 8
FB
s_waitcnt lgkmcnt(0)
s_barrier
v_lshl_add_u32 v19, v18, 2, v17
v_and_b32 v19, 15, v19
v_lshlrev_b32 v19, 4, v19
v_lshrrev_b32 v2, 7, v19
global_load_dwordx4 v[176:179], v106, %[wp] offset:-4096 sc1
v_lshl_add_u32 v2, v18, 1, v2
v_mul_u32_u24 v2, 0x8100, v2
v_add_u32 v2, v2, v19
s_lshl_b32 s64, s62, 7
v_xor_b32 v2, s64, v2
global_load_dwordx4 v[180:183], v106, %[wp] offset:-3072 sc1
v_lshrrev_b32 v19, 2, v17
v_cmp_eq_u32 vcc, v19, v18
v_mov_b32 v19, 0x10000
v_lshlrev_b32 v13, 4, v18
v_and_b32 v7, 3, v17
global_load_dwordx4 v[224:227], v107, %[wp] offset:0 sc1
v_lshl_add_u32 v7, v18, 2, v7
v_lshlrev_b32 v7, 4, v7
v_add_u32 v7, 61952, v7
v_add_u32 v8, 0x100, v13
v_mov_b32 v65, 1
global_load_dwordx4 v[228:231], v107, %[wp] offset:1024 sc1
v_mul_u32_u24 v28, 80, v18
v_lshlrev_b32 v31, 2, v16
s_add_u32 s68, %[glo], 1189888
s_addc_u32 s69, %[ghi], 0
s_lshl2_add_u32 s64, s96, s61
global_load_dwordx4 a[24:27], v109, %[wp] offset:-4096 sc1
s_lshl_b32 s64, s64, 8
s_lshr_b32 s65, %[pf], 5
s_and_b32 s65, s65, 0x780
s_add_u32 s65, s65, 9216
s_cmp_eq_u32 %[w], 0
global_load_dwordx4 a[28:31], v109, %[wp] offset:-3072 sc1
s_cselect_b32 %[wrap], s64, %[ca0]
s_cselect_b32 s65, s65, %[ca0]
v_mov_b32 v59, s65
s_lshl_b32 s96, s37, 6
s_xor_b32 s97, s96, 64
global_load_dwordx4 a[72:75], v110, %[wp] offset:0 sc1
v_add_u32 v6, 1, v6
v_xad_u32 v6, v6, s96, -1
s_lshl_b32 s64, s62, 8
s_add_u32 %[alo], %[alo], s64
s_addc_u32 %[ahi], %[ahi], 0
global_load_dwordx4 a[76:79], v110, %[wp] offset:1024 sc1
s_mul_i32 s64, s36, 6144
s_lshl_b32 s65, s37, 10
s_add_u32 s64, s64, s65
s_add_u32 s38, s64, 1024
s_mul_i32 s94, s36, 384
global_load_dwordx4 a[120:123], v112, %[wp] offset:-4096 sc1
s_lshl_b32 s59, %[w], 8
s_add_u32 s59, s59, 131072
s_mov_b32 s37, 1024
s_mov_b32 s74, 36352
global_load_dwordx4 a[124:127], v112, %[wp] offset:-3072 sc1
s_mov_b32 s76, 36352
s_mov_b32 s36, 0xffff
s_mov_b32 s54, 0x10001
s_mov_b32 s55, 0x10001
s_mov_b32 s58, 0
global_load_dwordx4 a[168:171], v113, %[wp] offset:0 sc1
s_mov_b32 s67, 0x44000000
s_mov_b32 %[nb], 0
s_mov_b32 s39, 0
s_mov_b32 s78, 0xaaaaaaaa
s_mov_b32 s79, 0xaaaaaaaa
global_load_dwordx4 a[172:175], v113, %[wp] offset:1024 sc1
s_mov_b32 s80, 0xcccccccc
s_mov_b32 s81, 0xcccccccc
v_mov_b64 v[248:249], 0
v_mov_b64 v[250:251], 0
v_mov_b64 v[252:253], 0
global_load_dwordx4 v[184:187], v106, %[wp] offset:-2048 sc1
v_mov_b64 v[10:11], 0
v_mov_b64 v[124:125], 0
v_mov_b64 v[126:127], 0
RF
s_mov_b32 exec_hi, 15
global_store_dword v105, v103, s[88:89] sc1
s_mov_b64 exec, 1
global_atomic_add v59, v65, s[68:69]
s_mov_b64 exec, -1
s_add_u32 s40, %[xlo], s94
s_addc_u32 s41, %[xhi], 0
s_add_u32 s42, %[hlo], 0
s_addc_u32 s43, %[hhi], 0
s_add_u32 s44, %[hlo], 786432
s_addc_u32 s45, %[hhi], 0
s_add_u32 s46, %[hlo], 1572864
s_addc_u32 s47, %[hhi], 0
s_add_u32 s88, s88, 256
s_addc_u32 s89, s89, 0
RV 128,14,36352
RV 132,15,36352
RV 136,14,38400
RV 140,15,38400
WV 21
RV 144,14,40448
RV 148,15,40448
RA 240,7,0
RA 244,7,256
RA 248,8,0
)"
R"(RA 252,8,64
WV 19
RV 152,14,0
RV 156,15,0
WV 17
RV 160,14,2048
RV 164,15,2048
WV 15
RV 168,14,4096
WL 10
RV 172,15,4096
WL 8
Y3 242,128,240,130,246,132
WL 7
RA 240,7,512
s_nop 1
s_nop 4
S0 54,40
S0 55,41
S 54,42
S 55,43
S 54,44
S 55,45
S 54,46
S 55,47
S 54,48
S 55,49
S 54,50
S 55,51
global_load_dwordx4 v[188:191], v106, %[wp] offset:-1024 sc1
LZA 248,0
WL 0
RA 248,8,128
global_load_dwordx4 v[232:235], v107, %[wp] offset:2048 sc1
Y3 244,134,242,136,240,138
RA 244,7,768
RA 240,7,1024
s_nop 1
s_nop 4
S 54,40
S 55,41
S 54,42
S 55,43
S 54,44
S 55,45
S 54,46
S 55,47
S 54,48
S 55,49
S 54,50
S 55,51
global_load_dwordx4 v[236:239], v107, %[wp] offset:3072 sc1
LSA 252,1
WL 2
RA 252,8,192
global_load_dwordx4 a[32:35], v109, %[wp] offset:-2048 sc1
LSA 248,2
WL 1
RA 248,8,256
global_load_dwordx4 a[36:39], v109, %[wp] offset:-1024 sc1
Y3 246,140,244,142,242,144
RA 244,7,1280
s_nop 1
s_nop 5
S 54,40
S 55,41
S 54,42
S 55,43
S 54,44
S 55,45
S 54,46
S 55,47
S 54,48
S 55,49
S 54,50
S 55,51
global_load_dwordx4 a[80:83], v110, %[wp] offset:2048 sc1
WL 2
LSA 252,3
WL 0
RA 252,8,320
global_load_dwordx4 a[84:87], v110, %[wp] offset:3072 sc1
Y3 240,146,246,148,244,150
RA 240,7,1536
RA 244,7,1792
s_nop 1
s_nop 4
S 54,40
S 55,41
S 54,42
S 55,43
S 54,44
S 55,45
S 54,46
S 55,47
S 54,48
S 55,49
S 54,50
S 55,51
global_load_dwordx4 a[128:131], v112, %[wp] offset:-2048 sc1
LSA 248,4
WL 2
RA 248,8,384
global_load_dwordx4 a[132:135], v112, %[wp] offset:-1024 sc1
LSA 252,5
WL 1
RA 252,8,448
global_load_dwordx4 a[176:179], v113, %[wp] offset:2048 sc1
Y3 242,152,240,154,246,156
RA 240,7,2048
s_nop 1
s_nop 5
S 54,40
S 55,41
S 54,42
S 55,43
S 54,44
S 55,45
S 54,46
S 55,47
S 54,48
S 55,49
S 54,50
S 55,51
global_load_dwordx4 a[180:183], v113, %[wp] offset:3072 sc1
WL 2
LSA 248,6
WL 0
RA 248,8,512
global_load_dwordx4 v[192:195], v106, %[wp] offset:0 sc1
s_mov_b32 s64, s63
s_mov_b32 s63, s62
s_mov_b32 s62, s61
s_mov_b32 s61, s60
s_mov_b32 s60, s57
s_lshl_b32 s53, s60, 5
s_add_u32 s48, s82, s53
s_addc_u32 s49, s83, 0
s_add_u32 s57, s57, 1
EL_
Y3 244,158,242,160,240,162
RA 244,7,2304
RA 240,7,2560
s_nop 1
s_nop 4
S 54,40
S 55,41
S 54,42
S 55,43
S 54,44
S 55,45
S 54,46
S 55,47
S 54,48
S 55,49
S 54,50
S 55,51
global_load_dwordx4 v[196:199], v106, %[wp] offset:1024 sc1
LSA 252,7
WL 2
RA 252,8,576
global_load_dwordx4 v[240:243], v108, %[wp] offset:-4096 sc1
LSA 248,8
WL 1
RA 248,8,640
global_load_dwordx4 v[244:247], v108, %[wp] offset:-3072 sc1
Y3 246,164,244,166,242,168
RA 244,7,2816
s_nop 1
s_nop 5
S 54,40
S 55,41
S 54,42
S 55,43
S 54,44
S 55,45
S 54,46
S 55,47
S 54,48
S 55,49
S 54,50
S 55,51
global_load_dwordx4 a[40:43], v109, %[wp] offset:0 sc1
WL 2
LSA 252,9
WL 0
RA 252,8,704
global_load_dwordx4 a[44:47], v109, %[wp] offset:1024 sc1
Y3 240,170,246,172,244,174
s_nop 1
s_nop 6
S 54,40
S 55,41
S 54,42
S 55,43
S 54,44
S 55,45
S 54,46
S 55,47
S 54,48
S 55,49
S 54,50
S 55,51
global_load_dwordx4 a[88:91], v111, %[wp] offset:-4096 sc1
LSA 248,10
WL 0
global_load_dwordx4 a[92:95], v111, %[wp] offset:-3072 sc1
LSA 252,11
global_load_dwordx4 a[136:139], v112, %[wp] offset:0 sc1
v_add_f32 v52, v54, v55
s_nop 5
v_mul_f32 v36, 0xbe2aaaab, v36
v_mul_f32 v37, 0x3c800000, v36
v_mov_b32 v53, v52
s_nop 1
v_permlane32_swap_b32 v52, v53
s_nop 1
v_add_f32 v52, v52, v53
v_mov_b32 v53, v52
s_nop 1
v_permlane16_swap_b32 v52, v53
s_nop 1
v_add_f32 v52, v52, v53
v_mul_f32 v52, 0x352aaaab, v52
v_fma_f32 v52, -v37, v37, v52
C 52,52,52
v_lshlrev_b32 v52, 16, v52
v_add_f32 v52, 0x3727c5ac, v52
C 52,52,52
v_lshlrev_b32 v52, 16, v52
v_sqrt_f32 v52, v52
s_nop 0
C 52,52,52
v_lshlrev_b32 v52, 16, v52
v_cmp_ge_f32 s[70:71], s67, v52
s_nop 1
v_cndmask_b32 v52, 0, v52, s[70:71]
v_mul_f32 v54, 0x44000000, v52
v_cmp_ge_f32 s[70:71], v54, |v36|
s_nop 1
v_cndmask_b32 v52, 0, v52, s[70:71]
v_rcp_f32 v52, v52
s_nop 0
v_mul_f32 v52, 0x42800000, v52
s_nop 1
v_mov_b32_dpp v53, v52 quad_perm:[1,1,3,3] row_mask:0xf bank_mask:0xf
v_cvt_pk_f16_f32 v55, v52, v53
ds_bpermute_b32 v40, v28, v55 offset:0
ds_bpermute_b32 v41, v28, v55 offset:8
v_readlane_b32 s72, v32, s58
v_readlane_b32 s73, v33, s58
s_nop 1
v_mov_b32 v42, s72
v_mov_b32 v43, s73
s_nop 1
WL 0
RS
global_load_dwordx4 a[140:143], v112, %[wp] offset:1024 sc1
s_add_u32 s56, s56, 36352
v_add_u32 v14, 36352, v14
v_add_u32 v15, 36352, v15
s_sub_u32 s53, s37, s56
v_add_u32 v1, s53, v14
v_xor_b32 v3, 64, v1
WV 10
v_min_u32 v9, v22, v23
v_cmp_eq_u32 vcc, 0, v9
s_nop 4
CB 0,0
v_add_u32 v4, v22, v6
v_add_u32 v5, v23, v6
s_mov_b32 s64, s63
s_mov_b32 s63, s62
s_mov_b32 s62, s61
s_mov_b32 s61, s60
s_mov_b32 s60, s57
s_lshl_b32 s53, s60, 5
s_add_u32 s48, s82, s53
s_addc_u32 s49, s83, 0
s_add_u32 s57, s57, 1
EL_
global_load_dword v34, v12, s[88:89] offset:0 sc1
global_load_dword v35, v12, s[88:89] offset:16 sc1
s_add_u32 m0, s38, 0
s_nop 0
DX 4,0
s_add_u32 m0, s38, 1920
s_nop 0
DX 4,128
s_add_u32 m0, s38, 3840
s_nop 0
DX 4,256
s_add_u32 m0, s38, 12288
s_nop 0
DX 5,0
s_add_u32 m0, s38, 14208
s_nop 0
DX 5,128
s_add_u32 m0, s38, 16128
s_nop 0
DX 5,256
s_add_u32 s38, s38, s76
s_xor_b32 s76, s76, 64512
s_and_b32 s38, s38, 0x1ffff
global_load_dwordx4 a[184:187], v114, %[wp] offset:-4096 sc1
global_load_dwordx4 a[188:191], v114, %[wp] offset:-3072 sc1
global_load_dwordx4 v[200:203], v106, %[wp] offset:2048 sc1
global_load_dwordx4 v[204:207], v106, %[wp] offset:3072 sc1
global_load_dwordx4 a[0:3], v108, %[wp] offset:-2048 sc1
global_load_dwordx4 a[4:7], v108, %[wp] offset:-1024 sc1
global_load_dwordx4 a[48:51], v109, %[wp] offset:2048 sc1
global_load_dwordx4 a[52:55], v109, %[wp] offset:3072 sc1
global_load_dwordx4 a[96:99], v111, %[wp] offset:-2048 sc1
global_load_dwordx4 a[100:103], v111, %[wp] offset:-1024 sc1
global_load_dwordx4 a[144:147], v112, %[wp] offset:2048 sc1
global_load_dwordx4 a[148:151], v112, %[wp] offset:3072 sc1
global_load_dwordx4 a[192:195], v114, %[wp] offset:-2048 sc1
global_load_dwordx4 a[196:199], v114, %[wp] offset:-1024 sc1
global_load_dwordx4 v[208:211], v107, %[wp] offset:-4096 sc1
global_load_dwordx4 v[212:215], v107, %[wp] offset:-3072 sc1
WV 24
v_min_u32 v9, v22, v23
v_cmp_eq_u32 vcc, 0, v9
s_nop 4
CB 0,1
v_add_u32 v120, v22, v6
v_add_u32 v121, v23, v6
s_mov_b32 s64, s63
s_mov_b32 s63, s62
s_mov_b32 s62, s61
s_mov_b32 s61, s60
s_mov_b32 s60, s57
.Lk1p1:
s_cbranch_execz .Lk1p5
s_lshl_b32 s53, s60, 5
s_add_u32 s48, s82, s53
s_addc_u32 s49, s83, 0
s_add_u32 s57, s57, 1
EL_
s_add_u32 s86, s86, 1024
s_addc_u32 s87, s87, 0
s_add_u32 s58, s58, 1
WV 24
v_add_u32 v34, v34, v6
WV 23
v_add_u32 v35, v35, v6
v_xor_b32 v34, s96, v34
v_xor_b32 v35, s97, v35
s_cmp_lt_u32 s58, 8
s_cselect_b32 s53, 256, 0
s_add_u32 s88, s88, s53
s_addc_u32 s89, s89, 0
global_load_dwordx4 a[8:11], v108, %[wp] offset:0 sc1
global_load_dwordx4 a[12:15], v108, %[wp] offset:1024 sc1
global_load_dwordx4 a[56:59], v110, %[wp] offset:-4096 sc1
global_load_dwordx4 a[60:63], v110, %[wp] offset:-3072 sc1
global_load_dwordx4 a[104:107], v111, %[wp] offset:0 sc1
global_load_dwordx4 a[108:111], v111, %[wp] offset:1024 sc1
global_load_dwordx4 a[152:155], v113, %[wp] offset:-4096 sc1
global_load_dwordx4 a[156:159], v113, %[wp] offset:-3072 sc1
global_load_dwordx4 a[200:203], v114, %[wp] offset:0 sc1
global_load_dwordx4 a[204:207], v114, %[wp] offset:1024 sc1
global_load_dwordx4 v[216:219], v107, %[wp] offset:-2048 sc1
global_load_dwordx4 v[220:223], v107, %[wp] offset:-1024 sc1
WV 12
v_min_u32 v9, v22, v23
v_cmp_eq_u32 vcc, 0, v9
s_nop 4
CB 0,2
v_add_u32 v122, v22, v6
v_add_u32 v123, v23, v6
s_mov_b32 s64, s63
s_mov_b32 s63, s62
s_mov_b32 s62, s61
s_mov_b32 s61, s60
s_mov_b32 s60, s57
s_lshl_b32 s53, s60, 5
s_add_u32 s48, s82, s53
s_addc_u32 s49, s83, 0
s_add_u32 s57, s57, 1
EL_
global_load_dwordx4 a[16:19], v108, %[wp] offset:2048 sc1
global_load_dwordx4 a[20:23], v108, %[wp] offset:3072 sc1
global_load_dwordx4 a[64:67], v110, %[wp] offset:-2048 sc1
global_load_dwordx4 a[68:71], v110, %[wp] offset:-1024 sc1
global_load_dwordx4 a[112:115], v111, %[wp] offset:2048 sc1
global_load_dwordx4 a[116:119], v111, %[wp] offset:3072 sc1
global_load_dwordx4 a[160:163], v113, %[wp] offset:-2048 sc1
global_load_dwordx4 a[164:167], v113, %[wp] offset:-1024 sc1
global_load_dwordx4 a[208:211], v114, %[wp] offset:2048 sc1
global_load_dwordx4 a[212:215], v114, %[wp] offset:3072 sc1
s_lshl_b32 s53, s63, 7
s_add_u32 s50, s84, s53
s_addc_u32 s51, s85, 0
s_mov_b32 m0, s59
s_nop 0
DR
s_xor_b32 s59, s59, 1024
v_add_u32 v20, s59, v13
s_nop 0
)"
R"(s_barrier
RA 216,1,0
RA 220,3,0
RA 224,1,2048
RA 228,3,2048
s_mov_b32 s66, 7
WL 3
Z 0,12,0
WL 2
M 0,13,1
s_add_u32 m0, s38, 0
Z 1,24,0
DX 120,0
M 1,25,1
s_add_u32 m0, s38, 1920
RA 232,1,4096
Z 2,36,0
DX 120,128
M 2,37,1
s_add_u32 m0, s38, 3840
Z 3,48,0
DX 120,256
M 3,49,1
s_add_u32 m0, s38, 12288
Z 4,60,0
DX 121,0
M 4,61,1
s_add_u32 m0, s38, 14208
RA 236,3,4096
Z 5,72,0
WL 2
DX 121,128
M 5,73,1
s_add_u32 m0, s38, 16128
M 0,14,2
DX 121,256
M 0,15,3
s_add_u32 s38, s38, s76
M 1,26,2
s_xor_b32 s76, s76, 64512
M 1,27,3
s_and_b32 s38, s38, 0x1ffff
RA 216,1,6144
M 2,38,2
WV 17
v_min_u32 v9, v22, v23
v_cmp_eq_u32 vcc, 0, v9
M 2,39,3
M 3,50,2
M 3,51,3
CB 0,3
v_add_u32 v4, v22, v6
v_add_u32 v5, v23, v6
M 4,62,2
s_mov_b32 s64, s63
v_xor_b32 v20, 0x400, v20
M 4,63,3
s_mov_b32 s63, s62
RA 220,3,6144
M 5,74,2
WL 2
M 5,75,3
s_mov_b32 s62, s61
M 0,16,4
s_mov_b32 s61, s60
M 0,17,5
s_mov_b32 s60, s57
M 1,28,4
s_lshl_b32 s53, s60, 5
M 1,29,5
s_add_u32 s48, s82, s53
RA 224,1,8192
M 2,40,4
s_addc_u32 s49, s83, 0
M 2,41,5
s_add_u32 s57, s57, 1
EL_
M 3,52,4
s_add_u32 s37, s37, s74
M 3,53,5
s_xor_b32 s74, s74, 64512
M 4,64,4
s_and_b32 s37, s37, 0x1ffff
M 4,65,5
s_sub_u32 s52, s37, s56
RA 228,3,8192
M 5,76,4
WL 2
M 5,77,5
s_add_u32 m0, s38, 0
M 0,18,0
DX 122,0
M 0,19,1
s_add_u32 m0, s38, 1920
M 1,30,0
DX 122,128
M 1,31,1
s_add_u32 m0, s38, 3840
RA 232,1,10240
M 2,42,0
DX 122,256
M 2,43,1
s_add_u32 m0, s38, 12288
M 3,54,0
WV 10
RV 116,20,0
M 3,55,1
DX 123,0
M 4,66,0
s_add_u32 m0, s38, 14208
M 4,67,1
RA 236,3,10240
M 5,78,0
WL 3
DX 123,128
M 5,79,1
s_add_u32 m0, s38, 16128
M 0,20,2
WL 1
DX 123,256
v_cmp_eq_u32 vcc, 0, v116
M 0,21,3
s_add_u32 s38, s38, s76
M 1,32,2
s_xor_b32 s76, s76, 64512
M 1,33,3
CB 1,4
RA 216,1,12288
M 2,44,2
s_and_b32 s38, s38, 0x1ffff
M 2,45,3
s_lshl_b32 s53, s63, 7
M 3,56,2
s_add_u32 s50, s84, s53
M 3,57,3
s_addc_u32 s51, s85, 0
M 4,68,2
s_mov_b32 m0, s59
M 4,69,3
RA 220,3,12288
M 5,80,2
WL 2
DR
M 5,81,3
s_xor_b32 s59, s59, 1024
M 0,22,4
s_add_u32 m0, s38, 0
M 0,23,5
DX 4,0
M 1,34,4
s_add_u32 m0, s38, 1920
M 1,35,5
RA 224,1,14336
DX 4,128
M 2,46,4
s_add_u32 m0, s38, 3840
FL 16,56,116
FH 104,57,116
M 2,47,5
DX 4,256
LS_ 16
M 3,58,4
s_add_u32 m0, s38, 12288
LA 16
LS_ 104
M 3,59,5
LC_ 104
DX 5,0
M 4,70,4
s_add_u32 m0, s38, 14208
M 4,71,5
RA 228,3,14336
DX 5,128
M 5,82,4
WL 4
FL 17,58,117
FH 105,59,117
M 5,83,5
LS_ 17
Z 6,12,0
LA 17
LS_ 105
M 6,13,1
LC_ 105
Z 7,24,0
WL 3
LO 104,16,104
HZ 24,104
M 7,25,1
s_add_u32 m0, s38, 16128
RA 232,1,16384
Z 8,36,0
DX 5,256
FL 18,60,116
FH 106,61,116
M 8,37,1
LS_ 18
Z 9,48,0
LA 18
LS_ 106
M 9,49,1
LC_ 106
v_add_u32 v118, v118, v2
Z 10,60,0
WL 3
LO 105,17,105
HS 24,105
M 10,61,1
s_add_u32 s38, s38, s76
RA 236,3,16384
Z 11,72,0
FL 19,62,117
FH 107,63,117
M 11,73,1
s_xor_b32 s76, s76, 64512
LS_ 19
M 6,14,2
s_and_b32 s38, s38, 0x1ffff
LA 19
LS_ 107
M 6,15,3
LC_ 107
M 7,26,2
WL 3
LO 106,18,106
HZ 25,106
M 7,27,3
RA 216,1,18432
M 8,38,2
FL 16,64,116
FH 108,65,116
M 8,39,3
LS_ 16
M 9,50,2
LA 16
LS_ 108
M 9,51,3
LC_ 108
M 10,62,2
WL 3
LO 107,19,107
HS 25,107
M 10,63,3
RA 220,3,18432
M 11,74,2
FL 17,66,117
FH 109,67,117
M 11,75,3
ST 104,118,42
LS_ 17
M 6,16,4
LA 17
LS_ 109
M 6,17,5
LC_ 109
M 7,28,4
WL 3
LO 108,16,108
HZ 26,108
M 7,29,5
s_add_u32 m0, s56, 0
RA 224,1,20480
M 8,40,4
global_load_lds_dwordx4 v34, %[xp] offset:0
FL 18,68,116
FH 110,69,116
M 8,41,5
s_add_u32 m0, s56, 1024
LS_ 18
M 9,52,4
LA 18
global_load_lds_dwordx4 v35, %[xp] offset:0
LS_ 110
M 9,53,5
s_add_u32 m0, s56, 1920
LC_ 110
XW 116,0
M 10,64,4
WL 3
global_load_lds_dwordx4 v34, %[xp] offset:128
LO 109,17,109
HS 26,109
M 10,65,5
s_add_u32 m0, s56, 2944
RA 228,3,20480
M 11,76,4
global_load_lds_dwordx4 v35, %[xp] offset:128
FL 19,70,117
FH 111,71,117
M 11,77,5
LS_ 19
M 6,18,0
LA 19
LS_ 111
M 6,19,1
LC_ 111
M 7,30,0
WL 3
LO 110,18,110
HZ 27,110
M 7,31,1
RA 232,1,22528
M 8,42,0
FL 16,72,116
FH 112,73,116
M 8,43,1
RV 120,20,64
LS_ 16
M 9,54,0
WL 2
LA 16
LS_ 112
M 9,55,1
WV 19
LC_ 112
v_add_u32 v1, s52, v14
M 10,66,0
s_barrier
LO 111,19,111
HS 27,111
M 10,67,1
RA 236,3,22528
M 11,78,0
FL 17,74,117
FH 113,75,117
M 11,79,1
ST 108,21,44
LS_ 17
M 6,20,2
LA 17
LS_ 113
M 6,21,3
WL 4
LC_ 113
v_cmp_eq_u32 vcc, 0, v120
M 7,32,2
WL 3
LO 112,16,112
HZ 29,112
M 7,33,3
RA 216,1,0
v_xor_b32 v3, 64, v1
M 8,44,2
CB 2,5
FL 18,76,116
FH 114,77,116
M 8,45,3
LS_ 18
M 9,56,2
LA 18
LS_ 114
M 9,57,3
LC_ 114
XW 116,1
M 10,68,2
WL 3
LO 113,17,113
HS 29,113
M 10,69,3
RA 220,3,0
M 11,80,2
FL 19,78,117
FH 115,79,117
M 11,81,3
LS_ 19
M 6,22,4
LA 19
LS_ 115
M 6,23,5
LC_ 115
M 7,34,4
WL 3
LO 114,18,114
HZ 30,114
M 7,35,5
RA 224,1,2048
M 8,46,4
WV 13
FL 16,80,120
FH 104,81,120
M 8,47,5
s_barrier
LS_ 16
M 9,58,4
LA 16
LS_ 104
M 9,59,5
LC_ 104
M 10,70,4
WL 3
LO 115,19,115
HS 30,115
M 10,71,5
RA 228,3,2048
M 11,82,4
FL 17,82,121
FH 105,83,121
M 11,83,5
SG
ST 112,21,46
)"
R"(LS_ 17
Z 0,12,0
LA 17
LS_ 105
M 0,13,1
LC_ 105
Z 1,24,0
WL 3
LO 104,16,104
HS 24,104
M 1,25,1
RA 232,1,4096
v_min_u32 v9, v22, v23
Z 2,36,0
FL 18,84,120
FH 106,85,120
M 2,37,1
LS_ 18
v_cmp_eq_u32 vcc, 0, v9
Z 3,48,0
LA 18
LS_ 106
M 3,49,1
LC_ 106
v_add_u32 v122, v122, v2
Z 4,60,0
WL 3
LO 105,17,105
HS 24,105
M 4,61,1
CB 0,6
RA 236,3,4096
v_add_u32 v4, v22, v6
Z 5,72,0
FL 19,86,121
FH 107,87,121
M 5,73,1
LS_ 19
v_add_u32 v5, v23, v6
M 0,14,2
s_mov_b32 s64, s63
LA 19
LS_ 107
M 0,15,3
s_mov_b32 s63, s62
LC_ 107
M 1,26,2
WL 3
LO 106,18,106
HS 25,106
M 1,27,3
s_mov_b32 s62, s61
RA 216,1,6144
M 2,38,2
s_mov_b32 s61, s60
FL 16,88,120
FH 108,89,120
M 2,39,3
s_mov_b32 s60, s57
LS_ 16
M 3,50,2
s_lshl_b32 s53, s60, 5
LA 16
LS_ 108
M 3,51,3
s_add_u32 s48, s82, s53
LC_ 108
M 4,62,2
WL 3
LO 107,19,107
HS 25,107
M 4,63,3
s_addc_u32 s49, s83, 0
RA 220,3,6144
v_xor_b32 v20, 0x400, v20
M 5,74,2
FL 17,90,121
FH 109,91,121
M 5,75,3
s_add_u32 s57, s57, 1
ST 104,122,42
LS_ 17
M 0,16,4
LA 17
LS_ 109
M 0,17,5
LC_ 109
EL_
M 1,28,4
WL 3
LO 108,16,108
HS 26,108
M 1,29,5
RA 224,1,8192
M 2,40,4
FL 18,92,120
FH 110,93,120
M 2,41,5
.Lk1p2:
s_cbranch_execz .Lk1p6
LS_ 18
M 3,52,4
s_add_u32 s37, s37, s74
LA 18
LS_ 110
M 3,53,5
s_xor_b32 s74, s74, 64512
LC_ 110
XW 120,0
M 4,64,4
WL 3
LO 109,17,109
HS 26,109
M 4,65,5
s_and_b32 s37, s37, 0x1ffff
RA 228,3,8192
M 5,76,4
FL 19,94,121
FH 111,95,121
M 5,77,5
s_sub_u32 s52, s37, s56
LS_ 19
M 0,18,0
s_lshl_b32 s53, s63, 7
LA 19
LS_ 111
M 0,19,1
s_add_u32 s50, s84, s53
LC_ 111
M 1,30,0
WL 3
LO 110,18,110
HS 27,110
M 1,31,1
s_addc_u32 s51, s85, 0
RA 232,1,10240
M 2,42,0
s_mov_b32 m0, s59
FL 16,96,120
FH 112,97,120
M 2,43,1
DR
LS_ 16
M 3,54,0
s_xor_b32 s59, s59, 1024
LA 16
LS_ 112
M 3,55,1
WV 16
LC_ 112
RV 116,20,0
M 4,66,0
WL 4
LO 111,19,111
HS 27,111
M 4,67,1
s_add_u32 m0, s38, 0
RA 236,3,10240
M 5,78,0
DX 4,0
FL 17,98,121
FH 113,99,121
M 5,79,1
ST 108,21,44
LS_ 17
M 0,20,2
LA 17
LS_ 113
M 0,21,3
WL 2
LC_ 113
v_cmp_eq_u32 vcc, 0, v116
M 1,32,2
LO 112,16,112
HS 29,112
M 1,33,3
s_add_u32 m0, s38, 1920
RA 216,1,12288
M 2,44,2
CB 1,7
DX 4,128
FL 18,100,120
FH 114,101,120
M 2,45,3
LS_ 18
M 3,56,2
LA 18
LS_ 114
M 3,57,3
LC_ 114
XW 120,1
M 4,68,2
WL 3
LO 113,17,113
HS 29,113
M 4,69,3
s_add_u32 m0, s38, 3840
RA 220,3,12288
M 5,80,2
DX 4,256
FL 19,102,121
FH 115,103,121
M 5,81,3
LS_ 19
M 0,22,4
LA 19
LS_ 115
M 0,23,5
LC_ 115
M 1,34,4
WL 3
LO 114,18,114
HS 30,114
M 1,35,5
s_add_u32 m0, s38, 12288
RA 224,1,14336
M 2,46,4
DX 5,0
FL 16,56,116
FH 104,57,116
M 2,47,5
LS_ 16
M 3,58,4
LA 16
LS_ 104
M 3,59,5
LC_ 104
RA 240,7,0
M 4,70,4
WL 4
LO 115,19,115
HS 30,115
M 4,71,5
s_add_u32 m0, s38, 14208
RA 228,3,14336
RA 244,7,256
M 5,82,4
DX 5,128
FL 17,58,117
FH 105,59,117
M 5,83,5
ST 112,21,46
LS_ 17
Z 6,12,0
LA 17
LS_ 105
M 6,13,1
LC_ 105
RA 248,8,0
Z 7,24,0
WL 6
LO 104,16,104
HS 24,104
M 7,25,1
s_add_u32 m0, s38, 16128
RA 232,1,16384
RA 252,8,64
Z 8,36,0
DX 5,256
FL 18,60,116
FH 106,61,116
M 8,37,1
WV 15
RV 128,14,0
LS_ 18
Z 9,48,0
LA 18
LS_ 106
M 9,49,1
LC_ 106
v_add_u32 v118, v118, v2
Z 10,60,0
WL 6
LO 105,17,105
HS 24,105
M 10,61,1
s_add_u32 s38, s38, s76
RA 236,3,16384
RV 132,15,0
Z 11,72,0
FL 19,62,117
FH 107,63,117
M 11,73,1
s_xor_b32 s76, s76, 64512
LS_ 19
M 6,14,2
s_and_b32 s38, s38, 0x1ffff
LA 19
LS_ 107
M 6,15,3
LC_ 107
M 7,26,2
WL 4
LO 106,18,106
HS 25,106
M 7,27,3
RA 216,1,18432
M 8,38,2
FL 16,64,116
FH 108,65,116
M 8,39,3
LS_ 16
M 9,50,2
LA 16
LS_ 108
M 9,51,3
LC_ 108
M 10,62,2
WL 3
LO 107,19,107
HS 25,107
M 10,63,3
RA 220,3,18432
M 11,74,2
FL 17,66,117
FH 109,67,117
M 11,75,3
ST 104,118,42
LS_ 17
M 6,16,4
LA 17
LS_ 109
M 6,17,5
LC_ 109
M 7,28,4
WL 3
LO 108,16,108
HS 26,108
M 7,29,5
s_add_u32 m0, s56, 3840
RA 224,1,20480
Y3 242,128,240,130,246,132
global_load_lds_dwordx4 v34, %[xp] offset:256
RA 240,7,512
M 8,40,4
s_add_u32 m0, s56, 4864
FL 18,68,116
FH 110,69,116
M 8,41,5
global_load_lds_dwordx4 v35, %[xp] offset:256
LS_ 18
M 9,52,4
s_add_u32 m0, s56, -384
LA 18
LS_ 110
M 9,53,5
LC_ 110
global_load_lds_dwordx4 v34, %[xp] offset:384
XW 116,0
M 10,64,4
WL 4
LO 109,17,109
HS 26,109
M 10,65,5
s_add_u32 m0, s56, 640
RA 228,3,20480
S0 54,40
M 11,76,4
global_load_lds_dwordx4 v35, %[xp] offset:384
FL 19,70,117
FH 111,71,117
M 11,77,5
LS_ 19
S0 55,41
M 6,18,0
LA 19
LS_ 111
M 6,19,1
LC_ 111
S 54,42
M 7,30,0
WL 3
LO 110,18,110
HS 27,110
M 7,31,1
RA 232,1,22528
S 55,43
LZA 248,0
RA 248,8,128
S 54,44
M 8,42,0
FL 16,72,116
FH 112,73,116
M 8,43,1
RV 120,20,64
LS_ 16
M 9,54,0
LA 16
LS_ 112
M 9,55,1
LC_ 112
v_add_u32 v1, s52, v14
M 10,66,0
WL 5
LO 111,19,111
HS 27,111
M 10,67,1
RA 236,3,22528
S 55,45
M 11,78,0
)"
R"(FL 17,74,117
FH 113,75,117
M 11,79,1
ST 108,21,44
LS_ 17
M 6,20,2
LA 17
LS_ 113
M 6,21,3
WL 4
LC_ 113
v_cmp_eq_u32 vcc, 0, v120
M 7,32,2
WL 3
LO 112,16,112
HS 29,112
M 7,33,3
RA 216,1,0
v_xor_b32 v3, 64, v1
M 8,44,2
CB 2,8
FL 18,76,116
FH 114,77,116
M 8,45,3
LS_ 18
S 54,46
M 9,56,2
LA 18
LS_ 114
M 9,57,3
LC_ 114
XW 116,1
M 10,68,2
WL 3
LO 113,17,113
HS 29,113
M 10,69,3
RA 220,3,0
S 55,47
M 11,80,2
FL 19,78,117
FH 115,79,117
M 11,81,3
LS_ 19
S 54,48
M 6,22,4
LA 19
LS_ 115
M 6,23,5
LC_ 115
S 55,49
M 7,34,4
WL 3
LO 114,18,114
HS 30,114
M 7,35,5
RA 224,1,2048
S 54,50
M 8,46,4
FL 16,80,120
FH 104,81,120
M 8,47,5
s_barrier
LS_ 16
S 55,51
M 9,58,4
LA 16
LS_ 104
M 9,59,5
LC_ 104
M 10,70,4
WL 3
LO 115,19,115
HS 30,115
M 10,71,5
RA 228,3,2048
M 11,82,4
FL 17,82,121
FH 105,83,121
M 11,83,5
SF
ST 112,21,46
LS_ 17
Z 0,12,0
LA 17
LS_ 105
M 0,13,1
LC_ 105
Z 1,24,0
WL 3
LO 104,16,104
HS 24,104
M 1,25,1
WV 16
RA 232,1,4096
v_min_u32 v9, v22, v23
Z 2,36,0
FL 18,84,120
FH 106,85,120
M 2,37,1
LS_ 18
v_cmp_eq_u32 vcc, 0, v9
Z 3,48,0
LA 18
LS_ 106
M 3,49,1
LC_ 106
v_add_u32 v122, v122, v2
Z 4,60,0
WL 3
LO 105,17,105
HS 24,105
M 4,61,1
CB 0,9
RA 236,3,4096
v_add_u32 v4, v22, v6
Z 5,72,0
FL 19,86,121
FH 107,87,121
M 5,73,1
LS_ 19
v_add_u32 v5, v23, v6
M 0,14,2
s_mov_b32 s64, s63
LA 19
LS_ 107
M 0,15,3
s_mov_b32 s63, s62
LC_ 107
HY 248,24,0
M 1,26,2
WL 3
LO 106,18,106
HS 25,106
M 1,27,3
s_mov_b32 s62, s61
RA 216,1,6144
HY 248,24,1
M 2,38,2
s_mov_b32 s61, s60
FL 16,88,120
FH 108,89,120
M 2,39,3
s_mov_b32 s60, s57
LS_ 16
M 3,50,2
s_lshl_b32 s53, s60, 5
LA 16
LS_ 108
M 3,51,3
s_add_u32 s48, s82, s53
LC_ 108
M 4,62,2
WL 3
LO 107,19,107
HS 25,107
M 4,63,3
s_addc_u32 s49, s83, 0
RA 220,3,6144
v_xor_b32 v20, 0x400, v20
M 5,74,2
FL 17,90,121
FH 109,91,121
M 5,75,3
s_add_u32 s57, s57, 1
ST 104,122,42
LS_ 17
M 0,16,4
LA 17
LS_ 109
M 0,17,5
LC_ 109
HY 250,25,0
M 1,28,4
WL 3
LO 108,16,108
HS 26,108
M 1,29,5
RA 224,1,8192
HY 250,25,1
M 2,40,4
FL 18,92,120
FH 110,93,120
M 2,41,5
EL_
LS_ 18
M 3,52,4
s_add_u32 s37, s37, s74
LA 18
LS_ 110
M 3,53,5
s_xor_b32 s74, s74, 64512
LC_ 110
XW 120,0
M 4,64,4
WL 3
LO 109,17,109
HS 26,109
M 4,65,5
s_and_b32 s37, s37, 0x1ffff
RA 228,3,8192
HY 252,26,0
M 5,76,4
FL 19,94,121
FH 111,95,121
M 5,77,5
s_sub_u32 s52, s37, s56
LS_ 19
HY 252,26,1
M 0,18,0
s_lshl_b32 s53, s63, 7
LA 19
LS_ 111
M 0,19,1
s_add_u32 s50, s84, s53
LC_ 111
M 1,30,0
WL 3
LO 110,18,110
HS 27,110
M 1,31,1
s_addc_u32 s51, s85, 0
RA 232,1,10240
M 2,42,0
s_mov_b32 m0, s59
FL 16,96,120
FH 112,97,120
M 2,43,1
DR
LS_ 16
M 3,54,0
s_xor_b32 s59, s59, 1024
LA 16
LS_ 112
M 3,55,1
WV 18
LC_ 112
RV 116,20,0
M 4,66,0
WL 4
LO 111,19,111
HS 27,111
M 4,67,1
s_add_u32 m0, s38, 0
RA 236,3,10240
HY 10,27,0
M 5,78,0
FL 17,98,121
FH 113,99,121
M 5,79,1
.Lk1p3:
s_cbranch_execz .Lk1p7
ST 108,21,44
LS_ 17
M 0,20,2
LA 17
LS_ 113
M 0,21,3
WL 2
LC_ 113
v_cmp_eq_u32 vcc, 0, v116
M 1,32,2
LO 112,16,112
HS 29,112
M 1,33,3
RA 216,1,12288
HY 10,27,1
M 2,44,2
CB 1,10
FL 18,100,120
FH 114,101,120
M 2,45,3
DX 4,0
LS_ 18
M 3,56,2
s_add_u32 m0, s38, 1920
LA 18
LS_ 114
M 3,57,3
LC_ 114
XW 120,1
M 4,68,2
WL 3
LO 113,17,113
HS 29,113
M 4,69,3
RA 220,3,12288
HY 124,29,0
M 5,80,2
FL 19,102,121
FH 115,103,121
M 5,81,3
DX 4,128
LS_ 19
M 0,22,4
s_add_u32 m0, s38, 3840
LA 19
LS_ 115
M 0,23,5
LC_ 115
RV 136,14,2048
M 1,34,4
WL 4
LO 114,18,114
HS 30,114
M 1,35,5
RA 224,1,14336
RV 140,15,2048
M 2,46,4
FL 16,56,116
FH 104,57,116
M 2,47,5
DX 4,256
LS_ 16
M 3,58,4
s_add_u32 m0, s38, 12288
LA 16
LS_ 104
M 3,59,5
WV 11
LC_ 104
RV 144,14,4096
M 4,70,4
WL 6
LO 115,19,115
HS 30,115
M 4,71,5
RA 228,3,14336
RV 148,15,4096
M 5,82,4
FL 17,58,117
FH 105,59,117
M 5,83,5
ST 112,21,46
LS_ 17
Z 6,12,0
LA 17
LS_ 105
M 6,13,1
LC_ 105
HY 124,29,1
Z 7,24,0
WL 5
LO 104,16,104
HZ 24,104
M 7,25,1
RA 232,1,16384
HY 126,30,0
Y3 244,134,242,136,240,138
DX 5,0
HY 126,30,1
Z 8,36,0
s_add_u32 m0, s38, 14208
FL 18,60,116
FH 106,61,116
M 8,37,1
RA 244,7,768
LS_ 18
Z 9,48,0
LA 18
LS_ 106
M 9,49,1
LC_ 106
v_add_u32 v118, v118, v2
Z 10,60,0
WL 4
LO 105,17,105
HS 24,105
M 10,61,1
RA 236,3,16384
RA 240,7,1024
Z 11,72,0
FL 19,62,117
FH 107,63,117
M 11,73,1
DX 5,128
LS_ 19
M 6,14,2
s_add_u32 m0, s38, 16128
LA 19
LS_ 107
M 6,15,3
LC_ 107
S 54,40
M 7,26,2
WL 4
LO 106,18,106
HZ 25,106
M 7,27,3
RA 216,1,18432
S 55,41
LSA 252,1
RA 252,8,192
S 54,42
M 8,38,2
FL 16,64,116
FH 108,65,116
M 8,39,3
DX 5,256
LS_ 16
)"
R"(M 9,50,2
s_add_u32 s38, s38, s76
LA 16
LS_ 108
M 9,51,3
s_xor_b32 s76, s76, 64512
LC_ 108
S 55,43
M 10,62,2
WL 4
LO 107,19,107
HS 25,107
M 10,63,3
s_and_b32 s38, s38, 0x1ffff
RA 220,3,18432
S 54,44
M 11,74,2
FL 17,66,117
FH 109,67,117
M 11,75,3
SU 104,118,42
LS_ 17
M 6,16,4
LA 17
LS_ 109
M 6,17,5
LC_ 109
S 55,45
M 7,28,4
WL 3
LO 108,16,108
HZ 26,108
M 7,29,5
s_add_u32 m0, s56, 1536
RA 224,1,20480
S 54,46
LSA 248,2
RA 248,8,256
S 55,47
M 8,40,4
FL 18,68,116
FH 110,69,116
M 8,41,5
global_load_lds_dwordx4 v34, %[xp] offset:512
LS_ 18
M 9,52,4
s_add_u32 m0, s56, 2560
LA 18
LS_ 110
M 9,53,5
LC_ 110
XW 116,0
M 10,64,4
WL 4
LO 109,17,109
HS 26,109
M 10,65,5
RA 228,3,20480
S 54,48
M 11,76,4
FL 19,70,117
FH 111,71,117
M 11,77,5
global_load_lds_dwordx4 v35, %[xp] offset:512
LS_ 19
M 6,18,0
s_add_u32 m0, s56, 3456
LA 19
LS_ 111
M 6,19,1
LC_ 111
S 55,49
M 7,30,0
WL 3
LO 110,18,110
HZ 27,110
M 7,31,1
RA 232,1,22528
S 54,50
M 8,42,0
FL 16,72,116
FH 112,73,116
M 8,43,1
RV 120,20,64
LS_ 16
M 9,54,0
LA 16
LS_ 112
M 9,55,1
LC_ 112
v_add_u32 v1, s52, v14
M 10,66,0
WL 4
LO 111,19,111
HS 27,111
M 10,67,1
RA 236,3,22528
S 55,51
M 11,78,0
FL 17,74,117
FH 113,75,117
M 11,79,1
SU 108,21,44
LS_ 17
M 6,20,2
LA 17
LS_ 113
M 6,21,3
WL 4
LC_ 113
v_cmp_eq_u32 vcc, 0, v120
M 7,32,2
WL 3
LO 112,16,112
HZ 29,112
M 7,33,3
RA 216,1,0
v_xor_b32 v3, 64, v1
M 8,44,2
CB 2,11
FL 18,76,116
FH 114,77,116
M 8,45,3
global_load_lds_dwordx4 v34, %[xp] offset:640
LS_ 18
M 9,56,2
s_add_u32 m0, s56, 4480
LA 18
LS_ 114
M 9,57,3
LC_ 114
XW 116,1
M 10,68,2
WL 3
LO 113,17,113
HS 29,113
M 10,69,3
RA 220,3,0
M 11,80,2
FL 19,78,117
FH 115,79,117
M 11,81,3
global_load_lds_dwordx4 v35, %[xp] offset:640
LS_ 19
M 6,22,4
LA 19
LS_ 115
M 6,23,5
LC_ 115
M 7,34,4
WL 3
LO 114,18,114
HZ 30,114
M 7,35,5
RA 224,1,2048
Y3 246,140,244,142,242,144
RA 244,7,1280
M 8,46,4
FL 16,80,120
FH 104,81,120
M 8,47,5
s_barrier
LS_ 16
M 9,58,4
LA 16
LS_ 104
M 9,59,5
LC_ 104
S 54,40
M 10,70,4
WL 4
LO 115,19,115
HS 30,115
M 10,71,5
RA 228,3,2048
S 55,41
M 11,82,4
FL 17,82,121
FH 105,83,121
M 11,83,5
1:
SU 112,21,46
LS_ 17
Z 0,12,0
LA 17
LS_ 105
M 0,13,1
LC_ 105
S 54,42
Z 1,24,0
WL 3
LO 104,16,104
HS 24,104
M 1,25,1
WV 16
RA 232,1,4096
v_min_u32 v9, v22, v23
LSA 252,3
RA 252,8,320
v_cmp_eq_u32 vcc, 0, v9
Z 2,36,0
FL 18,84,120
FH 106,85,120
M 2,37,1
LS_ 18
S 55,43
Z 3,48,0
CB 0,12
LA 18
LS_ 106
M 3,49,1
LC_ 106
v_add_u32 v122, v122, v2
Z 4,60,0
WL 4
LO 105,17,105
HS 24,105
M 4,61,1
RA 236,3,4096
v_add_u32 v4, v22, v6
Z 5,72,0
FL 19,86,121
FH 107,87,121
M 5,73,1
LS_ 19
v_add_u32 v5, v23, v6
M 0,14,2
s_mov_b32 s64, s63
LA 19
LS_ 107
M 0,15,3
s_mov_b32 s63, s62
LC_ 107
S 54,44
M 1,26,2
WL 3
LO 106,18,106
HS 25,106
M 1,27,3
s_mov_b32 s62, s61
RA 216,1,6144
S 55,45
M 2,38,2
s_mov_b32 s61, s60
FL 16,88,120
FH 108,89,120
M 2,39,3
s_mov_b32 s60, s57
LS_ 16
S 54,46
M 3,50,2
s_lshl_b32 s53, s60, 5
LA 16
LS_ 108
M 3,51,3
s_add_u32 s48, s82, s53
LC_ 108
S 55,47
M 4,62,2
WL 3
LO 107,19,107
HS 25,107
M 4,63,3
s_addc_u32 s49, s83, 0
RA 220,3,6144
v_xor_b32 v20, 0x400, v20
M 5,74,2
FL 17,90,121
FH 109,91,121
M 5,75,3
s_add_u32 s57, s57, 1
SU 104,122,42
LS_ 17
M 0,16,4
LA 17
LS_ 109
M 0,17,5
LC_ 109
S 54,48
M 1,28,4
WL 3
LO 108,16,108
HS 26,108
M 1,29,5
RA 224,1,8192
S 55,49
M 2,40,4
FL 18,92,120
FH 110,93,120
M 2,41,5
EL_
LS_ 18
M 3,52,4
s_add_u32 s37, s37, s74
LA 18
LS_ 110
M 3,53,5
s_xor_b32 s74, s74, 64512
LC_ 110
XW 120,0
M 4,64,4
WL 3
LO 109,17,109
HS 26,109
M 4,65,5
s_and_b32 s37, s37, 0x1ffff
RA 228,3,8192
S 54,50
M 5,76,4
FL 19,94,121
FH 111,95,121
M 5,77,5
s_sub_u32 s52, s37, s56
LS_ 19
S 55,51
M 0,18,0
s_lshl_b32 s53, s63, 7
LA 19
LS_ 111
M 0,19,1
s_add_u32 s50, s84, s53
LC_ 111
M 1,30,0
WL 3
LO 110,18,110
HS 27,110
M 1,31,1
s_addc_u32 s51, s85, 0
RA 232,1,10240
M 2,42,0
s_mov_b32 m0, s59
FL 16,96,120
FH 112,97,120
M 2,43,1
DR
LS_ 16
M 3,54,0
s_xor_b32 s59, s59, 1024
LA 16
LS_ 112
M 3,55,1
WV 18
LC_ 112
RV 116,20,0
M 4,66,0
WL 4
LO 111,19,111
HS 27,111
M 4,67,1
s_add_u32 m0, s38, 0
RA 236,3,10240
M 5,78,0
FL 17,98,121
FH 113,99,121
M 5,79,1
SU 108,21,44
LS_ 17
M 0,20,2
LA 17
LS_ 113
M 0,21,3
WL 2
LC_ 113
v_cmp_eq_u32 vcc, 0, v116
M 1,32,2
LO 112,16,112
HS 29,112
M 1,33,3
WV 8
RA 216,1,12288
RV 152,14,0
Y3 240,146,246,148,244,150
CB 1,13
RV 156,15,0
RV 160,14,2048
M 2,44,2
FL 18,100,120
FH 114,101,120
M 2,45,3
RV 164,15,2048
LS_ 18
M 3,56,2
LA 18
LS_ 114
M 3,57,3
WV 5
LC_ 114
RV 168,14,4096
M 4,68,2
WL 8
LO 113,17,113
HS 29,113
M 4,69,3
RA 220,3,12288
RV 172,15,4096
M 5,80,2
FL 19,102,121
FH 115,103,121
M 5,81,3
LS_ 19
)"
R"(XW 120,1
M 0,22,4
.Lk1p4:
s_cbranch_execz .Lk1p8
LA 19
LS_ 115
M 0,23,5
LC_ 115
DX 4,0
M 1,34,4
WL 5
LO 114,18,114
HS 30,114
M 1,35,5
s_add_u32 m0, s38, 1920
RA 224,1,14336
RA 240,7,1536
LSA 248,4
RA 248,8,384
RA 244,7,1792
M 2,46,4
FL 16,56,116
FH 104,57,116
M 2,47,5
DX 4,128
LS_ 16
M 3,58,4
s_add_u32 m0, s38, 3840
LA 16
LS_ 104
M 3,59,5
LC_ 104
S 54,40
M 4,70,4
WL 6
LO 115,19,115
HS 30,115
M 4,71,5
RA 228,3,14336
S 55,41
M 5,82,4
FL 17,58,117
FH 105,59,117
M 5,83,5
SU 112,21,46
LS_ 17
Z 6,12,0
LA 17
LS_ 105
M 6,13,1
LC_ 105
S 54,42
Z 7,24,0
WL 3
LO 104,16,104
HS 24,104
M 7,25,1
RA 232,1,16384
S 55,43
LSA 252,5
RA 252,8,448
S 54,44
Z 8,36,0
FL 18,60,116
FH 106,61,116
M 8,37,1
DX 4,256
LS_ 18
Z 9,48,0
s_add_u32 m0, s38, 12288
LA 18
LS_ 106
M 9,49,1
LC_ 106
v_add_u32 v118, v118, v2
Z 10,60,0
WL 4
LO 105,17,105
HS 24,105
M 10,61,1
RA 236,3,16384
S 55,45
Z 11,72,0
FL 19,62,117
FH 107,63,117
M 11,73,1
DX 5,0
LS_ 19
M 6,14,2
s_add_u32 m0, s38, 14208
LA 19
LS_ 107
M 6,15,3
LC_ 107
S 54,46
M 7,26,2
WL 3
LO 106,18,106
HS 25,106
M 7,27,3
RA 216,1,18432
S 55,47
M 8,38,2
FL 16,64,116
FH 108,65,116
M 8,39,3
DX 5,128
LS_ 16
M 9,50,2
s_add_u32 m0, s38, 16128
LA 16
LS_ 108
M 9,51,3
LC_ 108
S 54,48
M 10,62,2
WL 3
LO 107,19,107
HS 25,107
M 10,63,3
RA 220,3,18432
S 55,49
M 11,74,2
FL 17,66,117
FH 109,67,117
M 11,75,3
ST 104,118,42
LS_ 17
M 6,16,4
LA 17
LS_ 109
M 6,17,5
LC_ 109
S 54,50
M 7,28,4
WL 3
LO 108,16,108
HS 26,108
M 7,29,5
RA 224,1,20480
S 55,51
M 8,40,4
FL 18,68,116
FH 110,69,116
M 8,41,5
DX 5,256
LS_ 18
M 9,52,4
s_add_u32 s38, s38, s76
LA 18
LS_ 110
M 9,53,5
s_xor_b32 s76, s76, 64512
LC_ 110
XW 116,0
M 10,64,4
WL 3
LO 109,17,109
HS 26,109
M 10,65,5
s_and_b32 s38, s38, 0x1ffff
RA 228,3,20480
M 11,76,4
FL 19,70,117
FH 111,71,117
M 11,77,5
LS_ 19
M 6,18,0
LA 19
LS_ 111
M 6,19,1
LC_ 111
M 7,30,0
WL 3
LO 110,18,110
HS 27,110
M 7,31,1
RA 232,1,22528
Y3 242,152,240,154,246,156
RA 240,7,2048
M 8,42,0
FL 16,72,116
FH 112,73,116
M 8,43,1
RV 120,20,64
LS_ 16
M 9,54,0
LA 16
LS_ 112
M 9,55,1
LC_ 112
v_add_u32 v1, s52, v14
M 10,66,0
WL 5
LO 111,19,111
HS 27,111
M 10,67,1
RA 236,3,22528
S 54,40
M 11,78,0
FL 17,74,117
FH 113,75,117
M 11,79,1
ST 108,21,44
LS_ 17
M 6,20,2
LA 17
LS_ 113
M 6,21,3
WL 4
LC_ 113
v_cmp_eq_u32 vcc, 0, v120
M 7,32,2
WL 3
LO 112,16,112
HS 29,112
M 7,33,3
RA 216,1,0
v_xor_b32 v3, 64, v1
LSA 248,6
CB 2,14
RA 248,8,512
S 55,41
M 8,44,2
FL 18,76,116
FH 114,77,116
M 8,45,3
LS_ 18
S 54,42
M 9,56,2
LA 18
LS_ 114
M 9,57,3
LC_ 114
XW 116,1
M 10,68,2
WL 4
LO 113,17,113
HS 29,113
M 10,69,3
RA 220,3,0
S 55,43
M 11,80,2
FL 19,78,117
FH 115,79,117
M 11,81,3
LS_ 19
S 54,44
M 6,22,4
LA 19
LS_ 115
M 6,23,5
LC_ 115
S 55,45
M 7,34,4
WL 3
LO 114,18,114
HS 30,114
M 7,35,5
RA 224,1,2048
S 54,46
M 8,46,4
FL 16,80,120
FH 104,81,120
M 8,47,5
s_barrier
LS_ 16
S 55,47
M 9,58,4
LA 16
LS_ 104
M 9,59,5
LC_ 104
S 54,48
M 10,70,4
WL 3
LO 115,19,115
HS 30,115
M 10,71,5
RA 228,3,2048
S 55,49
M 11,82,4
FL 17,82,121
FH 105,83,121
M 11,83,5
ST 112,21,46
LS_ 17
Z 0,12,0
LA 17
LS_ 105
M 0,13,1
LC_ 105
S 54,50
Z 1,24,0
WL 3
LO 104,16,104
HS 24,104
M 1,25,1
WV 12
RA 232,1,4096
v_min_u32 v9, v22, v23
Z 2,36,0
FL 18,84,120
FH 106,85,120
M 2,37,1
LS_ 18
v_cmp_eq_u32 vcc, 0, v9
Z 3,48,0
LA 18
LS_ 106
M 3,49,1
LC_ 106
v_add_u32 v122, v122, v2
Z 4,60,0
WL 3
LO 105,17,105
HS 24,105
M 4,61,1
CB 0,15
RA 236,3,4096
v_add_u32 v4, v22, v6
Z 5,72,0
FL 19,86,121
FH 107,87,121
M 5,73,1
LS_ 19
v_add_u32 v5, v23, v6
M 0,14,2
s_mov_b32 s64, s63
LA 19
LS_ 107
M 0,15,3
s_mov_b32 s63, s62
LC_ 107
HY 248,24,0
M 1,26,2
WL 3
LO 106,18,106
HS 25,106
M 1,27,3
s_mov_b32 s62, s61
RA 216,1,6144
HY 248,24,1
M 2,38,2
s_mov_b32 s61, s60
FL 16,88,120
FH 108,89,120
M 2,39,3
s_mov_b32 s60, s57
LS_ 16
S 55,51
M 3,50,2
s_lshl_b32 s53, s60, 5
LA 16
LS_ 108
M 3,51,3
s_add_u32 s48, s82, s53
LC_ 108
M 4,62,2
WL 3
LO 107,19,107
HS 25,107
M 4,63,3
s_addc_u32 s49, s83, 0
RA 220,3,6144
v_xor_b32 v20, 0x400, v20
M 5,74,2
FL 17,90,121
FH 109,91,121
M 5,75,3
s_add_u32 s57, s57, 1
ST 104,122,42
LS_ 17
M 0,16,4
LA 17
LS_ 109
M 0,17,5
LC_ 109
HY 250,25,0
M 1,28,4
WL 3
LO 108,16,108
HS 26,108
M 1,29,5
RA 224,1,8192
HY 250,25,1
Y3 244,158,242,160,240,162
EL_
RA 244,7,2304
M 2,40,4
FL 18,92,120
FH 110,93,120
M 2,41,5
RA 240,7,2560
LS_ 18
M 3,52,4
s_add_u32 s37, s37, s74
LA 18
LS_ 110
M 3,53,5
)"
R"(s_xor_b32 s74, s74, 64512
LC_ 110
XW 120,0
M 4,64,4
WL 5
LO 109,17,109
HS 26,109
M 4,65,5
s_and_b32 s37, s37, 0x1ffff
RA 228,3,8192
HY 252,26,0
M 5,76,4
FL 19,94,121
FH 111,95,121
M 5,77,5
s_sub_u32 s52, s37, s56
LS_ 19
HY 252,26,1
M 0,18,0
s_lshl_b32 s53, s63, 7
LA 19
LS_ 111
M 0,19,1
s_add_u32 s50, s84, s53
LC_ 111
S 54,40
M 1,30,0
WL 3
LO 110,18,110
HS 27,110
M 1,31,1
s_addc_u32 s51, s85, 0
RA 232,1,10240
S 55,41
LSA 252,7
s_mov_b32 m0, s59
RA 252,8,576
S 54,42
M 2,42,0
FL 16,96,120
FH 112,97,120
M 2,43,1
DR
LS_ 16
M 3,54,0
s_xor_b32 s59, s59, 1024
LA 16
LS_ 112
M 3,55,1
WV 14
LC_ 112
RV 116,20,0
M 4,66,0
WL 5
LO 111,19,111
HS 27,111
M 4,67,1
s_add_u32 m0, s38, 0
RA 236,3,10240
HY 10,27,0
M 5,78,0
FL 17,98,121
FH 113,99,121
M 5,79,1
ST 108,21,44
LS_ 17
M 0,20,2
LA 17
LS_ 113
M 0,21,3
WL 2
LC_ 113
v_cmp_eq_u32 vcc, 0, v116
M 1,32,2
LO 112,16,112
HS 29,112
M 1,33,3
RA 216,1,12288
HY 10,27,1
LSA 248,8
CB 1,16
RA 248,8,640
S 55,43
M 2,44,2
FL 18,100,120
FH 114,101,120
M 2,45,3
DX 4,0
LS_ 18
M 3,56,2
s_add_u32 m0, s38, 1920
LA 18
LS_ 114
M 3,57,3
LC_ 114
XW 120,1
M 4,68,2
WL 4
LO 113,17,113
HS 29,113
M 4,69,3
RA 220,3,12288
HY 124,29,0
M 5,80,2
FL 19,102,121
FH 115,103,121
M 5,81,3
DX 4,128
LS_ 19
M 0,22,4
s_add_u32 m0, s38, 3840
LA 19
LS_ 115
M 0,23,5
LC_ 115
HY 124,29,1
M 1,34,4
WL 3
LO 114,18,114
HS 30,114
M 1,35,5
RA 224,1,14336
S 54,44
M 2,46,4
FL 16,56,116
FH 104,57,116
M 2,47,5
DX 4,256
LS_ 16
M 3,58,4
s_add_u32 m0, s38, 12288
LA 16
LS_ 104
M 3,59,5
LC_ 104
S 55,45
M 4,70,4
WL 3
LO 115,19,115
HS 30,115
M 4,71,5
RA 228,3,14336
HY 126,30,0
M 5,82,4
FL 17,58,117
FH 105,59,117
M 5,83,5
ST 112,21,46
LS_ 17
Z 6,12,0
LA 17
LS_ 105
M 6,13,1
LC_ 105
HY 126,30,1
Z 7,24,0
WL 3
LO 104,16,104
HZ 24,104
M 7,25,1
.Lk1p5:
s_cbranch_execz .Lk1p9
RA 232,1,16384
S 54,46
Z 8,36,0
FL 18,60,116
FH 106,61,116
M 8,37,1
DX 5,0
LS_ 18
Z 9,48,0
s_add_u32 m0, s38, 14208
LA 18
LS_ 106
M 9,49,1
LC_ 106
v_add_u32 v118, v118, v2
Z 10,60,0
WL 3
LO 105,17,105
HS 24,105
M 10,61,1
RA 236,3,16384
S 55,47
Z 11,72,0
FL 19,62,117
FH 107,63,117
M 11,73,1
DX 5,128
LS_ 19
M 6,14,2
s_add_u32 m0, s38, 16128
LA 19
LS_ 107
M 6,15,3
LC_ 107
S 54,48
M 7,26,2
WL 3
LO 106,18,106
HZ 25,106
M 7,27,3
RA 216,1,18432
S 55,49
M 8,38,2
FL 16,64,116
FH 108,65,116
M 8,39,3
DX 5,256
LS_ 16
M 9,50,2
s_add_u32 s38, s38, s76
LA 16
LS_ 108
M 9,51,3
s_xor_b32 s76, s76, 64512
LC_ 108
S 54,50
M 10,62,2
WL 3
LO 107,19,107
HS 25,107
M 10,63,3
s_and_b32 s38, s38, 0x1ffff
RA 220,3,18432
S 55,51
M 11,74,2
FL 17,66,117
FH 109,67,117
M 11,75,3
ST 104,118,42
LS_ 17
M 6,16,4
LA 17
LS_ 109
M 6,17,5
LC_ 109
M 7,28,4
WL 3
LO 108,16,108
HZ 26,108
M 7,29,5
RA 224,1,20480
M 8,40,4
FL 18,68,116
FH 110,69,116
M 8,41,5
LS_ 18
M 9,52,4
LA 18
LS_ 110
M 9,53,5
LC_ 110
XW 116,0
M 10,64,4
WL 3
LO 109,17,109
HS 26,109
M 10,65,5
RA 228,3,20480
M 11,76,4
FL 19,70,117
FH 111,71,117
M 11,77,5
LS_ 19
M 6,18,0
LA 19
LS_ 111
M 6,19,1
LC_ 111
M 7,30,0
WL 3
LO 110,18,110
HZ 27,110
M 7,31,1
RA 232,1,22528
M 8,42,0
FL 16,72,116
FH 112,73,116
M 8,43,1
RV 120,20,64
LS_ 16
M 9,54,0
LA 16
LS_ 112
M 9,55,1
LC_ 112
v_add_u32 v1, s52, v14
M 10,66,0
WL 4
LO 111,19,111
HS 27,111
M 10,67,1
RA 236,3,22528
Y3 246,164,244,166,242,168
RA 244,7,2816
M 11,78,0
FL 17,74,117
FH 113,75,117
M 11,79,1
ST 108,21,44
LS_ 17
M 6,20,2
LA 17
LS_ 113
M 6,21,3
WL 5
LC_ 113
v_cmp_eq_u32 vcc, 0, v120
M 7,32,2
WL 4
LO 112,16,112
HZ 29,112
M 7,33,3
RA 216,1,0
v_xor_b32 v3, 64, v1
M 8,44,2
CB 2,17
FL 18,76,116
FH 114,77,116
M 8,45,3
LS_ 18
S 54,40
M 9,56,2
LA 18
LS_ 114
M 9,57,3
LC_ 114
XW 116,1
M 10,68,2
WL 3
LO 113,17,113
HS 29,113
M 10,69,3
RA 220,3,0
S 55,41
LSA 252,9
RA 252,8,704
S 54,42
M 11,80,2
FL 19,78,117
FH 115,79,117
M 11,81,3
LS_ 19
S 55,43
M 6,22,4
LA 19
LS_ 115
M 6,23,5
LC_ 115
S 54,44
M 7,34,4
WL 4
LO 114,18,114
HZ 30,114
M 7,35,5
RA 224,1,2048
S 55,45
M 8,46,4
WV 15
FL 16,80,120
FH 104,81,120
M 8,47,5
s_barrier
LS_ 16
S 54,46
M 9,58,4
LA 16
LS_ 104
M 9,59,5
LC_ 104
S 55,47
M 10,70,4
WL 3
LO 115,19,115
HS 30,115
M 10,71,5
RA 228,3,2048
S 54,48
M 11,82,4
FL 17,82,121
FH 105,83,121
M 11,83,5
ST 112,21,46
LS_ 17
Z 0,12,0
LA 17
LS_ 105
M 0,13,1
LC_ 105
S 55,49
Z 1,24,0
WL 3
LO 104,16,104
HS 24,104
M 1,25,1
WV 12
RA 232,1,4096
v_min_u32 v9, v22, v23
Z 2,36,0
)"
R"(FL 18,84,120
FH 106,85,120
M 2,37,1
LS_ 18
v_cmp_eq_u32 vcc, 0, v9
Z 3,48,0
LA 18
LS_ 106
M 3,49,1
LC_ 106
v_add_u32 v122, v122, v2
Z 4,60,0
WL 3
LO 105,17,105
HS 24,105
M 4,61,1
CB 0,18
RA 236,3,4096
v_add_u32 v4, v22, v6
Z 5,72,0
FL 19,86,121
FH 107,87,121
M 5,73,1
LS_ 19
v_add_u32 v5, v23, v6
M 0,14,2
s_mov_b32 s64, s63
LA 19
LS_ 107
M 0,15,3
s_mov_b32 s63, s62
LC_ 107
S 54,50
M 1,26,2
WL 3
LO 106,18,106
HS 25,106
M 1,27,3
s_mov_b32 s62, s61
RA 216,1,6144
S 55,51
M 2,38,2
s_mov_b32 s61, s60
FL 16,88,120
FH 108,89,120
M 2,39,3
s_mov_b32 s60, s57
LS_ 16
M 3,50,2
s_lshl_b32 s53, s60, 5
LA 16
LS_ 108
M 3,51,3
s_add_u32 s48, s82, s53
LC_ 108
M 4,62,2
WL 3
LO 107,19,107
HS 25,107
M 4,63,3
s_addc_u32 s49, s83, 0
RA 220,3,6144
v_xor_b32 v20, 0x400, v20
M 5,74,2
FL 17,90,121
FH 109,91,121
M 5,75,3
s_add_u32 s57, s57, 1
ST 104,122,42
LS_ 17
M 0,16,4
LA 17
LS_ 109
M 0,17,5
LC_ 109
M 1,28,4
WL 3
LO 108,16,108
HS 26,108
M 1,29,5
RA 224,1,8192
M 2,40,4
FL 18,92,120
FH 110,93,120
M 2,41,5
EL_
LS_ 18
M 3,52,4
s_add_u32 s37, s37, s74
LA 18
LS_ 110
M 3,53,5
s_xor_b32 s74, s74, 64512
LC_ 110
XW 120,0
M 4,64,4
WL 3
LO 109,17,109
HS 26,109
M 4,65,5
s_and_b32 s37, s37, 0x1ffff
RA 228,3,8192
Y3 240,170,246,172,244,174
s_sub_u32 s52, s37, s56
M 5,76,4
FL 19,94,121
FH 111,95,121
M 5,77,5
s_lshl_b32 s53, s63, 7
LS_ 19
M 0,18,0
s_add_u32 s50, s84, s53
LA 19
LS_ 111
M 0,19,1
s_addc_u32 s51, s85, 0
LC_ 111
S 54,40
M 1,30,0
WL 3
LO 110,18,110
HS 27,110
M 1,31,1
s_mov_b32 m0, s59
RA 232,1,10240
S 55,41
M 2,42,0
FL 16,96,120
FH 112,97,120
M 2,43,1
DR
LS_ 16
M 3,54,0
s_xor_b32 s59, s59, 1024
LA 16
LS_ 112
M 3,55,1
WV 14
LC_ 112
RV 116,20,0
M 4,66,0
WL 4
LO 111,19,111
HS 27,111
M 4,67,1
s_add_u32 m0, s38, 0
RA 236,3,10240
S 54,42
LSA 248,10
S 55,43
S 54,44
M 5,78,0
FL 17,98,121
FH 113,99,121
M 5,79,1
ST 108,21,44
LS_ 17
M 0,20,2
LA 17
LS_ 113
M 0,21,3
WL 2
LC_ 113
v_cmp_eq_u32 vcc, 0, v116
M 1,32,2
LO 112,16,112
HS 29,112
M 1,33,3
RA 216,1,12288
S 55,45
M 2,44,2
CB 1,19
FL 18,100,120
FH 114,101,120
M 2,45,3
DX 4,0
LS_ 18
M 3,56,2
s_add_u32 m0, s38, 1920
LA 18
LS_ 114
M 3,57,3
LC_ 114
XW 120,1
M 4,68,2
WL 3
LO 113,17,113
HS 29,113
M 4,69,3
RA 220,3,12288
S 54,46
LSA 252,11
DX 4,128
S 55,47
M 5,80,2
FL 19,102,121
FH 115,103,121
M 5,81,3
s_add_u32 m0, s38, 3840
LS_ 19
S 54,48
M 0,22,4
LA 19
LS_ 115
M 0,23,5
LC_ 115
DX 4,256
M 1,34,4
WL 3
LO 114,18,114
HS 30,114
M 1,35,5
s_add_u32 m0, s38, 12288
RA 224,1,14336
S 55,49
M 2,46,4
FL 16,56,116
FH 104,57,116
M 2,47,5
DX 5,0
LS_ 16
M 3,58,4
LA 16
LS_ 104
M 3,59,5
LC_ 104
S 54,50
M 4,70,4
WL 3
LO 115,19,115
HS 30,115
M 4,71,5
s_add_u32 m0, s38, 14208
RA 228,3,14336
S 55,51
M 5,82,4
FL 17,58,117
FH 105,59,117
M 5,83,5
ST 112,21,46
LS_ 17
Z 6,12,0
LA 17
LS_ 105
M 6,13,1
LC_ 105
Z 7,24,0
WL 3
LO 104,16,104
HS 24,104
M 7,25,1
RA 232,1,16384
Z 8,36,0
FL 18,60,116
FH 106,61,116
M 8,37,1
DX 5,128
LS_ 18
Z 9,48,0
s_add_u32 m0, s38, 16128
LA 18
LS_ 106
M 9,49,1
LC_ 106
v_add_u32 v118, v118, v2
Z 10,60,0
WL 3
LO 105,17,105
HS 24,105
M 10,61,1
RA 236,3,16384
Z 11,72,0
FL 19,62,117
FH 107,63,117
M 11,73,1
DX 5,256
LS_ 19
M 6,14,2
s_add_u32 s38, s38, s76
LA 19
LS_ 107
M 6,15,3
s_xor_b32 s76, s76, 64512
LC_ 107
M 7,26,2
WL 3
LO 106,18,106
HS 25,106
M 7,27,3
s_and_b32 s38, s38, 0x1ffff
RA 216,1,18432
M 8,38,2
FL 16,64,116
FH 108,65,116
M 8,39,3
LS_ 16
M 9,50,2
LA 16
LS_ 108
M 9,51,3
LC_ 108
M 10,62,2
WL 3
LO 107,19,107
HS 25,107
M 10,63,3
RA 220,3,18432
M 11,74,2
FL 17,66,117
FH 109,67,117
M 11,75,3
ST 104,118,42
LS_ 17
M 6,16,4
.Lk1p6:
s_cbranch_execz .Lk1p10
LA 17
LS_ 109
M 6,17,5
LC_ 109
M 7,28,4
WL 3
LO 108,16,108
HS 26,108
M 7,29,5
RA 224,1,20480
v_add_f32 v52, v54, v55
M 8,40,4
FL 18,68,116
FH 110,69,116
M 8,41,5
LS_ 18
v_mul_f32 v36, 0xbe2aaaab, v36
M 9,52,4
LA 18
LS_ 110
M 9,53,5
LC_ 110
XW 116,0
M 10,64,4
WL 3
LO 109,17,109
HS 26,109
M 10,65,5
RA 228,3,20480
v_mul_f32 v37, 0x3c800000, v36
M 11,76,4
FL 19,70,117
FH 111,71,117
M 11,77,5
LS_ 19
v_mov_b32 v53, v52
M 6,18,0
LA 19
LS_ 111
M 6,19,1
LC_ 111
v_permlane32_swap_b32 v52, v53
M 7,30,0
WL 3
LO 110,18,110
HS 27,110
M 7,31,1
RA 232,1,22528
v_add_f32 v52, v52, v53
M 8,42,0
FL 16,72,116
FH 112,73,116
M 8,43,1
RV 120,20,64
LS_ 16
M 9,54,0
LA 16
LS_ 112
M 9,55,1
LC_ 112
v_add_u32 v1, s52, v14
M 10,66,0
WL 4
LO 111,19,111
HS 27,111
M 10,67,1
RA 236,3,22528
v_mov_b32 v53, v52
M 11,78,0
FL 17,74,117
FH 113,75,117
M 11,79,1
ST 108,21,44
LS_ 17
M 6,20,2
LA 17
LS_ 113
M 6,21,3
WL 4
LC_ 113
v_cmp_eq_u32 vcc, 0, v120
M 7,32,2
WL 3
LO 112,16,112
)"
R"(HS 29,112
M 7,33,3
RA 216,1,0
v_xor_b32 v3, 64, v1
M 8,44,2
CB 2,20
FL 18,76,116
FH 114,77,116
M 8,45,3
LS_ 18
v_permlane16_swap_b32 v52, v53
M 9,56,2
LA 18
LS_ 114
M 9,57,3
LC_ 114
XW 116,1
M 10,68,2
WL 3
LO 113,17,113
HS 29,113
M 10,69,3
RA 220,3,0
v_add_f32 v52, v52, v53
M 11,80,2
FL 19,78,117
FH 115,79,117
M 11,81,3
LS_ 19
v_mul_f32 v52, 0x352aaaab, v52
M 6,22,4
LA 19
LS_ 115
M 6,23,5
LC_ 115
v_fma_f32 v52, -v37, v37, v52
M 7,34,4
WL 3
LO 114,18,114
HS 30,114
M 7,35,5
RA 224,1,2048
C 52,52,52
M 8,46,4
WV 16
FL 16,80,120
FH 104,81,120
M 8,47,5
s_barrier
LS_ 16
v_lshlrev_b32 v52, 16, v52
M 9,58,4
LA 16
LS_ 104
M 9,59,5
LC_ 104
v_add_f32 v52, 0x3727c5ac, v52
M 10,70,4
WL 3
LO 115,19,115
HS 30,115
M 10,71,5
RA 228,3,2048
C 52,52,52
M 11,82,4
FL 17,82,121
FH 105,83,121
M 11,83,5
ST 112,21,46
LS_ 17
Z 0,12,0
LA 17
LS_ 105
M 0,13,1
LC_ 105
v_lshlrev_b32 v52, 16, v52
Z 1,24,0
WL 3
LO 104,16,104
HS 24,104
M 1,25,1
WV 12
RA 232,1,4096
v_min_u32 v9, v22, v23
Z 2,36,0
FL 18,84,120
FH 106,85,120
M 2,37,1
LS_ 18
v_cmp_eq_u32 vcc, 0, v9
Z 3,48,0
LA 18
LS_ 106
M 3,49,1
LC_ 106
v_add_u32 v122, v122, v2
Z 4,60,0
WL 3
LO 105,17,105
HS 24,105
M 4,61,1
CB 0,21
RA 236,3,4096
v_add_u32 v4, v22, v6
Z 5,72,0
FL 19,86,121
FH 107,87,121
M 5,73,1
LS_ 19
v_add_u32 v5, v23, v6
M 0,14,2
s_mov_b32 s64, s63
LA 19
LS_ 107
M 0,15,3
s_mov_b32 s63, s62
LC_ 107
HY 248,24,0
M 1,26,2
WL 3
LO 106,18,106
HS 25,106
M 1,27,3
s_mov_b32 s62, s61
RA 216,1,6144
HY 248,24,1
M 2,38,2
s_mov_b32 s61, s60
FL 16,88,120
FH 108,89,120
M 2,39,3
s_mov_b32 s60, s57
LS_ 16
v_sqrt_f32 v52, v52
M 3,50,2
s_lshl_b32 s53, s60, 5
LA 16
LS_ 108
M 3,51,3
s_add_u32 s48, s82, s53
LC_ 108
C 52,52,52
M 4,62,2
WL 3
LO 107,19,107
HS 25,107
M 4,63,3
s_addc_u32 s49, s83, 0
RA 220,3,6144
v_xor_b32 v20, 0x400, v20
M 5,74,2
FL 17,90,121
FH 109,91,121
M 5,75,3
s_add_u32 s57, s57, 1
ST 104,122,42
LS_ 17
M 0,16,4
LA 17
LS_ 109
M 0,17,5
LC_ 109
HY 250,25,0
M 1,28,4
WL 3
LO 108,16,108
HS 26,108
M 1,29,5
RA 224,1,8192
HY 250,25,1
M 2,40,4
FL 18,92,120
FH 110,93,120
M 2,41,5
EL_
LS_ 18
M 3,52,4
s_add_u32 s37, s37, s74
LA 18
LS_ 110
M 3,53,5
s_xor_b32 s74, s74, 64512
LC_ 110
XW 120,0
M 4,64,4
WL 3
LO 109,17,109
HS 26,109
M 4,65,5
s_and_b32 s37, s37, 0x1ffff
RA 228,3,8192
HY 252,26,0
M 5,76,4
FL 19,94,121
FH 111,95,121
M 5,77,5
s_sub_u32 s52, s37, s56
LS_ 19
HY 252,26,1
M 0,18,0
s_lshl_b32 s53, s63, 7
LA 19
LS_ 111
M 0,19,1
s_add_u32 s50, s84, s53
LC_ 111
v_lshlrev_b32 v52, 16, v52
M 1,30,0
WL 3
LO 110,18,110
HS 27,110
M 1,31,1
s_addc_u32 s51, s85, 0
RA 232,1,10240
v_cmp_ge_f32 s[70:71], s67, v52
M 2,42,0
s_mov_b32 m0, s59
FL 16,96,120
FH 112,97,120
M 2,43,1
DR
LS_ 16
M 3,54,0
s_xor_b32 s59, s59, 1024
LA 16
LS_ 112
M 3,55,1
WV 14
LC_ 112
RV 116,20,0
M 4,66,0
WL 4
LO 111,19,111
HS 27,111
M 4,67,1
s_add_u32 m0, s38, 0
RA 236,3,10240
HY 10,27,0
M 5,78,0
FL 17,98,121
FH 113,99,121
M 5,79,1
ST 108,21,44
LS_ 17
M 0,20,2
LA 17
LS_ 113
M 0,21,3
WL 2
LC_ 113
v_cmp_eq_u32 vcc, 0, v116
M 1,32,2
LO 112,16,112
HS 29,112
M 1,33,3
RA 216,1,12288
HY 10,27,1
M 2,44,2
CB 1,22
FL 18,100,120
FH 114,101,120
M 2,45,3
DX 4,0
LS_ 18
M 3,56,2
s_add_u32 m0, s38, 1920
LA 18
LS_ 114
M 3,57,3
LC_ 114
XW 120,1
M 4,68,2
WL 3
LO 113,17,113
HS 29,113
M 4,69,3
RA 220,3,12288
HY 124,29,0
M 5,80,2
FL 19,102,121
FH 115,103,121
M 5,81,3
DX 4,128
LS_ 19
M 0,22,4
s_add_u32 m0, s38, 3840
LA 19
LS_ 115
M 0,23,5
LC_ 115
HY 124,29,1
M 1,34,4
WL 3
LO 114,18,114
HS 30,114
M 1,35,5
RA 224,1,14336
v_cndmask_b32 v52, 0, v52, s[70:71]
M 2,46,4
FL 16,56,116
FH 104,57,116
M 2,47,5
DX 4,256
LS_ 16
M 3,58,4
s_add_u32 m0, s38, 12288
LA 16
LS_ 104
M 3,59,5
LC_ 104
v_mul_f32 v54, 0x44000000, v52
M 4,70,4
WL 3
LO 115,19,115
HS 30,115
M 4,71,5
RA 228,3,14336
HY 126,30,0
M 5,82,4
FL 17,58,117
FH 105,59,117
M 5,83,5
ST 112,21,46
LS_ 17
Z 6,12,0
LA 17
LS_ 105
M 6,13,1
LC_ 105
HY 126,30,1
Z 7,24,0
WL 3
LO 104,16,104
HZ 24,104
M 7,25,1
RA 232,1,16384
v_cmp_ge_f32 s[70:71], v54, |v36|
Z 8,36,0
FL 18,60,116
FH 106,61,116
M 8,37,1
DX 5,0
LS_ 18
Z 9,48,0
s_add_u32 m0, s38, 14208
LA 18
LS_ 106
M 9,49,1
LC_ 106
v_add_u32 v118, v118, v2
Z 10,60,0
WL 3
LO 105,17,105
HS 24,105
M 10,61,1
RA 236,3,16384
v_cndmask_b32 v52, 0, v52, s[70:71]
Z 11,72,0
FL 19,62,117
FH 107,63,117
M 11,73,1
DX 5,128
LS_ 19
M 6,14,2
s_add_u32 m0, s38, 16128
LA 19
LS_ 107
M 6,15,3
LC_ 107
v_rcp_f32 v52, v52
M 7,26,2
WL 3
LO 106,18,106
HZ 25,106
M 7,27,3
RA 216,1,18432
v_mul_f32 v52, 0x42800000, v52
M 8,38,2
FL 16,64,116
FH 108,65,116
M 8,39,3
DX 5,256
LS_ 16
M 9,50,2
s_add_u32 s38, s38, s76
LA 16
LS_ 108
M 9,51,3
s_xor_b32 s76, s76, 64512
LC_ 108
v_mov_b32_dpp v53, v52 quad_perm:[1,1,3,3] row_mask:0xf bank_mask:0xf
M 10,62,2
WL 3
LO 107,19,107
HS 25,107
M 10,63,3
s_and_b32 s38, s38, 0x1ffff
RA 220,3,18432
v_cvt_pk_f16_f32 v55, v52, v53
M 11,74,2
)"
R"(FL 17,66,117
FH 109,67,117
M 11,75,3
SU 104,118,42
LS_ 17
M 6,16,4
LA 17
LS_ 109
M 6,17,5
LC_ 109
ds_bpermute_b32 v40, v28, v55 offset:0
M 7,28,4
WL 4
LO 108,16,108
HZ 26,108
M 7,29,5
RA 224,1,20480
ds_bpermute_b32 v41, v28, v55 offset:8
M 8,40,4
FL 18,68,116
FH 110,69,116
M 8,41,5
global_load_dword v34, v12, s[88:89] offset:0 sc1
LS_ 18
M 9,52,4
LA 18
LS_ 110
M 9,53,5
LC_ 110
XW 116,0
M 10,64,4
WL 5
LO 109,17,109
HS 26,109
M 10,65,5
RA 228,3,20480
v_readlane_b32 s72, v32, s58
M 11,76,4
FL 19,70,117
FH 111,71,117
M 11,77,5
global_load_dword v35, v12, s[88:89] offset:16 sc1
LS_ 19
M 6,18,0
LA 19
LS_ 111
M 6,19,1
LC_ 111
v_readlane_b32 s73, v33, s58
M 7,30,0
WL 3
LO 110,18,110
HZ 27,110
M 7,31,1
RA 232,1,22528
v_mov_b32 v42, s72
M 8,42,0
FL 16,72,116
FH 112,73,116
M 8,43,1
RV 120,20,64
LS_ 16
M 9,54,0
LA 16
LS_ 112
M 9,55,1
.Lk1p7:
s_cbranch_execz .Lk1p11
LC_ 112
v_add_u32 v1, s52, v14
M 10,66,0
WL 4
LO 111,19,111
HS 27,111
M 10,67,1
RA 236,3,22528
v_mov_b32 v43, s73
M 11,78,0
FL 17,74,117
FH 113,75,117
M 11,79,1
SU 108,21,44
LS_ 17
M 6,20,2
LA 17
LS_ 113
M 6,21,3
WL 4
LC_ 113
v_cmp_eq_u32 vcc, 0, v120
M 7,32,2
WL 3
LO 112,16,112
HZ 29,112
M 7,33,3
RA 216,1,0
v_xor_b32 v3, 64, v1
M 8,44,2
CB 2,23
FL 18,76,116
FH 114,77,116
M 8,45,3
RS
LS_ 18
M 9,56,2
LA 18
LS_ 114
M 9,57,3
LC_ 114
XW 116,1
M 10,68,2
WL 3
LO 113,17,113
HS 29,113
M 10,69,3
RA 220,3,0
M 11,80,2
FL 19,78,117
FH 115,79,117
M 11,81,3
LS_ 19
M 6,22,4
LA 19
LS_ 115
M 6,23,5
LC_ 115
M 7,34,4
WL 3
LO 114,18,114
HZ 30,114
M 7,35,5
RA 224,1,2048
M 8,46,4
WV 19
FL 16,80,120
FH 104,81,120
M 8,47,5
s_barrier
LS_ 16
M 9,58,4
LA 16
LS_ 104
M 9,59,5
LC_ 104
M 10,70,4
WL 3
LO 115,19,115
HS 30,115
M 10,71,5
RA 228,3,2048
M 11,82,4
FL 17,82,121
FH 105,83,121
M 11,83,5
SU 112,21,46
LS_ 17
Z 0,12,0
LA 17
LS_ 105
M 0,13,1
LC_ 105
Z 1,24,0
WL 3
LO 104,16,104
HS 24,104
M 1,25,1
WV 15
RA 232,1,4096
v_min_u32 v9, v22, v23
Z 2,36,0
FL 18,84,120
FH 106,85,120
M 2,37,1
LS_ 18
v_cmp_eq_u32 vcc, 0, v9
Z 3,48,0
LA 18
LS_ 106
M 3,49,1
LC_ 106
v_add_u32 v122, v122, v2
Z 4,60,0
WL 3
LO 105,17,105
HS 24,105
M 4,61,1
CB 0,24
RA 236,3,4096
v_add_u32 v4, v22, v6
Z 5,72,0
FL 19,86,121
FH 107,87,121
M 5,73,1
LS_ 19
v_add_u32 v5, v23, v6
M 0,14,2
s_mov_b32 s64, s63
LA 19
LS_ 107
M 0,15,3
s_mov_b32 s63, s62
LC_ 107
M 1,26,2
WL 3
LO 106,18,106
HS 25,106
M 1,27,3
s_mov_b32 s62, s61
RA 216,1,6144
M 2,38,2
s_mov_b32 s61, s60
FL 16,88,120
FH 108,89,120
M 2,39,3
s_mov_b32 s60, s57
LS_ 16
M 3,50,2
s_lshl_b32 s53, s60, 5
LA 16
LS_ 108
M 3,51,3
s_add_u32 s48, s82, s53
LC_ 108
M 4,62,2
WL 3
LO 107,19,107
HS 25,107
M 4,63,3
s_addc_u32 s49, s83, 0
RA 220,3,6144
v_xor_b32 v20, 0x400, v20
M 5,74,2
FL 17,90,121
FH 109,91,121
M 5,75,3
s_add_u32 s57, s57, 1
SU 104,122,42
LS_ 17
M 0,16,4
LA 17
LS_ 109
M 0,17,5
LC_ 109
M 1,28,4
WL 3
LO 108,16,108
HS 26,108
M 1,29,5
RA 224,1,8192
M 2,40,4
FL 18,92,120
FH 110,93,120
M 2,41,5
EL_
LS_ 18
M 3,52,4
s_add_u32 s37, s37, s74
LA 18
LS_ 110
M 3,53,5
s_xor_b32 s74, s74, 64512
LC_ 110
XW 120,0
M 4,64,4
WL 3
LO 109,17,109
HS 26,109
M 4,65,5
s_and_b32 s37, s37, 0x1ffff
RA 228,3,8192
M 5,76,4
FL 19,94,121
FH 111,95,121
M 5,77,5
s_sub_u32 s52, s37, s56
LS_ 19
M 0,18,0
s_lshl_b32 s53, s63, 7
LA 19
LS_ 111
M 0,19,1
s_add_u32 s50, s84, s53
LC_ 111
M 1,30,0
WL 3
LO 110,18,110
HS 27,110
M 1,31,1
s_addc_u32 s51, s85, 0
RA 232,1,10240
M 2,42,0
s_mov_b32 m0, s59
FL 16,96,120
FH 112,97,120
M 2,43,1
DR
LS_ 16
M 3,54,0
s_xor_b32 s59, s59, 1024
LA 16
LS_ 112
M 3,55,1
WV 17
LC_ 112
RV 116,20,0
M 4,66,0
WL 4
LO 111,19,111
HS 27,111
M 4,67,1
s_add_u32 m0, s38, 0
RA 236,3,10240
M 5,78,0
FL 17,98,121
FH 113,99,121
M 5,79,1
SU 108,21,44
LS_ 17
M 0,20,2
LA 17
LS_ 113
M 0,21,3
WL 2
LC_ 113
v_cmp_eq_u32 vcc, 0, v116
M 1,32,2
LO 112,16,112
HS 29,112
M 1,33,3
RA 216,1,12288
M 2,44,2
CB 1,25
FL 18,100,120
FH 114,101,120
M 2,45,3
DX 4,0
LS_ 18
M 3,56,2
s_add_u32 m0, s38, 1920
LA 18
LS_ 114
M 3,57,3
LC_ 114
XW 120,1
M 4,68,2
WL 3
LO 113,17,113
HS 29,113
M 4,69,3
RA 220,3,12288
M 5,80,2
FL 19,102,121
FH 115,103,121
M 5,81,3
DX 4,128
LS_ 19
M 0,22,4
s_add_u32 m0, s38, 3840
LA 19
LS_ 115
M 0,23,5
LC_ 115
M 1,34,4
WL 3
LO 114,18,114
HS 30,114
M 1,35,5
RA 224,1,14336
M 2,46,4
FL 16,56,116
FH 104,57,116
M 2,47,5
DX 4,256
LS_ 16
M 3,58,4
s_add_u32 m0, s38, 12288
LA 16
LS_ 104
M 3,59,5
LC_ 104
M 4,70,4
WL 3
LO 115,19,115
HS 30,115
M 4,71,5
RA 228,3,14336
M 5,82,4
FL 17,58,117
FH 105,59,117
M 5,83,5
SU 112,21,46
LS_ 17
Z 6,12,0
LA 17
LS_ 105
M 6,13,1
LC_ 105
Z 7,24,0
WL 3
LO 104,16,104
)"
R"(HS 24,104
M 7,25,1
RA 232,1,16384
Z 8,36,0
FL 18,60,116
FH 106,61,116
M 8,37,1
DX 5,0
LS_ 18
Z 9,48,0
s_add_u32 m0, s38, 14208
LA 18
LS_ 106
M 9,49,1
LC_ 106
v_add_u32 v118, v118, v2
Z 10,60,0
WL 3
LO 105,17,105
HS 24,105
M 10,61,1
RA 236,3,16384
Z 11,72,0
FL 19,62,117
FH 107,63,117
M 11,73,1
DX 5,128
LS_ 19
M 6,14,2
s_add_u32 m0, s38, 16128
LA 19
LS_ 107
M 6,15,3
LC_ 107
M 7,26,2
WL 3
LO 106,18,106
HS 25,106
M 7,27,3
RA 216,1,18432
M 8,38,2
FL 16,64,116
FH 108,65,116
M 8,39,3
DX 5,256
LS_ 16
M 9,50,2
s_add_u32 s38, s38, s76
LA 16
LS_ 108
M 9,51,3
s_xor_b32 s76, s76, 64512
LC_ 108
M 10,62,2
WL 3
LO 107,19,107
HS 25,107
M 10,63,3
s_and_b32 s38, s38, 0x1ffff
RA 220,3,18432
M 11,74,2
FL 17,66,117
FH 109,67,117
M 11,75,3
WV 12
ST 104,118,42
LS_ 17
M 6,16,4
s_add_u32 s86, s86, 1024
LA 17
LS_ 109
M 6,17,5
s_addc_u32 s87, s87, 0
LC_ 109
M 7,28,4
WL 3
LO 108,16,108
HS 26,108
M 7,29,5
s_add_u32 s58, s58, 1
RA 224,1,20480
M 8,40,4
FL 18,68,116
FH 110,69,116
M 8,41,5
LS_ 18
v_add_u32 v34, v34, v6
M 9,52,4
LA 18
LS_ 110
M 9,53,5
LC_ 110
XW 116,0
M 10,64,4
WL 3
LO 109,17,109
HS 26,109
M 10,65,5
RA 228,3,20480
v_add_u32 v35, v35, v6
M 11,76,4
FL 19,70,117
FH 111,71,117
M 11,77,5
LS_ 19
v_xor_b32 v34, s96, v34
M 6,18,0
LA 19
LS_ 111
M 6,19,1
LC_ 111
v_xor_b32 v35, s97, v35
M 7,30,0
WL 3
LO 110,18,110
HS 27,110
M 7,31,1
s_cmp_lt_u32 s58, 8
RA 232,1,22528
M 8,42,0
s_cselect_b32 s53, 256, 0
FL 16,72,116
FH 112,73,116
M 8,43,1
s_add_u32 s88, s88, s53
RV 120,20,64
LS_ 16
M 9,54,0
s_addc_u32 s89, s89, 0
LA 16
LS_ 112
M 9,55,1
LC_ 112
v_add_u32 v1, s52, v14
M 10,66,0
WL 4
LO 111,19,111
HS 27,111
M 10,67,1
RA 236,3,22528
M 11,78,0
FL 17,74,117
FH 113,75,117
M 11,79,1
ST 108,21,44
LS_ 17
M 6,20,2
LA 17
LS_ 113
M 6,21,3
WL 4
LC_ 113
v_cmp_eq_u32 vcc, 0, v120
M 7,32,2
WL 3
LO 112,16,112
HS 29,112
M 7,33,3
RA 216,1,0
v_xor_b32 v3, 64, v1
M 8,44,2
CB 2,26
FL 18,76,116
FH 114,77,116
M 8,45,3
LS_ 18
M 9,56,2
LA 18
LS_ 114
M 9,57,3
LC_ 114
XW 116,1
M 10,68,2
WL 3
LO 113,17,113
HS 29,113
M 10,69,3
RA 220,3,0
M 11,80,2
FL 19,78,117
FH 115,79,117
M 11,81,3
LS_ 19
M 6,22,4
LA 19
LS_ 115
M 6,23,5
LC_ 115
M 7,34,4
WL 3
LO 114,18,114
HS 30,114
M 7,35,5
RA 224,1,2048
M 8,46,4
FL 16,80,120
FH 104,81,120
M 8,47,5
s_barrier
LS_ 16
M 9,58,4
LA 16
LS_ 104
M 9,59,5
LC_ 104
M 10,70,4
WL 3
LO 115,19,115
HS 30,115
M 10,71,5
RA 228,3,2048
M 11,82,4
s_sub_u32 s66, s66, 1
FL 17,82,121
FH 105,83,121
M 11,83,5
s_cbranch_scc1 7f
ST 112,21,46
LS_ 17
Z 0,12,0
.Lk1p8:
s_cbranch_execz .Lk1p12
LA 17
LS_ 105
M 0,13,1
LC_ 105
Z 1,24,0
WL 3
LO 104,16,104
HS 24,104
M 1,25,1
WV 12
RA 232,1,4096
v_min_u32 v9, v22, v23
Z 2,36,0
FL 18,84,120
FH 106,85,120
M 2,37,1
LS_ 18
v_cmp_eq_u32 vcc, 0, v9
Z 3,48,0
LA 18
LS_ 106
M 3,49,1
LC_ 106
v_add_u32 v122, v122, v2
Z 4,60,0
WL 3
LO 105,17,105
HS 24,105
M 4,61,1
CB 0,27
RA 236,3,4096
v_add_u32 v4, v22, v6
Z 5,72,0
FL 19,86,121
FH 107,87,121
M 5,73,1
LS_ 19
v_add_u32 v5, v23, v6
M 0,14,2
s_mov_b32 s64, s63
LA 19
LS_ 107
M 0,15,3
s_mov_b32 s63, s62
LC_ 107
HY 248,24,0
M 1,26,2
WL 3
LO 106,18,106
HS 25,106
M 1,27,3
s_mov_b32 s62, s61
RA 216,1,6144
HY 248,24,1
M 2,38,2
s_mov_b32 s61, s60
FL 16,88,120
FH 108,89,120
M 2,39,3
s_mov_b32 s60, s57
LS_ 16
M 3,50,2
s_lshl_b32 s53, s60, 5
LA 16
LS_ 108
M 3,51,3
s_add_u32 s48, s82, s53
LC_ 108
M 4,62,2
WL 3
LO 107,19,107
HS 25,107
M 4,63,3
s_addc_u32 s49, s83, 0
RA 220,3,6144
v_xor_b32 v20, 0x400, v20
M 5,74,2
FL 17,90,121
FH 109,91,121
M 5,75,3
s_add_u32 s57, s57, 1
ST 104,122,42
LS_ 17
M 0,16,4
LA 17
LS_ 109
M 0,17,5
LC_ 109
HY 250,25,0
M 1,28,4
WL 3
LO 108,16,108
HS 26,108
M 1,29,5
RA 224,1,8192
HY 250,25,1
M 2,40,4
FL 18,92,120
FH 110,93,120
M 2,41,5
EL_
LS_ 18
M 3,52,4
s_add_u32 s37, s37, s74
LA 18
LS_ 110
M 3,53,5
s_xor_b32 s74, s74, 64512
LC_ 110
XW 120,0
M 4,64,4
WL 3
LO 109,17,109
HS 26,109
M 4,65,5
s_and_b32 s37, s37, 0x1ffff
RA 228,3,8192
HY 252,26,0
M 5,76,4
FL 19,94,121
FH 111,95,121
M 5,77,5
s_sub_u32 s52, s37, s56
LS_ 19
HY 252,26,1
M 0,18,0
s_lshl_b32 s53, s63, 7
LA 19
LS_ 111
M 0,19,1
s_add_u32 s50, s84, s53
LC_ 111
M 1,30,0
WL 3
LO 110,18,110
HS 27,110
M 1,31,1
s_addc_u32 s51, s85, 0
RA 232,1,10240
M 2,42,0
s_mov_b32 m0, s59
FL 16,96,120
FH 112,97,120
M 2,43,1
DR
LS_ 16
M 3,54,0
s_xor_b32 s59, s59, 1024
LA 16
LS_ 112
M 3,55,1
WV 14
LC_ 112
RV 116,20,0
M 4,66,0
WL 4
LO 111,19,111
HS 27,111
M 4,67,1
s_add_u32 m0, s38, 0
RA 236,3,10240
HY 10,27,0
M 5,78,0
FL 17,98,121
FH 113,99,121
M 5,79,1
ST 108,21,44
LS_ 17
M 0,20,2
LA 17
LS_ 113
M 0,21,3
WL 2
LC_ 113
v_cmp_eq_u32 vcc, 0, v116
M 1,32,2
LO 112,16,112
HS 29,112
)"
R"(M 1,33,3
RA 216,1,12288
HY 10,27,1
M 2,44,2
CB 1,28
FL 18,100,120
FH 114,101,120
M 2,45,3
DX 4,0
LS_ 18
M 3,56,2
s_add_u32 m0, s38, 1920
LA 18
LS_ 114
M 3,57,3
LC_ 114
XW 120,1
M 4,68,2
WL 3
LO 113,17,113
HS 29,113
M 4,69,3
RA 220,3,12288
HY 124,29,0
M 5,80,2
FL 19,102,121
FH 115,103,121
M 5,81,3
DX 4,128
LS_ 19
M 0,22,4
s_add_u32 m0, s38, 3840
LA 19
LS_ 115
M 0,23,5
LC_ 115
HY 124,29,1
M 1,34,4
WL 3
LO 114,18,114
HS 30,114
M 1,35,5
RA 224,1,14336
M 2,46,4
FL 16,56,116
FH 104,57,116
M 2,47,5
DX 4,256
LS_ 16
M 3,58,4
s_add_u32 m0, s38, 12288
LA 16
LS_ 104
M 3,59,5
LC_ 104
M 4,70,4
WL 3
LO 115,19,115
HS 30,115
M 4,71,5
RA 228,3,14336
HY 126,30,0
M 5,82,4
FL 17,58,117
FH 105,59,117
M 5,83,5
ST 112,21,46
LS_ 17
Z 6,12,0
LA 17
LS_ 105
M 6,13,1
LC_ 105
HY 126,30,1
Z 7,24,0
WL 3
LO 104,16,104
HZ 24,104
M 7,25,1
RA 232,1,16384
Z 8,36,0
FL 18,60,116
FH 106,61,116
M 8,37,1
DX 5,0
LS_ 18
Z 9,48,0
s_add_u32 m0, s38, 14208
LA 18
LS_ 106
M 9,49,1
LC_ 106
v_add_u32 v118, v118, v2
Z 10,60,0
WL 3
LO 105,17,105
HS 24,105
M 10,61,1
RA 236,3,16384
Z 11,72,0
FL 19,62,117
FH 107,63,117
M 11,73,1
DX 5,128
LS_ 19
M 6,14,2
s_add_u32 m0, s38, 16128
LA 19
LS_ 107
M 6,15,3
LC_ 107
M 7,26,2
WL 3
LO 106,18,106
HZ 25,106
M 7,27,3
RA 216,1,18432
M 8,38,2
FL 16,64,116
FH 108,65,116
M 8,39,3
DX 5,256
LS_ 16
M 9,50,2
s_add_u32 s38, s38, s76
LA 16
LS_ 108
M 9,51,3
s_xor_b32 s76, s76, 64512
LC_ 108
M 10,62,2
WL 3
LO 107,19,107
HS 25,107
M 10,63,3
s_and_b32 s38, s38, 0x1ffff
RA 220,3,18432
M 11,74,2
FL 17,66,117
FH 109,67,117
M 11,75,3
ST 104,118,42
LS_ 17
M 6,16,4
LA 17
LS_ 109
M 6,17,5
LC_ 109
M 7,28,4
WL 3
LO 108,16,108
HZ 26,108
M 7,29,5
s_add_u32 m0, s56, 0
RA 224,1,20480
M 8,40,4
FL 18,68,116
FH 110,69,116
M 8,41,5
global_load_lds_dwordx4 v34, %[xp] offset:0
LS_ 18
M 9,52,4
s_add_u32 m0, s56, 1024
LA 18
LS_ 110
M 9,53,5
LC_ 110
XW 116,0
M 10,64,4
WL 3
LO 109,17,109
HS 26,109
M 10,65,5
RA 228,3,20480
M 11,76,4
FL 19,70,117
FH 111,71,117
M 11,77,5
global_load_lds_dwordx4 v35, %[xp] offset:0
LS_ 19
M 6,18,0
s_add_u32 m0, s56, 1920
LA 19
LS_ 111
M 6,19,1
LC_ 111
M 7,30,0
WL 3
LO 110,18,110
HZ 27,110
M 7,31,1
RA 232,1,22528
M 8,42,0
FL 16,72,116
FH 112,73,116
M 8,43,1
RV 120,20,64
LS_ 16
M 9,54,0
LA 16
LS_ 112
M 9,55,1
LC_ 112
v_add_u32 v1, s52, v14
M 10,66,0
WL 4
LO 111,19,111
HS 27,111
M 10,67,1
RA 236,3,22528
M 11,78,0
FL 17,74,117
FH 113,75,117
M 11,79,1
ST 108,21,44
LS_ 17
M 6,20,2
LA 17
LS_ 113
M 6,21,3
WL 4
LC_ 113
v_cmp_eq_u32 vcc, 0, v120
M 7,32,2
WL 3
LO 112,16,112
HZ 29,112
M 7,33,3
RA 216,1,0
v_xor_b32 v3, 64, v1
M 8,44,2
CB 2,29
FL 18,76,116
FH 114,77,116
M 8,45,3
global_load_lds_dwordx4 v34, %[xp] offset:128
LS_ 18
M 9,56,2
s_add_u32 m0, s56, 2944
LA 18
LS_ 114
M 9,57,3
LC_ 114
XW 116,1
M 10,68,2
WL 3
LO 113,17,113
HS 29,113
M 10,69,3
RA 220,3,0
M 11,80,2
FL 19,78,117
FH 115,79,117
M 11,81,3
global_load_lds_dwordx4 v35, %[xp] offset:128
LS_ 19
M 6,22,4
LA 19
LS_ 115
M 6,23,5
LC_ 115
M 7,34,4
WL 3
LO 114,18,114
HZ 30,114
M 7,35,5
RA 224,1,2048
M 8,46,4
WV 20
FL 16,80,120
FH 104,81,120
M 8,47,5
s_barrier
LS_ 16
M 9,58,4
LA 16
LS_ 104
M 9,59,5
LC_ 104
M 10,70,4
WL 3
LO 115,19,115
HS 30,115
M 10,71,5
RA 228,3,2048
M 11,82,4
FL 17,82,121
FH 105,83,121
M 11,83,5
ST 112,21,46
LS_ 17
Z 0,12,0
LA 17
LS_ 105
M 0,13,1
LC_ 105
Z 1,24,0
WL 3
LO 104,16,104
HS 24,104
M 1,25,1
WV 16
RA 232,1,4096
v_min_u32 v9, v22, v23
Z 2,36,0
FL 18,84,120
FH 106,85,120
M 2,37,1
LS_ 18
v_cmp_eq_u32 vcc, 0, v9
Z 3,48,0
LA 18
LS_ 106
M 3,49,1
LC_ 106
v_add_u32 v122, v122, v2
Z 4,60,0
WL 3
LO 105,17,105
HS 24,105
M 4,61,1
CB 0,30
RA 236,3,4096
v_add_u32 v4, v22, v6
Z 5,72,0
FL 19,86,121
FH 107,87,121
M 5,73,1
LS_ 19
v_add_u32 v5, v23, v6
M 0,14,2
s_mov_b32 s64, s63
LA 19
LS_ 107
M 0,15,3
s_mov_b32 s63, s62
LC_ 107
M 1,26,2
WL 3
LO 106,18,106
HS 25,106
M 1,27,3
s_mov_b32 s62, s61
RA 216,1,6144
M 2,38,2
s_mov_b32 s61, s60
FL 16,88,120
FH 108,89,120
M 2,39,3
s_mov_b32 s60, s57
LS_ 16
M 3,50,2
s_lshl_b32 s53, s60, 5
LA 16
LS_ 108
M 3,51,3
s_add_u32 s48, s82, s53
LC_ 108
M 4,62,2
WL 3
LO 107,19,107
HS 25,107
M 4,63,3
s_addc_u32 s49, s83, 0
RA 220,3,6144
v_xor_b32 v20, 0x400, v20
M 5,74,2
FL 17,90,121
FH 109,91,121
M 5,75,3
s_add_u32 s57, s57, 1
ST 104,122,42
LS_ 17
M 0,16,4
.Lk1p9:
s_cbranch_execz .Lk1r0
LA 17
LS_ 109
M 0,17,5
LC_ 109
M 1,28,4
WL 3
LO 108,16,108
HS 26,108
M 1,29,5
RA 224,1,8192
M 2,40,4
FL 18,92,120
FH 110,93,120
M 2,41,5
EL_
LS_ 18
M 3,52,4
s_add_u32 s37, s37, s74
LA 18
LS_ 110
M 3,53,5
s_xor_b32 s74, s74, 64512
LC_ 110
XW 120,0
)"
R"(M 4,64,4
WL 3
LO 109,17,109
HS 26,109
M 4,65,5
s_and_b32 s37, s37, 0x1ffff
RA 228,3,8192
M 5,76,4
FL 19,94,121
FH 111,95,121
M 5,77,5
s_sub_u32 s52, s37, s56
LS_ 19
M 0,18,0
s_lshl_b32 s53, s63, 7
LA 19
LS_ 111
M 0,19,1
s_add_u32 s50, s84, s53
LC_ 111
M 1,30,0
WL 3
LO 110,18,110
HS 27,110
M 1,31,1
s_addc_u32 s51, s85, 0
RA 232,1,10240
M 2,42,0
s_mov_b32 m0, s59
FL 16,96,120
FH 112,97,120
M 2,43,1
DR
LS_ 16
M 3,54,0
s_xor_b32 s59, s59, 1024
LA 16
LS_ 112
M 3,55,1
WV 18
LC_ 112
RV 116,20,0
M 4,66,0
WL 4
LO 111,19,111
HS 27,111
M 4,67,1
s_add_u32 m0, s38, 0
RA 236,3,10240
M 5,78,0
FL 17,98,121
FH 113,99,121
M 5,79,1
ST 108,21,44
LS_ 17
M 0,20,2
LA 17
LS_ 113
M 0,21,3
WL 2
LC_ 113
v_cmp_eq_u32 vcc, 0, v116
M 1,32,2
LO 112,16,112
HS 29,112
M 1,33,3
RA 216,1,12288
M 2,44,2
CB 1,31
FL 18,100,120
FH 114,101,120
M 2,45,3
DX 4,0
LS_ 18
M 3,56,2
s_add_u32 m0, s38, 1920
LA 18
LS_ 114
M 3,57,3
LC_ 114
XW 120,1
M 4,68,2
WL 3
LO 113,17,113
HS 29,113
M 4,69,3
RA 220,3,12288
M 5,80,2
FL 19,102,121
FH 115,103,121
M 5,81,3
DX 4,128
LS_ 19
M 0,22,4
s_add_u32 m0, s38, 3840
LA 19
LS_ 115
M 0,23,5
LC_ 115
M 1,34,4
WL 3
LO 114,18,114
HS 30,114
M 1,35,5
RA 224,1,14336
M 2,46,4
FL 16,56,116
FH 104,57,116
M 2,47,5
DX 4,256
LS_ 16
M 3,58,4
s_add_u32 m0, s38, 12288
LA 16
LS_ 104
M 3,59,5
LC_ 104
RA 240,7,0
M 4,70,4
WL 4
LO 115,19,115
HS 30,115
M 4,71,5
RA 228,3,14336
RA 244,7,256
M 5,82,4
FL 17,58,117
FH 105,59,117
M 5,83,5
ST 112,21,46
LS_ 17
Z 6,12,0
LA 17
LS_ 105
M 6,13,1
LC_ 105
RA 248,8,0
Z 7,24,0
WL 6
LO 104,16,104
HS 24,104
M 7,25,1
RA 232,1,16384
RA 252,8,64
Z 8,36,0
FL 18,60,116
FH 106,61,116
M 8,37,1
DX 5,0
LS_ 18
Z 9,48,0
s_add_u32 m0, s38, 14208
LA 18
LS_ 106
M 9,49,1
LC_ 106
v_add_u32 v118, v118, v2
Z 10,60,0
WL 5
LO 105,17,105
HS 24,105
M 10,61,1
WV 13
RA 236,3,16384
RV 128,14,0
Z 11,72,0
FL 19,62,117
FH 107,63,117
M 11,73,1
DX 5,128
LS_ 19
M 6,14,2
s_add_u32 m0, s38, 16128
LA 19
LS_ 107
M 6,15,3
LC_ 107
RV 132,15,0
M 7,26,2
WL 5
LO 106,18,106
HS 25,106
M 7,27,3
RA 216,1,18432
M 8,38,2
FL 16,64,116
FH 108,65,116
M 8,39,3
DX 5,256
LS_ 16
M 9,50,2
s_add_u32 s38, s38, s76
LA 16
LS_ 108
M 9,51,3
s_xor_b32 s76, s76, 64512
LC_ 108
M 10,62,2
WL 4
LO 107,19,107
HS 25,107
M 10,63,3
s_and_b32 s38, s38, 0x1ffff
RA 220,3,18432
M 11,74,2
FL 17,66,117
FH 109,67,117
M 11,75,3
ST 104,118,42
LS_ 17
M 6,16,4
LA 17
LS_ 109
M 6,17,5
LC_ 109
M 7,28,4
WL 3
LO 108,16,108
HS 26,108
M 7,29,5
s_add_u32 m0, s56, 3840
RA 224,1,20480
Y3 242,128,240,130,246,132
global_load_lds_dwordx4 v34, %[xp] offset:256
RA 240,7,512
M 8,40,4
s_add_u32 m0, s56, 4864
FL 18,68,116
FH 110,69,116
M 8,41,5
LS_ 18
M 9,52,4
LA 18
LS_ 110
M 9,53,5
LC_ 110
XW 116,0
M 10,64,4
WL 4
LO 109,17,109
HS 26,109
M 10,65,5
RA 228,3,20480
S0 54,40
M 11,76,4
FL 19,70,117
FH 111,71,117
M 11,77,5
global_load_lds_dwordx4 v35, %[xp] offset:256
LS_ 19
M 6,18,0
s_add_u32 m0, s56, -384
LA 19
LS_ 111
M 6,19,1
LC_ 111
S0 55,41
M 7,30,0
WL 3
LO 110,18,110
HS 27,110
M 7,31,1
RA 232,1,22528
S 54,42
LZA 248,0
RA 248,8,128
S 55,43
M 8,42,0
FL 16,72,116
FH 112,73,116
M 8,43,1
RV 120,20,64
LS_ 16
M 9,54,0
LA 16
LS_ 112
M 9,55,1
LC_ 112
v_add_u32 v1, s52, v14
M 10,66,0
WL 5
LO 111,19,111
HS 27,111
M 10,67,1
RA 236,3,22528
S 54,44
M 11,78,0
FL 17,74,117
FH 113,75,117
M 11,79,1
ST 108,21,44
LS_ 17
M 6,20,2
LA 17
LS_ 113
M 6,21,3
WL 4
LC_ 113
v_cmp_eq_u32 vcc, 0, v120
M 7,32,2
WL 3
LO 112,16,112
HS 29,112
M 7,33,3
RA 216,1,0
v_xor_b32 v3, 64, v1
M 8,44,2
CB 2,32
FL 18,76,116
FH 114,77,116
M 8,45,3
global_load_lds_dwordx4 v34, %[xp] offset:384
LS_ 18
M 9,56,2
s_add_u32 m0, s56, 640
LA 18
LS_ 114
M 9,57,3
LC_ 114
XW 116,1
M 10,68,2
WL 3
LO 113,17,113
HS 29,113
M 10,69,3
RA 220,3,0
S 55,45
M 11,80,2
FL 19,78,117
FH 115,79,117
M 11,81,3
global_load_lds_dwordx4 v35, %[xp] offset:384
LS_ 19
M 6,22,4
LA 19
LS_ 115
M 6,23,5
LC_ 115
S 54,46
M 7,34,4
WL 3
LO 114,18,114
HS 30,114
M 7,35,5
RA 224,1,2048
S 55,47
M 8,46,4
FL 16,80,120
FH 104,81,120
M 8,47,5
s_barrier
LS_ 16
S 54,48
M 9,58,4
LA 16
LS_ 104
M 9,59,5
LC_ 104
S 55,49
M 10,70,4
WL 3
LO 115,19,115
HS 30,115
M 10,71,5
RA 228,3,2048
S 54,50
M 11,82,4
FL 17,82,121
FH 105,83,121
M 11,83,5
ST 112,21,46
LS_ 17
Z 0,12,0
LA 17
LS_ 105
M 0,13,1
LC_ 105
S 55,51
Z 1,24,0
WL 3
LO 104,16,104
HS 24,104
M 1,25,1
WV 16
RA 232,1,4096
v_min_u32 v9, v22, v23
Z 2,36,0
FL 18,84,120
FH 106,85,120
M 2,37,1
LS_ 18
v_cmp_eq_u32 vcc, 0, v9
Z 3,48,0
LA 18
LS_ 106
M 3,49,1
LC_ 106
v_add_u32 v122, v122, v2
Z 4,60,0
WL 3
)"
R"(LO 105,17,105
HS 24,105
M 4,61,1
CB 0,33
RA 236,3,4096
v_add_u32 v4, v22, v6
Z 5,72,0
FL 19,86,121
FH 107,87,121
M 5,73,1
LS_ 19
v_add_u32 v5, v23, v6
M 0,14,2
s_mov_b32 s64, s63
LA 19
LS_ 107
M 0,15,3
s_mov_b32 s63, s62
LC_ 107
HY 248,24,0
M 1,26,2
WL 3
LO 106,18,106
HS 25,106
M 1,27,3
s_mov_b32 s62, s61
RA 216,1,6144
HY 248,24,1
M 2,38,2
s_mov_b32 s61, s60
FL 16,88,120
FH 108,89,120
M 2,39,3
s_mov_b32 s60, s57
LS_ 16
M 3,50,2
s_lshl_b32 s53, s60, 5
LA 16
LS_ 108
M 3,51,3
s_add_u32 s48, s82, s53
LC_ 108
M 4,62,2
WL 3
LO 107,19,107
HS 25,107
M 4,63,3
s_addc_u32 s49, s83, 0
RA 220,3,6144
v_xor_b32 v20, 0x400, v20
M 5,74,2
FL 17,90,121
FH 109,91,121
M 5,75,3
s_add_u32 s57, s57, 1
ST 104,122,42
LS_ 17
M 0,16,4
LA 17
LS_ 109
M 0,17,5
LC_ 109
HY 250,25,0
M 1,28,4
WL 3
LO 108,16,108
HS 26,108
M 1,29,5
RA 224,1,8192
HY 250,25,1
M 2,40,4
FL 18,92,120
FH 110,93,120
M 2,41,5
EL_
LS_ 18
M 3,52,4
s_add_u32 s37, s37, s74
LA 18
LS_ 110
M 3,53,5
s_xor_b32 s74, s74, 64512
LC_ 110
XW 120,0
M 4,64,4
WL 3
LO 109,17,109
HS 26,109
M 4,65,5
s_and_b32 s37, s37, 0x1ffff
RA 228,3,8192
HY 252,26,0
M 5,76,4
FL 19,94,121
FH 111,95,121
M 5,77,5
s_sub_u32 s52, s37, s56
LS_ 19
HY 252,26,1
M 0,18,0
s_lshl_b32 s53, s63, 7
LA 19
LS_ 111
M 0,19,1
s_add_u32 s50, s84, s53
LC_ 111
M 1,30,0
WL 3
LO 110,18,110
HS 27,110
M 1,31,1
s_addc_u32 s51, s85, 0
RA 232,1,10240
M 2,42,0
s_mov_b32 m0, s59
FL 16,96,120
FH 112,97,120
M 2,43,1
DR
LS_ 16
M 3,54,0
s_xor_b32 s59, s59, 1024
LA 16
LS_ 112
M 3,55,1
WV 18
LC_ 112
RV 116,20,0
M 4,66,0
WL 4
LO 111,19,111
.Lk1p10:
s_cbranch_execz .Lk1r0
HS 27,111
M 4,67,1
s_add_u32 m0, s38, 0
RA 236,3,10240
HY 10,27,0
M 5,78,0
FL 17,98,121
FH 113,99,121
M 5,79,1
ST 108,21,44
LS_ 17
M 0,20,2
LA 17
LS_ 113
M 0,21,3
WL 2
LC_ 113
v_cmp_eq_u32 vcc, 0, v116
M 1,32,2
LO 112,16,112
HS 29,112
M 1,33,3
RA 216,1,12288
HY 10,27,1
M 2,44,2
CB 1,34
FL 18,100,120
FH 114,101,120
M 2,45,3
DX 4,0
LS_ 18
M 3,56,2
s_add_u32 m0, s38, 1920
LA 18
LS_ 114
M 3,57,3
LC_ 114
XW 120,1
M 4,68,2
WL 3
LO 113,17,113
HS 29,113
M 4,69,3
RA 220,3,12288
HY 124,29,0
M 5,80,2
FL 19,102,121
FH 115,103,121
M 5,81,3
DX 4,128
LS_ 19
M 0,22,4
s_add_u32 m0, s38, 3840
LA 19
LS_ 115
M 0,23,5
LC_ 115
RV 136,14,2048
M 1,34,4
WL 4
LO 114,18,114
HS 30,114
M 1,35,5
RA 224,1,14336
RV 140,15,2048
M 2,46,4
FL 16,56,116
FH 104,57,116
M 2,47,5
DX 4,256
LS_ 16
M 3,58,4
s_add_u32 m0, s38, 12288
LA 16
LS_ 104
M 3,59,5
WV 11
LC_ 104
RV 144,14,4096
M 4,70,4
WL 6
LO 115,19,115
HS 30,115
M 4,71,5
RA 228,3,14336
RV 148,15,4096
M 5,82,4
FL 17,58,117
FH 105,59,117
M 5,83,5
ST 112,21,46
LS_ 17
Z 6,12,0
LA 17
LS_ 105
M 6,13,1
LC_ 105
HY 124,29,1
Z 7,24,0
WL 5
LO 104,16,104
HZ 24,104
M 7,25,1
RA 232,1,16384
HY 126,30,0
Y3 244,134,242,136,240,138
DX 5,0
HY 126,30,1
Z 8,36,0
s_add_u32 m0, s38, 14208
FL 18,60,116
FH 106,61,116
M 8,37,1
RA 244,7,768
LS_ 18
Z 9,48,0
LA 18
LS_ 106
M 9,49,1
LC_ 106
v_add_u32 v118, v118, v2
Z 10,60,0
WL 4
LO 105,17,105
HS 24,105
M 10,61,1
RA 236,3,16384
RA 240,7,1024
Z 11,72,0
FL 19,62,117
FH 107,63,117
M 11,73,1
DX 5,128
LS_ 19
M 6,14,2
s_add_u32 m0, s38, 16128
LA 19
LS_ 107
M 6,15,3
LC_ 107
S 54,40
M 7,26,2
WL 4
LO 106,18,106
HZ 25,106
M 7,27,3
RA 216,1,18432
S 55,41
LSA 252,1
RA 252,8,192
S 54,42
M 8,38,2
FL 16,64,116
FH 108,65,116
M 8,39,3
DX 5,256
LS_ 16
M 9,50,2
s_add_u32 s38, s38, s76
LA 16
LS_ 108
M 9,51,3
s_xor_b32 s76, s76, 64512
LC_ 108
S 55,43
M 10,62,2
WL 4
LO 107,19,107
HS 25,107
M 10,63,3
s_and_b32 s38, s38, 0x1ffff
RA 220,3,18432
S 54,44
M 11,74,2
FL 17,66,117
FH 109,67,117
M 11,75,3
SU 104,118,42
LS_ 17
M 6,16,4
LA 17
LS_ 109
M 6,17,5
LC_ 109
S 55,45
M 7,28,4
WL 3
LO 108,16,108
HZ 26,108
M 7,29,5
s_add_u32 m0, s56, 1536
RA 224,1,20480
S 54,46
LSA 248,2
RA 248,8,256
S 55,47
M 8,40,4
FL 18,68,116
FH 110,69,116
M 8,41,5
global_load_lds_dwordx4 v34, %[xp] offset:512
LS_ 18
M 9,52,4
s_add_u32 m0, s56, 2560
LA 18
LS_ 110
M 9,53,5
LC_ 110
XW 116,0
M 10,64,4
WL 4
LO 109,17,109
HS 26,109
M 10,65,5
RA 228,3,20480
S 54,48
M 11,76,4
FL 19,70,117
FH 111,71,117
M 11,77,5
global_load_lds_dwordx4 v35, %[xp] offset:512
LS_ 19
M 6,18,0
s_add_u32 m0, s56, 3456
LA 19
LS_ 111
M 6,19,1
LC_ 111
S 55,49
M 7,30,0
WL 3
LO 110,18,110
HZ 27,110
M 7,31,1
RA 232,1,22528
S 54,50
M 8,42,0
FL 16,72,116
FH 112,73,116
M 8,43,1
RV 120,20,64
LS_ 16
M 9,54,0
LA 16
LS_ 112
M 9,55,1
LC_ 112
v_add_u32 v1, s52, v14
M 10,66,0
WL 4
LO 111,19,111
HS 27,111
M 10,67,1
RA 236,3,22528
S 55,51
M 11,78,0
FL 17,74,117
FH 113,75,117
M 11,79,1
SU 108,21,44
LS_ 17
M 6,20,2
LA 17
LS_ 113
M 6,21,3
WL 4
LC_ 113
)"
R"(v_cmp_eq_u32 vcc, 0, v120
M 7,32,2
WL 3
LO 112,16,112
HZ 29,112
M 7,33,3
RA 216,1,0
v_xor_b32 v3, 64, v1
M 8,44,2
CB 2,35
FL 18,76,116
FH 114,77,116
M 8,45,3
global_load_lds_dwordx4 v34, %[xp] offset:640
LS_ 18
M 9,56,2
s_add_u32 m0, s56, 4480
LA 18
LS_ 114
M 9,57,3
LC_ 114
XW 116,1
M 10,68,2
WL 3
LO 113,17,113
HS 29,113
M 10,69,3
RA 220,3,0
M 11,80,2
FL 19,78,117
FH 115,79,117
M 11,81,3
global_load_lds_dwordx4 v35, %[xp] offset:640
LS_ 19
M 6,22,4
LA 19
LS_ 115
M 6,23,5
LC_ 115
M 7,34,4
WL 3
LO 114,18,114
HZ 30,114
M 7,35,5
RA 224,1,2048
Y3 246,140,244,142,242,144
RA 244,7,1280
M 8,46,4
FL 16,80,120
FH 104,81,120
M 8,47,5
s_barrier
LS_ 16
M 9,58,4
LA 16
LS_ 104
M 9,59,5
LC_ 104
S 54,40
M 10,70,4
WL 4
LO 115,19,115
HS 30,115
M 10,71,5
RA 228,3,2048
S 55,41
M 11,82,4
FL 17,82,121
FH 105,83,121
M 11,83,5
s_branch 1b
7:
SE
s_mov_b64 exec, 0
s_cmp_eq_u32 %[w], 0
s_cbranch_scc1 .Lk1p13
s_cmp_eq_u32 %[w], 1
s_cbranch_scc1 .Lk1r1
s_cmp_eq_u32 %[w], 2
s_cbranch_scc1 .Lk1r1
s_branch .Lk1r1
.Lk1r1:
s_mov_b64 exec, -1
ST 112,21,46
LS_ 17
Z 0,12,0
LA 17
LS_ 105
M 0,13,1
LC_ 105
Z 1,24,0
WL 3
LO 104,16,104
HS 24,104
M 1,25,1
WV 12
RA 232,1,4096
v_min_u32 v9, v22, v23
Z 2,36,0
FL 18,84,120
FH 106,85,120
M 2,37,1
AT_
LS_ 18
Z 3,48,0
LA 18
LS_ 106
M 3,49,1
LC_ 106
v_add_u32 v122, v122, v2
Z 4,60,0
WL 3
LO 105,17,105
HS 24,105
M 4,61,1
RA 236,3,4096
v_cmp_eq_u32 vcc, 0, v9
Z 5,72,0
FL 19,86,121
FH 107,87,121
M 5,73,1
LS_ 19
HY 248,24,0
M 0,14,2
CB 0,36
LA 19
LS_ 107
M 0,15,3
LC_ 107
v_add_u32 v4, v22, v6
M 1,26,2
WL 3
LO 106,18,106
HS 25,106
M 1,27,3
RA 216,1,6144
v_add_u32 v5, v23, v6
M 2,38,2
s_mov_b32 s64, s63
FL 16,88,120
FH 108,89,120
M 2,39,3
s_mov_b32 s63, s62
LS_ 16
HY 248,24,1
M 3,50,2
s_mov_b32 s62, s61
LA 16
LS_ 108
M 3,51,3
s_mov_b32 s61, s60
LC_ 108
v_cvt_f64_f32 v[248:249], v248
M 4,62,2
WL 3
LO 107,19,107
HS 25,107
M 4,63,3
s_mov_b32 s60, s57
RA 220,3,6144
v_xor_b32 v20, 0x400, v20
M 5,74,2
FL 17,90,121
FH 109,91,121
M 5,75,3
s_lshl_b32 s53, s60, 5
ST 104,122,42
LS_ 17
M 0,16,4
s_add_u32 s48, s82, s53
LA 17
LS_ 109
M 0,17,5
s_addc_u32 s49, s83, 0
LC_ 109
HY 250,25,0
M 1,28,4
WL 3
LO 108,16,108
HS 26,108
M 1,29,5
s_add_u32 s57, s57, 1
RA 224,1,8192
HY 250,25,1
M 2,40,4
FL 18,92,120
FH 110,93,120
M 2,41,5
EL_
LS_ 18
M 3,52,4
s_add_u32 s37, s37, s74
LA 18
LS_ 110
M 3,53,5
s_xor_b32 s74, s74, 64512
LC_ 110
XW 120,0
M 4,64,4
WL 3
LO 109,17,109
HS 26,109
M 4,65,5
s_and_b32 s37, s37, 0x1ffff
RA 228,3,8192
v_cvt_f64_f32 v[250:251], v250
M 5,76,4
FL 19,94,121
FH 111,95,121
M 5,77,5
s_sub_u32 s52, s37, s56
LS_ 19
HY 252,26,0
M 0,18,0
s_lshl_b32 s53, s63, 7
LA 19
LS_ 111
M 0,19,1
s_add_u32 s50, s84, s53
LC_ 111
HY 252,26,1
M 1,30,0
WL 3
LO 110,18,110
HS 27,110
M 1,31,1
s_addc_u32 s51, s85, 0
RA 232,1,10240
v_cvt_f64_f32 v[252:253], v252
M 2,42,0
s_mov_b32 m0, s59
FL 16,96,120
FH 112,97,120
M 2,43,1
DR
LS_ 16
M 3,54,0
s_xor_b32 s59, s59, 1024
LA 16
LS_ 112
M 3,55,1
WV 15
LC_ 112
RV 116,20,0
M 4,66,0
WL 4
LO 111,19,111
HS 27,111
M 4,67,1
s_add_u32 m0, s38, 0
RA 236,3,10240
HY 10,27,0
M 5,78,0
FL 17,98,121
FH 113,99,121
M 5,79,1
ST 108,21,44
LS_ 17
M 0,20,2
LA 17
LS_ 113
M 0,21,3
WL 2
LC_ 113
v_cmp_eq_u32 vcc, 0, v116
M 1,32,2
.Lk1p11:
s_cbranch_execz .Lk1r0
LO 112,16,112
HS 29,112
M 1,33,3
RA 216,1,12288
HY 10,27,1
M 2,44,2
CB 1,37
FL 18,100,120
FH 114,101,120
M 2,45,3
DX 4,0
LS_ 18
M 3,56,2
s_add_u32 m0, s38, 1920
LA 18
LS_ 114
M 3,57,3
LC_ 114
XW 120,1
M 4,68,2
WL 3
LO 113,17,113
HS 29,113
M 4,69,3
RA 220,3,12288
v_cvt_f64_f32 v[10:11], v10
M 5,80,2
FL 19,102,121
FH 115,103,121
M 5,81,3
DX 4,128
LS_ 19
M 0,22,4
s_add_u32 m0, s38, 3840
LA 19
LS_ 115
M 0,23,5
LC_ 115
HY 124,29,0
M 1,34,4
WL 3
LO 114,18,114
HS 30,114
M 1,35,5
RA 224,1,14336
HY 124,29,1
M 2,46,4
FL 16,56,116
FH 104,57,116
M 2,47,5
DX 4,256
LS_ 16
M 3,58,4
s_add_u32 m0, s38, 12288
LA 16
LS_ 104
M 3,59,5
LC_ 104
v_cvt_f64_f32 v[124:125], v124
M 4,70,4
WL 3
LO 115,19,115
HS 30,115
M 4,71,5
RA 228,3,14336
HY 126,30,0
M 5,82,4
FL 17,58,117
FH 105,59,117
M 5,83,5
ST 112,21,46
LS_ 17
Z 6,12,0
LA 17
LS_ 105
M 6,13,1
LC_ 105
HY 126,30,1
Z 7,24,0
WL 3
LO 104,16,104
HZ 24,104
M 7,25,1
RA 232,1,16384
v_cvt_f64_f32 v[126:127], v126
Z 8,36,0
FL 18,60,116
FH 106,61,116
M 8,37,1
DX 5,0
LS_ 18
Z 9,48,0
s_add_u32 m0, s38, 14208
LA 18
LS_ 106
M 9,49,1
LC_ 106
v_add_u32 v118, v118, v2
Z 10,60,0
WL 3
LO 105,17,105
HS 24,105
M 10,61,1
RA 236,3,16384
Z 11,72,0
FL 19,62,117
FH 107,63,117
M 11,73,1
DX 5,128
LS_ 19
M 6,14,2
s_add_u32 m0, s38, 16128
LA 19
LS_ 107
M 6,15,3
LC_ 107
M 7,26,2
WL 3
LO 106,18,106
HZ 25,106
M 7,27,3
RA 216,1,18432
M 8,38,2
FL 16,64,116
FH 108,65,116
M 8,39,3
DX 5,256
LS_ 16
M 9,50,2
s_add_u32 s38, s38, s76
LA 16
LS_ 108
)"
R"(M 9,51,3
s_xor_b32 s76, s76, 64512
LC_ 108
M 10,62,2
WL 3
LO 107,19,107
HS 25,107
M 10,63,3
s_and_b32 s38, s38, 0x1ffff
RA 220,3,18432
M 11,74,2
FL 17,66,117
FH 109,67,117
M 11,75,3
SU 104,118,42
LS_ 17
M 6,16,4
LA 17
LS_ 109
M 6,17,5
LC_ 109
M 7,28,4
WL 3
LO 108,16,108
HZ 26,108
M 7,29,5
RA 224,1,20480
M 8,40,4
FL 18,68,116
FH 110,69,116
M 8,41,5
LS_ 18
M 9,52,4
LA 18
LS_ 110
M 9,53,5
LC_ 110
XW 116,0
M 10,64,4
WL 3
LO 109,17,109
HS 26,109
M 10,65,5
RA 228,3,20480
M 11,76,4
FL 19,70,117
FH 111,71,117
M 11,77,5
LS_ 19
M 6,18,0
LA 19
LS_ 111
M 6,19,1
LC_ 111
M 7,30,0
WL 3
LO 110,18,110
HZ 27,110
M 7,31,1
RA 232,1,22528
M 8,42,0
FL 16,72,116
FH 112,73,116
M 8,43,1
RV 120,20,64
LS_ 16
M 9,54,0
LA 16
LS_ 112
M 9,55,1
LC_ 112
v_add_u32 v1, s52, v14
M 10,66,0
WL 4
LO 111,19,111
HS 27,111
M 10,67,1
RA 236,3,22528
M 11,78,0
FL 17,74,117
FH 113,75,117
M 11,79,1
SU 108,21,44
LS_ 17
M 6,20,2
LA 17
LS_ 113
M 6,21,3
WV 13
LC_ 113
CW
M 7,32,2
WL 4
LO 112,16,112
HZ 29,112
M 7,33,3
RA 216,1,0
v_cmp_eq_u32 vcc, 0, v120
M 8,44,2
FL 18,76,116
FH 114,77,116
M 8,45,3
LS_ 18
v_xor_b32 v3, 64, v1
M 9,56,2
CB 2,38
LA 18
LS_ 114
M 9,57,3
LC_ 114
XW 116,1
M 10,68,2
WL 4
LO 113,17,113
HS 29,113
M 10,69,3
RA 220,3,0
M 11,80,2
FL 19,78,117
FH 115,79,117
M 11,81,3
LS_ 19
M 6,22,4
LA 19
LS_ 115
M 6,23,5
LC_ 115
M 7,34,4
WL 3
LO 114,18,114
HZ 30,114
M 7,35,5
RA 224,1,2048
M 8,46,4
FL 16,80,120
FH 104,81,120
M 8,47,5
s_barrier
ds_read_b32 v40, v41
LS_ 16
M 9,58,4
LA 16
LS_ 104
M 9,59,5
LC_ 104
v_xor_b32 v41, 64, v41
M 10,70,4
WL 4
LO 115,19,115
HS 30,115
M 10,71,5
RA 228,3,2048
M 11,82,4
FL 17,82,121
FH 105,83,121
M 11,83,5
E2
SU 112,21,46
LS_ 17
Z 0,12,0
LA 17
LS_ 105
M 0,13,1
WL 4
LC_ 105
v_readlane_b32 s72, v40, 1
Z 1,24,0
WL 3
LO 104,16,104
HS 24,104
M 1,25,1
WV 12
RA 232,1,4096
v_min_u32 v9, v22, v23
Z 2,36,0
FL 18,84,120
FH 106,85,120
M 2,37,1
AT_
LS_ 18
Z 3,48,0
LA 18
LS_ 106
M 3,49,1
LC_ 106
v_add_u32 v122, v122, v2
Z 4,60,0
WL 3
LO 105,17,105
HS 24,105
M 4,61,1
RA 236,3,4096
v_cmp_eq_u32 vcc, 0, v9
Z 5,72,0
FL 19,86,121
FH 107,87,121
M 5,73,1
LS_ 19
HW 254,24
M 0,14,2
CB 0,39
LA 19
LS_ 107
M 0,15,3
LC_ 107
v_add_u32 v4, v22, v6
M 1,26,2
WL 3
LO 106,18,106
HS 25,106
M 1,27,3
RA 216,1,6144
v_add_u32 v5, v23, v6
M 2,38,2
s_mov_b32 s64, s63
FL 16,88,120
FH 108,89,120
M 2,39,3
s_mov_b32 s63, s62
LS_ 16
v_cvt_f64_f32 v[254:255], v254
M 3,50,2
s_mov_b32 s62, s61
LA 16
LS_ 108
M 3,51,3
s_mov_b32 s61, s60
LC_ 108
v_add_f64 v[248:249], v[248:249], v[254:255]
M 4,62,2
WL 3
LO 107,19,107
HS 25,107
M 4,63,3
s_add_u32 %[nb], %[nb], 1
RA 220,3,6144
v_xor_b32 v20, 0x400, v20
M 5,74,2
FL 17,90,121
FH 109,91,121
M 5,75,3
s_cmp_ge_u32 s72, 96
SU 104,122,42
LS_ 17
M 0,16,4
s_addc_u32 s39, s39, 0
LA 17
LS_ 109
M 0,17,5
s_min_u32 s72, s72, 95
LC_ 109
HW 254,25
M 1,28,4
WL 3
LO 108,16,108
HS 26,108
M 1,29,5
s_bfe_u32 s53, s72, 0x20003
RA 224,1,8192
v_cvt_f64_f32 v[254:255], v254
M 2,40,4
s_bitcmp1_b32 s90, s53
FL 18,92,120
FH 110,93,120
M 2,41,5
s_cselect_b32 s73, 2304, 0
LS_ 18
v_add_f64 v[250:251], v[250:251], v[254:255]
M 3,52,4
s_and_b32 s53, s72, 31
LA 18
LS_ 110
M 3,53,5
s_mul_i32 s53, s53, 72
LC_ 110
XW 120,0
M 4,64,4
WL 3
LO 109,17,109
HS 26,109
M 4,65,5
s_lshr_b32 s72, s72, 5
RA 228,3,8192
HW 254,26
M 5,76,4
FL 19,94,121
FH 111,95,121
M 5,77,5
s_add_u32 s53, s53, s72
LS_ 19
v_cvt_f64_f32 v[254:255], v254
M 0,18,0
s_add_u32 s53, s53, s73
LA 19
LS_ 111
M 0,19,1
s_add_u32 s57, s53, 69
LC_ 111
v_add_f64 v[252:253], v[252:253], v[254:255]
M 1,30,0
WL 3
LO 110,18,110
HS 27,110
M 1,31,1
s_cmp_eq_u32 s39, 0
RA 232,1,10240
M 2,42,0
s_cselect_b32 s57, s57, s61
FL 16,96,120
FH 112,97,120
M 2,43,1
s_mov_b32 s60, s57
LS_ 16
M 3,54,0
s_lshl_b32 s53, s60, 5
LA 16
LS_ 112
M 3,55,1
s_add_u32 s48, s82, s53
LC_ 112
WV 13
RV 116,20,0
M 4,66,0
WL 4
LO 111,19,111
HS 27,111
M 4,67,1
s_addc_u32 s49, s83, 0
RA 236,3,10240
HW 254,27
M 5,78,0
FL 17,98,121
FH 113,99,121
M 5,79,1
s_add_u32 s57, s57, 1
SU 108,21,44
LS_ 17
M 0,20,2
LA 17
LS_ 113
M 0,21,3
WL 2
LC_ 113
v_cmp_eq_u32 vcc, 0, v116
M 1,32,2
LO 112,16,112
HS 29,112
M 1,33,3
RA 216,1,12288
v_cvt_f64_f32 v[254:255], v254
M 2,44,2
CB 1,40
FL 18,100,120
FH 114,101,120
M 2,45,3
EL_
LS_ 18
M 3,56,2
s_add_u32 s37, s37, s74
LA 18
LS_ 114
M 3,57,3
s_xor_b32 s74, s74, 64512
LC_ 114
XW 120,1
M 4,68,2
WL 3
LO 113,17,113
HS 29,113
M 4,69,3
s_and_b32 s37, s37, 0x1ffff
RA 220,3,12288
v_add_f64 v[10:11], v[10:11], v[254:255]
M 5,80,2
FL 19,102,121
FH 115,103,121
M 5,81,3
s_sub_u32 s52, s37, s56
LS_ 19
HW 254,29
M 0,22,4
s_lshl_b32 s53, s63, 7
LA 19
LS_ 115
M 0,23,5
s_add_u32 s50, s84, s53
LC_ 115
v_cvt_f64_f32 v[254:255], v254
M 1,34,4
WL 3
LO 114,18,114
HS 30,114
M 1,35,5
s_addc_u32 s51, s85, 0
RA 224,1,14336
v_add_f64 v[124:125], v[124:125], v[254:255]
M 2,46,4
s_mov_b32 m0, s59
FL 16,56,116
FH 104,57,116
)"
R"(M 2,47,5
DR
LS_ 16
M 3,58,4
s_xor_b32 s59, s59, 1024
LA 16
LS_ 104
M 3,59,5
s_add_u32 m0, s38, 0
LC_ 104
M 4,70,4
WL 3
LO 115,19,115
HS 30,115
M 4,71,5
.Lk1p12:
s_cbranch_execz .Lk1r0
RA 228,3,14336
HW 254,30
M 5,82,4
FL 17,58,117
FH 105,59,117
M 5,83,5
SU 112,21,46
LS_ 17
Z 6,12,0
LA 17
LS_ 105
M 6,13,1
LC_ 105
v_cvt_f64_f32 v[254:255], v254
Z 7,24,0
WL 3
LO 104,16,104
HZ 24,104
M 7,25,1
RA 232,1,16384
v_add_f64 v[126:127], v[126:127], v[254:255]
Z 8,36,0
FL 18,60,116
FH 106,61,116
M 8,37,1
DX 4,0
LS_ 18
Z 9,48,0
s_add_u32 m0, s38, 1920
LA 18
LS_ 106
M 9,49,1
LC_ 106
v_add_u32 v118, v118, v2
Z 10,60,0
WL 3
LO 105,17,105
HS 24,105
M 10,61,1
RA 236,3,16384
Z 11,72,0
FL 19,62,117
FH 107,63,117
M 11,73,1
DX 4,128
LS_ 19
M 6,14,2
s_add_u32 m0, s38, 3840
LA 19
LS_ 107
M 6,15,3
LC_ 107
M 7,26,2
WL 3
LO 106,18,106
HZ 25,106
M 7,27,3
RA 216,1,18432
M 8,38,2
FL 16,64,116
FH 108,65,116
M 8,39,3
DX 4,256
LS_ 16
M 9,50,2
s_add_u32 m0, s38, 12288
LA 16
LS_ 108
M 9,51,3
LC_ 108
M 10,62,2
WL 3
LO 107,19,107
HS 25,107
M 10,63,3
RA 220,3,18432
M 11,74,2
FL 17,66,117
FH 109,67,117
M 11,75,3
SU 104,118,42
LS_ 17
M 6,16,4
LA 17
LS_ 109
M 6,17,5
LC_ 109
M 7,28,4
WL 3
LO 108,16,108
HZ 26,108
M 7,29,5
RA 224,1,20480
M 8,40,4
FL 18,68,116
FH 110,69,116
M 8,41,5
DX 5,0
LS_ 18
M 9,52,4
s_add_u32 m0, s38, 14208
LA 18
LS_ 110
M 9,53,5
LC_ 110
XW 116,0
M 10,64,4
WL 3
LO 109,17,109
HS 26,109
M 10,65,5
RA 228,3,20480
M 11,76,4
FL 19,70,117
FH 111,71,117
M 11,77,5
DX 5,128
LS_ 19
M 6,18,0
s_add_u32 m0, s38, 16128
LA 19
LS_ 111
M 6,19,1
LC_ 111
M 7,30,0
WL 3
LO 110,18,110
HZ 27,110
M 7,31,1
RA 232,1,22528
M 8,42,0
FL 16,72,116
FH 112,73,116
M 8,43,1
RV 120,20,64
LS_ 16
M 9,54,0
LA 16
LS_ 112
M 9,55,1
LC_ 112
v_add_u32 v1, s52, v14
M 10,66,0
WL 4
LO 111,19,111
HS 27,111
M 10,67,1
RA 236,3,22528
M 11,78,0
FL 17,74,117
FH 113,75,117
M 11,79,1
SU 108,21,44
LS_ 17
M 6,20,2
LA 17
LS_ 113
M 6,21,3
WV 12
LC_ 113
CW
M 7,32,2
WL 4
LO 112,16,112
HZ 29,112
M 7,33,3
RA 216,1,0
v_cmp_eq_u32 vcc, 0, v120
M 8,44,2
FL 18,76,116
FH 114,77,116
M 8,45,3
LS_ 18
v_xor_b32 v3, 64, v1
M 9,56,2
CB 2,41
LA 18
LS_ 114
M 9,57,3
LC_ 114
XW 116,1
M 10,68,2
WL 4
LO 113,17,113
HS 29,113
M 10,69,3
RA 220,3,0
M 11,80,2
FL 19,78,117
FH 115,79,117
M 11,81,3
DX 5,256
LS_ 19
M 6,22,4
s_add_u32 s38, s38, s76
LA 19
LS_ 115
M 6,23,5
s_xor_b32 s76, s76, 64512
LC_ 115
M 7,34,4
WL 3
LO 114,18,114
HZ 30,114
M 7,35,5
s_and_b32 s38, s38, 0x1ffff
RA 224,1,2048
M 8,46,4
FL 16,80,120
FH 104,81,120
M 8,47,5
s_barrier
ds_read_b32 v40, v41
LS_ 16
M 9,58,4
LA 16
LS_ 104
M 9,59,5
LC_ 104
v_xor_b32 v41, 64, v41
M 10,70,4
WL 4
LO 115,19,115
HS 30,115
M 10,71,5
RA 228,3,2048
M 11,82,4
FL 17,82,121
FH 105,83,121
M 11,83,5
2:
SU 112,21,46
LS_ 17
Z 0,12,0
LA 17
LS_ 105
M 0,13,1
WL 4
LC_ 105
v_readlane_b32 s72, v40, 1
Z 1,24,0
WL 3
LO 104,16,104
HS 24,104
M 1,25,1
WV 11
RA 232,1,4096
v_min_u32 v9, v22, v23
Z 2,36,0
FL 18,84,120
FH 106,85,120
M 2,37,1
AT_
LS_ 18
Z 3,48,0
LA 18
LS_ 106
M 3,49,1
LC_ 106
v_add_u32 v122, v122, v2
Z 4,60,0
WL 3
LO 105,17,105
HS 24,105
M 4,61,1
RA 236,3,4096
v_cmp_eq_u32 vcc, 0, v9
Z 5,72,0
FL 19,86,121
FH 107,87,121
M 5,73,1
LS_ 19
HW 254,24
M 0,14,2
CB 0,42
LA 19
LS_ 107
M 0,15,3
LC_ 107
v_add_u32 v4, v22, v6
M 1,26,2
WL 3
LO 106,18,106
HS 25,106
M 1,27,3
RA 216,1,6144
v_add_u32 v5, v23, v6
M 2,38,2
s_mov_b32 s64, s63
FL 16,88,120
FH 108,89,120
M 2,39,3
s_mov_b32 s63, s62
LS_ 16
v_cvt_f64_f32 v[254:255], v254
M 3,50,2
s_mov_b32 s62, s61
LA 16
LS_ 108
M 3,51,3
s_mov_b32 s61, s60
LC_ 108
v_add_f64 v[248:249], v[248:249], v[254:255]
M 4,62,2
WL 3
LO 107,19,107
HS 25,107
M 4,63,3
s_add_u32 %[nb], %[nb], 1
RA 220,3,6144
v_xor_b32 v20, 0x400, v20
M 5,74,2
FL 17,90,121
FH 109,91,121
M 5,75,3
s_cmp_ge_u32 s72, 96
SU 104,122,42
LS_ 17
M 0,16,4
s_addc_u32 s39, s39, 0
LA 17
LS_ 109
M 0,17,5
s_min_u32 s72, s72, 95
LC_ 109
HW 254,25
M 1,28,4
WL 3
LO 108,16,108
HS 26,108
M 1,29,5
s_bfe_u32 s53, s72, 0x20003
RA 224,1,8192
v_cvt_f64_f32 v[254:255], v254
M 2,40,4
s_bitcmp1_b32 s90, s53
FL 18,92,120
FH 110,93,120
M 2,41,5
s_cselect_b32 s73, 2304, 0
LS_ 18
v_add_f64 v[250:251], v[250:251], v[254:255]
M 3,52,4
s_and_b32 s53, s72, 31
LA 18
LS_ 110
M 3,53,5
s_mul_i32 s53, s53, 72
LC_ 110
XW 120,0
M 4,64,4
WL 3
LO 109,17,109
HS 26,109
M 4,65,5
s_lshr_b32 s72, s72, 5
RA 228,3,8192
HW 254,26
M 5,76,4
FL 19,94,121
FH 111,95,121
M 5,77,5
s_add_u32 s53, s53, s72
LS_ 19
v_cvt_f64_f32 v[254:255], v254
M 0,18,0
s_add_u32 s53, s53, s73
LA 19
LS_ 111
M 0,19,1
s_add_u32 s57, s53, 69
LC_ 111
v_add_f64 v[252:253], v[252:253], v[254:255]
M 1,30,0
WL 3
LO 110,18,110
HS 27,110
M 1,31,1
s_cmp_eq_u32 s39, 0
RA 232,1,10240
M 2,42,0
s_cselect_b32 s57, s57, s61
FL 16,96,120
)"
R"(FH 112,97,120
M 2,43,1
s_mov_b32 s60, s57
LS_ 16
M 3,54,0
s_lshl_b32 s53, s60, 5
LA 16
LS_ 112
M 3,55,1
s_add_u32 s48, s82, s53
LC_ 112
WV 12
RV 116,20,0
M 4,66,0
WL 4
LO 111,19,111
HS 27,111
M 4,67,1
s_addc_u32 s49, s83, 0
RA 236,3,10240
HW 254,27
M 5,78,0
FL 17,98,121
FH 113,99,121
M 5,79,1
s_add_u32 s57, s57, 1
SU 108,21,44
LS_ 17
M 0,20,2
LA 17
LS_ 113
M 0,21,3
WL 2
LC_ 113
v_cmp_eq_u32 vcc, 0, v116
M 1,32,2
LO 112,16,112
HS 29,112
M 1,33,3
RA 216,1,12288
v_cvt_f64_f32 v[254:255], v254
M 2,44,2
CB 1,43
FL 18,100,120
FH 114,101,120
M 2,45,3
EL_
LS_ 18
M 3,56,2
s_add_u32 s37, s37, s74
LA 18
LS_ 114
M 3,57,3
s_xor_b32 s74, s74, 64512
LC_ 114
XW 120,1
M 4,68,2
WL 3
LO 113,17,113
HS 29,113
M 4,69,3
s_and_b32 s37, s37, 0x1ffff
RA 220,3,12288
v_add_f64 v[10:11], v[10:11], v[254:255]
M 5,80,2
FL 19,102,121
FH 115,103,121
M 5,81,3
s_sub_u32 s52, s37, s56
LS_ 19
HW 254,29
M 0,22,4
s_lshl_b32 s53, s63, 7
LA 19
LS_ 115
M 0,23,5
s_add_u32 s50, s84, s53
LC_ 115
v_cvt_f64_f32 v[254:255], v254
M 1,34,4
WL 3
LO 114,18,114
HS 30,114
M 1,35,5
s_addc_u32 s51, s85, 0
RA 224,1,14336
v_add_f64 v[124:125], v[124:125], v[254:255]
M 2,46,4
s_mov_b32 m0, s59
FL 16,56,116
FH 104,57,116
M 2,47,5
DR
LS_ 16
M 3,58,4
s_xor_b32 s59, s59, 1024
LA 16
LS_ 104
M 3,59,5
s_add_u32 m0, s38, 0
LC_ 104
M 4,70,4
WL 3
LO 115,19,115
HS 30,115
M 4,71,5
RA 228,3,14336
HW 254,30
M 5,82,4
FL 17,58,117
FH 105,59,117
M 5,83,5
SU 112,21,46
LS_ 17
Z 6,12,0
LA 17
LS_ 105
M 6,13,1
LC_ 105
v_cvt_f64_f32 v[254:255], v254
Z 7,24,0
WL 3
LO 104,16,104
HZ 24,104
M 7,25,1
RA 232,1,16384
v_add_f64 v[126:127], v[126:127], v[254:255]
Z 8,36,0
FL 18,60,116
FH 106,61,116
M 8,37,1
DX 4,0
LS_ 18
Z 9,48,0
s_add_u32 m0, s38, 1920
LA 18
LS_ 106
M 9,49,1
LC_ 106
v_add_u32 v118, v118, v2
Z 10,60,0
WL 3
LO 105,17,105
HS 24,105
M 10,61,1
RA 236,3,16384
Z 11,72,0
FL 19,62,117
FH 107,63,117
M 11,73,1
DX 4,128
LS_ 19
M 6,14,2
s_add_u32 m0, s38, 3840
LA 19
LS_ 107
M 6,15,3
LC_ 107
M 7,26,2
WL 3
LO 106,18,106
HZ 25,106
M 7,27,3
RA 216,1,18432
M 8,38,2
FL 16,64,116
FH 108,65,116
M 8,39,3
.Lk1p13:
s_cbranch_execz .Lk1r1
DX 4,256
LS_ 16
M 9,50,2
s_add_u32 m0, s38, 12288
LA 16
LS_ 108
M 9,51,3
LC_ 108
M 10,62,2
WL 3
LO 107,19,107
HS 25,107
M 10,63,3
RA 220,3,18432
M 11,74,2
FL 17,66,117
FH 109,67,117
M 11,75,3
SU 104,118,42
LS_ 17
M 6,16,4
LA 17
LS_ 109
M 6,17,5
LC_ 109
M 7,28,4
WL 3
LO 108,16,108
HZ 26,108
M 7,29,5
RA 224,1,20480
M 8,40,4
FL 18,68,116
FH 110,69,116
M 8,41,5
DX 5,0
LS_ 18
M 9,52,4
s_add_u32 m0, s38, 14208
LA 18
LS_ 110
M 9,53,5
LC_ 110
XW 116,0
M 10,64,4
WL 3
LO 109,17,109
HS 26,109
M 10,65,5
RA 228,3,20480
M 11,76,4
FL 19,70,117
FH 111,71,117
M 11,77,5
DX 5,128
LS_ 19
M 6,18,0
s_add_u32 m0, s38, 16128
LA 19
LS_ 111
M 6,19,1
LC_ 111
M 7,30,0
WL 3
LO 110,18,110
HZ 27,110
M 7,31,1
RA 232,1,22528
M 8,42,0
FL 16,72,116
FH 112,73,116
M 8,43,1
RV 120,20,64
LS_ 16
M 9,54,0
LA 16
LS_ 112
M 9,55,1
LC_ 112
v_add_u32 v1, s52, v14
M 10,66,0
WL 4
LO 111,19,111
HS 27,111
M 10,67,1
RA 236,3,22528
M 11,78,0
FL 17,74,117
FH 113,75,117
M 11,79,1
SU 108,21,44
LS_ 17
M 6,20,2
LA 17
LS_ 113
M 6,21,3
WV 12
LC_ 113
CW
M 7,32,2
WL 4
LO 112,16,112
HZ 29,112
M 7,33,3
RA 216,1,0
v_cmp_eq_u32 vcc, 0, v120
M 8,44,2
FL 18,76,116
FH 114,77,116
M 8,45,3
LS_ 18
v_xor_b32 v3, 64, v1
M 9,56,2
CB 2,44
LA 18
LS_ 114
M 9,57,3
LC_ 114
XW 116,1
M 10,68,2
WL 4
LO 113,17,113
HS 29,113
M 10,69,3
RA 220,3,0
M 11,80,2
FL 19,78,117
FH 115,79,117
M 11,81,3
DX 5,256
LS_ 19
M 6,22,4
s_add_u32 s38, s38, s76
LA 19
LS_ 115
M 6,23,5
s_xor_b32 s76, s76, 64512
LC_ 115
M 7,34,4
WL 3
LO 114,18,114
HZ 30,114
M 7,35,5
s_and_b32 s38, s38, 0x1ffff
RA 224,1,2048
M 8,46,4
FL 16,80,120
FH 104,81,120
M 8,47,5
s_barrier
ds_read_b32 v40, v41
LS_ 16
M 9,58,4
LA 16
LS_ 104
M 9,59,5
LC_ 104
v_xor_b32 v41, 64, v41
M 10,70,4
WL 4
LO 115,19,115
HS 30,115
M 10,71,5
RA 228,3,2048
M 11,82,4
s_cmp_lt_u32 s39, 4
FL 17,82,121
FH 105,83,121
M 11,83,5
s_cbranch_scc1 2b
SU 112,21,46
LS_ 17
LA 17
LS_ 105
LC_ 105
WL 3
LO 104,16,104
HS 24,104
FL 18,84,120
FH 106,85,120
LS_ 18
LA 18
LS_ 106
v_add_u32 v122, v122, v2
LC_ 106
WL 2
LO 105,17,105
HS 24,105
HW 254,24
v_cvt_f64_f32 v[254:255], v254
v_add_f64 v[248:249], v[248:249], v[254:255]
FL 19,86,121
FH 107,87,121
LS_ 19
LA 19
LS_ 107
LC_ 107
WL 2
LO 106,18,106
HS 25,106
FL 16,88,120
FH 108,89,120
LS_ 16
LA 16
LS_ 108
LC_ 108
WL 2
LO 107,19,107
HS 25,107
HW 254,25
v_cvt_f64_f32 v[254:255], v254
v_add_f64 v[250:251], v[250:251], v[254:255]
FL 17,90,121
FH 109,91,121
SU 104,122,42
LS_ 17
LA 17
LS_ 109
LC_ 109
WL 2
LO 108,16,108
HS 26,108
FL 18,92,120
FH 110,93,120
LS_ 18
LA 18
LS_ 110
XW 120,0
LC_ 110
WL 2
)"
R"(LO 109,17,109
HS 26,109
HW 254,26
v_cvt_f64_f32 v[254:255], v254
v_add_f64 v[252:253], v[252:253], v[254:255]
FL 19,94,121
FH 111,95,121
LS_ 19
LA 19
LS_ 111
LC_ 111
WL 2
LO 110,18,110
HS 27,110
FL 16,96,120
FH 112,97,120
LS_ 16
LA 16
LS_ 112
LC_ 112
WL 2
LO 111,19,111
HS 27,111
HW 254,27
v_cvt_f64_f32 v[254:255], v254
v_add_f64 v[10:11], v[10:11], v[254:255]
FL 17,98,121
FH 113,99,121
SU 108,21,44
LS_ 17
LA 17
LS_ 113
LC_ 113
WL 2
LO 112,16,112
HS 29,112
FL 18,100,120
FH 114,101,120
LS_ 18
LA 18
LS_ 114
XW 120,1
LC_ 114
WL 2
LO 113,17,113
HS 29,113
HW 254,29
v_cvt_f64_f32 v[254:255], v254
v_add_f64 v[124:125], v[124:125], v[254:255]
FL 19,102,121
FH 115,103,121
LS_ 19
LA 19
LS_ 115
LC_ 115
WL 2
LO 114,18,114
HS 30,114
WL 0
LO 115,19,115
HS 30,115
HW 254,30
v_cvt_f64_f32 v[254:255], v254
v_add_f64 v[126:127], v[126:127], v[254:255]
SU 112,21,46
v_mov_b64 v[52:53], 0
s_nop 1
v_permlane16_swap_b32 v248, v250
v_permlane16_swap_b32 v249, v251
v_permlane16_swap_b32 v252, v10
v_permlane16_swap_b32 v253, v11
v_permlane16_swap_b32 v124, v126
v_permlane16_swap_b32 v125, v127
s_nop 1
v_add_f64 v[248:249], v[248:249], v[250:251]
v_add_f64 v[252:253], v[252:253], v[10:11]
v_add_f64 v[124:125], v[124:125], v[126:127]
s_nop 1
v_permlane32_swap_b32 v248, v252
v_permlane32_swap_b32 v249, v253
v_permlane32_swap_b32 v124, v52
v_permlane32_swap_b32 v125, v53
s_nop 1
v_add_f64 v[248:249], v[248:249], v[252:253]
v_add_f64 v[124:125], v[124:125], v[52:53]
s_nop 1
v_and_b32 v56, 0x7c, v31
v_lshlrev_b32 v56, 1, v56
v_lshl_add_u32 v56, v12, 9, v56
v_add_u32 v57, 0x2000, v56
s_mov_b32 s48, %[alo]
s_mov_b32 s49, %[ahi]
global_atomic_add_f64 v56, v[248:249], s[48:49]
s_mov_b32 exec_hi, 0
global_atomic_add_f64 v57, v[124:125], s[48:49]
s_mov_b32 exec_hi, -1
s_waitcnt vmcnt(6) lgkmcnt(0)
8:
k1_end:
.purgem M
.purgem Z
.purgem LZ
.purgem LS
.purgem LD
.purgem GZ
.purgem GS
.purgem S0
.purgem S
.purgem LS_
.purgem FL
.purgem FH
.purgem LU
.purgem VL_
.purgem VP
.purgem VQ
.purgem VS
.purgem FL_
.purgem FB
.purgem E2
.purgem DN
.purgem RF
.purgem SN
.purgem FX
.purgem C
.purgem LA
.purgem LC_
.purgem LO
.purgem HZ
.purgem HS
.purgem RA
.purgem RV
.purgem DW
.purgem DF
.purgem PFN
.purgem PFW
.purgem WL
.purgem WV
.purgem ST
.purgem SU
.purgem DX
.purgem DR
.purgem EL_
.purgem RS
.purgem Y3
.purgem LZA
.purgem LSA
.purgem CK
.purgem CB
.purgem CS
.purgem XW
.purgem HY
.purgem HW
.purgem AT_
.purgem CW
.purgem DC
.purgem SE
.purgem SG
.purgem SF
.purgem PL
)"
      : [nb] "=&s"(nb), [xp] "=&s"(xp), [wp] "=&s"(wp), [xlo] "=&s"(t[0]), [xhi] "=&s"(t[1]), [hlo] "=&s"(t[2]), [hhi] "=&s"(t[3]), [glo] "=&s"(t[4]), [ghi] "=&s"(t[5]), [d] "=&s"(t[8]), [wrap] "=&s"(t[9]), [rst] "=&s"(t[10]), [pos0] "=&s"(t[11]), [s2] "=&s"(t[17]), [alo] "=&s"(t[18]), [ahi] "=&s"(t[19]), [ca0] "=&s"(t[20]), [pf] "=&s"(t[21])
      : [ka] "s"(__builtin_amdgcn_kernarg_segment_ptr()), [b] "s"(b), [w] "s"(w), [pm] "s"(pm)
      : K1_CLOB);
  TS(6);
  MK_END(1);
  const unsigned pg = w & 1, nh = w >> 1, cl = (b & 7) * 1024;
  MK_START2(4); TS(9);
  const uint64_t pp = uni64((uint64_t)g_prm + (blockIdx.x * 4 + w) * 256), ap = (uint64_t)g_buf + K1_G_ACC + w * 1024;
  const unsigned wm = lds0 + K2_IM0 + w * 3072, hm = lds0 + pg * K2_NU * 2048 + nh * 1024, hx = nh << 7, pw = ((b >> 3) * 4 + 36 - 11 * w) * 1024;
  const unsigned pm2 = lds0 + K2_PF_OFF, sb = lds0 + K2_S16_OFF, sw = sb + w * 256;
  const uint64_t tb = uni64((uint64_t)(g_bias + 2048)), ta = uni64((uint64_t)g_buf + K1_G_TAB);
  uint64_t flg;
#define K2_OPS [pp] "s"(pp), [cl] "s"(cl), [sh] "s"((b >> 2) & 14), [tb] "s"(tb), [ta] "s"(ta), [alo] "s"((unsigned)ap), [ahi] "s"((unsigned)(ap >> 32)), [wm] "s"(wm), [hm] "s"(hm), [hx] "s"(hx), [w] "s"(w), [pw] "s"(pw), \
         [sb] "s"(sb), [sw] "s"(sw), [pm] "s"(pm2)
  asm volatile(
R"(.macro FL
global_load_dword v21, v22, s[98:99] sc1
.endm
.macro MA t,a,b
v_mfma_f32_16x16x32_f16 a[\t:\t+3], a[\a:\a+3], a[\b:\b+3], a[\t:\t+3]
.endm
.macro MV t,a,b
v_mfma_f32_16x16x32_f16 v[\t:\t+3], a[\a:\a+3], a[\b:\b+3], v[\t:\t+3]
.endm
.macro ZA t,a,b
v_mfma_f32_16x16x32_f16 a[\t:\t+3], a[\a:\a+3], a[\b:\b+3], a[252:255]
.endm
.macro ZV t,a,b
v_mfma_f32_16x16x32_f16 v[\t:\t+3], a[\a:\a+3], a[\b:\b+3], v[28:31]
.endm
.macro SB d,b
v_sub_f32 v[\d], v[\d], v[\b]
.endm
.macro TR d,a,o
ds_read_b64_tr_b16 a[\d:\d+1], v[\a] offset:1024*\o
.endm
.macro LA d,o
global_load_dwordx4 a[\d:\d+3], v0, s[44:45] offset:1024*\o sc1
.endm
.macro RV d,a,o
ds_read_b128 v[\d:\d+3], v[\a] offset:\o
.endm
.macro PB d,s,h
v_pk_mul_f16 v[\d], v[\d], v[\s] op_sel:[0,\h] op_sel_hi:[1,\h]
.endm
.macro DH o
global_load_lds_dwordx4 v1, s[40:41] offset:1024*\o nt sc1
.endm
.macro DG
global_load_lds_dwordx4 v0, s[46:47] sc1
.endm
.macro DX o
global_load_lds_dwordx4 v0, s[40:41] offset:1024*\o
.endm
.macro ST d,o
global_store_dwordx4 v0, v[\d:\d+3], s[42:43] offset:1024*\o nt sc1
.endm
.macro QO r,b
s_bfe_u32 s42, s50, 0x10000+\b
s_lshl2_add_u32 s42, s42, %[w]
s_lshl2_add_u32 s42, s42, 0x40000
s_bfe_u32 s[\r], 0xfe987610, s42
s_bfe_u32 s[\r+1], 0xda43cb52, s42
s_mul_i32 s[\r], s[\r], 3072
s_mul_i32 s[\r+1], s[\r+1], 3072
.endm
.macro AN d
v_and_b32 v[\d], s39, v[\d]
.endm
.macro AW a,s
v_accvgpr_write_b32 a[\a], v[\s]
.endm
.macro AR d,a
v_accvgpr_read_b32 v[\d], a[\a]
.endm
.macro UL d,x
v_lshlrev_b32 v[\d], 16, v[\x]
.endm
.macro UH d,x
v_and_b32 v[\d], s38, v[\x]
.endm
.macro AD d,b
v_add_f32 v[\d], v[\b], v[\d]
.endm
.macro PA a
s_mov_b32 m0, %[pm]
s_getpc_b64 s[46:47]
s_andn2_b32 s46, s46, 0x7f
s_add_u32 s46, s46, s81
s_addc_u32 s47, s47, 0
s_add_u32 s46, s46, \a
s_addc_u32 s47, s47, 0
.endm
.macro DF
global_load_lds_dwordx4 v0, s[46:47]
.endm
.macro CV d,a,b
v_cvt_pk_bf16_f32 v[\d], v[\a], v[\b]
.endm
.macro PM d,s
v_pk_mul_f16 v[\d], v[\d], v[\s]
.endm
.macro GA r
s_mov_b64 exec, 1
global_atomic_add v\r, v\r, v[\r+1], %[ta] sc0
s_mov_b64 exec, -1
.endm
.macro GW r
v_readfirstlane_b32 s46, v\r
v_lshlrev_b32 v\r, 6, v0
s_mov_b32 s47, 0
s_sub_u32 s42, s46, %[w]
s_cmp_lg_u32 s42, 31
s_cbranch_scc1 32f
v_mov_b32 v13, 0x4800
s_mov_b64 exec, 1
global_atomic_add v13, v13, v[\r+1], %[ta] sc0
s_mov_b64 exec, -1
s_waitcnt vmcnt(0)
v_readfirstlane_b32 s42, v13
s_nop 0
s_cmp_lg_u32 s42, 7
s_cbranch_scc1 32f
s_mov_b64 exec, 0xff
global_store_dword v\r, v[\r+1], %[ta] sc1
s_mov_b64 exec, -1
32:
.endm
.macro GE r
s_cmp_ge_u32 s46, 0
s_cbranch_scc1 29f
s_mov_b32 s47, 1
.endm
.macro GP
s_branch 27f
26:
s_sleep 3
27:
global_load_dword v13, v6, %[ta] sc1
s_sub_u32 s80, s80, 1
s_waitcnt vmcnt(0)
v_cmp_ne_u32 vcc, 1, v13
s_cbranch_scc1 28f
s_nop 3
s_cbranch_vccnz 26b
28:
.endm
.macro FS
global_store_dword v0, v21, s[96:97] sc1
.endm
.macro FB
s_cbranch_vccnz 21b
22:
.endm
.macro FG
s_or_b64 s[54:55], s[42:43], vcc
s_cbranch_scc1 23b
24:
.endm
.macro FX
s_branch 25f
21:
s_sleep 4
FL
s_waitcnt vmcnt(0)
v_cmp_ne_u32 vcc, 1, v21
s_sub_u32 s80, s80, 1
s_cbranch_scc1 22f
s_nop 2
s_cbranch_vccnz 21b
s_branch 22f
23:
s_cmp_eq_u64 s[42:43], 0
s_cselect_b32 s54, 4, 0
v_mov_b32 v8, s54
v_mov_b32 v9, 1
s_mov_b64 exec, 1
global_store_dword v8, v9, s[78:79] offset:384 sc1
s_mov_b64 exec, -1
s_nop 4
s_branch 24f
25:
.endm
.macro SX
s_sub_u32 s82, s42, s82
s_sub_u32 s54, s54, s100
s_mul_i32 s82, s82, 10
s_mul_i32 s54, s54, 225
s_sub_u32 s82, s54, s82
.endm
.macro SZ n
s_bitcmp1_b32 s82, 31
s_cbranch_scc1 .Lsy\n
.Lsz\n:
.endm
.macro SY n
.Lsy\n:
s_sleep 2
s_branch .Lsz\n
.endm
.macro WL n
s_waitcnt lgkmcnt(\n)
.endm
.macro WV n
s_waitcnt vmcnt(\n)
.endm
s_branch .Lk2s
.p2align 12
.Lk2s:
k2_code:
.Lk2p0:
s_cbranch_execz .Lk2p4
s_memtime s[82:83]
s_memrealtime s[100:101]
s_nop 0
s_load_dwordx16 s[48:63], %[pp], 0x0
s_load_dwordx16 s[64:79], %[pp], 0x40
s_load_dwordx16 s[84:99], %[pp], 0x80
v_mbcnt_lo_u32_b32 v7, -1, 0
v_mbcnt_hi_u32_b32 v7, -1, v7
v_lshlrev_b32 v0, 4, v7
v_lshrrev_b32 v1, 3, v7
v_mul_u32_u24 v1, 0x8100, v1
v_and_b32 v8, 0xff, v0
v_add_u32 v1, v1, v8
v_xor_b32 v1, %[hx], v1
v_add_u32 v188, 0x1000, v0
v_add_u32 v189, 0x2000, v0
v_mov_b32 v6, %[cl]
s_lshl1_add_u32 s46, %[sh], %[w]
s_lshl_b32 s46, s46, 7
s_add_u32 s46, s46, 12288
v_mov_b32 v210, s46
v_mov_b32 v211, 1
v_and_b32 v8, 15, v7
v_lshrrev_b32 v9, 4, v7
v_and_b32 v10, 3, v7
v_lshrrev_b32 v11, 2, v7
v_lshl_add_u32 v11, v10, 2, v11
v_and_b32 v11, 15, v11
v_lshlrev_b32 v11, 4, v11
v_lshl_add_u32 v11, v10, 8, v11
v_lshrrev_b32 v10, 1, v7
v_and_b32 v10, 8, v10
v_add_u32 v2, v10, v11
v_xor_b32 v3, 8, v2
v_add_u32 v4, 36864, v2
v_add_u32 v5, 36864, v3
s_mov_b32 s38, 0xffff0000
s_mov_b32 s80, 0x40000
s_mov_b32 s39, 0xfff8fff8
v_bfe_u32 v10, v7, 1, 2
v_bfe_u32 v11, v7, 3, 1
v_bfe_u32 v12, v7, 1, 1
v_xor_b32 v11, v11, v12
v_and_b32 v12, 1, v7
v_lshl_add_u32 v11, v11, 1, v12
v_lshl_add_u32 v10, v10, 2, v11
v_lshl_add_u32 v10, v9, 4, v10
v_lshl_add_u32 v190, v10, 2, %[sw]
v_lshl_add_u32 v191, v7, 2, %[sw]
s_waitcnt lgkmcnt(0)
s_mov_b32 s81, s52
v_min_u32 v209, 23, v7
v_lshlrev_b32 v209, 2, v209
global_load_dword v209, v209, %[tb] sc1
v_lshlrev_b32 v10, 2, v7
global_load_dword v160, v10, s[92:93] offset:0
global_load_dword v161, v10, s[92:93] offset:1024
global_load_dword v162, v10, s[92:93] offset:2048
global_load_dwordx4 v[212:215], v0, s[88:89] sc1
s_mov_b64 exec, 0xff
global_load_dwordx4 v[216:219], v0, s[88:89] offset:1024 sc1
s_mov_b64 exec, -1
WV 6
s_barrier
GA 210
v_mul_u32_u24 v10, 0xc00, v9
s_lshl_b32 s42, %[w], 7
s_add_u32 s42, s42, 73728
v_lshl_add_u32 v18, v8, 3, v10
v_add_u32 v18, s42, v18
s_lshl_b32 s42, %[w], 6
s_add_u32 s42, s42, 74240
v_lshl_add_u32 v19, v8, 2, v10
v_add_u32 v19, s42, v19
v_add_u32 v20, %[wm], v0
v_add_u32 v23, %[hm], v0
v_lshlrev_b32 v10, 2, v9
v_sub_u32 v10, v7, v10
v_and_b32 v10, 15, v10
s_lshr_b32 s42, %[hx], 1
s_add_u32 s42, s42, %[sb]
v_lshl_add_u32 v230, v10, 1, s42
s_movk_i32 s42, 1366
v_add_u32 v228, s51, v7
v_lshrrev_b32 v11, 6, v228
v_mul_u32_u24 v12, s42, v11
v_lshrrev_b32 v12, 16, v12
v_and_b32 v11, 1, v11
v_lshl_add_u32 v11, v12, 1, v11
v_bfe_u32 v228, v228, 4, 2
v_lshl_add_u32 v228, v11, 2, v228
v_lshl_add_u32 v228, v228, 4, %[sb]
v_add_u32 v229, s51, v7
v_add_u32 v229, 64, v229
v_lshrrev_b32 v11, 6, v229
v_mul_u32_u24 v12, s42, v11
v_lshrrev_b32 v12, 16, v12
v_and_b32 v11, 1, v11
v_lshl_add_u32 v11, v12, 1, v11
v_bfe_u32 v229, v229, 4, 2
v_lshl_add_u32 v229, v11, 2, v229
v_lshl_add_u32 v229, v229, 4, %[sb]
WV 0
GW 210
s_bitcmp1_b32 s48, 0
s_cselect_b64 s[64:65], s[58:59], s[56:57]
s_bitcmp1_b32 s48, 16
s_cselect_b32 s40, 16384, 0
s_add_u32 s64, s64, s40
s_addc_u32 s65, s65, 0
s_add_u32 m0, %[hm], 0
s_add_u32 s40, s64, 0
s_addc_u32 s41, s65, 0
DH 0
DH 2
s_add_u32 m0, %[hm], 4096
s_nop 0
v_xor_b32 v1, 0x100, v1
s_add_u32 s40, s64, 0
s_addc_u32 s41, s65, 0
DH 0
s_add_u32 s44, s86, 4096
s_addc_u32 s45, s87, 0
LA 0,-4
LA 4,-3
LA 8,-2
LA 12,-1
LA 16,0
LA 20,1
s_add_u32 s44, s44, 6144
s_addc_u32 s45, s45, 0
LA 24,-4
LA 28,-3
LA 32,-2
LA 36,-1
LA 40,0
LA 44,1
v_lshlrev_b32 v11, 4, v8
global_load_dwordx4 v[112:115], v11, s[94:95] offset:0 sc1
global_load_dwordx4 v[116:119], v11, s[94:95] offset:256 sc1
global_load_dwordx4 v[120:123], v11, s[94:95] offset:512 sc1
global_load_dwordx4 v[124:127], v11, s[94:95] offset:768 sc1
global_load_dwordx4 v[128:131], v11, s[94:95] offset:1024 sc1
global_load_dwordx4 v[132:135], v11, s[94:95] offset:1280 sc1
GE 210
s_mov_b32 m0, %[pm]
s_add_u32 s40, %[pw], 61440
s_add_u32 s40, s40, s86
s_addc_u32 s41, s87, 0
global_load_lds_dwordx4 v0, s[40:41] sc1
s_add_u32 m0, %[hm], 6144
s_add_u32 s40, s64, 2048
s_addc_u32 s41, s65, 0
DH 0
DH 2
s_add_u32 m0, %[hm], 10240
s_add_u32 s40, s64, 6144
s_addc_u32 s41, s65, 0
DH 0
s_add_u32 s44, s86, 53248
s_addc_u32 s45, s87, 0
LA 48,-4
LA 52,-3
LA 56,-2
LA 60,-1
LA 64,0
LA 68,1
s_add_u32 s44, s44, 6144
s_addc_u32 s45, s45, 0
LA 72,-4
LA 76,-3
LA 80,-2
LA 84,-1
LA 88,0
LA 92,1
s_add_u32 m0, %[hm], 12288
v_xor_b32 v1, 0x300, v1
s_add_u32 s40, s64, 0
s_addc_u32 s41, s65, 0
DH 0
DH 2
s_add_u32 m0, %[hm], 16384
s_add_u32 s40, s64, 4096
s_addc_u32 s41, s65, 0
DH 0
s_add_u32 m0, %[hm], 18432
s_add_u32 s40, s64, 6144
s_addc_u32 s41, s65, 0
DH 0
s_add_u32 m0, %[hm], 24576
v_xor_b32 v1, 0x100, v1
s_add_u32 s40, s64, 0
s_addc_u32 s41, s65, 0
DH 0
DH 2
s_add_u32 m0, %[hm], 28672
s_add_u32 s40, s64, 4096
s_addc_u32 s41, s65, 0
DH 0
s_add_u32 m0, %[hm], 30720
s_add_u32 s40, s64, 6144
s_addc_u32 s41, s65, 0
DH 0
)"
R"(29:
s_branch 25f
SY 0
SY 1
SY 2
SY 3
SY 4
SY 5
SY 6
SY 7
SY 8
SY 9
SY 10
SY 11
SY 12
SY 13
SY 14
SY 15
SY 16
SY 17
SY 18
SY 19
FX
v_min_u32_dpp v209, v209, v209 row_shr:1 row_mask:0xf bank_mask:0xf
s_nop 1
v_min_u32_dpp v209, v209, v209 row_shr:2 row_mask:0xf bank_mask:0xf
s_nop 1
v_min_u32_dpp v209, v209, v209 row_shr:4 row_mask:0xf bank_mask:0xf
s_nop 1
v_min_u32_dpp v209, v209, v209 row_shr:8 row_mask:0xf bank_mask:0xf
s_nop 1
v_min_u32_dpp v209, v209, v209 row_bcast:15 row_mask:0xa bank_mask:0xf
s_nop 1
v_readlane_b32 s53, v209, 31
WV 5
v_add_f32 v112, v112, v113
v_add_f32 v114, v114, v115
WV 4
v_add_f32 v116, v116, v117
v_add_f32 v118, v118, v119
WV 3
v_add_f32 v120, v120, v121
v_add_f32 v122, v122, v123
WV 2
v_add_f32 v124, v124, v125
v_add_f32 v126, v126, v127
WV 1
v_add_f32 v128, v128, v129
v_add_f32 v130, v130, v131
WV 0
v_add_f32 v132, v132, v133
v_add_f32 v134, v134, v135
v_add_f32 v112, v112, v114
v_add_f32 v116, v116, v118
v_add_f32 v120, v120, v122
v_add_f32 v124, v124, v126
v_add_f32 v128, v128, v130
v_add_f32 v132, v132, v134
v_sub_f32 v32, s53, v112
v_sub_f32 v33, s53, v116
v_sub_f32 v34, s53, v120
v_sub_f32 v35, s53, v124
v_sub_f32 v36, s53, v128
v_sub_f32 v37, s53, v132
v_mov_b32 v28, s53
v_mov_b32 v29, s53
v_mov_b32 v30, s53
v_mov_b32 v31, s53
AW 252,28
AW 253,28
AW 254,28
AW 255,28
v_mov_b32 v195, 0x7f800000
UL 176,160
UL 180,161
UL 184,162
UH 177,160
UH 181,161
UH 185,162
v_mov_b32 v193, 0
GP
s_mov_b32 s42, %[alo]
s_mov_b32 s43, %[ahi]
global_load_dwordx4 v[112:115], v0, s[42:43] sc1
global_load_dwordx4 v[116:119], v188, s[42:43] sc1
global_load_dwordx4 v[120:123], v189, s[42:43] sc1
s_add_u32 s42, s42, 12288
s_addc_u32 s43, s43, 0
global_load_dwordx4 v[124:127], v0, s[42:43] sc1
global_load_dwordx4 v[128:131], v188, s[42:43] sc1
global_load_dwordx4 v[132:135], v189, s[42:43] sc1
s_add_u32 s42, s42, 12288
s_addc_u32 s43, s43, 0
global_load_dwordx4 v[136:139], v0, s[42:43] sc1
global_load_dwordx4 v[140:143], v188, s[42:43] sc1
global_load_dwordx4 v[144:147], v189, s[42:43] sc1
s_add_u32 s42, s42, 12288
s_addc_u32 s43, s43, 0
global_load_dwordx4 v[148:151], v0, s[42:43] sc1
global_load_dwordx4 v[152:155], v188, s[42:43] sc1
global_load_dwordx4 v[156:159], v189, s[42:43] sc1
s_nop 0
RV 232,23,0
RV 236,23,2048
RV 240,23,4096
s_mov_b32 s42, 0x3e100000
WV 8
v_add_f64 v[112:113], v[112:113], v[124:125]
WV 7
v_add_f64 v[116:117], v[116:117], v[128:129]
WV 6
v_add_f64 v[120:121], v[120:121], v[132:133]
v_add_f64 v[114:115], v[114:115], v[126:127]
v_add_f64 v[118:119], v[118:119], v[130:131]
v_add_f64 v[122:123], v[122:123], v[134:135]
WV 5
v_add_f64 v[112:113], v[112:113], v[136:137]
WV 4
v_add_f64 v[116:117], v[116:117], v[140:141]
WV 3
v_add_f64 v[120:121], v[120:121], v[144:145]
v_add_f64 v[114:115], v[114:115], v[138:139]
v_add_f64 v[118:119], v[118:119], v[142:143]
v_add_f64 v[122:123], v[122:123], v[146:147]
WV 2
v_add_f64 v[112:113], v[112:113], v[148:149]
WV 1
v_add_f64 v[116:117], v[116:117], v[152:153]
WV 0
v_add_f64 v[120:121], v[120:121], v[156:157]
v_add_f64 v[114:115], v[114:115], v[150:151]
v_add_f64 v[118:119], v[118:119], v[154:155]
v_add_f64 v[122:123], v[122:123], v[158:159]
v_cvt_f32_f64 v164, v[112:113]
v_cvt_f32_f64 v168, v[116:117]
v_cvt_f32_f64 v172, v[120:121]
s_bitcmp1_b32 s47, 0
s_cbranch_scc1 30f
v_cvt_f32_f64 v165, v[114:115]
s_mov_b32 m0, %[pm]
s_add_u32 s40, %[pw], 61440
s_add_u32 s40, s40, s86
s_addc_u32 s41, s87, 0
global_load_lds_dwordx4 v0, s[40:41] sc1
v_cvt_f32_f64 v169, v[118:119]
v_cvt_f32_f64 v173, v[122:123]
v_mul_f32 v166, 0x4091c518, v164
v_mul_f32 v170, 0x4091c518, v168
s_add_u32 m0, %[hm], 6144
s_add_u32 s40, s64, 2048
s_addc_u32 s41, s65, 0
DH 0
v_mul_f32 v174, 0x4091c518, v172
v_mul_f32 v167, 0x4091c518, v165
v_mul_f32 v171, 0x4091c518, v169
v_mul_f32 v175, 0x4091c518, v173
DH 2
CV 167,166,167
CV 171,170,171
CV 175,174,175
UL 166,167
s_add_u32 m0, %[hm], 10240
s_add_u32 s40, s64, 6144
s_addc_u32 s41, s65, 0
DH 0
UL 170,171
UL 174,175
UH 167,167
UH 171,171
s_add_u32 s44, s86, 53248
s_addc_u32 s45, s87, 0
LA 48,-4
UH 175,175
v_add_f32 v166, 0x358637bd, v166
v_add_f32 v170, 0x358637bd, v170
v_add_f32 v174, 0x358637bd, v174
LA 52,-3
v_add_f32 v167, 0x358637bd, v167
v_add_f32 v171, 0x358637bd, v171
v_add_f32 v175, 0x358637bd, v175
CV 167,166,167
LA 56,-2
CV 171,170,171
CV 175,174,175
UL 166,167
UL 170,171
LA 60,-1
UL 174,175
UH 167,167
UH 171,171
UH 175,175
LA 64,0
v_sqrt_f32 v166, v166
v_sqrt_f32 v170, v170
v_sqrt_f32 v174, v174
v_sqrt_f32 v167, v167
LA 68,1
v_sqrt_f32 v171, v171
v_sqrt_f32 v175, v175
CV 167,166,167
CV 171,170,171
s_add_u32 s44, s44, 6144
s_addc_u32 s45, s45, 0
LA 72,-4
CV 175,174,175
UL 166,167
UL 170,171
UL 174,175
LA 76,-3
UH 167,167
UH 171,171
UH 175,175
v_cmp_le_f32 vcc, s42, v164
v_cmp_le_f32 s[54:55], s42, v165
s_nop 0
v_cndmask_b32 v164, v195, v166, vcc
v_cndmask_b32 v165, v195, v167, s[54:55]
LA 80,-2
v_add_f32 v164, v164, v165
v_cmp_le_f32 vcc, s42, v168
v_cmp_le_f32 s[54:55], s42, v169
s_nop 0
v_cndmask_b32 v168, v195, v170, vcc
v_cndmask_b32 v169, v195, v171, s[54:55]
v_add_f32 v168, v168, v169
v_cmp_le_f32 vcc, s42, v172
v_cmp_le_f32 s[54:55], s42, v173
s_nop 0
v_cndmask_b32 v172, v195, v174, vcc
v_cndmask_b32 v173, v195, v175, s[54:55]
LA 84,-1
v_add_f32 v172, v172, v173
v_add_f32 v192, v164, v168
v_add_f32 v192, v192, v172
s_nop 1
s_memtime s[42:43]
s_memrealtime s[54:55]
v_add_f32_dpp v192, v192, v192 row_shr:1 row_mask:0xf bank_mask:0xf
s_nop 1
v_add_f32_dpp v192, v192, v192 row_shr:2 row_mask:0xf bank_mask:0xf
s_nop 1
v_add_f32_dpp v192, v192, v192 row_shr:4 row_mask:0xf bank_mask:0xf
s_nop 1
v_add_f32_dpp v192, v192, v192 row_shr:8 row_mask:0xf bank_mask:0xf
s_nop 1
v_add_f32_dpp v192, v192, v192 row_bcast:15 row_mask:0xa bank_mask:0xf
s_nop 1
v_add_f32_dpp v192, v192, v192 row_bcast:31 row_mask:0xc bank_mask:0xf
ds_write_b32 v191, v192 offset:4096
v_mov_b32 v6, %[sb]
s_waitcnt lgkmcnt(0)
s_barrier
SX
ds_read_b32 v196, v6 offset:4348
ds_read_b32 v197, v6 offset:4604
ds_read_b32 v198, v6 offset:4860
ds_read_b32 v199, v6 offset:5116
WL 0
v_add_f32 v196, v196, v197
v_add_f32 v198, v198, v199
LA 88,0
v_add_f32 v196, v196, v198
v_mul_f32 v194, 0x3a2aaaab, v196
v_cmp_nlt_f32 s[42:43], v196, v195
CV 194,194,194
UL 194,194
LA 92,1
v_add_f32 v194, 0x358637bd, v194
CV 194,194,194
UL 194,194
v_rcp_f32 v194, v194
s_nop 0
v_mul_f32 v166, v194, v166
s_add_u32 m0, %[hm], 12288
v_xor_b32 v1, 0x300, v1
s_add_u32 s40, s64, 0
s_addc_u32 s41, s65, 0
DH 0
v_mul_f32 v170, v194, v170
v_mul_f32 v174, v194, v174
v_mul_f32 v167, v194, v167
v_mul_f32 v171, v194, v171
DH 2
v_mul_f32 v175, v194, v175
CV 167,166,167
CV 171,170,171
CV 175,174,175
s_add_u32 m0, %[hm], 16384
s_add_u32 s40, s64, 4096
s_addc_u32 s41, s65, 0
DH 0
UL 166,167
UL 170,171
UL 174,175
UH 167,167
s_add_u32 m0, %[hm], 18432
s_add_u32 s40, s64, 6144
s_addc_u32 s41, s65, 0
DH 0
UH 171,171
UH 175,175
v_fma_f32 v166, v176, v166, 1.0
v_fma_f32 v170, v180, v170, 1.0
s_add_u32 m0, %[hm], 24576
v_xor_b32 v1, 0x100, v1
s_add_u32 s40, s64, 0
s_addc_u32 s41, s65, 0
DH 0
v_fma_f32 v174, v184, v174, 1.0
v_fma_f32 v167, v177, v167, 1.0
v_fma_f32 v171, v181, v171, 1.0
v_fma_f32 v175, v185, v175, 1.0
DH 2
v_mul_f32 v166, 0x400898a7, v166
v_mul_f32 v170, 0x400898a7, v170
v_mul_f32 v174, 0x400898a7, v174
v_mul_f32 v167, 0x400898a7, v167
s_add_u32 m0, %[hm], 28672
s_add_u32 s40, s64, 4096
s_addc_u32 s41, s65, 0
DH 0
v_mul_f32 v171, 0x400898a7, v171
v_mul_f32 v175, 0x400898a7, v175
v_cvt_pk_f16_f32 v176, v166, v167
v_cvt_pk_f16_f32 v180, v170, v171
s_add_u32 m0, %[hm], 30720
s_add_u32 s40, s64, 6144
s_addc_u32 s41, s65, 0
DH 0
s_branch 31f
30:
v_cvt_f32_f64 v165, v[114:115]
v_cvt_f32_f64 v169, v[118:119]
v_cvt_f32_f64 v173, v[122:123]
v_mul_f32 v166, 0x4091c518, v164
v_mul_f32 v170, 0x4091c518, v168
v_mul_f32 v174, 0x4091c518, v172
v_mul_f32 v167, 0x4091c518, v165
v_mul_f32 v171, 0x4091c518, v169
v_mul_f32 v175, 0x4091c518, v173
CV 167,166,167
CV 171,170,171
CV 175,174,175
UL 166,167
UL 170,171
UL 174,175
UH 167,167
UH 171,171
UH 175,175
v_add_f32 v166, 0x358637bd, v166
v_add_f32 v170, 0x358637bd, v170
v_add_f32 v174, 0x358637bd, v174
v_add_f32 v167, 0x358637bd, v167
v_add_f32 v171, 0x358637bd, v171
v_add_f32 v175, 0x358637bd, v175
CV 167,166,167
CV 171,170,171
CV 175,174,175
UL 166,167
UL 170,171
UL 174,175
UH 167,167
UH 171,171
UH 175,175
v_sqrt_f32 v166, v166
v_sqrt_f32 v170, v170
v_sqrt_f32 v174, v174
v_sqrt_f32 v167, v167
v_sqrt_f32 v171, v171
v_sqrt_f32 v175, v175
CV 167,166,167
CV 171,170,171
CV 175,174,175
UL 166,167
UL 170,171
UL 174,175
UH 167,167
UH 171,171
UH 175,175
v_cmp_le_f32 vcc, s42, v164
v_cmp_le_f32 s[54:55], s42, v165
s_nop 0
v_cndmask_b32 v164, v195, v166, vcc
v_cndmask_b32 v165, v195, v167, s[54:55]
v_add_f32 v164, v164, v165
v_cmp_le_f32 vcc, s42, v168
v_cmp_le_f32 s[54:55], s42, v169
s_nop 0
v_cndmask_b32 v168, v195, v170, vcc
v_cndmask_b32 v169, v195, v171, s[54:55]
v_add_f32 v168, v168, v169
v_cmp_le_f32 vcc, s42, v172
v_cmp_le_f32 s[54:55], s42, v173
s_nop 0
v_cndmask_b32 v172, v195, v174, vcc
v_cndmask_b32 v173, v195, v175, s[54:55]
v_add_f32 v172, v172, v173
v_add_f32 v192, v164, v168
v_add_f32 v192, v192, v172
s_nop 1
)"
R"(s_memtime s[42:43]
s_memrealtime s[54:55]
v_add_f32_dpp v192, v192, v192 row_shr:1 row_mask:0xf bank_mask:0xf
s_nop 1
v_add_f32_dpp v192, v192, v192 row_shr:2 row_mask:0xf bank_mask:0xf
s_nop 1
v_add_f32_dpp v192, v192, v192 row_shr:4 row_mask:0xf bank_mask:0xf
s_nop 1
v_add_f32_dpp v192, v192, v192 row_shr:8 row_mask:0xf bank_mask:0xf
s_nop 1
v_add_f32_dpp v192, v192, v192 row_bcast:15 row_mask:0xa bank_mask:0xf
s_nop 1
v_add_f32_dpp v192, v192, v192 row_bcast:31 row_mask:0xc bank_mask:0xf
ds_write_b32 v191, v192 offset:4096
v_mov_b32 v6, %[sb]
s_waitcnt lgkmcnt(0)
s_barrier
SX
ds_read_b32 v196, v6 offset:4348
ds_read_b32 v197, v6 offset:4604
ds_read_b32 v198, v6 offset:4860
ds_read_b32 v199, v6 offset:5116
WL 0
v_add_f32 v196, v196, v197
v_add_f32 v198, v198, v199
v_add_f32 v196, v196, v198
v_mul_f32 v194, 0x3a2aaaab, v196
v_cmp_nlt_f32 s[42:43], v196, v195
CV 194,194,194
UL 194,194
v_add_f32 v194, 0x358637bd, v194
CV 194,194,194
UL 194,194
v_rcp_f32 v194, v194
s_nop 0
v_mul_f32 v166, v194, v166
v_mul_f32 v170, v194, v170
.Lk2p1:
s_cbranch_execz .Lk2p5
v_mul_f32 v174, v194, v174
v_mul_f32 v167, v194, v167
v_mul_f32 v171, v194, v171
v_mul_f32 v175, v194, v175
CV 167,166,167
CV 171,170,171
CV 175,174,175
UL 166,167
UL 170,171
UL 174,175
UH 167,167
UH 171,171
UH 175,175
v_fma_f32 v166, v176, v166, 1.0
v_fma_f32 v170, v180, v170, 1.0
v_fma_f32 v174, v184, v174, 1.0
v_fma_f32 v167, v177, v167, 1.0
v_fma_f32 v171, v181, v171, 1.0
v_fma_f32 v175, v185, v175, 1.0
v_mul_f32 v166, 0x400898a7, v166
v_mul_f32 v170, 0x400898a7, v170
v_mul_f32 v174, 0x400898a7, v174
v_mul_f32 v167, 0x400898a7, v167
v_mul_f32 v171, 0x400898a7, v171
v_mul_f32 v175, 0x400898a7, v175
v_cvt_pk_f16_f32 v176, v166, v167
v_cvt_pk_f16_f32 v180, v170, v171
31:
v_cvt_pk_f16_f32 v184, v174, v175
ds_write_b32 v190, v176 offset:0
ds_write_b32 v190, v180 offset:1024
ds_write_b32 v190, v184 offset:2048
v_max3_f32 v193, |v166|, |v167|, v193
v_max3_f32 v193, |v170|, |v171|, v193
v_max3_f32 v193, |v174|, |v175|, v193
ds_write_b32 v191, v176 offset:3072
v_cmp_lt_f32 vcc, 0x43000000, v193
WL 0
s_barrier
ds_read_u16 v112, v230 offset:3072
ds_read_u16 v113, v230 offset:3104
ds_read_u16 v114, v230 offset:3200
ds_read_u16 v115, v230 offset:3232
ds_read_u16 v116, v230 offset:3328
ds_read_u16 v117, v230 offset:3360
ds_read_u16 v118, v230 offset:3456
ds_read_u16 v119, v230 offset:3488
ds_read_b128 v[220:223], v228
ds_read_b128 v[224:227], v229
FG
s_bitcmp1_b32 s49, 0
s_cselect_b64 s[66:67], s[62:63], s[60:61]
s_add_u32 m0, %[hm], 20480
v_xor_b32 v1, 0x100, v1
s_add_u32 s40, s66, 0
s_addc_u32 s41, s67, 0
DH 0
DH 2
WL 8
v_lshl_or_b32 v10, v113, 16, v112
WL 6
v_lshl_or_b32 v11, v115, 16, v114
PB 232,10,0
PB 233,10,0
PB 234,10,1
PB 235,10,1
ds_write_b128 v23, v[232:235] offset:0
PB 236,10,0
PB 237,10,0
PB 238,10,1
PB 239,10,1
ds_write_b128 v23, v[236:239] offset:2048
PB 240,11,0
PB 241,11,0
PB 242,11,1
PB 243,11,1
ds_write_b128 v23, v[240:243] offset:4096
WL 7
v_lshl_or_b32 v12, v117, 16, v116
WL 5
v_lshl_or_b32 v13, v119, 16, v118
WL 4
PM 212,220
PM 213,221
PM 214,222
PM 215,223
WL 3
PM 216,224
PM 217,225
PM 218,226
PM 219,227
WL 0
s_barrier
TR 96,2,0
TR 98,3,0
TR 100,2,1
TR 102,3,1
TR 104,4,0
TR 106,5,0
TR 108,4,1
TR 110,5,1
TR 112,2,2
TR 114,3,2
TR 116,2,3
TR 118,3,3
s_mov_b32 s42, 0x40003
v_pk_add_u16 v212, v212, s42
v_pk_add_u16 v213, v213, s42
v_pk_add_u16 v214, v214, s42
v_pk_add_u16 v215, v215, s42
v_pk_add_u16 v216, v216, s42
v_pk_add_u16 v217, v217, s42
v_pk_add_u16 v218, v218, s42
v_pk_add_u16 v219, v219, s42
AN 212
AN 213
AN 214
AN 215
AN 216
AN 217
AN 218
AN 219
global_store_dwordx4 v0, v[212:215], s[90:91] sc1
s_mov_b64 exec, 0xff
global_store_dwordx4 v0, v[216:219], s[90:91] offset:1024 sc1
s_mov_b64 exec, -1
WL 8
ZV 40,96,0
WV 26
RV 6,23,6144
MV 40,100,4
ZV 44,96,8
MV 44,100,12
ZV 48,96,16
MV 48,100,20
WL 0
PB 6,11,0
PB 7,11,0
ZV 52,96,24
PB 8,11,1
PB 9,11,1
MV 52,100,28
ds_write_b128 v23, v[6:9] offset:6144
ZV 56,96,32
MV 56,100,36
ZV 60,96,40
MV 60,100,44
TR 96,4,2
SZ 0
ZV 84,104,40
TR 98,5,2
MV 84,108,44
ZV 80,104,32
WV 25
RV 24,23,8192
MV 80,108,36
TR 100,4,3
ZV 76,104,24
TR 102,5,3
MV 76,108,28
ZV 72,104,16
MV 72,108,20
WL 2
PB 24,11,0
PB 25,11,0
ZV 68,104,8
PB 26,11,1
PB 27,11,1
MV 68,108,12
ds_write_b128 v23, v[24:27] offset:8192
ZV 64,104,0
MV 64,108,4
TR 104,2,4
SZ 1
ZV 88,112,0
TR 106,3,4
MV 88,116,4
ZV 92,112,8
MV 92,116,12
TR 108,2,5
ZV 96,112,16
TR 110,3,5
MV 96,116,20
WV 24
RV 6,23,10240
ZV 100,112,24
MV 100,116,28
ZV 104,112,32
MV 104,116,36
ZV 108,112,40
WL 0
PB 6,11,0
PB 7,11,0
MV 108,116,44
TR 112,4,4
PB 8,11,1
SZ 2
ZA 140,96,40
TR 114,5,4
PB 9,11,1
MA 140,100,44
ds_write_b128 v23, v[6:9] offset:10240
ZA 136,96,32
MA 136,100,36
TR 116,4,5
ZA 132,96,24
TR 118,5,5
MA 132,100,28
ZA 128,96,16
MA 128,100,20
ZA 124,96,8
MA 124,100,12
WL 0
s_barrier
ZA 120,96,0
MA 120,100,4
WV 23
TR 96,2,6
SZ 3
MV 40,104,48
WV 21
TR 98,3,6
MV 40,108,52
s_add_u32 s44, s86, 102400
s_addc_u32 s45, s87, 0
LA 0,-4
MV 44,104,56
WV 21
MV 44,108,60
WV 20
TR 100,2,7
LA 4,-3
MV 48,104,64
WV 20
TR 102,3,7
MV 48,108,68
WV 19
LA 8,-2
MV 52,104,72
WV 19
MV 52,108,76
WV 18
LA 12,-1
MV 56,104,80
WV 18
MV 56,108,84
WV 17
LA 16,0
MV 60,104,88
WV 17
MV 60,108,92
TR 104,4,6
LA 20,1
SZ 4
MV 84,112,88
TR 106,5,6
MV 84,116,92
s_add_u32 s44, s44, 6144
s_addc_u32 s45, s45, 0
LA 24,-4
MV 80,112,80
MV 80,116,84
TR 108,4,7
LA 28,-3
MV 76,112,72
TR 110,5,7
MV 76,116,76
LA 32,-2
MV 72,112,64
MV 72,116,68
LA 36,-1
MV 68,112,56
MV 68,116,60
LA 40,0
MV 64,112,48
MV 64,116,52
WL 4
TR 112,2,8
LA 44,1
SZ 5
MV 88,96,48
TR 114,3,8
MV 88,100,52
s_bitcmp1_b32 s49, 0
s_cselect_b64 s[66:67], s[62:63], s[60:61]
MV 92,96,56
s_add_u32 m0, %[hm], 32768
v_xor_b32 v1, 0x100, v1
MV 92,100,60
TR 116,2,9
MV 96,96,64
TR 118,3,9
MV 96,100,68
s_add_u32 s40, s66, 0
s_addc_u32 s41, s67, 0
DH 0
MV 100,96,72
WV 24
RV 24,23,12288
MV 100,100,76
DH 2
MV 104,96,80
MV 104,100,84
s_bitcmp1_b32 s48, 0
s_cselect_b64 s[64:65], s[58:59], s[56:57]
MV 108,96,88
s_bitcmp1_b32 s48, 16
s_cselect_b32 s40, 16384, 0
MV 108,100,92
TR 96,4,8
WL 1
PB 24,12,0
SZ 6
MA 140,104,88
TR 98,5,8
PB 25,12,0
MA 140,108,92
s_add_u32 s64, s64, s40
s_addc_u32 s65, s65, 0
PB 26,12,1
MA 136,104,80
s_add_u32 m0, %[hm], 0
v_xor_b32 v1, 0x700, v1
PB 27,12,1
MA 136,108,84
TR 100,4,9
MA 132,104,72
TR 102,5,9
MA 132,108,76
s_add_u32 s40, s64, 0
s_addc_u32 s41, s65, 0
DH 0
MA 128,104,64
ds_write_b128 v23, v[24:27] offset:12288
MA 128,108,68
WV 25
DH 2
MA 124,104,56
s_add_u32 m0, %[hm], 4096
RV 6,23,14336
MA 124,108,60
s_add_u32 s40, s64, 4096
s_addc_u32 s41, s65, 0
DH 0
MA 120,104,48
MA 120,108,52
TR 104,2,10
SZ 7
ZV 112,112,48
TR 106,3,10
MV 112,116,52
WL 2
PB 6,12,0
PB 7,12,0
ZV 116,112,56
PB 8,12,1
PB 9,12,1
MV 116,116,60
TR 108,2,11
ZV 120,112,64
TR 110,3,11
)"
R"(MV 120,116,68
ds_write_b128 v23, v[6:9] offset:14336
ZV 124,112,72
WV 26
RV 24,23,16384
MV 124,116,76
ZV 128,112,80
MV 128,116,84
ZV 132,112,88
MV 132,116,92
TR 112,4,10
WL 1
PB 24,12,0
SZ 8
ZV 156,96,88
TR 114,5,10
PB 25,12,0
MV 156,100,92
PB 26,12,1
PB 27,12,1
ZV 152,96,80
ds_write_b128 v23, v[24:27] offset:16384
MV 152,100,84
TR 116,4,11
ZV 148,96,72
TR 118,5,11
MV 148,100,76
ZV 144,96,64
MV 144,100,68
ZV 140,96,56
WV 17
MV 140,100,60
WL 0
s_barrier
v_mov_b32 v21, 1
s_mov_b64 exec, 1
FS
s_mov_b64 exec, -1
ZV 136,96,48
MV 136,100,52
TR 96,2,12
SZ 9
ZV 160,104,48
TR 98,3,12
MV 160,108,52
ZV 164,104,56
MV 164,108,60
TR 100,2,13
ZV 168,104,64
TR 102,3,13
MV 168,108,68
ZV 172,104,72
MV 172,108,76
ZV 176,104,80
MV 176,108,84
ZV 180,104,88
MV 180,108,92
TR 104,4,12
SZ 10
ZA 164,112,88
TR 106,5,12
MA 164,116,92
ZA 160,112,80
MA 160,116,84
TR 108,4,13
ZA 156,112,72
TR 110,5,13
MA 156,116,76
ZA 152,112,64
MA 152,116,68
ZA 148,112,56
MA 148,116,60
ZA 144,112,48
WV 6
MA 144,116,52
WL 4
TR 112,2,14
SZ 11
MV 40,96,0
TR 114,3,14
MV 40,100,4
s_add_u32 s44, s86, 151552
s_addc_u32 s45, s87, 0
LA 48,-4
MV 44,96,8
RV 6,23,18432
MV 44,100,12
TR 116,2,15
MV 48,96,16
TR 118,3,15
MV 48,100,20
MV 52,96,24
LA 52,-3
MV 52,100,28
WL 2
PB 6,12,0
PB 7,12,0
MV 56,96,32
PB 8,12,1
PB 9,12,1
MV 56,100,36
ds_write_b128 v23, v[6:9] offset:18432
MV 60,96,40
MV 60,100,44
TR 96,4,14
LA 56,-2
MV 84,104,40
TR 98,5,14
MV 84,108,44
MV 80,104,32
RV 24,23,20480
MV 80,108,36
TR 100,4,15
MV 76,104,24
TR 102,5,15
LA 60,-1
MV 76,108,28
QO 88,0
MV 72,104,16
MV 72,108,20
WL 2
PB 24,12,0
PB 25,12,0
MV 68,104,8
PB 26,12,1
PB 27,12,1
MV 68,108,12
LA 64,0
MV 64,104,0
ds_write_b128 v23, v[24:27] offset:20480
MV 64,108,4
TR 104,2,16
MV 88,112,0
TR 106,3,16
MV 88,116,4
MV 92,112,8
LA 68,1
MV 92,116,12
TR 108,2,17
MV 96,112,16
TR 110,3,17
MV 96,116,20
s_add_u32 s44, s44, 6144
s_addc_u32 s45, s45, 0
LA 72,-4
SZ 12
MV 100,112,24
RV 6,23,22528
MV 100,116,28
QO 90,4
MV 104,112,32
MV 104,116,36
MV 108,112,40
LA 76,-3
MV 108,116,44
TR 112,4,16
WL 1
PB 6,12,0
MA 140,96,40
TR 114,5,16
PB 7,12,0
MA 140,100,44
PB 8,12,1
PB 9,12,1
MA 136,96,32
ds_write_b128 v23, v[6:9] offset:22528
MA 136,100,36
TR 116,4,17
LA 80,-2
MA 132,96,24
TR 118,5,17
MA 132,100,28
MA 128,96,16
MA 128,100,20
MA 124,96,8
LA 84,-1
MA 124,100,12
WL 0
s_barrier
MA 120,96,0
.Lk2p2:
s_cbranch_execz .Lk2p6
MA 120,100,4
TR 96,2,18
MV 112,104,0
TR 98,3,18
MV 112,108,4
LA 88,0
MV 116,104,8
MV 116,108,12
TR 100,2,19
MV 120,104,16
TR 102,3,19
MV 120,108,20
MV 124,104,24
LA 92,1
MV 124,108,28
MV 128,104,32
MV 128,108,36
s_add_u32 m0, %[hm], 6144
SZ 13
MV 132,104,40
s_add_u32 s40, s64, 6144
s_addc_u32 s41, s65, 0
DH 0
MV 132,108,44
TR 104,4,18
MV 156,112,40
s_add_u32 m0, %[hm], 8192
TR 106,5,18
MV 156,116,44
s_add_u32 s40, s66, 0
s_addc_u32 s41, s67, 0
DH 0
MV 152,112,32
MV 152,116,36
TR 108,4,19
DH 2
MV 148,112,24
TR 110,5,19
MV 148,116,28
s_add_u32 m0, %[hm], 12288
v_xor_b32 v1, 0x100, v1
MV 144,112,16
s_add_u32 s40, s64, 0
s_addc_u32 s41, s65, 0
DH 0
MV 144,116,20
MV 140,112,8
MV 140,116,12
MV 136,112,0
MV 136,116,4
WL 4
TR 112,2,20
DH 2
MV 160,96,0
TR 114,3,20
MV 160,100,4
RV 24,23,24576
MV 164,96,8
s_add_u32 m0, %[hm], 16384
MV 164,100,12
TR 116,2,21
MV 168,96,16
TR 118,3,21
MV 168,100,20
s_add_u32 s40, s64, 4096
s_addc_u32 s41, s65, 0
DH 0
MV 172,96,24
WL 2
PB 24,13,0
PB 25,13,0
MV 172,100,28
PB 26,13,1
PB 27,13,1
MV 176,96,32
ds_write_b128 v23, v[24:27] offset:24576
MV 176,100,36
MV 180,96,40
MV 180,100,44
TR 96,4,20
MA 164,104,40
TR 98,5,20
MA 164,108,44
MA 160,104,32
RV 6,23,26624
MA 160,108,36
TR 100,4,21
SZ 14
MA 156,104,24
TR 102,5,21
MA 156,108,28
MA 152,104,16
MA 152,108,20
WL 2
PB 6,13,0
PB 7,13,0
MA 148,104,8
PB 8,13,1
PB 9,13,1
MA 148,108,12
ds_write_b128 v23, v[6:9] offset:26624
MA 144,104,0
MA 144,108,4
TR 104,2,22
ZV 184,112,0
TR 106,3,22
MV 184,116,4
ZV 188,112,8
MV 188,116,12
TR 108,2,23
ZV 192,112,16
TR 110,3,23
MV 192,116,20
RV 24,23,28672
ZV 196,112,24
MV 196,116,28
ZV 200,112,32
MV 200,116,36
ZV 204,112,40
WL 0
PB 24,13,0
PB 25,13,0
MV 204,116,44
TR 112,4,22
PB 26,13,1
ZV 228,96,40
TR 114,5,22
PB 27,13,1
MV 228,100,44
ds_write_b128 v23, v[24:27] offset:28672
ZV 224,96,32
MV 224,100,36
TR 116,4,23
ZV 220,96,24
TR 118,5,23
MV 220,100,28
ZV 216,96,16
MV 216,100,20
SZ 15
ZV 212,96,8
MV 212,100,12
WL 0
s_barrier
ZV 208,96,0
MV 208,100,4
TR 96,2,24
ZV 232,104,0
TR 98,3,24
MV 232,108,4
ZV 236,104,8
MV 236,108,12
TR 100,2,25
ZV 240,104,16
TR 102,3,25
MV 240,108,20
ZV 244,104,24
MV 244,108,28
ZV 248,104,32
MV 248,108,36
ZV 252,104,40
MV 252,108,44
TR 104,4,24
ZA 188,112,40
TR 106,5,24
MA 188,116,44
ZA 184,112,32
MA 184,116,36
TR 108,4,25
ZA 180,112,24
TR 110,5,25
MA 180,116,28
ZA 176,112,16
v_lshlrev_b32 v22, 4, v0
MA 176,116,20
FL
ZA 172,112,8
MA 172,116,12
ZA 168,112,0
WV 7
MA 168,116,4
WL 4
TR 112,2,26
SZ 16
MV 40,96,48
TR 114,3,26
MV 40,100,52
RV 6,23,30720
MV 44,96,56
MV 44,100,60
TR 116,2,27
MV 48,96,64
TR 118,3,27
MV 48,100,68
MV 52,96,72
WL 2
PB 6,13,0
PB 7,13,0
MV 52,100,76
PB 8,13,1
PB 9,13,1
MV 56,96,80
ds_write_b128 v23, v[6:9] offset:30720
MV 56,100,84
RV 24,23,32768
MV 60,96,88
MV 60,100,92
TR 96,4,26
MV 84,104,88
TR 98,5,26
MV 84,108,92
MV 80,104,80
WL 2
PB 24,13,0
PB 25,13,0
MV 80,108,84
TR 100,4,27
PB 26,13,1
MV 76,104,72
)"
R"(TR 102,5,27
PB 27,13,1
MV 76,108,76
ds_write_b128 v23, v[24:27] offset:32768
MV 72,104,64
RV 6,23,34816
MV 72,108,68
MV 68,104,56
MV 68,108,60
MV 64,104,48
MV 64,108,52
TR 104,2,28
WL 1
PB 6,13,0
MV 88,112,48
TR 106,3,28
PB 7,13,0
MV 88,116,52
PB 8,13,1
PB 9,13,1
MV 92,112,56
ds_write_b128 v23, v[6:9] offset:34816
MV 92,116,60
TR 108,2,29
MV 96,112,64
TR 110,3,29
MV 96,116,68
MV 100,112,72
MV 100,116,76
MV 104,112,80
MV 104,116,84
MV 108,112,88
MV 108,116,92
TR 112,4,28
MA 140,96,88
TR 114,5,28
MA 140,100,92
MA 136,96,80
WV 0
v_cmp_ne_u32 vcc, 1, v21
MA 136,100,84
TR 116,4,29
MA 132,96,72
TR 118,5,29
MA 132,100,76
FB
MA 128,96,64
MA 128,100,68
MA 124,96,56
MA 124,100,60
WL 0
s_barrier
MA 120,96,48
s_add_u32 s44, s84, 200704
s_addc_u32 s45, s85, 0
LA 0,-4
MA 120,100,52
TR 96,2,30
MV 112,104,48
TR 98,3,30
MV 112,108,52
MV 116,104,56
MV 116,108,60
TR 100,2,31
LA 4,-3
MV 120,104,64
TR 102,3,31
MV 120,108,68
MV 124,104,72
MV 124,108,76
MV 128,104,80
LA 8,-2
MV 128,108,84
MV 132,104,88
MV 132,108,92
TR 104,4,30
MV 156,112,88
TR 106,5,30
MV 156,116,92
LA 12,-1
MV 152,112,80
MV 152,116,84
TR 108,4,31
MV 148,112,72
TR 110,5,31
MV 148,116,76
MV 144,112,64
LA 16,0
MV 144,116,68
MV 140,112,56
MV 140,116,60
MV 136,112,48
MV 136,116,52
WL 4
TR 112,2,32
LA 20,1
SZ 17
MV 160,96,48
TR 114,3,32
MV 160,100,52
MV 164,96,56
s_add_u32 s44, s44, 6144
s_addc_u32 s45, s45, 0
LA 24,-4
MV 164,100,60
TR 116,2,33
MV 168,96,64
TR 118,3,33
MV 168,100,68
MV 172,96,72
MV 172,100,76
LA 28,-3
MV 176,96,80
MV 176,100,84
MV 180,96,88
MV 180,100,92
WL 4
TR 96,4,32
MA 164,104,88
TR 98,5,32
LA 32,-2
MA 164,108,92
MA 160,104,80
MA 160,108,84
TR 100,4,33
MA 156,104,72
TR 102,5,33
MA 156,108,76
LA 36,-1
MA 152,104,64
MA 152,108,68
MA 148,104,56
MA 148,108,60
MA 144,104,48
LA 40,0
MA 144,108,52
WL 4
TR 104,2,34
MV 184,112,48
TR 106,3,34
MV 184,116,52
MV 188,112,56
MV 188,116,60
TR 108,2,35
LA 44,1
MV 192,112,64
TR 110,3,35
MV 192,116,68
MV 196,112,72
s_add_u32 m0, %[hm], 18432
MV 196,116,76
s_add_u32 s40, s64, 6144
s_addc_u32 s41, s65, 0
DH 0
MV 200,112,80
MV 200,116,84
MV 204,112,88
s_add_u32 m0, %[hm], 20480
MV 204,116,92
WL 4
TR 112,4,34
MV 228,96,88
TR 114,5,34
MV 228,100,92
s_add_u32 s40, s66, 0
s_addc_u32 s41, s67, 0
DH 0
MV 224,96,80
MV 224,100,84
TR 116,4,35
MV 220,96,72
TR 118,5,35
MV 220,100,76
MV 216,96,64
DH 2
MV 216,100,68
MV 212,96,56
MV 212,100,60
WL 0
s_barrier
MV 208,96,48
MV 208,100,52
TR 96,2,0
MV 232,104,48
TR 98,3,0
MV 232,108,52
s_mov_b64 exec, s[76:77]
global_load_dwordx4 v[6:9], v0, s[78:79] sc1
s_mov_b64 exec, -1
MV 236,104,56
MV 236,108,60
TR 100,2,1
MV 240,104,64
TR 102,3,1
MV 240,108,68
MV 244,104,72
MV 244,108,76
MV 248,104,80
MV 248,108,84
MV 252,104,88
MV 252,108,92
TR 104,4,0
MA 188,112,88
TR 106,5,0
MA 188,116,92
MA 184,112,80
MA 184,116,84
TR 108,4,1
MA 180,112,72
TR 110,5,1
MA 180,116,76
MA 176,112,64
MA 176,116,68
MA 172,112,56
MA 172,116,60
MA 168,112,48
WV 4
MA 168,116,52
WL 4
TR 112,2,2
SZ 18
MV 40,96,0
TR 114,3,2
MV 40,100,4
s_add_u32 s44, s84, 249856
s_addc_u32 s45, s85, 0
LA 48,-4
MV 44,96,8
MV 44,100,12
TR 116,2,3
MV 48,96,16
TR 118,3,3
MV 48,100,20
MV 52,96,24
LA 52,-3
MV 52,100,28
MV 56,96,32
MV 56,100,36
MV 60,96,40
MV 60,100,44
WL 4
TR 96,4,2
LA 56,-2
MV 84,104,40
TR 98,5,2
MV 84,108,44
MV 80,104,32
MV 80,108,36
TR 100,4,3
MV 76,104,24
TR 102,5,3
LA 60,-1
MV 76,108,28
MV 72,104,16
MV 72,108,20
MV 68,104,8
MV 68,108,12
LA 64,0
MV 64,104,0
MV 64,108,4
WL 4
TR 104,2,4
MV 88,112,0
TR 106,3,4
MV 88,116,4
MV 92,112,8
LA 68,1
MV 92,116,12
TR 108,2,5
MV 96,112,16
TR 110,3,5
MV 96,116,20
s_add_u32 s44, s44, 6144
s_addc_u32 s45, s45, 0
LA 72,-4
MV 100,112,24
MV 100,116,28
MV 104,112,32
MV 104,116,36
MV 108,112,40
LA 76,-3
MV 108,116,44
WL 4
TR 112,4,4
MA 140,96,40
TR 114,5,4
MA 140,100,44
MA 136,96,32
MA 136,100,36
TR 116,4,5
LA 80,-2
MA 132,96,24
TR 118,5,5
MA 132,100,28
MA 128,96,16
MA 128,100,20
MA 124,96,8
LA 84,-1
MA 124,100,12
WL 0
s_barrier
MA 120,96,0
MA 120,100,4
TR 96,2,6
MV 112,104,0
TR 98,3,6
MV 112,108,4
LA 88,0
MV 116,104,8
MV 116,108,12
TR 100,2,7
MV 120,104,16
TR 102,3,7
MV 120,108,20
.Lk2p3:
s_cbranch_execz .Lk2p7
MV 124,104,24
LA 92,1
MV 124,108,28
MV 128,104,32
MV 128,108,36
s_mov_b32 m0, %[pm]
MV 132,104,40
s_add_u32 s46, %[pw], 131072
MV 132,108,44
TR 104,4,6
MV 156,112,40
TR 106,5,6
MV 156,116,44
s_add_u32 s46, s46, s84
s_addc_u32 s47, s85, 0
DG
MV 152,112,32
MV 152,116,36
TR 108,4,7
MV 148,112,24
s_add_u32 m0, %[hm], 24576
TR 110,5,7
v_xor_b32 v1, 0x300, v1
MV 148,116,28
s_add_u32 s40, s64, 0
s_addc_u32 s41, s65, 0
DH 0
MV 144,112,16
MV 144,116,20
MV 140,112,8
MV 140,116,12
MV 136,112,0
DH 2
MV 136,116,4
WL 4
TR 112,2,8
MV 160,96,0
TR 114,3,8
MV 160,100,4
s_add_u32 m0, %[hm], 28672
MV 164,96,8
s_add_u32 s40, s64, 4096
s_addc_u32 s41, s65, 0
DH 0
MV 164,100,12
TR 116,2,9
MV 168,96,16
WV 16
TR 118,3,9
MV 168,100,20
s_add_u32 m0, %[wm], 0
v_or_b32 v6, v6, v7
s_mov_b64 exec, s[76:77]
v_cmp_ne_u32 %[flg], 0, v6
s_mov_b64 exec, -1
MV 172,96,24
s_add_u32 s40, s88, 0
MV 172,100,28
s_add_u32 s40, s40, s68
s_addc_u32 s41, s69, 0
DX 0
MV 176,96,32
MV 176,100,36
MV 180,96,40
MV 180,100,44
WL 4
TR 96,4,8
MA 164,104,40
TR 98,5,8
DX 1
MA 164,108,44
MA 160,104,32
MA 160,108,36
TR 100,4,9
MA 156,104,24
TR 102,5,9
MA 156,108,28
DX 2
MA 152,104,16
MA 152,108,20
MA 148,104,8
MA 148,108,12
s_add_u32 m0, %[hm], 30720
MA 144,104,0
)"
R"(s_add_u32 s40, s64, 6144
s_addc_u32 s41, s65, 0
DH 0
MA 144,108,4
WL 4
TR 104,2,10
MV 184,112,0
TR 106,3,10
MV 184,116,4
s_add_u32 m0, %[hm], 32768
MV 188,112,8
s_add_u32 s40, s66, 0
s_addc_u32 s41, s67, 0
DH 0
MV 188,116,12
TR 108,2,11
MV 192,112,16
TR 110,3,11
MV 192,116,20
MV 196,112,24
MV 196,116,28
DH 2
MV 200,112,32
MV 200,116,36
MV 204,112,40
MV 204,116,44
WL 4
TR 112,4,10
MV 228,96,40
TR 114,5,10
MV 228,100,44
MV 224,96,32
MV 224,100,36
TR 116,4,11
MV 220,96,24
TR 118,5,11
MV 220,100,28
MV 216,96,16
MV 216,100,20
MV 212,96,8
MV 212,100,12
WL 0
s_barrier
MV 208,96,0
MV 208,100,4
TR 96,2,12
MV 232,104,0
TR 98,3,12
MV 232,108,4
MV 236,104,8
MV 236,108,12
TR 100,2,13
MV 240,104,16
TR 102,3,13
MV 240,108,20
MV 244,104,24
MV 244,108,28
MV 248,104,32
MV 248,108,36
MV 252,104,40
MV 252,108,44
TR 104,4,12
MA 188,112,40
TR 106,5,12
MA 188,116,44
MA 184,112,32
MA 184,116,36
TR 108,4,13
MA 180,112,24
TR 110,5,13
MA 180,116,28
MA 176,112,16
MA 176,116,20
MA 172,112,8
MA 172,116,12
MA 168,112,0
WV 10
MA 168,116,4
WL 4
TR 112,2,14
SZ 19
MV 40,96,48
TR 114,3,14
MV 40,100,52
s_add_u32 s44, s84, 299008
s_addc_u32 s45, s85, 0
LA 0,-4
MV 44,96,56
MV 44,100,60
TR 116,2,15
MV 48,96,64
TR 118,3,15
MV 48,100,68
MV 52,96,72
LA 4,-3
MV 52,100,76
MV 56,96,80
MV 56,100,84
MV 60,96,88
MV 60,100,92
WL 4
TR 96,4,14
LA 8,-2
MV 84,104,88
TR 98,5,14
MV 84,108,92
MV 80,104,80
MV 80,108,84
TR 100,4,15
MV 76,104,72
TR 102,5,15
LA 12,-1
MV 76,108,76
MV 72,104,64
MV 72,108,68
MV 68,104,56
MV 68,108,60
LA 16,0
MV 64,104,48
MV 64,108,52
WL 4
TR 104,2,16
MV 88,112,48
TR 106,3,16
MV 88,116,52
MV 92,112,56
LA 20,1
MV 92,116,60
TR 108,2,17
MV 96,112,64
TR 110,3,17
MV 96,116,68
s_add_u32 s44, s44, 6144
s_addc_u32 s45, s45, 0
MV 100,112,72
MV 100,116,76
LA 24,-4
MV 104,112,80
MV 104,116,84
MV 108,112,88
MV 108,116,92
WL 4
TR 112,4,16
MA 140,96,88
TR 114,5,16
MA 140,100,92
LA 28,-3
MA 136,96,80
MA 136,100,84
TR 116,4,17
MA 132,96,72
TR 118,5,17
MA 132,100,76
MA 128,96,64
LA 32,-2
MA 128,100,68
MA 124,96,56
WV 12
MA 124,100,60
WL 0
s_barrier
ds_read_b64 v[12:13], v18 offset:0
MA 120,96,48
ds_read_b32 v16, v19 offset:0
MA 120,100,52
TR 96,2,18
MV 112,104,48
TR 98,3,18
MV 112,108,52
LA 36,-1
MV 116,104,56
ds_read_b64 v[14:15], v18 offset:768
MV 116,108,60
TR 100,2,19
MV 120,104,64
TR 102,3,19
MV 120,108,68
ds_read_b32 v17, v19 offset:768
MV 124,104,72
LA 40,0
MV 124,108,76
MV 128,104,80
MV 128,108,84
MV 132,104,88
WL 6
UL 6,12
UH 7,12
MV 132,108,92
TR 104,4,18
UL 8,13
MV 156,112,88
TR 106,5,18
AD 40,6
MV 156,116,92
LA 44,1
MV 152,112,80
AD 44,7
AD 48,8
MV 152,116,84
TR 108,4,19
UH 6,13
MV 148,112,72
s_mov_b32 m0, %[pm]
TR 110,5,19
UL 7,16
MV 148,116,76
s_add_u32 s46, %[pw], 262144
UH 8,16
MV 144,112,64
s_add_u32 s46, s46, s84
s_addc_u32 s47, s85, 0
MV 144,116,68
DG
MV 140,112,56
ds_read_b64 v[12:13], v18 offset:1536
MV 140,116,60
ds_read_b32 v16, v19 offset:1536
AD 52,6
MV 136,112,48
s_add_u32 m0, %[hm], 0
v_xor_b32 v1, 0x100, v1
AD 56,7
MV 136,116,52
WL 6
TR 112,2,20
AD 60,8
MV 160,96,48
TR 114,3,20
UL 6,14
MV 160,100,52
s_add_u32 s40, s64, 0
s_addc_u32 s41, s65, 0
UH 7,14
MV 164,96,56
DH 0
MV 164,100,60
TR 116,2,21
UL 8,15
MV 168,96,64
TR 118,3,21
AD 41,6
MV 168,100,68
AD 45,7
AD 49,8
MV 172,96,72
UH 6,15
UL 7,17
MV 172,100,76
DH 2
MV 176,96,80
UH 8,17
MV 176,100,84
ds_read_b64 v[14:15], v18 offset:2304
MV 180,96,88
s_add_u32 m0, %[hm], 4096
ds_read_b32 v17, v19 offset:2304
AD 53,6
MV 180,100,92
WL 6
TR 96,4,20
AD 57,7
MA 164,104,88
TR 98,5,20
AD 61,8
MA 164,108,92
s_add_u32 s40, s64, 4096
s_addc_u32 s41, s65, 0
UL 6,12
MA 160,104,80
DH 0
MA 160,108,84
TR 100,4,21
UH 7,12
MA 156,104,72
TR 102,5,21
UL 8,13
MA 156,108,76
s_add_u32 m0, %[wm], 12288
AD 42,6
AD 46,7
MA 152,104,64
s_add_u32 s40, s88, 98304
AD 50,8
UH 6,13
MA 152,108,68
s_add_u32 s40, s40, s68
s_addc_u32 s41, s69, 0
UL 7,16
MA 148,104,56
DX 0
MA 148,108,60
UH 8,16
AD 54,6
MA 144,104,48
AD 58,7
AD 62,8
MA 144,108,52
WL 3
TR 104,2,22
UL 6,14
MV 184,112,48
TR 106,3,22
UH 7,14
MV 184,116,52
DX 1
MV 188,112,56
UL 8,15
AD 43,6
MV 188,116,60
TR 108,2,23
AD 47,7
MV 192,112,64
TR 110,3,23
AD 51,8
MV 192,116,68
UH 6,15
UL 7,17
MV 196,112,72
DX 2
MV 196,116,76
UH 8,17
AD 55,6
MV 200,112,80
AD 59,7
AD 63,8
MV 200,116,84
MV 204,112,88
MV 204,116,92
WL 4
TR 112,4,22
MV 228,96,88
TR 114,5,22
MV 228,100,92
MV 224,96,80
MV 224,100,84
TR 116,4,23
MV 220,96,72
TR 118,5,23
MV 220,100,76
MV 216,96,64
MV 216,100,68
MV 212,96,56
MV 212,100,60
MV 208,96,48
MV 208,100,52
WL 4
TR 96,2,24
MV 232,104,48
TR 98,3,24
MV 232,108,52
MV 236,104,56
MV 236,108,60
TR 100,2,25
MV 240,104,64
TR 102,3,25
MV 240,108,68
MV 244,104,72
MV 244,108,76
MV 248,104,80
MV 248,108,84
MV 252,104,88
MV 252,108,92
WL 4
TR 104,4,24
MA 188,112,88
TR 106,5,24
MA 188,116,92
MA 184,112,80
MA 184,116,84
TR 108,4,25
MA 180,112,72
TR 110,5,25
MA 180,116,76
MA 176,112,64
MA 176,116,68
MA 172,112,56
MA 172,116,60
MA 168,112,48
WV 7
MA 168,116,52
WL 4
TR 112,2,26
MV 40,96,0
TR 114,3,26
MV 40,100,4
s_add_u32 s44, s84, 348160
s_addc_u32 s45, s85, 0
MV 44,96,8
LA 48,-4
MV 44,100,12
TR 116,2,27
MV 48,96,16
TR 118,3,27
MV 48,100,20
MV 52,96,24
MV 52,100,28
LA 52,-3
MV 56,96,32
MV 56,100,36
MV 60,96,40
)"
R"(MV 60,100,44
WL 4
TR 96,4,26
MV 84,104,40
TR 98,5,26
MV 84,108,44
LA 56,-2
MV 80,104,32
MV 80,108,36
TR 100,4,27
MV 76,104,24
TR 102,5,27
MV 76,108,28
MV 72,104,16
LA 60,-1
MV 72,108,20
MV 68,104,8
MV 68,108,12
MV 64,104,0
MV 64,108,4
WL 4
TR 104,2,28
MV 88,112,0
TR 106,3,28
MV 88,116,4
LA 64,0
MV 92,112,8
MV 92,116,12
TR 108,2,29
MV 96,112,16
TR 110,3,29
MV 96,116,20
MV 100,112,24
LA 68,1
MV 100,116,28
MV 104,112,32
MV 104,116,36
s_add_u32 s44, s44, 6144
s_addc_u32 s45, s45, 0
MV 108,112,40
MV 108,116,44
WL 4
TR 112,4,28
MA 140,96,40
TR 114,5,28
MA 140,100,44
LA 72,-4
MA 136,96,32
MA 136,100,36
TR 116,4,29
MA 132,96,24
TR 118,5,29
MA 132,100,28
MA 128,96,16
LA 76,-3
MA 128,100,20
MA 124,96,8
WV 8
MA 124,100,12
WL 0
s_barrier
ds_read_b64 v[12:13], v18 offset:12288
MA 120,96,0
ds_read_b32 v16, v19 offset:12288
MA 120,100,4
TR 96,2,30
MV 112,104,0
TR 98,3,30
MV 112,108,4
LA 80,-2
MV 116,104,8
ds_read_b64 v[14:15], v18 offset:13056
MV 116,108,12
TR 100,2,31
MV 120,104,16
TR 102,3,31
MV 120,108,20
ds_read_b32 v17, v19 offset:13056
MV 124,104,24
LA 84,-1
MV 124,108,28
.Lk2p4:
s_cbranch_execz .Lk2p8
MV 128,104,32
MV 128,108,36
MV 132,104,40
WL 6
UL 6,12
UH 7,12
MV 132,108,44
TR 104,4,30
UL 8,13
MV 156,112,40
TR 106,5,30
AD 64,6
MV 156,116,44
LA 88,0
MV 152,112,32
AD 68,7
AD 72,8
MV 152,116,36
TR 108,4,31
UH 6,13
MV 148,112,24
TR 110,5,31
UL 7,16
MV 148,116,28
UH 8,16
MV 144,112,16
LA 92,1
MV 144,116,20
ds_read_b64 v[12:13], v18 offset:13824
MV 140,112,8
WL 5
ds_read_b32 v16, v19 offset:13824
AD 76,6
MV 140,116,12
s_add_u32 m0, %[hm], 6144
AD 80,7
AD 84,8
MV 136,112,0
s_add_u32 s40, s64, 6144
s_addc_u32 s41, s65, 0
UL 6,14
MV 136,116,4
TR 112,2,32
UH 7,14
MV 160,96,0
TR 114,3,32
UL 8,15
MV 160,100,4
DH 0
MV 164,96,8
AD 65,6
AD 69,7
MV 164,100,12
TR 116,2,33
AD 73,8
MV 168,96,16
s_add_u32 m0, %[hm], 8192
TR 118,3,33
UH 6,15
MV 168,100,20
s_add_u32 s40, s66, 0
s_addc_u32 s41, s67, 0
UL 7,17
MV 172,96,24
DH 0
MV 172,100,28
UH 8,17
MV 176,96,32
ds_read_b64 v[14:15], v18 offset:14592
MV 176,100,36
ds_read_b32 v17, v19 offset:14592
AD 77,6
MV 180,96,40
AD 81,7
AD 85,8
MV 180,100,44
WL 6
TR 96,4,32
UL 6,12
MA 164,104,40
TR 98,5,32
UH 7,12
MA 164,108,44
DH 2
MA 160,104,32
UL 8,13
AD 66,6
MA 160,108,36
TR 100,4,33
AD 70,7
MA 156,104,24
TR 102,5,33
AD 74,8
MA 156,108,28
s_mov_b32 m0, %[pm]
UH 6,13
UL 7,16
MA 152,104,16
s_add_u32 s46, %[pw], 393216
UH 8,16
AD 78,6
MA 152,108,20
s_add_u32 s46, s46, s84
s_addc_u32 s47, s85, 0
AD 82,7
MA 148,104,8
DG
MA 148,108,12
WL 4
AD 86,8
UL 6,14
MA 144,104,0
UH 7,14
UL 8,15
MA 144,108,4
TR 104,2,34
AD 67,6
MV 184,112,0
TR 106,3,34
AD 71,7
MV 184,116,4
s_bitcmp1_b32 s48, 3
s_cselect_b64 s[64:65], s[58:59], s[56:57]
AD 75,8
MV 188,112,8
s_bitcmp1_b32 s48, 19
s_cselect_b32 s40, 16384, 0
UH 6,15
MV 188,116,12
TR 108,2,35
UL 7,17
MV 192,112,16
TR 110,3,35
UH 8,17
MV 192,116,20
s_add_u32 s64, s64, s40
s_addc_u32 s65, s65, 0
AD 79,6
MV 196,112,24
s_add_u32 m0, %[hm], 12288
v_xor_b32 v1, 0x700, v1
AD 83,7
MV 196,116,28
s_add_u32 s40, s64, 786432
s_addc_u32 s41, s65, 0
AD 87,8
MV 200,112,32
DH 0
MV 200,116,36
MV 204,112,40
MV 204,116,44
WL 4
TR 112,4,34
MV 228,96,40
TR 114,5,34
MV 228,100,44
DH 2
MV 224,96,32
MV 224,100,36
TR 116,4,35
MV 220,96,24
s_add_u32 m0, %[hm], 16384
TR 118,5,35
MV 220,100,28
s_add_u32 s40, s64, 790528
s_addc_u32 s41, s65, 0
MV 216,96,16
DH 0
MV 216,100,20
MV 212,96,8
MV 212,100,12
MV 208,96,0
MV 208,100,4
WL 4
TR 96,2,0
MV 232,104,0
TR 98,3,0
MV 232,108,4
MV 236,104,8
MV 236,108,12
TR 100,2,1
MV 240,104,16
TR 102,3,1
MV 240,108,20
MV 244,104,24
MV 244,108,28
MV 248,104,32
MV 248,108,36
MV 252,104,40
MV 252,108,44
WL 4
TR 104,4,0
MA 188,112,40
TR 106,5,0
MA 188,116,44
MA 184,112,32
MA 184,116,36
TR 108,4,1
MA 180,112,24
TR 110,5,1
MA 180,116,28
MA 176,112,16
MA 176,116,20
MA 172,112,8
MA 172,116,12
MA 168,112,0
WV 7
MA 168,116,4
WL 4
TR 112,2,2
MV 40,96,48
TR 114,3,2
MV 40,100,52
s_add_u32 s44, s84, 397312
s_addc_u32 s45, s85, 0
MV 44,96,56
LA 0,-4
MV 44,100,60
TR 116,2,3
MV 48,96,64
TR 118,3,3
MV 48,100,68
MV 52,96,72
MV 52,100,76
LA 4,-3
MV 56,96,80
MV 56,100,84
MV 60,96,88
MV 60,100,92
WL 4
TR 96,4,2
MV 84,104,88
TR 98,5,2
MV 84,108,92
LA 8,-2
MV 80,104,80
MV 80,108,84
TR 100,4,3
MV 76,104,72
TR 102,5,3
MV 76,108,76
MV 72,104,64
LA 12,-1
MV 72,108,68
MV 68,104,56
MV 68,108,60
MV 64,104,48
MV 64,108,52
WL 4
TR 104,2,4
MV 88,112,48
TR 106,3,4
MV 88,116,52
LA 16,0
MV 92,112,56
MV 92,116,60
TR 108,2,5
MV 96,112,64
TR 110,3,5
MV 96,116,68
MV 100,112,72
LA 20,1
MV 100,116,76
MV 104,112,80
MV 104,116,84
s_add_u32 s44, s44, 6144
s_addc_u32 s45, s45, 0
MV 108,112,88
MV 108,116,92
WL 4
TR 112,4,4
MA 140,96,88
TR 114,5,4
MA 140,100,92
LA 24,-4
MA 136,96,80
MA 136,100,84
TR 116,4,5
MA 132,96,72
TR 118,5,5
MA 132,100,76
MA 128,96,64
LA 28,-3
MA 128,100,68
MA 124,96,56
WV 8
MA 124,100,60
WL 0
s_barrier
MA 120,96,48
MA 120,100,52
TR 96,2,6
MV 112,104,48
TR 98,3,6
MV 112,108,52
LA 32,-2
MV 116,104,56
MV 116,108,60
TR 100,2,7
MV 120,104,64
TR 102,3,7
MV 120,108,68
MV 124,104,72
LA 36,-1
MV 124,108,76
MV 128,104,80
MV 128,108,84
MV 132,104,88
MV 132,108,92
TR 104,4,6
MV 156,112,88
TR 106,5,6
MV 156,116,92
LA 40,0
MV 152,112,80
MV 152,116,84
TR 108,4,7
)"
R"(MV 148,112,72
TR 110,5,7
MV 148,116,76
MV 144,112,64
LA 44,1
MV 144,116,68
MV 140,112,56
MV 140,116,60
s_add_u32 m0, %[hm], 18432
MV 136,112,48
s_add_u32 s40, s64, 792576
s_addc_u32 s41, s65, 0
MV 136,116,52
WL 4
TR 112,2,8
MV 160,96,48
TR 114,3,8
MV 160,100,52
DH 0
MV 164,96,56
MV 164,100,60
TR 116,2,9
MV 168,96,64
TR 118,3,9
MV 168,100,68
s_bitcmp1_b32 s49, 3
s_cselect_b64 s[66:67], s[62:63], s[60:61]
MV 172,96,72
s_add_u32 m0, %[hm], 20480
MV 172,100,76
s_add_u32 s40, s66, 786432
s_addc_u32 s41, s67, 0
MV 176,96,80
DH 0
MV 176,100,84
MV 180,96,88
MV 180,100,92
WL 4
TR 96,4,8
MA 164,104,88
TR 98,5,8
MA 164,108,92
DH 2
MA 160,104,80
MA 160,108,84
TR 100,4,9
MA 156,104,72
TR 102,5,9
MA 156,108,76
s_mov_b32 m0, %[pm]
MA 152,104,64
s_add_u32 s46, %[pw], 524288
MA 152,108,68
s_add_u32 s46, s46, s84
s_addc_u32 s47, s85, 0
MA 148,104,56
DG
MA 148,108,60
MA 144,104,48
MA 144,108,52
WL 4
TR 104,2,10
MV 184,112,48
s_add_u32 m0, %[hm], 24576
TR 106,3,10
v_xor_b32 v1, 0x100, v1
MV 184,116,52
s_add_u32 s40, s64, 786432
s_addc_u32 s41, s65, 0
MV 188,112,56
DH 0
MV 188,116,60
TR 108,2,11
MV 192,112,64
TR 110,3,11
MV 192,116,68
MV 196,112,72
MV 196,116,76
DH 2
MV 200,112,80
MV 200,116,84
MV 204,112,88
s_add_u32 m0, %[hm], 28672
MV 204,116,92
WL 4
TR 112,4,10
MV 228,96,88
TR 114,5,10
MV 228,100,92
s_add_u32 s40, s64, 790528
s_addc_u32 s41, s65, 0
MV 224,96,80
DH 0
MV 224,100,84
TR 116,4,11
MV 220,96,72
TR 118,5,11
MV 220,100,76
s_add_u32 m0, %[wm], 0
MV 216,96,64
s_add_u32 s40, s89, 0
MV 216,100,68
s_add_u32 s40, s40, s68
s_addc_u32 s41, s69, 0
MV 212,96,56
DX 0
MV 212,100,60
MV 208,96,48
MV 208,100,52
WL 4
TR 96,2,12
MV 232,104,48
TR 98,3,12
MV 232,108,52
DX 1
MV 236,104,56
MV 236,108,60
TR 100,2,13
MV 240,104,64
TR 102,3,13
MV 240,108,68
MV 244,104,72
DX 2
MV 244,108,76
MV 248,104,80
MV 248,108,84
MV 252,104,88
s_add_u32 m0, %[hm], 30720
MV 252,108,92
WL 4
TR 104,4,12
MA 188,112,88
TR 106,5,12
MA 188,116,92
s_add_u32 s40, s64, 792576
s_addc_u32 s41, s65, 0
MA 184,112,80
MA 184,116,84
TR 108,4,13
MA 180,112,72
TR 110,5,13
MA 180,116,76
MA 176,112,64
MA 176,116,68
MA 172,112,56
MA 172,116,60
MA 168,112,48
WV 10
MA 168,116,52
WL 4
TR 112,2,14
MV 40,96,0
TR 114,3,14
MV 40,100,4
s_add_u32 s44, s84, 446464
s_addc_u32 s45, s85, 0
MV 44,96,8
LA 48,-4
MV 44,100,12
TR 116,2,15
MV 48,96,16
TR 118,3,15
MV 48,100,20
MV 52,96,24
MV 52,100,28
LA 52,-3
MV 56,96,32
MV 56,100,36
MV 60,96,40
MV 60,100,44
WL 4
TR 96,4,14
MV 84,104,40
TR 98,5,14
MV 84,108,44
LA 56,-2
MV 80,104,32
MV 80,108,36
TR 100,4,15
MV 76,104,24
TR 102,5,15
MV 76,108,28
MV 72,104,16
LA 60,-1
MV 72,108,20
MV 68,104,8
MV 68,108,12
MV 64,104,0
MV 64,108,4
WL 4
TR 104,2,16
MV 88,112,0
TR 106,3,16
MV 88,116,4
LA 64,0
MV 92,112,8
MV 92,116,12
TR 108,2,17
MV 96,112,16
TR 110,3,17
MV 96,116,20
MV 100,112,24
LA 68,1
MV 100,116,28
MV 104,112,32
MV 104,116,36
s_add_u32 s44, s44, 6144
s_addc_u32 s45, s45, 0
MV 108,112,40
MV 108,116,44
WL 4
TR 112,4,16
MA 140,96,40
TR 114,5,16
MA 140,100,44
LA 72,-4
MA 136,96,32
MA 136,100,36
TR 116,4,17
MA 132,96,24
TR 118,5,17
MA 132,100,28
MA 128,96,16
LA 76,-3
MA 128,100,20
MA 124,96,8
WV 8
MA 124,100,12
WL 0
s_barrier
ds_read_b64 v[12:13], v18 offset:0
MA 120,96,0
ds_read_b32 v16, v19 offset:0
MA 120,100,4
TR 96,2,18
MV 112,104,0
TR 98,3,18
MV 112,108,4
LA 80,-2
MV 116,104,8
ds_read_b64 v[14:15], v18 offset:768
MV 116,108,12
TR 100,2,19
MV 120,104,16
TR 102,3,19
MV 120,108,20
ds_read_b32 v17, v19 offset:768
MV 124,104,24
LA 84,-1
MV 124,108,28
.Lk2p5:
s_cbranch_execz .Lk2r0
MV 128,104,32
MV 128,108,36
MV 132,104,40
WL 6
UL 6,12
UH 7,12
MV 132,108,44
TR 104,4,18
UL 8,13
MV 156,112,40
TR 106,5,18
AD 88,6
MV 156,116,44
LA 88,0
MV 152,112,32
AD 92,7
AD 96,8
MV 152,116,36
TR 108,4,19
UH 6,13
MV 148,112,24
TR 110,5,19
UL 7,16
MV 148,116,28
UH 8,16
MV 144,112,16
LA 92,1
MV 144,116,20
ds_read_b64 v[12:13], v18 offset:1536
MV 140,112,8
ds_read_b32 v16, v19 offset:1536
AD 100,6
MV 140,116,12
AD 104,7
AD 108,8
MV 136,112,0
WL 6
UL 6,14
UH 7,14
MV 136,116,4
TR 112,2,20
UL 8,15
MV 160,96,0
TR 114,3,20
AD 89,6
MV 160,100,4
DH 0
MV 164,96,8
AD 93,7
AD 97,8
MV 164,100,12
TR 116,2,21
UH 6,15
MV 168,96,16
s_add_u32 m0, %[hm], 32768
TR 118,3,21
UL 7,17
MV 168,100,20
s_add_u32 s40, s66, 786432
s_addc_u32 s41, s67, 0
UH 8,17
MV 172,96,24
DH 0
MV 172,100,28
ds_read_b64 v[14:15], v18 offset:2304
MV 176,96,32
ds_read_b32 v17, v19 offset:2304
AD 101,6
MV 176,100,36
AD 105,7
AD 109,8
MV 180,96,40
WL 6
UL 6,12
UH 7,12
MV 180,100,44
TR 96,4,20
UL 8,13
MA 164,104,40
TR 98,5,20
AD 90,6
MA 164,108,44
DH 2
MA 160,104,32
AD 94,7
AD 98,8
MA 160,108,36
TR 100,4,21
UH 6,13
MA 156,104,24
WL 3
TR 102,5,21
UL 7,16
MA 156,108,28
s_mov_b32 m0, %[pm]
UH 8,16
AD 102,6
MA 152,104,16
s_add_u32 s46, %[pw], 655360
AD 106,7
AD 110,8
MA 152,108,20
s_add_u32 s46, s46, s84
s_addc_u32 s47, s85, 0
UL 6,14
MA 148,104,8
DG
MA 148,108,12
UH 7,14
UL 8,15
MA 144,104,0
AD 91,6
AD 95,7
MA 144,108,4
TR 104,2,22
AD 99,8
MV 184,112,0
s_add_u32 m0, %[hm], 0
TR 106,3,22
v_xor_b32 v1, 0x300, v1
MV 184,116,4
s_add_u32 s40, s64, 786432
s_addc_u32 s41, s65, 0
UH 6,15
MV 188,112,8
DH 0
MV 188,116,12
TR 108,2,23
UL 7,17
MV 192,112,16
TR 110,3,23
UH 8,17
MV 192,116,20
AD 103,6
AD 107,7
MV 196,112,24
AD 111,8
MV 196,116,28
DH 2
MV 200,112,32
MV 200,116,36
MV 204,112,40
s_add_u32 m0, %[hm], 4096
MV 204,116,44
WL 4
TR 112,4,22
MV 228,96,40
TR 114,5,22
)"
R"(MV 228,100,44
s_add_u32 s40, s64, 790528
s_addc_u32 s41, s65, 0
MV 224,96,32
DH 0
MV 224,100,36
TR 116,4,23
MV 220,96,24
TR 118,5,23
MV 220,100,28
s_add_u32 m0, %[wm], 12288
MV 216,96,16
s_add_u32 s40, s89, 98304
MV 216,100,20
s_add_u32 s40, s40, s68
s_addc_u32 s41, s69, 0
MV 212,96,8
DX 0
MV 212,100,12
MV 208,96,0
MV 208,100,4
WL 4
TR 96,2,24
MV 232,104,0
TR 98,3,24
MV 232,108,4
DX 1
MV 236,104,8
MV 236,108,12
TR 100,2,25
MV 240,104,16
TR 102,3,25
MV 240,108,20
MV 244,104,24
DX 2
MV 244,108,28
MV 248,104,32
MV 248,108,36
MV 252,104,40
s_add_u32 m0, %[hm], 6144
MV 252,108,44
WL 4
TR 104,4,24
MA 188,112,40
TR 106,5,24
MA 188,116,44
s_add_u32 s40, s64, 792576
s_addc_u32 s41, s65, 0
MA 184,112,32
MA 184,116,36
TR 108,4,25
MA 180,112,24
TR 110,5,25
MA 180,116,28
MA 176,112,16
MA 176,116,20
MA 172,112,8
MA 172,116,12
MA 168,112,0
WV 10
MA 168,116,4
WL 4
TR 112,2,26
MV 40,96,48
TR 114,3,26
MV 40,100,52
s_add_u32 s44, s84, 495616
s_addc_u32 s45, s85, 0
MV 44,96,56
LA 0,-4
MV 44,100,60
TR 116,2,27
MV 48,96,64
TR 118,3,27
MV 48,100,68
MV 52,96,72
MV 52,100,76
LA 4,-3
MV 56,96,80
MV 56,100,84
MV 60,96,88
MV 60,100,92
WL 4
TR 96,4,26
MV 84,104,88
TR 98,5,26
MV 84,108,92
LA 8,-2
MV 80,104,80
MV 80,108,84
TR 100,4,27
MV 76,104,72
TR 102,5,27
MV 76,108,76
MV 72,104,64
LA 12,-1
MV 72,108,68
MV 68,104,56
MV 68,108,60
MV 64,104,48
MV 64,108,52
WL 4
TR 104,2,28
MV 88,112,48
TR 106,3,28
MV 88,116,52
LA 16,0
MV 92,112,56
MV 92,116,60
TR 108,2,29
MV 96,112,64
TR 110,3,29
MV 96,116,68
MV 100,112,72
LA 20,1
MV 100,116,76
MV 104,112,80
MV 104,116,84
s_add_u32 s44, s44, 6144
s_addc_u32 s45, s45, 0
MV 108,112,88
MV 108,116,92
WL 4
TR 112,4,28
MA 140,96,88
TR 114,5,28
MA 140,100,92
LA 24,-4
MA 136,96,80
MA 136,100,84
TR 116,4,29
MA 132,96,72
TR 118,5,29
MA 132,100,76
MA 128,96,64
LA 28,-3
MA 128,100,68
MA 124,96,56
WV 8
MA 124,100,60
WL 0
s_barrier
MA 120,96,48
MA 120,100,52
TR 96,2,30
MV 112,104,48
TR 98,3,30
MV 112,108,52
LA 32,-2
MV 116,104,56
ds_read_b64 v[12:13], v18 offset:12288
MV 116,108,60
TR 100,2,31
MV 120,104,64
TR 102,3,31
MV 120,108,68
ds_read_b32 v16, v19 offset:12288
MV 124,104,72
LA 36,-1
MV 124,108,76
ds_read_b64 v[14:15], v18 offset:13056
MV 128,104,80
ds_read_b32 v17, v19 offset:13056
MV 128,108,84
MV 132,104,88
MV 132,108,92
TR 104,4,30
MV 156,112,88
TR 106,5,30
MV 156,116,92
LA 40,0
MV 152,112,80
WL 4
UL 6,12
UH 7,12
MV 152,116,84
TR 108,4,31
UL 8,13
MV 148,112,72
TR 110,5,31
AR 9,120
MV 148,116,76
AR 10,124
AR 11,128
MV 144,112,64
LA 44,1
MV 144,116,68
AD 9,6
AD 10,7
MV 140,112,56
AD 11,8
AW 120,9
MV 140,116,60
AW 124,10
AW 128,11
MV 136,112,48
UH 6,13
UL 7,16
MV 136,116,52
TR 112,2,32
UH 8,16
MV 160,96,48
TR 114,3,32
MV 160,100,52
DH 0
MV 164,96,56
ds_read_b64 v[12:13], v18 offset:13824
MV 164,100,60
TR 116,2,33
MV 168,96,64
s_add_u32 m0, %[hm], 8192
TR 118,3,33
MV 168,100,68
s_add_u32 s40, s66, 786432
s_addc_u32 s41, s67, 0
MV 172,96,72
DH 0
MV 172,100,76
ds_read_b32 v16, v19 offset:13824
AR 9,132
MV 176,96,80
AR 10,136
AR 11,140
MV 176,100,84
AD 9,6
AD 10,7
MV 180,96,88
AD 11,8
AW 132,9
MV 180,100,92
WL 6
TR 96,4,32
AW 136,10
MA 164,104,88
TR 98,5,32
AW 140,11
MA 164,108,92
DH 2
MA 160,104,80
UL 6,14
UH 7,14
MA 160,108,84
TR 100,4,33
UL 8,15
MA 156,104,72
PA 20480
TR 102,5,33
AR 9,121
MA 156,108,76
AR 10,125
AR 11,129
MA 152,104,64
DF
MA 152,108,68
s_mov_b32 m0, %[pm]
AD 9,6
AD 10,7
MA 148,104,56
s_add_u32 s46, %[pw], 786432
AD 11,8
AW 121,9
MA 148,108,60
s_add_u32 s46, s46, s84
s_addc_u32 s47, s85, 0
AW 125,10
MA 144,104,48
AW 129,11
UH 6,15
MA 144,108,52
WL 3
TR 104,2,34
UL 7,17
MV 184,112,48
TR 106,3,34
UH 8,17
MV 184,116,52
DG
MV 188,112,56
ds_read_b64 v[14:15], v18 offset:14592
MV 188,116,60
TR 108,2,35
MV 192,112,64
s_add_u32 m0, %[hm], 12288
TR 110,3,35
v_xor_b32 v1, 0x100, v1
MV 192,116,68
s_add_u32 s40, s64, 786432
s_addc_u32 s41, s65, 0
MV 196,112,72
DH 0
MV 196,116,76
ds_read_b32 v17, v19 offset:14592
AR 9,133
MV 200,112,80
AR 10,137
AR 11,141
MV 200,116,84
AD 9,6
AD 10,7
MV 204,112,88
AD 11,8
AW 133,9
MV 204,116,92
WL 6
TR 112,4,34
AW 137,10
MV 228,96,88
TR 114,5,34
AW 141,11
MV 228,100,92
DH 2
MV 224,96,80
UL 6,12
UH 7,12
MV 224,100,84
TR 116,4,35
UL 8,13
MV 220,96,72
s_add_u32 m0, %[hm], 16384
TR 118,5,35
AR 9,122
MV 220,100,76
s_add_u32 s40, s64, 790528
s_addc_u32 s41, s65, 0
AR 10,126
MV 216,96,64
DH 0
MV 216,100,68
AR 11,130
AD 9,6
MV 212,96,56
AD 10,7
AD 11,8
MV 212,100,60
AW 122,9
AW 126,10
MV 208,96,48
s_add_u32 m0, %[hm], 18432
AW 130,11
UH 6,13
MV 208,100,52
WL 3
TR 96,2,0
UL 7,16
MV 232,104,48
TR 98,3,0
UH 8,16
MV 232,108,52
s_add_u32 s40, s64, 792576
s_addc_u32 s41, s65, 0
AR 9,134
MV 236,104,56
DH 0
MV 236,108,60
TR 100,2,1
AR 10,138
MV 240,104,64
TR 102,3,1
AR 11,142
MV 240,108,68
s_add_u32 m0, %[hm], 20480
AD 9,6
AD 10,7
MV 244,104,72
s_add_u32 s40, s66, 786432
s_addc_u32 s41, s67, 0
AD 11,8
MV 244,108,76
DH 0
MV 248,104,80
AW 134,9
AW 138,10
MV 248,108,84
AW 142,11
UL 6,14
MV 252,104,88
UH 7,14
UL 8,15
MV 252,108,92
WL 3
TR 104,4,0
AR 9,123
MA 188,112,88
TR 106,5,0
AR 10,127
MA 188,116,92
AR 11,131
AD 9,6
MA 184,112,80
AD 10,7
AD 11,8
MA 184,116,84
TR 108,4,1
AW 123,9
MA 180,112,72
TR 110,5,1
AW 127,10
MA 180,116,76
AW 131,11
UH 6,15
)"
R"(MA 176,112,64
UL 7,17
UH 8,17
MA 176,116,68
AR 9,135
AR 10,139
MA 172,112,56
AR 11,143
AD 9,6
MA 172,116,60
AD 10,7
AD 11,8
MA 168,112,48
WV 10
AW 135,9
AW 139,10
MA 168,116,52
WL 4
TR 112,2,2
AW 143,11
MV 40,96,0
TR 114,3,2
MV 40,100,4
s_add_u32 s44, s84, 544768
s_addc_u32 s45, s85, 0
MV 44,96,8
LA 48,-4
MV 44,100,12
TR 116,2,3
MV 48,96,16
.Lk2p6:
s_cbranch_execz .Lk2r0
TR 118,3,3
MV 48,100,20
MV 52,96,24
MV 52,100,28
LA 52,-3
MV 56,96,32
MV 56,100,36
MV 60,96,40
MV 60,100,44
WL 4
TR 96,4,2
MV 84,104,40
TR 98,5,2
MV 84,108,44
LA 56,-2
MV 80,104,32
MV 80,108,36
TR 100,4,3
MV 76,104,24
TR 102,5,3
MV 76,108,28
MV 72,104,16
LA 60,-1
MV 72,108,20
MV 68,104,8
MV 68,108,12
MV 64,104,0
MV 64,108,4
WL 4
TR 104,2,4
MV 88,112,0
TR 106,3,4
MV 88,116,4
LA 64,0
MV 92,112,8
MV 92,116,12
TR 108,2,5
MV 96,112,16
TR 110,3,5
MV 96,116,20
MV 100,112,24
LA 68,1
MV 100,116,28
MV 104,112,32
MV 104,116,36
s_add_u32 s44, s44, 6144
s_addc_u32 s45, s45, 0
MV 108,112,40
MV 108,116,44
WL 4
TR 112,4,4
MA 140,96,40
TR 114,5,4
MA 140,100,44
LA 72,-4
MA 136,96,32
MA 136,100,36
TR 116,4,5
MA 132,96,24
TR 118,5,5
MA 132,100,28
MA 128,96,16
LA 76,-3
MA 128,100,20
MA 124,96,8
WV 10
MA 124,100,12
WL 0
s_barrier
MA 120,96,0
MA 120,100,4
TR 96,2,6
MV 112,104,0
TR 98,3,6
MV 112,108,4
LA 80,-2
MV 116,104,8
MV 116,108,12
TR 100,2,7
MV 120,104,16
TR 102,3,7
MV 120,108,20
MV 124,104,24
LA 84,-1
MV 124,108,28
MV 128,104,32
MV 128,108,36
MV 132,104,40
MV 132,108,44
TR 104,4,6
MV 156,112,40
TR 106,5,6
MV 156,116,44
LA 88,0
MV 152,112,32
MV 152,116,36
TR 108,4,7
MV 148,112,24
TR 110,5,7
MV 148,116,28
MV 144,112,16
LA 92,1
MV 144,116,20
MV 140,112,8
MV 140,116,12
MV 136,112,0
MV 136,116,4
WL 4
TR 112,2,8
MV 160,96,0
TR 114,3,8
MV 160,100,4
DH 2
MV 164,96,8
MV 164,100,12
TR 116,2,9
MV 168,96,16
TR 118,3,9
MV 168,100,20
s_mov_b32 m0, %[pm]
MV 172,96,24
s_add_u32 s46, %[pw], 917504
MV 172,100,28
s_add_u32 s46, s46, s84
s_addc_u32 s47, s85, 0
MV 176,96,32
DG
MV 176,100,36
MV 180,96,40
MV 180,100,44
WL 4
TR 96,4,8
MA 164,104,40
s_add_u32 m0, %[hm], 24576
TR 98,5,8
v_xor_b32 v1, 0x700, v1
MA 164,108,44
s_add_u32 s40, s64, 786432
s_addc_u32 s41, s65, 0
MA 160,104,32
DH 0
MA 160,108,36
TR 100,4,9
MA 156,104,24
TR 102,5,9
MA 156,108,28
MA 152,104,16
MA 152,108,20
DH 2
MA 148,104,8
MA 148,108,12
MA 144,104,0
s_add_u32 m0, %[hm], 28672
MA 144,108,4
WL 4
TR 104,2,10
MV 184,112,0
TR 106,3,10
MV 184,116,4
s_add_u32 s40, s64, 790528
s_addc_u32 s41, s65, 0
MV 188,112,8
DH 0
MV 188,116,12
TR 108,2,11
MV 192,112,16
TR 110,3,11
MV 192,116,20
s_add_u32 m0, %[wm], 0
MV 196,112,24
s_add_u32 s40, s88, 49152
MV 196,116,28
s_add_u32 s40, s40, s68
s_addc_u32 s41, s69, 0
MV 200,112,32
DX 0
MV 200,116,36
MV 204,112,40
MV 204,116,44
WL 4
TR 112,4,10
MV 228,96,40
TR 114,5,10
MV 228,100,44
DX 1
MV 224,96,32
MV 224,100,36
TR 116,4,11
MV 220,96,24
TR 118,5,11
MV 220,100,28
MV 216,96,16
DX 2
MV 216,100,20
MV 212,96,8
MV 212,100,12
MV 208,96,0
s_add_u32 m0, %[hm], 30720
MV 208,100,4
WL 4
TR 96,2,12
MV 232,104,0
TR 98,3,12
MV 232,108,4
s_add_u32 s40, s64, 792576
s_addc_u32 s41, s65, 0
MV 236,104,8
DH 0
MV 236,108,12
TR 100,2,13
MV 240,104,16
TR 102,3,13
MV 240,108,20
s_add_u32 m0, %[hm], 32768
MV 244,104,24
s_add_u32 s40, s66, 786432
s_addc_u32 s41, s67, 0
MV 244,108,28
DH 0
MV 248,104,32
MV 248,108,36
MV 252,104,40
MV 252,108,44
WL 4
TR 104,4,12
MA 188,112,40
TR 106,5,12
MA 188,116,44
MA 184,112,32
MA 184,116,36
TR 108,4,13
MA 180,112,24
TR 110,5,13
MA 180,116,28
MA 176,112,16
MA 176,116,20
MA 172,112,8
MA 172,116,12
MA 168,112,0
WV 10
MA 168,116,4
WL 4
TR 112,2,14
MV 40,96,48
TR 114,3,14
MV 40,100,52
s_add_u32 s44, s84, 593920
s_addc_u32 s45, s85, 0
MV 44,96,56
LA 0,-4
MV 44,100,60
TR 116,2,15
MV 48,96,64
TR 118,3,15
MV 48,100,68
MV 52,96,72
MV 52,100,76
LA 4,-3
MV 56,96,80
MV 56,100,84
MV 60,96,88
MV 60,100,92
WL 4
TR 96,4,14
MV 84,104,88
TR 98,5,14
MV 84,108,92
LA 8,-2
MV 80,104,80
MV 80,108,84
TR 100,4,15
MV 76,104,72
TR 102,5,15
MV 76,108,76
MV 72,104,64
LA 12,-1
MV 72,108,68
MV 68,104,56
MV 68,108,60
MV 64,104,48
MV 64,108,52
WL 4
TR 104,2,16
MV 88,112,48
TR 106,3,16
MV 88,116,52
LA 16,0
MV 92,112,56
MV 92,116,60
TR 108,2,17
MV 96,112,64
TR 110,3,17
MV 96,116,68
MV 100,112,72
LA 20,1
MV 100,116,76
MV 104,112,80
MV 104,116,84
s_add_u32 s44, s44, 6144
s_addc_u32 s45, s45, 0
MV 108,112,88
MV 108,116,92
WL 4
TR 112,4,16
MA 140,96,88
TR 114,5,16
MA 140,100,92
LA 24,-4
MA 136,96,80
MA 136,100,84
TR 116,4,17
MA 132,96,72
TR 118,5,17
MA 132,100,76
MA 128,96,64
LA 28,-3
MA 128,100,68
MA 124,96,56
WV 10
MA 124,100,60
WL 0
s_barrier
MA 120,96,48
MA 120,100,52
TR 96,2,18
MV 112,104,48
TR 98,3,18
MV 112,108,52
LA 32,-2
MV 116,104,56
MV 116,108,60
TR 100,2,19
MV 120,104,64
TR 102,3,19
MV 120,108,68
MV 124,104,72
LA 36,-1
MV 124,108,76
MV 128,104,80
MV 128,108,84
MV 132,104,88
MV 132,108,92
TR 104,4,18
MV 156,112,88
TR 106,5,18
MV 156,116,92
LA 40,0
MV 152,112,80
ds_read_b64 v[12:13], v18 offset:0
MV 152,116,84
TR 108,4,19
MV 148,112,72
TR 110,5,19
MV 148,116,76
ds_read_b32 v16, v19 offset:0
MV 144,112,64
LA 44,1
MV 144,116,68
ds_read_b64 v[14:15], v18 offset:768
MV 140,112,56
ds_read_b32 v17, v19 offset:768
MV 140,116,60
MV 136,112,48
MV 136,116,52
WL 8
TR 112,2,20
MV 160,96,48
TR 114,3,20
)"
R"(MV 160,100,52
DH 2
MV 164,96,56
WL 4
UL 6,12
UH 7,12
MV 164,100,60
TR 116,2,21
UL 8,13
MV 168,96,64
TR 118,3,21
AD 112,6
MV 168,100,68
s_mov_b32 m0, %[pm]
AD 116,7
AD 120,8
MV 172,96,72
s_add_u32 s46, %[pw], 1048576
UH 6,13
UL 7,16
MV 172,100,76
s_add_u32 s46, s46, s84
s_addc_u32 s47, s85, 0
UH 8,16
MV 176,96,80
DG
MV 176,100,84
ds_read_b64 v[12:13], v18 offset:1536
MV 180,96,88
ds_read_b32 v16, v19 offset:1536
AD 124,6
MV 180,100,92
TR 96,4,20
AD 128,7
MA 164,104,88
s_add_u32 m0, %[hm], 0
TR 98,5,20
v_xor_b32 v1, 0x100, v1
MA 164,108,92
s_add_u32 s40, s64, 786432
s_addc_u32 s41, s65, 0
AD 132,8
MA 160,104,80
DH 0
MA 160,108,84
WL 5
TR 100,4,21
UL 6,14
MA 156,104,72
TR 102,5,21
UH 7,14
MA 156,108,76
UL 8,15
AD 113,6
MA 152,104,64
AD 117,7
AD 121,8
MA 152,108,68
DH 2
MA 148,104,56
UH 6,15
UL 7,17
MA 148,108,60
UH 8,17
MA 144,104,48
s_add_u32 m0, %[hm], 4096
ds_read_b64 v[14:15], v18 offset:2304
MA 144,108,52
WL 5
TR 104,2,22
MV 184,112,48
TR 106,3,22
MV 184,116,52
s_add_u32 s40, s64, 790528
s_addc_u32 s41, s65, 0
MV 188,112,56
DH 0
MV 188,116,60
TR 108,2,23
MV 192,112,64
TR 110,3,23
MV 192,116,68
s_add_u32 m0, %[wm], 12288
ds_read_b32 v17, v19 offset:2304
AD 125,6
MV 196,112,72
s_add_u32 s40, s88, 147456
AD 129,7
AD 133,8
MV 196,116,76
s_add_u32 s40, s40, s68
s_addc_u32 s41, s69, 0
UL 6,12
MV 200,112,80
DX 0
MV 200,116,84
UH 7,12
UL 8,13
MV 204,112,88
AD 114,6
AD 118,7
MV 204,116,92
WL 6
TR 112,4,22
AD 122,8
MV 228,96,88
TR 114,5,22
UH 6,13
MV 228,100,92
DX 1
MV 224,96,80
UL 7,16
UH 8,16
MV 224,100,84
TR 116,4,23
AD 126,6
MV 220,96,72
TR 118,5,23
AD 130,7
MV 220,100,76
WL 4
AD 134,8
UL 6,14
MV 216,96,64
DX 2
MV 216,100,68
UH 7,14
UL 8,15
MV 212,96,56
AD 115,6
AD 119,7
MV 212,100,60
AD 123,8
UH 6,15
MV 208,96,48
s_add_u32 m0, %[hm], 6144
UL 7,17
UH 8,17
MV 208,100,52
TR 96,2,24
AD 127,6
MV 232,104,48
TR 98,3,24
AD 131,7
MV 232,108,52
s_add_u32 s40, s64, 792576
s_addc_u32 s41, s65, 0
AD 135,8
MV 236,104,56
DH 0
MV 236,108,60
TR 100,2,25
MV 240,104,64
TR 102,3,25
MV 240,108,68
s_add_u32 m0, %[hm], 8192
MV 244,104,72
s_add_u32 s40, s66, 786432
s_addc_u32 s41, s67, 0
MV 244,108,76
DH 0
MV 248,104,80
MV 248,108,84
MV 252,104,88
MV 252,108,92
WL 4
TR 104,4,24
MA 188,112,88
TR 106,5,24
MA 188,116,92
MA 184,112,80
MA 184,116,84
TR 108,4,25
MA 180,112,72
TR 110,5,25
MA 180,116,76
MA 176,112,64
MA 176,116,68
MA 172,112,56
MA 172,116,60
MA 168,112,48
WV 10
MA 168,116,52
WL 4
TR 112,2,26
MV 40,96,0
TR 114,3,26
MV 40,100,4
s_add_u32 s44, s84, 643072
s_addc_u32 s45, s85, 0
MV 44,96,8
LA 48,-4
MV 44,100,12
.Lk2p7:
s_cbranch_execz .Lk2r0
TR 116,2,27
MV 48,96,16
TR 118,3,27
MV 48,100,20
MV 52,96,24
MV 52,100,28
LA 52,-3
MV 56,96,32
MV 56,100,36
MV 60,96,40
MV 60,100,44
WL 4
TR 96,4,26
MV 84,104,40
TR 98,5,26
MV 84,108,44
LA 56,-2
MV 80,104,32
MV 80,108,36
TR 100,4,27
MV 76,104,24
TR 102,5,27
MV 76,108,28
MV 72,104,16
LA 60,-1
MV 72,108,20
MV 68,104,8
MV 68,108,12
MV 64,104,0
MV 64,108,4
WL 4
TR 104,2,28
MV 88,112,0
TR 106,3,28
MV 88,116,4
LA 64,0
MV 92,112,8
MV 92,116,12
TR 108,2,29
MV 96,112,16
TR 110,3,29
MV 96,116,20
MV 100,112,24
LA 68,1
MV 100,116,28
MV 104,112,32
MV 104,116,36
s_add_u32 s44, s44, 6144
s_addc_u32 s45, s45, 0
MV 108,112,40
MV 108,116,44
WL 4
TR 112,4,28
MA 140,96,40
TR 114,5,28
MA 140,100,44
LA 72,-4
MA 136,96,32
MA 136,100,36
TR 116,4,29
MA 132,96,24
TR 118,5,29
MA 132,100,28
MA 128,96,16
LA 76,-3
MA 128,100,20
MA 124,96,8
WV 10
MA 124,100,12
WL 0
s_barrier
MA 120,96,0
MA 120,100,4
TR 96,2,30
MV 112,104,0
TR 98,3,30
MV 112,108,4
LA 80,-2
MV 116,104,8
MV 116,108,12
TR 100,2,31
MV 120,104,16
TR 102,3,31
MV 120,108,20
MV 124,104,24
LA 84,-1
MV 124,108,28
MV 128,104,32
MV 128,108,36
MV 132,104,40
MV 132,108,44
TR 104,4,30
MV 156,112,40
TR 106,5,30
MV 156,116,44
LA 88,0
MV 152,112,32
MV 152,116,36
TR 108,4,31
MV 148,112,24
TR 110,5,31
MV 148,116,28
MV 144,112,16
LA 92,1
MV 144,116,20
MV 140,112,8
MV 140,116,12
MV 136,112,0
MV 136,116,4
WL 4
TR 112,2,32
MV 160,96,0
TR 114,3,32
MV 160,100,4
DH 2
MV 164,96,8
ds_read_b64 v[12:13], v18 offset:12288
MV 164,100,12
TR 116,2,33
MV 168,96,16
TR 118,3,33
MV 168,100,20
s_add_u32 m0, %[hm], 12288
v_xor_b32 v1, 0x300, v1
MV 172,96,24
s_add_u32 s40, s64, 786432
s_addc_u32 s41, s65, 0
MV 172,100,28
DH 0
MV 176,96,32
ds_read_b32 v16, v19 offset:12288
MV 176,100,36
ds_read_b64 v[14:15], v18 offset:13056
MV 180,96,40
ds_read_b32 v17, v19 offset:13056
MV 180,100,44
WL 8
TR 96,4,32
MA 164,104,40
TR 98,5,32
MA 164,108,44
DH 2
MA 160,104,32
MA 160,108,36
WL 4
TR 100,4,33
UL 6,12
MA 156,104,24
s_add_u32 m0, %[hm], 16384
TR 102,5,33
UH 7,12
MA 156,108,28
s_add_u32 s40, s64, 790528
s_addc_u32 s41, s65, 0
UL 8,13
MA 152,104,16
DH 0
MA 152,108,20
AD 136,6
AD 140,7
MA 148,104,8
AD 144,8
UH 6,13
MA 148,108,12
UL 7,16
UH 8,16
MA 144,104,0
s_add_u32 m0, %[hm], 18432
ds_read_b64 v[12:13], v18 offset:13824
MA 144,108,4
TR 104,2,34
MV 184,112,0
TR 106,3,34
MV 184,116,4
s_add_u32 s40, s64, 792576
s_addc_u32 s41, s65, 0
MV 188,112,8
DH 0
MV 188,116,12
TR 108,2,35
MV 192,112,16
TR 110,3,35
MV 192,116,20
s_add_u32 m0, %[hm], 20480
ds_read_b32 v16, v19 offset:13824
AD 148,6
MV 196,112,24
s_add_u32 s40, s66, 786432
s_addc_u32 s41, s67, 0
AD 152,7
MV 196,116,28
DH 0
MV 200,112,32
WL 6
AD 156,8
UL 6,14
MV 200,116,36
UH 7,14
UL 8,15
MV 204,112,40
AD 137,6
AD 141,7
MV 204,116,44
TR 112,4,34
AD 145,8
)"
R"(MV 228,96,40
TR 114,5,34
UH 6,15
MV 228,100,44
DH 2
MV 224,96,32
UL 7,17
UH 8,17
MV 224,100,36
TR 116,4,35
MV 220,96,24
TR 118,5,35
MV 220,100,28
s_add_u32 m0, %[hm], 24576
v_xor_b32 v1, 0x100, v1
MV 216,96,16
s_add_u32 s40, s64, 786432
s_addc_u32 s41, s65, 0
MV 216,100,20
DH 0
MV 212,96,8
ds_read_b64 v[14:15], v18 offset:14592
MV 212,100,12
ds_read_b32 v17, v19 offset:14592
AD 149,6
MV 208,96,0
AD 153,7
AD 157,8
MV 208,100,4
WL 6
TR 96,2,0
UL 6,12
MV 232,104,0
TR 98,3,0
UH 7,12
MV 232,108,4
DH 2
MV 236,104,8
UL 8,13
AD 138,6
MV 236,108,12
TR 100,2,1
AD 142,7
MV 240,104,16
s_add_u32 m0, %[hm], 28672
TR 102,3,1
AD 146,8
MV 240,108,20
s_add_u32 s40, s64, 790528
s_addc_u32 s41, s65, 0
UH 6,13
MV 244,104,24
DH 0
MV 244,108,28
UL 7,16
UH 8,16
MV 248,104,32
WL 4
AD 150,6
AD 154,7
MV 248,108,36
s_add_u32 m0, %[wm], 0
AD 158,8
UL 6,14
MV 252,104,40
s_add_u32 s40, s89, 49152
UH 7,14
UL 8,15
MV 252,108,44
TR 104,4,0
AD 139,6
MA 188,112,40
TR 106,5,0
AD 143,7
MA 188,116,44
s_add_u32 s40, s40, s68
s_addc_u32 s41, s69, 0
AD 147,8
MA 184,112,32
UH 6,15
UL 7,17
MA 184,116,36
TR 108,4,1
UH 8,17
MA 180,112,24
TR 110,5,1
AD 151,6
MA 180,116,28
AD 155,7
AD 159,8
MA 176,112,16
MA 176,116,20
MA 172,112,8
MA 172,116,12
MA 168,112,0
WV 10
MA 168,116,4
WL 4
TR 112,2,2
MV 40,96,48
TR 114,3,2
MV 40,100,52
s_add_u32 s44, s84, 692224
s_addc_u32 s45, s85, 0
MV 44,96,56
LA 0,-4
MV 44,100,60
TR 116,2,3
MV 48,96,64
TR 118,3,3
MV 48,100,68
MV 52,96,72
MV 52,100,76
LA 4,-3
MV 56,96,80
MV 56,100,84
MV 60,96,88
MV 60,100,92
WL 4
TR 96,4,2
MV 84,104,88
TR 98,5,2
MV 84,108,92
LA 8,-2
MV 80,104,80
MV 80,108,84
TR 100,4,3
MV 76,104,72
TR 102,5,3
MV 76,108,76
MV 72,104,64
LA 12,-1
MV 72,108,68
MV 68,104,56
MV 68,108,60
MV 64,104,48
MV 64,108,52
WL 4
TR 104,2,4
MV 88,112,48
TR 106,3,4
MV 88,116,52
LA 16,0
MV 92,112,56
MV 92,116,60
TR 108,2,5
MV 96,112,64
TR 110,3,5
MV 96,116,68
MV 100,112,72
LA 20,1
MV 100,116,76
MV 104,112,80
MV 104,116,84
s_add_u32 s44, s44, 6144
s_addc_u32 s45, s45, 0
MV 108,112,88
MV 108,116,92
WL 4
TR 112,4,4
MA 140,96,88
TR 114,5,4
MA 140,100,92
LA 24,-4
MA 136,96,80
MA 136,100,84
TR 116,4,5
MA 132,96,72
TR 118,5,5
MA 132,100,76
MA 128,96,64
LA 28,-3
MA 128,100,68
MA 124,96,56
WV 14
MA 124,100,60
WL 0
s_barrier
MA 120,96,48
MA 120,100,52
TR 96,2,6
MV 112,104,48
TR 98,3,6
MV 112,108,52
LA 32,-2
MV 116,104,56
MV 116,108,60
TR 100,2,7
MV 120,104,64
TR 102,3,7
MV 120,108,68
MV 124,104,72
LA 36,-1
MV 124,108,76
MV 128,104,80
MV 128,108,84
MV 132,104,88
MV 132,108,92
TR 104,4,6
MV 156,112,88
TR 106,5,6
MV 156,116,92
LA 40,0
MV 152,112,80
MV 152,116,84
TR 108,4,7
MV 148,112,72
TR 110,5,7
MV 148,116,76
MV 144,112,64
LA 44,1
MV 144,116,68
MV 140,112,56
MV 140,116,60
MV 136,112,48
MV 136,116,52
WL 4
TR 112,2,8
MV 160,96,48
TR 114,3,8
MV 160,100,52
DX 0
MV 164,96,56
MV 164,100,60
TR 116,2,9
MV 168,96,64
TR 118,3,9
MV 168,100,68
MV 172,96,72
DX 1
MV 172,100,76
MV 176,96,80
MV 176,100,84
MV 180,96,88
MV 180,100,92
WL 4
TR 96,4,8
MA 164,104,88
TR 98,5,8
MA 164,108,92
DX 2
MA 160,104,80
MA 160,108,84
TR 100,4,9
MA 156,104,72
PA 20480
TR 102,5,9
MA 156,108,76
MA 152,104,64
DF
MA 152,108,68
s_add_u32 m0, %[hm], 30720
MA 148,104,56
s_add_u32 s40, s64, 792576
s_addc_u32 s41, s65, 0
MA 148,108,60
MA 144,104,48
MA 144,108,52
WL 4
TR 104,2,10
MV 184,112,48
TR 106,3,10
MV 184,116,52
DH 0
MV 188,112,56
MV 188,116,60
TR 108,2,11
MV 192,112,64
s_add_u32 m0, %[hm], 32768
TR 110,3,11
MV 192,116,68
s_add_u32 s40, s66, 786432
s_addc_u32 s41, s67, 0
MV 196,112,72
DH 0
MV 196,116,76
MV 200,112,80
MV 200,116,84
MV 204,112,88
MV 204,116,92
WL 4
TR 112,4,10
MV 228,96,88
TR 114,5,10
MV 228,100,92
DH 2
MV 224,96,80
MV 224,100,84
TR 116,4,11
MV 220,96,72
TR 118,5,11
MV 220,100,76
s_bitcmp1_b32 s48, 6
s_cselect_b64 s[64:65], s[58:59], s[56:57]
MV 216,96,64
s_bitcmp1_b32 s48, 22
s_cselect_b32 s40, 16384, 0
MV 216,100,68
s_add_u32 s64, s64, s40
s_addc_u32 s65, s65, 0
MV 212,96,56
s_add_u32 m0, %[hm], 0
v_xor_b32 v1, 0x700, v1
MV 212,100,60
s_add_u32 s40, s64, 1572864
s_addc_u32 s41, s65, 0
MV 208,96,48
DH 0
MV 208,100,52
WL 4
TR 96,2,12
MV 232,104,48
TR 98,3,12
MV 232,108,52
MV 236,104,56
MV 236,108,60
TR 100,2,13
MV 240,104,64
TR 102,3,13
MV 240,108,68
DH 2
MV 244,104,72
MV 244,108,76
MV 248,104,80
s_add_u32 m0, %[hm], 4096
MV 248,108,84
s_add_u32 s40, s64, 1576960
s_addc_u32 s41, s65, 0
MV 252,104,88
DH 0
MV 252,108,92
WL 4
TR 104,4,12
MA 188,112,88
TR 106,5,12
MA 188,116,92
s_add_u32 m0, %[wm], 12288
MA 184,112,80
s_add_u32 s40, s89, 147456
MA 184,116,84
TR 108,4,13
MA 180,112,72
TR 110,5,13
MA 180,116,76
s_add_u32 s40, s40, s68
s_addc_u32 s41, s69, 0
MA 176,112,64
MA 176,116,68
MA 172,112,56
MA 172,116,60
MA 168,112,48
WV 10
MA 168,116,52
WL 4
TR 112,2,14
MV 40,96,0
.Lk2p8:
s_cbranch_execz .Lk2r0
TR 114,3,14
MV 40,100,4
s_add_u32 s44, s84, 741376
s_addc_u32 s45, s85, 0
MV 44,96,8
LA 48,-4
MV 44,100,12
TR 116,2,15
MV 48,96,16
TR 118,3,15
MV 48,100,20
MV 52,96,24
MV 52,100,28
LA 52,-3
MV 56,96,32
MV 56,100,36
MV 60,96,40
MV 60,100,44
WL 4
TR 96,4,14
MV 84,104,40
TR 98,5,14
MV 84,108,44
LA 56,-2
MV 80,104,32
MV 80,108,36
TR 100,4,15
MV 76,104,24
TR 102,5,15
MV 76,108,28
MV 72,104,16
LA 60,-1
MV 72,108,20
MV 68,104,8
MV 68,108,12
MV 64,104,0
MV 64,108,4
WL 4
TR 104,2,16
MV 88,112,0
TR 106,3,16
MV 88,116,4
)"
R"(LA 64,0
MV 92,112,8
MV 92,116,12
TR 108,2,17
MV 96,112,16
TR 110,3,17
MV 96,116,20
MV 100,112,24
LA 68,1
MV 100,116,28
MV 104,112,32
MV 104,116,36
s_add_u32 s44, s44, 6144
s_addc_u32 s45, s45, 0
MV 108,112,40
MV 108,116,44
WL 4
TR 112,4,16
MA 140,96,40
TR 114,5,16
MA 140,100,44
LA 72,-4
MA 136,96,32
MA 136,100,36
TR 116,4,17
MA 132,96,24
TR 118,5,17
MA 132,100,28
MA 128,96,16
LA 76,-3
MA 128,100,20
MA 124,96,8
WV 15
MA 124,100,12
WL 0
s_barrier
MA 120,96,0
MA 120,100,4
TR 96,2,18
MV 112,104,0
TR 98,3,18
MV 112,108,4
LA 80,-2
MV 116,104,8
MV 116,108,12
TR 100,2,19
MV 120,104,16
TR 102,3,19
MV 120,108,20
MV 124,104,24
LA 84,-1
MV 124,108,28
MV 128,104,32
MV 128,108,36
MV 132,104,40
MV 132,108,44
TR 104,4,18
MV 156,112,40
TR 106,5,18
MV 156,116,44
LA 88,0
MV 152,112,32
MV 152,116,36
TR 108,4,19
MV 148,112,24
TR 110,5,19
MV 148,116,28
MV 144,112,16
LA 92,1
MV 144,116,20
MV 140,112,8
MV 140,116,12
MV 136,112,0
MV 136,116,4
WL 4
TR 112,2,20
MV 160,96,0
TR 114,3,20
MV 160,100,4
DX 0
MV 164,96,8
MV 164,100,12
TR 116,2,21
MV 168,96,16
TR 118,3,21
MV 168,100,20
MV 172,96,24
DX 1
MV 172,100,28
MV 176,96,32
MV 176,100,36
MV 180,96,40
MV 180,100,44
WL 4
TR 96,4,20
MA 164,104,40
TR 98,5,20
MA 164,108,44
DX 2
MA 160,104,32
ds_read_b64 v[12:13], v18 offset:0
MA 160,108,36
TR 100,4,21
MA 156,104,24
TR 102,5,21
MA 156,108,28
s_add_u32 m0, %[hm], 6144
ds_read_b32 v16, v19 offset:0
MA 152,104,16
s_add_u32 s40, s64, 1579008
s_addc_u32 s41, s65, 0
MA 152,108,20
DH 0
MA 148,104,8
ds_read_b64 v[14:15], v18 offset:768
MA 148,108,12
ds_read_b32 v17, v19 offset:768
MA 144,104,0
s_bitcmp1_b32 s49, 6
s_cselect_b64 s[66:67], s[62:63], s[60:61]
MA 144,108,4
WL 8
TR 104,2,22
MV 184,112,0
s_add_u32 m0, %[hm], 8192
TR 106,3,22
MV 184,116,4
s_add_u32 s40, s66, 1572864
s_addc_u32 s41, s67, 0
MV 188,112,8
DH 0
MV 188,116,12
WL 4
TR 108,2,23
UL 6,12
MV 192,112,16
TR 110,3,23
UH 7,12
MV 192,116,20
UL 8,13
AD 160,6
MV 196,112,24
AD 164,7
AD 168,8
MV 196,116,28
DH 2
MV 200,112,32
UH 6,13
UL 7,16
MV 200,116,36
UH 8,16
MV 204,112,40
ds_read_b64 v[12:13], v18 offset:1536
MV 204,116,44
TR 112,4,22
MV 228,96,40
s_add_u32 m0, %[hm], 12288
TR 114,5,22
v_xor_b32 v1, 0x100, v1
MV 228,100,44
s_add_u32 s40, s64, 1572864
s_addc_u32 s41, s65, 0
MV 224,96,32
DH 0
MV 224,100,36
TR 116,4,23
MV 220,96,24
TR 118,5,23
MV 220,100,28
ds_read_b32 v16, v19 offset:1536
AD 172,6
MV 216,96,16
AD 176,7
AD 180,8
MV 216,100,20
DH 2
MV 212,96,8
WL 6
UL 6,14
UH 7,14
MV 212,100,12
UL 8,15
AD 161,6
MV 208,96,0
s_add_u32 m0, %[hm], 16384
AD 165,7
AD 169,8
MV 208,100,4
TR 96,2,24
UH 6,15
MV 232,104,0
TR 98,3,24
UL 7,17
MV 232,108,4
s_add_u32 s40, s64, 1576960
s_addc_u32 s41, s65, 0
UH 8,17
MV 236,104,8
DH 0
MV 236,108,12
TR 100,2,25
MV 240,104,16
TR 102,3,25
MV 240,108,20
ds_read_b64 v[14:15], v18 offset:2304
MV 244,104,24
ds_read_b32 v17, v19 offset:2304
AD 173,6
MV 244,108,28
AD 177,7
AD 181,8
MV 248,104,32
WL 6
UL 6,12
UH 7,12
MV 248,108,36
UL 8,13
AD 162,6
MV 252,104,40
AD 166,7
AD 170,8
MV 252,108,44
TR 104,4,24
UH 6,13
MA 188,112,40
TR 106,5,24
UL 7,16
MA 188,116,44
UH 8,16
AD 174,6
MA 184,112,32
AD 178,7
AD 182,8
MA 184,116,36
WL 2
TR 108,4,25
UL 6,14
MA 180,112,24
TR 110,5,25
UH 7,14
MA 180,116,28
UL 8,15
AD 163,6
MA 176,112,16
AD 167,7
AD 171,8
MA 176,116,20
UH 6,15
UL 7,17
MA 172,112,8
UH 8,17
AD 175,6
MA 172,116,12
AD 179,7
AD 183,8
MA 168,112,0
MA 168,116,4
WV 9
TR 112,2,26
MV 40,96,48
TR 114,3,26
MV 40,100,52
s_add_u32 s44, s84, 790528
s_addc_u32 s45, s85, 0
MV 44,96,56
LA 0,-4
MV 44,100,60
TR 116,2,27
MV 48,96,64
TR 118,3,27
MV 48,100,68
MV 52,96,72
MV 52,100,76
LA 4,-3
MV 56,96,80
MV 56,100,84
MV 60,96,88
MV 60,100,92
WL 4
TR 96,4,26
MV 84,104,88
TR 98,5,26
MV 84,108,92
LA 8,-2
MV 80,104,80
MV 80,108,84
TR 100,4,27
MV 76,104,72
TR 102,5,27
MV 76,108,76
MV 72,104,64
LA 12,-1
MV 72,108,68
MV 68,104,56
MV 68,108,60
MV 64,104,48
MV 64,108,52
WL 4
TR 104,2,28
MV 88,112,48
TR 106,3,28
MV 88,116,52
LA 16,0
MV 92,112,56
MV 92,116,60
TR 108,2,29
MV 96,112,64
TR 110,3,29
MV 96,116,68
MV 100,112,72
LA 20,1
MV 100,116,76
MV 104,112,80
MV 104,116,84
s_add_u32 s44, s44, 6144
s_addc_u32 s45, s45, 0
MV 108,112,88
MV 108,116,92
WL 4
TR 112,4,28
MA 140,96,88
TR 114,5,28
MA 140,100,92
LA 24,-4
MA 136,96,80
MA 136,100,84
s_mov_b64 exec, 0
s_cmp_eq_u32 %[w], 0
s_cbranch_scc1 .Lk2p9
s_cmp_eq_u32 %[w], 1
s_cbranch_scc1 .Lk2p10
s_cmp_eq_u32 %[w], 2
s_cbranch_scc1 .Lk2p11
s_branch .Lk2p12
.Lk2r1:
s_mov_b64 exec, -1
TR 116,4,29
MA 132,96,72
TR 118,5,29
MA 132,100,76
MA 128,96,64
LA 28,-3
MA 128,100,68
MA 124,96,56
WV 14
MA 124,100,60
WL 0
s_barrier
MA 120,96,48
s_add_u32 m0, %[hm], 18432
MA 120,100,52
TR 96,2,30
MV 112,104,48
TR 98,3,30
MV 112,108,52
LA 32,-2
MV 116,104,56
s_add_u32 s40, s64, 1579008
s_addc_u32 s41, s65, 0
MV 116,108,60
TR 100,2,31
MV 120,104,64
TR 102,3,31
MV 120,108,68
MV 124,104,72
LA 36,-1
MV 124,108,76
MV 128,104,80
MV 128,108,84
MV 132,104,88
MV 132,108,92
TR 104,4,30
MV 156,112,88
TR 106,5,30
MV 156,116,92
LA 40,0
MV 152,112,80
MV 152,116,84
TR 108,4,31
MV 148,112,72
TR 110,5,31
MV 148,116,76
MV 144,112,64
LA 44,1
MV 144,116,68
MV 140,112,56
MV 140,116,60
MV 136,112,48
MV 136,116,52
WL 4
TR 112,2,32
MV 160,96,48
TR 114,3,32
MV 160,100,52
DH 0
MV 164,96,56
MV 164,100,60
TR 116,2,33
MV 168,96,64
s_add_u32 m0, %[hm], 20480
)"
R"(TR 118,3,33
MV 168,100,68
s_add_u32 s40, s66, 1572864
s_addc_u32 s41, s67, 0
MV 172,96,72
DH 0
MV 172,100,76
MV 176,96,80
MV 176,100,84
MV 180,96,88
MV 180,100,92
WL 4
TR 96,4,32
MA 164,104,88
TR 98,5,32
MA 164,108,92
DH 2
MA 160,104,80
MA 160,108,84
TR 100,4,33
MA 156,104,72
TR 102,5,33
MA 156,108,76
s_add_u32 m0, %[hm], 24576
v_xor_b32 v1, 0x300, v1
MA 152,104,64
s_add_u32 s40, s64, 1572864
s_addc_u32 s41, s65, 0
MA 152,108,68
DH 0
MA 148,104,56
MA 148,108,60
MA 144,104,48
MA 144,108,52
WL 4
TR 104,2,34
MV 184,112,48
TR 106,3,34
MV 184,116,52
DH 2
MV 188,112,56
ds_read_b64 v[12:13], v18 offset:12288
MV 188,116,60
TR 108,2,35
MV 192,112,64
s_add_u32 m0, %[hm], 28672
TR 110,3,35
MV 192,116,68
s_add_u32 s40, s64, 1576960
s_addc_u32 s41, s65, 0
MV 196,112,72
DH 0
MV 196,116,76
ds_read_b32 v16, v19 offset:12288
MV 200,112,80
ds_read_b64 v[14:15], v18 offset:13056
MV 200,116,84
s_add_u32 m0, %[wm], 0
ds_read_b32 v17, v19 offset:13056
MV 204,112,88
s_add_u32 s40, s90, 0
MV 204,116,92
WL 8
TR 112,4,34
MV 228,96,88
TR 114,5,34
MV 228,100,92
s_add_u32 s40, s40, s70
s_addc_u32 s41, s71, 0
MV 224,96,80
DX 0
MV 224,100,84
WL 4
TR 116,4,35
UL 6,12
MV 220,96,72
TR 118,5,35
UH 7,12
MV 220,100,76
UL 8,13
AR 9,144
MV 216,96,64
AR 10,148
AR 11,152
MV 216,100,68
DX 1
MV 212,96,56
AD 9,6
AD 10,7
MV 212,100,60
AD 11,8
AW 144,9
MV 208,96,48
AW 148,10
AW 152,11
MV 208,100,52
TR 96,2,0
UH 6,13
MV 232,104,48
TR 98,3,0
UL 7,16
MV 232,108,52
DX 2
MV 236,104,56
UH 8,16
MV 236,108,60
TR 100,2,1
MV 240,104,64
TR 102,3,1
MV 240,108,68
ds_read_b64 v[12:13], v18 offset:13824
MV 244,104,72
ds_read_b32 v16, v19 offset:13824
AR 9,156
MV 244,108,76
AR 10,160
AR 11,164
MV 248,104,80
AD 9,6
AD 10,7
MV 248,108,84
AD 11,8
AW 156,9
MV 252,104,88
.Lk2p9:
s_cbranch_execz .Lk2r1
AW 160,10
AW 164,11
MV 252,108,92
WL 6
TR 104,4,0
UL 6,14
MA 188,112,88
TR 106,5,0
UH 7,14
MA 188,116,92
UL 8,15
AR 9,145
MA 184,112,80
AR 10,149
AR 11,153
MA 184,116,84
TR 108,4,1
AD 9,6
MA 180,112,72
TR 110,5,1
AD 10,7
MA 180,116,76
AD 11,8
AW 145,9
MA 176,112,64
AW 149,10
AW 153,11
MA 176,116,68
UH 6,15
UL 7,17
MA 172,112,56
UH 8,17
MA 172,116,60
ds_read_b64 v[14:15], v18 offset:14592
MA 168,112,48
WV 9
ds_read_b32 v17, v19 offset:14592
AR 9,157
MA 168,116,52
WL 6
TR 112,2,2
AR 10,161
MV 40,96,0
TR 114,3,2
AR 11,165
MV 40,100,4
s_add_u32 s44, s84, 839680
s_addc_u32 s45, s85, 0
AD 9,6
MV 44,96,8
LA 48,-4
MV 44,100,12
TR 116,2,3
AD 10,7
MV 48,96,16
TR 118,3,3
AD 11,8
MV 48,100,20
AW 157,9
AW 161,10
MV 52,96,24
AW 165,11
UL 6,12
MV 52,100,28
LA 52,-3
MV 56,96,32
UH 7,12
UL 8,13
MV 56,100,36
AR 9,146
AR 10,150
MV 60,96,40
AR 11,154
AD 9,6
MV 60,100,44
WL 4
TR 96,4,2
AD 10,7
MV 84,104,40
TR 98,5,2
AD 11,8
MV 84,108,44
LA 56,-2
MV 80,104,32
AW 146,9
AW 150,10
MV 80,108,36
TR 100,4,3
AW 154,11
MV 76,104,24
TR 102,5,3
UH 6,13
MV 76,108,28
UL 7,16
UH 8,16
MV 72,104,16
LA 60,-1
MV 72,108,20
AR 9,158
AR 10,162
MV 68,104,8
AR 11,166
AD 9,6
MV 68,108,12
AD 10,7
AD 11,8
MV 64,104,0
AW 158,9
AW 162,10
MV 64,108,4
WL 4
TR 104,2,4
AW 166,11
MV 88,112,0
TR 106,3,4
UL 6,14
MV 88,116,4
LA 64,0
MV 92,112,8
UH 7,14
UL 8,15
MV 92,116,12
TR 108,2,5
AR 9,147
MV 96,112,16
TR 110,3,5
AR 10,151
MV 96,116,20
AR 11,155
AD 9,6
MV 100,112,24
LA 68,1
MV 100,116,28
AD 10,7
AD 11,8
MV 104,112,32
AW 147,9
AW 151,10
MV 104,116,36
s_add_u32 s44, s44, 6144
s_addc_u32 s45, s45, 0
AW 155,11
MV 108,112,40
UH 6,15
UL 7,17
MV 108,116,44
WL 4
TR 112,4,4
UH 8,17
MA 140,96,40
TR 114,5,4
AR 9,159
MA 140,100,44
LA 72,-4
MA 136,96,32
AR 10,163
AR 11,167
MA 136,100,36
TR 116,4,5
AD 9,6
MA 132,96,24
TR 118,5,5
AD 10,7
MA 132,100,28
AD 11,8
AW 159,9
MA 128,96,16
LA 76,-3
MA 128,100,20
AW 163,10
AW 167,11
MA 124,96,8
MA 124,100,12
WL 0
s_barrier
MA 120,96,0
s_add_u32 m0, %[hm], 30720
MA 120,100,4
TR 96,2,6
MV 112,104,0
TR 98,3,6
MV 112,108,4
LA 80,-2
MV 116,104,8
s_add_u32 s40, s64, 1579008
s_addc_u32 s41, s65, 0
MV 116,108,12
TR 100,2,7
MV 120,104,16
TR 102,3,7
MV 120,108,20
MV 124,104,24
LA 84,-1
MV 124,108,28
MV 128,104,32
MV 128,108,36
MV 132,104,40
MV 132,108,44
TR 104,4,6
MV 156,112,40
TR 106,5,6
MV 156,116,44
LA 88,0
MV 152,112,32
MV 152,116,36
TR 108,4,7
MV 148,112,24
TR 110,5,7
MV 148,116,28
MV 144,112,16
LA 92,1
MV 144,116,20
MV 140,112,8
MV 140,116,12
MV 136,112,0
MV 136,116,4
WL 4
TR 112,2,8
MV 160,96,0
TR 114,3,8
MV 160,100,4
DH 0
MV 164,96,8
MV 164,100,12
TR 116,2,9
MV 168,96,16
s_add_u32 m0, %[hm], 32768
TR 118,3,9
MV 168,100,20
s_add_u32 s40, s66, 1572864
s_addc_u32 s41, s67, 0
MV 172,96,24
DH 0
MV 172,100,28
MV 176,96,32
MV 176,100,36
MV 180,96,40
MV 180,100,44
WL 4
TR 96,4,8
MA 164,104,40
TR 98,5,8
MA 164,108,44
DH 2
MA 160,104,32
MA 160,108,36
TR 100,4,9
MA 156,104,24
TR 102,5,9
MA 156,108,28
s_add_u32 m0, %[hm], 0
v_xor_b32 v1, 0x100, v1
MA 152,104,16
s_add_u32 s40, s64, 1572864
s_addc_u32 s41, s65, 0
MA 152,108,20
DH 0
MA 148,104,8
MA 148,108,12
MA 144,104,0
MA 144,108,4
WL 4
TR 104,2,10
MV 184,112,0
TR 106,3,10
MV 184,116,4
DH 2
MV 188,112,8
MV 188,116,12
TR 108,2,11
MV 192,112,16
s_add_u32 m0, %[hm], 4096
TR 110,3,11
MV 192,116,20
)"
R"(s_add_u32 s40, s64, 1576960
s_addc_u32 s41, s65, 0
MV 196,112,24
DH 0
MV 196,116,28
MV 200,112,32
MV 200,116,36
s_add_u32 m0, %[wm], 12288
MV 204,112,40
s_add_u32 s40, s90, 49152
MV 204,116,44
WL 4
TR 112,4,10
MV 228,96,40
TR 114,5,10
MV 228,100,44
s_add_u32 s40, s40, s70
s_addc_u32 s41, s71, 0
MV 224,96,32
DX 0
MV 224,100,36
TR 116,4,11
MV 220,96,24
TR 118,5,11
MV 220,100,28
MV 216,96,16
MV 216,100,20
DX 1
MV 212,96,8
MV 212,100,12
MV 208,96,0
MV 208,100,4
WL 4
TR 96,2,12
MV 232,104,0
TR 98,3,12
MV 232,108,4
DX 2
MV 236,104,8
MV 236,108,12
TR 100,2,13
MV 240,104,16
TR 102,3,13
MV 240,108,20
MV 244,104,24
MV 244,108,28
MV 248,104,32
MV 248,108,36
MV 252,104,40
MV 252,108,44
WL 4
TR 104,4,12
MA 188,112,40
TR 106,5,12
MA 188,116,44
MA 184,112,32
MA 184,116,36
TR 108,4,13
MA 180,112,24
TR 110,5,13
MA 180,116,28
MA 176,112,16
MA 176,116,20
MA 172,112,8
MA 172,116,12
MA 168,112,0
WV 9
MA 168,116,4
WL 4
TR 112,2,14
MV 40,96,48
TR 114,3,14
MV 40,100,52
s_add_u32 s44, s84, 888832
s_addc_u32 s45, s85, 0
MV 44,96,56
LA 0,-4
MV 44,100,60
TR 116,2,15
MV 48,96,64
TR 118,3,15
MV 48,100,68
MV 52,96,72
MV 52,100,76
LA 4,-3
MV 56,96,80
MV 56,100,84
MV 60,96,88
MV 60,100,92
WL 4
TR 96,4,14
MV 84,104,88
TR 98,5,14
MV 84,108,92
LA 8,-2
MV 80,104,80
MV 80,108,84
TR 100,4,15
MV 76,104,72
TR 102,5,15
MV 76,108,76
MV 72,104,64
LA 12,-1
MV 72,108,68
MV 68,104,56
MV 68,108,60
MV 64,104,48
MV 64,108,52
WL 4
TR 104,2,16
MV 88,112,48
TR 106,3,16
MV 88,116,52
LA 16,0
MV 92,112,56
MV 92,116,60
TR 108,2,17
MV 96,112,64
TR 110,3,17
MV 96,116,68
MV 100,112,72
LA 20,1
MV 100,116,76
MV 104,112,80
MV 104,116,84
s_add_u32 s44, s44, 6144
s_addc_u32 s45, s45, 0
MV 108,112,88
MV 108,116,92
WL 4
TR 112,4,16
MA 140,96,88
TR 114,5,16
MA 140,100,92
LA 24,-4
MA 136,96,80
MA 136,100,84
TR 116,4,17
MA 132,96,72
TR 118,5,17
MA 132,100,76
MA 128,96,64
LA 28,-3
MA 128,100,68
MA 124,96,56
MA 124,100,60
WL 0
s_barrier
MA 120,96,48
s_add_u32 m0, %[hm], 6144
MA 120,100,52
TR 96,2,18
MV 112,104,48
TR 98,3,18
MV 112,108,52
LA 32,-2
MV 116,104,56
s_add_u32 s40, s64, 1579008
s_addc_u32 s41, s65, 0
MV 116,108,60
TR 100,2,19
MV 120,104,64
TR 102,3,19
MV 120,108,68
MV 124,104,72
LA 36,-1
MV 124,108,76
MV 128,104,80
MV 128,108,84
MV 132,104,88
MV 132,108,92
TR 104,4,18
MV 156,112,88
TR 106,5,18
MV 156,116,92
LA 40,0
MV 152,112,80
MV 152,116,84
TR 108,4,19
MV 148,112,72
TR 110,5,19
MV 148,116,76
MV 144,112,64
LA 44,1
MV 144,116,68
MV 140,112,56
MV 140,116,60
MV 136,112,48
MV 136,116,52
WL 4
TR 112,2,20
MV 160,96,48
TR 114,3,20
MV 160,100,52
DH 0
MV 164,96,56
MV 164,100,60
TR 116,2,21
MV 168,96,64
s_add_u32 m0, %[hm], 8192
TR 118,3,21
MV 168,100,68
s_add_u32 s40, s66, 1572864
s_addc_u32 s41, s67, 0
MV 172,96,72
DH 0
MV 172,100,76
MV 176,96,80
MV 176,100,84
MV 180,96,88
MV 180,100,92
WL 4
TR 96,4,20
MA 164,104,88
TR 98,5,20
MA 164,108,92
DH 2
MA 160,104,80
MA 160,108,84
TR 100,4,21
MA 156,104,72
PA 20480
TR 102,5,21
MA 156,108,76
MA 152,104,64
DF
MA 152,108,68
s_add_u32 m0, %[hm], 12288
v_xor_b32 v1, 0x700, v1
MA 148,104,56
s_add_u32 s40, s64, 1572864
s_addc_u32 s41, s65, 0
MA 148,108,60
MA 144,104,48
MA 144,108,52
WL 4
TR 104,2,22
MV 184,112,48
TR 106,3,22
MV 184,116,52
DH 0
MV 188,112,56
MV 188,116,60
TR 108,2,23
MV 192,112,64
TR 110,3,23
MV 192,116,68
MV 196,112,72
DH 2
MV 196,116,76
MV 200,112,80
MV 200,116,84
s_add_u32 m0, %[hm], 16384
MV 204,112,88
s_add_u32 s40, s64, 1576960
s_addc_u32 s41, s65, 0
MV 204,116,92
WL 4
TR 112,4,22
MV 228,96,88
TR 114,5,22
MV 228,100,92
DH 0
MV 224,96,80
ds_read_b64 v[12:13], v18 offset:0
MV 224,100,84
TR 116,4,23
MV 220,96,72
TR 118,5,23
MV 220,100,76
ds_read_b32 v16, v19 offset:0
MV 216,96,64
.Lk2p10:
s_cbranch_execz .Lk2r1
ds_read_b64 v[14:15], v18 offset:768
MV 216,100,68
ds_read_b32 v17, v19 offset:768
MV 212,96,56
MV 212,100,60
MV 208,96,48
MV 208,100,52
WL 8
TR 96,2,24
MV 232,104,48
WL 3
TR 98,3,24
UL 6,12
MV 232,108,52
UH 7,12
UL 8,13
MV 236,104,56
AD 184,6
AD 188,7
MV 236,108,60
TR 100,2,25
AD 192,8
MV 240,104,64
TR 102,3,25
UH 6,13
MV 240,108,68
UL 7,16
UH 8,16
MV 244,104,72
ds_read_b64 v[12:13], v18 offset:1536
MV 244,108,76
ds_read_b32 v16, v19 offset:1536
AD 196,6
MV 248,104,80
AD 200,7
AD 204,8
MV 248,108,84
WL 6
UL 6,14
UH 7,14
MV 252,104,88
UL 8,15
AD 185,6
MV 252,108,92
TR 104,4,24
AD 189,7
MA 188,112,88
TR 106,5,24
AD 193,8
MA 188,116,92
UH 6,15
UL 7,17
MA 184,112,80
UH 8,17
MA 184,116,84
TR 108,4,25
MA 180,112,72
TR 110,5,25
MA 180,116,76
ds_read_b64 v[14:15], v18 offset:2304
MA 176,112,64
ds_read_b32 v17, v19 offset:2304
AD 197,6
MA 176,116,68
AD 201,7
AD 205,8
MA 172,112,56
WL 6
UL 6,12
UH 7,12
MA 172,116,60
UL 8,13
AD 186,6
MA 168,112,48
AD 190,7
AD 194,8
MA 168,116,52
WV 7
TR 112,2,26
UH 6,13
MV 40,96,0
TR 114,3,26
UL 7,16
MV 40,100,4
s_add_u32 s44, s84, 937984
s_addc_u32 s45, s85, 0
UH 8,16
MV 44,96,8
LA 48,-4
MV 44,100,12
TR 116,2,27
AD 198,6
MV 48,96,16
TR 118,3,27
AD 202,7
MV 48,100,20
WL 4
AD 206,8
UL 6,14
MV 52,96,24
UH 7,14
UL 8,15
MV 52,100,28
LA 52,-3
MV 56,96,32
AD 187,6
AD 191,7
MV 56,100,36
AD 195,8
UH 6,15
MV 60,96,40
UL 7,17
UH 8,17
MV 60,100,44
TR 96,4,26
AD 199,6
MV 84,104,40
TR 98,5,26
AD 203,7
MV 84,108,44
LA 56,-2
MV 80,104,32
AD 207,8
MV 80,108,36
TR 100,4,27
MV 76,104,24
)"
R"(TR 102,5,27
MV 76,108,28
MV 72,104,16
LA 60,-1
MV 72,108,20
MV 68,104,8
MV 68,108,12
MV 64,104,0
MV 64,108,4
WL 4
TR 104,2,28
MV 88,112,0
TR 106,3,28
MV 88,116,4
LA 64,0
MV 92,112,8
MV 92,116,12
TR 108,2,29
MV 96,112,16
TR 110,3,29
MV 96,116,20
MV 100,112,24
LA 68,1
MV 100,116,28
MV 104,112,32
MV 104,116,36
s_add_u32 s44, s44, 6144
s_addc_u32 s45, s45, 0
MV 108,112,40
MV 108,116,44
WL 4
TR 112,4,28
MA 140,96,40
TR 114,5,28
MA 140,100,44
LA 72,-4
MA 136,96,32
MA 136,100,36
TR 116,4,29
MA 132,96,24
TR 118,5,29
MA 132,100,28
MA 128,96,16
LA 76,-3
MA 128,100,20
MA 124,96,8
MA 124,100,12
WL 0
s_barrier
MA 120,96,0
s_add_u32 m0, %[hm], 18432
MA 120,100,4
TR 96,2,30
MV 112,104,0
TR 98,3,30
MV 112,108,4
LA 80,-2
MV 116,104,8
s_add_u32 s40, s64, 1579008
s_addc_u32 s41, s65, 0
MV 116,108,12
TR 100,2,31
MV 120,104,16
TR 102,3,31
MV 120,108,20
MV 124,104,24
LA 84,-1
MV 124,108,28
MV 128,104,32
MV 128,108,36
MV 132,104,40
MV 132,108,44
TR 104,4,30
MV 156,112,40
TR 106,5,30
MV 156,116,44
LA 88,0
MV 152,112,32
MV 152,116,36
TR 108,4,31
MV 148,112,24
TR 110,5,31
MV 148,116,28
MV 144,112,16
LA 92,1
MV 144,116,20
MV 140,112,8
MV 140,116,12
MV 136,112,0
MV 136,116,4
WL 4
TR 112,2,32
MV 160,96,0
TR 114,3,32
MV 160,100,4
DH 0
MV 164,96,8
MV 164,100,12
TR 116,2,33
MV 168,96,16
s_add_u32 m0, %[hm], 20480
TR 118,3,33
MV 168,100,20
s_add_u32 s40, s66, 1572864
s_addc_u32 s41, s67, 0
MV 172,96,24
DH 0
MV 172,100,28
MV 176,96,32
MV 176,100,36
MV 180,96,40
MV 180,100,44
WL 4
TR 96,4,32
MA 164,104,40
TR 98,5,32
MA 164,108,44
DH 2
MA 160,104,32
MA 160,108,36
TR 100,4,33
MA 156,104,24
TR 102,5,33
MA 156,108,28
s_add_u32 m0, %[hm], 24576
v_xor_b32 v1, 0x100, v1
MA 152,104,16
s_add_u32 s40, s64, 1572864
s_addc_u32 s41, s65, 0
MA 152,108,20
DH 0
MA 148,104,8
MA 148,108,12
MA 144,104,0
MA 144,108,4
WL 4
TR 104,2,34
MV 184,112,0
TR 106,3,34
MV 184,116,4
DH 2
MV 188,112,8
MV 188,116,12
TR 108,2,35
MV 192,112,16
s_add_u32 m0, %[hm], 28672
TR 110,3,35
MV 192,116,20
s_add_u32 s40, s64, 1576960
s_addc_u32 s41, s65, 0
MV 196,112,24
DH 0
MV 196,116,28
MV 200,112,32
MV 200,116,36
s_add_u32 m0, %[wm], 0
MV 204,112,40
s_add_u32 s40, s91, 0
MV 204,116,44
WL 4
TR 112,4,34
MV 228,96,40
TR 114,5,34
MV 228,100,44
s_add_u32 s40, s40, s70
s_addc_u32 s41, s71, 0
MV 224,96,32
DX 0
MV 224,100,36
TR 116,4,35
MV 220,96,24
TR 118,5,35
MV 220,100,28
MV 216,96,16
MV 216,100,20
DX 1
MV 212,96,8
MV 212,100,12
MV 208,96,0
MV 208,100,4
WL 4
TR 96,2,0
MV 232,104,0
TR 98,3,0
MV 232,108,4
DX 2
MV 236,104,8
ds_read_b64 v[12:13], v18 offset:12288
MV 236,108,12
TR 100,2,1
MV 240,104,16
TR 102,3,1
MV 240,108,20
ds_read_b32 v16, v19 offset:12288
MV 244,104,24
ds_read_b64 v[14:15], v18 offset:13056
MV 244,108,28
ds_read_b32 v17, v19 offset:13056
MV 248,104,32
MV 248,108,36
MV 252,104,40
MV 252,108,44
WL 8
TR 104,4,0
MA 188,112,40
WL 3
TR 106,5,0
UL 6,12
MA 188,116,44
UH 7,12
UL 8,13
MA 184,112,32
AD 208,6
AD 212,7
MA 184,116,36
TR 108,4,1
AD 216,8
MA 180,112,24
TR 110,5,1
UH 6,13
MA 180,116,28
UL 7,16
UH 8,16
MA 176,112,16
ds_read_b64 v[12:13], v18 offset:13824
MA 176,116,20
ds_read_b32 v16, v19 offset:13824
AD 220,6
MA 172,112,8
AD 224,7
AD 228,8
MA 172,116,12
WL 6
UL 6,14
UH 7,14
MA 168,112,0
UL 8,15
AD 209,6
MA 168,116,4
WV 9
TR 112,2,2
AD 213,7
MV 40,96,48
TR 114,3,2
AD 217,8
MV 40,100,52
s_add_u32 s44, s84, 987136
s_addc_u32 s45, s85, 0
UH 6,15
MV 44,96,56
LA 0,-4
MV 44,100,60
TR 116,2,3
UL 7,17
MV 48,96,64
TR 118,3,3
UH 8,17
MV 48,100,68
ds_read_b64 v[14:15], v18 offset:14592
MV 52,96,72
ds_read_b32 v17, v19 offset:14592
AD 221,6
MV 52,100,76
LA 4,-3
MV 56,96,80
AD 225,7
AD 229,8
MV 56,100,84
WL 6
UL 6,12
UH 7,12
MV 60,96,88
UL 8,13
AD 210,6
MV 60,100,92
TR 96,4,2
AD 214,7
MV 84,104,88
TR 98,5,2
AD 218,8
MV 84,108,92
LA 8,-2
MV 80,104,80
UH 6,13
UL 7,16
MV 80,108,84
TR 100,4,3
UH 8,16
MV 76,104,72
TR 102,5,3
AD 222,6
MV 76,108,76
AD 226,7
AD 230,8
MV 72,104,64
LA 12,-1
MV 72,108,68
WL 4
UL 6,14
UH 7,14
MV 68,104,56
UL 8,15
AD 211,6
MV 68,108,60
AD 215,7
AD 219,8
MV 64,104,48
UH 6,15
UL 7,17
MV 64,108,52
TR 104,2,4
UH 8,17
MV 88,112,48
TR 106,3,4
AD 223,6
MV 88,116,52
LA 16,0
MV 92,112,56
AD 227,7
AD 231,8
MV 92,116,60
TR 108,2,5
MV 96,112,64
TR 110,3,5
MV 96,116,68
MV 100,112,72
LA 20,1
MV 100,116,76
MV 104,112,80
MV 104,116,84
s_add_u32 s44, s44, 6144
s_addc_u32 s45, s45, 0
MV 108,112,88
MV 108,116,92
WL 4
TR 112,4,4
MA 140,96,88
TR 114,5,4
MA 140,100,92
LA 24,-4
MA 136,96,80
MA 136,100,84
TR 116,4,5
MA 132,96,72
TR 118,5,5
MA 132,100,76
MA 128,96,64
LA 28,-3
MA 128,100,68
MA 124,96,56
MA 124,100,60
WL 0
s_barrier
MA 120,96,48
s_add_u32 m0, %[hm], 30720
MA 120,100,52
TR 96,2,6
MV 112,104,48
TR 98,3,6
MV 112,108,52
LA 32,-2
MV 116,104,56
s_add_u32 s40, s64, 1579008
s_addc_u32 s41, s65, 0
MV 116,108,60
TR 100,2,7
MV 120,104,64
TR 102,3,7
MV 120,108,68
MV 124,104,72
LA 36,-1
MV 124,108,76
MV 128,104,80
MV 128,108,84
MV 132,104,88
MV 132,108,92
TR 104,4,6
MV 156,112,88
TR 106,5,6
MV 156,116,92
LA 40,0
MV 152,112,80
MV 152,116,84
TR 108,4,7
MV 148,112,72
TR 110,5,7
MV 148,116,76
MV 144,112,64
LA 44,1
MV 144,116,68
MV 140,112,56
MV 140,116,60
MV 136,112,48
MV 136,116,52
WL 4
TR 112,2,8
MV 160,96,48
TR 114,3,8
MV 160,100,52
DH 0
)"
R"(MV 164,96,56
MV 164,100,60
TR 116,2,9
MV 168,96,64
s_add_u32 m0, %[hm], 32768
TR 118,3,9
MV 168,100,68
s_add_u32 s40, s66, 1572864
s_addc_u32 s41, s67, 0
MV 172,96,72
DH 0
MV 172,100,76
MV 176,96,80
MV 176,100,84
MV 180,96,88
MV 180,100,92
WL 4
TR 96,4,8
MA 164,104,88
TR 98,5,8
MA 164,108,92
DH 2
MA 160,104,80
MA 160,108,84
TR 100,4,9
MA 156,104,72
TR 102,5,9
MA 156,108,76
s_add_u32 m0, %[hm], 0
v_xor_b32 v1, 0x300, v1
MA 152,104,64
s_add_u32 s40, s64, 1572864
s_addc_u32 s41, s65, 0
MA 152,108,68
DH 0
MA 148,104,56
MA 148,108,60
MA 144,104,48
MA 144,108,52
WL 4
TR 104,2,10
MV 184,112,48
TR 106,3,10
MV 184,116,52
DH 2
MV 188,112,56
MV 188,116,60
TR 108,2,11
MV 192,112,64
s_add_u32 m0, %[hm], 4096
TR 110,3,11
MV 192,116,68
s_add_u32 s40, s64, 1576960
s_addc_u32 s41, s65, 0
MV 196,112,72
.Lk2p11:
s_cbranch_execz .Lk2r1
DH 0
MV 196,116,76
MV 200,112,80
MV 200,116,84
s_add_u32 m0, %[wm], 12288
MV 204,112,88
s_add_u32 s40, s91, 49152
MV 204,116,92
WL 4
TR 112,4,10
MV 228,96,88
TR 114,5,10
MV 228,100,92
s_add_u32 s40, s40, s70
s_addc_u32 s41, s71, 0
MV 224,96,80
DX 0
MV 224,100,84
TR 116,4,11
MV 220,96,72
TR 118,5,11
MV 220,100,76
MV 216,96,64
MV 216,100,68
DX 1
MV 212,96,56
MV 212,100,60
MV 208,96,48
MV 208,100,52
WL 4
TR 96,2,12
MV 232,104,48
TR 98,3,12
MV 232,108,52
DX 2
MV 236,104,56
MV 236,108,60
TR 100,2,13
MV 240,104,64
TR 102,3,13
MV 240,108,68
MV 244,104,72
MV 244,108,76
MV 248,104,80
MV 248,108,84
MV 252,104,88
MV 252,108,92
WL 4
TR 104,4,12
MA 188,112,88
TR 106,5,12
MA 188,116,92
MA 184,112,80
MA 184,116,84
TR 108,4,13
MA 180,112,72
TR 110,5,13
MA 180,116,76
MA 176,112,64
MA 176,116,68
MA 172,112,56
MA 172,116,60
MA 168,112,48
WV 9
MA 168,116,52
WL 4
TR 112,2,14
MV 40,96,0
TR 114,3,14
MV 40,100,4
s_add_u32 s44, s84, 1036288
s_addc_u32 s45, s85, 0
MV 44,96,8
LA 48,-4
MV 44,100,12
TR 116,2,15
MV 48,96,16
TR 118,3,15
MV 48,100,20
MV 52,96,24
MV 52,100,28
LA 52,-3
MV 56,96,32
MV 56,100,36
MV 60,96,40
MV 60,100,44
WL 4
TR 96,4,14
MV 84,104,40
TR 98,5,14
MV 84,108,44
LA 56,-2
MV 80,104,32
MV 80,108,36
TR 100,4,15
MV 76,104,24
TR 102,5,15
MV 76,108,28
MV 72,104,16
LA 60,-1
MV 72,108,20
MV 68,104,8
MV 68,108,12
MV 64,104,0
MV 64,108,4
WL 4
TR 104,2,16
MV 88,112,0
TR 106,3,16
MV 88,116,4
LA 64,0
MV 92,112,8
MV 92,116,12
TR 108,2,17
MV 96,112,16
TR 110,3,17
MV 96,116,20
MV 100,112,24
LA 68,1
MV 100,116,28
MV 104,112,32
MV 104,116,36
s_add_u32 s44, s44, 6144
s_addc_u32 s45, s45, 0
MV 108,112,40
MV 108,116,44
WL 4
TR 112,4,16
MA 140,96,40
TR 114,5,16
MA 140,100,44
LA 72,-4
MA 136,96,32
MA 136,100,36
TR 116,4,17
MA 132,96,24
TR 118,5,17
MA 132,100,28
MA 128,96,16
LA 76,-3
MA 128,100,20
MA 124,96,8
MA 124,100,12
WL 0
s_barrier
ds_read_b64 v[12:13], v18 offset:0
MA 120,96,0
s_add_u32 m0, %[hm], 6144
ds_read_b32 v16, v19 offset:0
MA 120,100,4
TR 96,2,18
MV 112,104,0
TR 98,3,18
MV 112,108,4
LA 80,-2
MV 116,104,8
s_add_u32 s40, s64, 1579008
s_addc_u32 s41, s65, 0
MV 116,108,12
TR 100,2,19
MV 120,104,16
TR 102,3,19
MV 120,108,20
ds_read_b64 v[14:15], v18 offset:768
MV 124,104,24
LA 84,-1
MV 124,108,28
ds_read_b32 v17, v19 offset:768
MV 128,104,32
MV 128,108,36
MV 132,104,40
MV 132,108,44
TR 104,4,18
MV 156,112,40
WL 7
TR 106,5,18
UL 6,12
MV 156,116,44
LA 88,0
MV 152,112,32
UH 7,12
UL 8,13
MV 152,116,36
TR 108,4,19
AD 232,6
MV 148,112,24
TR 110,5,19
AD 236,7
MV 148,116,28
AD 240,8
UH 6,13
MV 144,112,16
LA 92,1
MV 144,116,20
UL 7,16
UH 8,16
MV 140,112,8
ds_read_b64 v[12:13], v18 offset:1536
MV 140,116,12
ds_read_b32 v16, v19 offset:1536
AD 244,6
MV 136,112,0
AD 248,7
AD 252,8
MV 136,116,4
WL 6
TR 112,2,20
UL 6,14
MV 160,96,0
TR 114,3,20
UH 7,14
MV 160,100,4
DH 0
MV 164,96,8
UL 8,15
AD 233,6
MV 164,100,12
TR 116,2,21
AD 237,7
MV 168,96,16
s_add_u32 m0, %[hm], 8192
TR 118,3,21
AD 241,8
MV 168,100,20
s_add_u32 s40, s66, 1572864
s_addc_u32 s41, s67, 0
UH 6,15
MV 172,96,24
DH 0
MV 172,100,28
UL 7,17
UH 8,17
MV 176,96,32
ds_read_b64 v[14:15], v18 offset:2304
MV 176,100,36
ds_read_b32 v17, v19 offset:2304
AD 245,6
MV 180,96,40
AD 249,7
AD 253,8
MV 180,100,44
WL 6
TR 96,4,20
UL 6,12
MA 164,104,40
TR 98,5,20
UH 7,12
MA 164,108,44
DH 2
MA 160,104,32
UL 8,13
AD 234,6
MA 160,108,36
TR 100,4,21
AD 238,7
MA 156,104,24
TR 102,5,21
AD 242,8
MA 156,108,28
s_add_u32 m0, %[hm], 12288
v_xor_b32 v1, 0x100, v1
UH 6,13
MA 152,104,16
s_add_u32 s40, s64, 1572864
s_addc_u32 s41, s65, 0
UL 7,16
MA 152,108,20
DH 0
MA 148,104,8
UH 8,16
AD 246,6
MA 148,108,12
AD 250,7
AD 254,8
MA 144,104,0
WL 4
UL 6,14
UH 7,14
MA 144,108,4
TR 104,2,22
UL 8,15
MV 184,112,0
TR 106,3,22
AD 235,6
MV 184,116,4
DH 2
MV 188,112,8
AD 239,7
AD 243,8
MV 188,116,12
TR 108,2,23
UH 6,15
MV 192,112,16
s_add_u32 m0, %[hm], 16384
TR 110,3,23
UL 7,17
MV 192,116,20
s_add_u32 s40, s64, 1576960
s_addc_u32 s41, s65, 0
UH 8,17
MV 196,112,24
DH 0
MV 196,116,28
AD 247,6
AD 251,7
MV 200,112,32
AD 255,8
MV 200,116,36
MV 204,112,40
MV 204,116,44
WL 4
TR 112,4,22
MV 228,96,40
TR 114,5,22
MV 228,100,44
MV 224,96,32
MV 224,100,36
TR 116,4,23
MV 220,96,24
TR 118,5,23
MV 220,100,28
MV 216,96,16
MV 216,100,20
MV 212,96,8
MV 212,100,12
MV 208,96,0
MV 208,100,4
WL 4
TR 96,2,24
MV 232,104,0
TR 98,3,24
MV 232,108,4
MV 236,104,8
MV 236,108,12
TR 100,2,25
MV 240,104,16
TR 102,3,25
MV 240,108,20
MV 244,104,24
MV 244,108,28
MV 248,104,32
MV 248,108,36
MV 252,104,40
)"
R"(MV 252,108,44
WL 4
TR 104,4,24
MA 188,112,40
TR 106,5,24
MA 188,116,44
MA 184,112,32
MA 184,116,36
TR 108,4,25
MA 180,112,24
TR 110,5,25
MA 180,116,28
MA 176,112,16
MA 176,116,20
MA 172,112,8
MA 172,116,12
MA 168,112,0
WV 6
MA 168,116,4
WL 4
TR 112,2,26
MV 40,96,48
TR 114,3,26
MV 40,100,52
s_add_u32 s44, s84, 1085440
s_addc_u32 s45, s85, 0
MV 44,96,56
LA 0,-4
MV 44,100,60
TR 116,2,27
MV 48,96,64
TR 118,3,27
MV 48,100,68
MV 52,96,72
MV 52,100,76
LA 4,-3
MV 56,96,80
MV 56,100,84
MV 60,96,88
MV 60,100,92
WL 4
TR 96,4,26
MV 84,104,88
TR 98,5,26
MV 84,108,92
LA 8,-2
MV 80,104,80
MV 80,108,84
TR 100,4,27
MV 76,104,72
TR 102,5,27
MV 76,108,76
MV 72,104,64
LA 12,-1
MV 72,108,68
MV 68,104,56
MV 68,108,60
MV 64,104,48
MV 64,108,52
WL 4
TR 104,2,28
MV 88,112,48
TR 106,3,28
MV 88,116,52
LA 16,0
MV 92,112,56
MV 92,116,60
TR 108,2,29
MV 96,112,64
TR 110,3,29
MV 96,116,68
MV 100,112,72
LA 20,1
MV 100,116,76
MV 104,112,80
MV 104,116,84
s_add_u32 s44, s44, 6144
s_addc_u32 s45, s45, 0
MV 108,112,88
MV 108,116,92
WL 4
TR 112,4,28
MA 140,96,88
TR 114,5,28
MA 140,100,92
LA 24,-4
MA 136,96,80
MA 136,100,84
TR 116,4,29
MA 132,96,72
TR 118,5,29
MA 132,100,76
MA 128,96,64
LA 28,-3
MA 128,100,68
MA 124,96,56
MA 124,100,60
WL 0
s_barrier
ds_read_b64 v[12:13], v18 offset:12288
ds_read_b32 v16, v19 offset:12288
ds_read_b64 v[14:15], v18 offset:13056
MA 120,96,48
s_add_u32 m0, %[hm], 18432
ds_read_b32 v17, v19 offset:13056
MA 120,100,52
TR 96,2,30
MV 112,104,48
TR 98,3,30
MV 112,108,52
LA 32,-2
MV 116,104,56
s_add_u32 s40, s64, 1579008
s_addc_u32 s41, s65, 0
MV 116,108,60
WL 4
TR 100,2,31
UL 6,12
UH 7,12
MV 120,104,64
TR 102,3,31
UL 8,13
AR 9,168
MV 120,108,68
AR 10,172
AR 11,176
AD 9,6
MV 124,104,72
LA 36,-1
AD 10,7
MV 124,108,76
AD 11,8
AW 168,9
AW 172,10
MV 128,104,80
AW 176,11
UH 6,13
UL 7,16
MV 128,108,84
UH 8,16
ds_read_b64 v[12:13], v18 offset:13824
ds_read_b32 v16, v19 offset:13824
MV 132,104,88
AR 9,180
AR 10,184
AR 11,188
MV 132,108,92
TR 104,4,30
AD 9,6
AD 10,7
MV 156,112,88
TR 106,5,30
AD 11,8
AW 180,9
MV 156,116,92
LA 40,0
AW 184,10
MV 152,112,80
WL 4
AW 188,11
UL 6,14
UH 7,14
MV 152,116,84
TR 108,4,31
UL 8,15
AR 9,169
MV 148,112,72
TR 110,5,31
AR 10,173
AR 11,177
MV 148,116,76
AD 9,6
AD 10,7
AD 11,8
MV 144,112,64
LA 44,1
AW 169,9
MV 144,116,68
AW 173,10
AW 177,11
UH 6,15
MV 140,112,56
UL 7,17
UH 8,17
ds_read_b64 v[14:15], v18 offset:14592
MV 140,116,60
ds_read_b32 v17, v19 offset:14592
AR 9,181
AR 10,185
MV 136,112,48
AR 11,189
AD 9,6
AD 10,7
MV 136,116,52
TR 112,2,32
AD 11,8
AW 181,9
MV 160,96,48
TR 114,3,32
AW 185,10
AW 189,11
MV 160,100,52
WL 5
DH 0
UL 6,12
MV 164,96,56
UH 7,12
UL 8,13
AR 9,170
MV 164,100,60
TR 116,2,33
AR 10,174
AR 11,178
MV 168,96,64
s_add_u32 m0, %[hm], 20480
TR 118,3,33
AD 9,6
AD 10,7
MV 168,100,68
s_add_u32 s40, s66, 1572864
s_addc_u32 s41, s67, 0
AD 11,8
AW 170,9
MV 172,96,72
DH 0
AW 174,10
MV 172,100,76
.Lk2p12:
s_cbranch_execz .Lk2r1
AW 178,11
UH 6,13
UL 7,16
MV 176,96,80
UH 8,16
AR 9,182
AR 10,186
MV 176,100,84
AR 11,190
AD 9,6
AD 10,7
MV 180,96,88
AD 11,8
AW 182,9
AW 186,10
MV 180,100,92
WL 2
TR 96,4,32
AW 190,11
UL 6,14
MA 164,104,88
TR 98,5,32
UH 7,14
UL 8,15
MA 164,108,92
DH 2
AR 9,171
MA 160,104,80
AR 10,175
AR 11,179
AD 9,6
MA 160,108,84
TR 100,4,33
AD 10,7
AD 11,8
MA 156,104,72
PA 20480
TR 102,5,33
AW 171,9
AW 175,10
MA 156,108,76
AW 179,11
UH 6,15
UL 7,17
MA 152,104,64
DF
UH 8,17
MA 152,108,68
s_bitcmp1_b32 s48, 1
s_cselect_b64 s[64:65], s[58:59], s[56:57]
AR 9,183
AR 10,187
MA 148,104,56
s_bitcmp1_b32 s48, 17
s_cselect_b32 s40, 16384, 0
AR 11,191
AD 9,6
MA 148,108,60
s_add_u32 s64, s64, s40
s_addc_u32 s65, s65, 0
AD 10,7
AD 11,8
MA 144,104,48
s_add_u32 m0, %[hm], 24576
v_xor_b32 v1, 0x700, v1
AW 183,9
AW 187,10
MA 144,108,52
WL 2
TR 104,2,34
AW 191,11
MV 184,112,48
TR 106,3,34
MV 184,116,52
s_add_u32 s40, s64, 262144
s_addc_u32 s41, s65, 0
MV 188,112,56
DH 0
MV 188,116,60
TR 108,2,35
MV 192,112,64
TR 110,3,35
MV 192,116,68
MV 196,112,72
MV 196,116,76
DH 2
MV 200,112,80
MV 200,116,84
MV 204,112,88
s_bitcmp1_b32 s48, 0
s_cselect_b64 s[64:65], s[58:59], s[56:57]
MV 204,116,92
WL 4
TR 112,4,34
MV 228,96,88
TR 114,5,34
MV 228,100,92
s_bitcmp1_b32 s48, 16
s_cselect_b32 s40, 16384, 0
MV 224,96,80
s_add_u32 s64, s64, s40
s_addc_u32 s65, s65, 0
MV 224,100,84
s_add_u32 m0, %[hm], 28672
TR 116,4,35
MV 220,96,72
TR 118,5,35
MV 220,100,76
s_add_u32 s40, s64, 4096
s_addc_u32 s41, s65, 0
MV 216,96,64
DH 0
MV 216,100,68
MV 212,96,56
MV 212,100,60
MV 208,96,48
MV 208,100,52
WL 4
TR 96,2,0
MV 232,104,48
TR 98,3,0
MV 232,108,52
MV 236,104,56
MV 236,108,60
TR 100,2,1
MV 240,104,64
TR 102,3,1
MV 240,108,68
MV 244,104,72
MV 244,108,76
MV 248,104,80
MV 248,108,84
MV 252,104,88
MV 252,108,92
WL 4
TR 104,4,0
MA 188,112,88
TR 106,5,0
MA 188,116,92
MA 184,112,80
MA 184,116,84
TR 108,4,1
MA 180,112,72
TR 110,5,1
MA 180,116,76
MA 176,112,64
MA 176,116,68
MA 172,112,56
MA 172,116,60
MA 168,112,48
WV 7
MA 168,116,52
WL 4
TR 112,2,2
MV 40,96,0
TR 114,3,2
MV 40,100,4
s_add_u32 s44, s84, 1134592
s_addc_u32 s45, s85, 0
MV 44,96,8
LA 48,-4
MV 44,100,12
TR 116,2,3
MV 48,96,16
TR 118,3,3
MV 48,100,20
MV 52,96,24
MV 52,100,28
LA 52,-3
MV 56,96,32
MV 56,100,36
)"
R"(MV 60,96,40
MV 60,100,44
WL 4
TR 96,4,2
MV 84,104,40
TR 98,5,2
MV 84,108,44
LA 56,-2
MV 80,104,32
MV 80,108,36
TR 100,4,3
MV 76,104,24
TR 102,5,3
MV 76,108,28
MV 72,104,16
LA 60,-1
MV 72,108,20
MV 68,104,8
MV 68,108,12
MV 64,104,0
MV 64,108,4
WL 4
TR 104,2,4
MV 88,112,0
TR 106,3,4
MV 88,116,4
LA 64,0
MV 92,112,8
MV 92,116,12
TR 108,2,5
MV 96,112,16
TR 110,3,5
MV 96,116,20
MV 100,112,24
LA 68,1
MV 100,116,28
MV 104,112,32
MV 104,116,36
s_add_u32 s44, s44, 6144
s_addc_u32 s45, s45, 0
MV 108,112,40
MV 108,116,44
WL 4
TR 112,4,4
MA 140,96,40
TR 114,5,4
MA 140,100,44
LA 72,-4
MA 136,96,32
s_mov_b64 exec, 0
s_cmp_eq_u32 %[w], 0
s_cbranch_scc1 .Lk2p13
s_cmp_eq_u32 %[w], 1
s_cbranch_scc1 .Lk2p14
s_cmp_eq_u32 %[w], 2
s_cbranch_scc1 .Lk2p15
s_branch .Lk2p16
.Lk2r2:
s_mov_b64 exec, -1
MA 136,100,36
TR 116,4,5
MA 132,96,24
TR 118,5,5
MA 132,100,28
MA 128,96,16
LA 76,-3
MA 128,100,20
MA 124,96,8
MA 124,100,12
WL 0
s_barrier
MA 120,96,0
s_add_u32 m0, %[hm], 30720
MA 120,100,4
TR 96,2,6
MV 112,104,0
TR 98,3,6
MV 112,108,4
LA 80,-2
MV 116,104,8
s_add_u32 s40, s64, 6144
s_addc_u32 s41, s65, 0
MV 116,108,12
TR 100,2,7
MV 120,104,16
TR 102,3,7
MV 120,108,20
MV 124,104,24
LA 84,-1
MV 124,108,28
MV 128,104,32
MV 128,108,36
MV 132,104,40
MV 132,108,44
TR 104,4,6
MV 156,112,40
TR 106,5,6
MV 156,116,44
LA 88,0
MV 152,112,32
MV 152,116,36
TR 108,4,7
MV 148,112,24
TR 110,5,7
MV 148,116,28
MV 144,112,16
LA 92,1
MV 144,116,20
MV 140,112,8
MV 140,116,12
MV 136,112,0
MV 136,116,4
WL 4
TR 112,2,8
MV 160,96,0
TR 114,3,8
MV 160,100,4
DH 0
MV 164,96,8
MV 164,100,12
TR 116,2,9
MV 168,96,16
TR 118,3,9
MV 168,100,20
s_bitcmp1_b32 s49, 0
s_cselect_b64 s[66:67], s[62:63], s[60:61]
MV 172,96,24
s_add_u32 m0, %[hm], 32768
MV 172,100,28
s_add_u32 s40, s66, 0
s_addc_u32 s41, s67, 0
MV 176,96,32
DH 0
MV 176,100,36
MV 180,96,40
MV 180,100,44
WL 4
TR 96,4,8
MA 164,104,40
TR 98,5,8
MA 164,108,44
DH 2
MA 160,104,32
MA 160,108,36
TR 100,4,9
MA 156,104,24
TR 102,5,9
MA 156,108,28
s_bitcmp1_b32 s48, 1
s_cselect_b64 s[64:65], s[58:59], s[56:57]
MA 152,104,16
s_bitcmp1_b32 s48, 17
s_cselect_b32 s40, 16384, 0
MA 152,108,20
s_add_u32 s64, s64, s40
s_addc_u32 s65, s65, 0
MA 148,104,8
s_add_u32 m0, %[hm], 0
v_xor_b32 v1, 0x100, v1
MA 148,108,12
s_add_u32 s40, s64, 262144
s_addc_u32 s41, s65, 0
MA 144,104,0
DH 0
MA 144,108,4
WL 4
TR 104,2,10
MV 184,112,0
TR 106,3,10
MV 184,116,4
MV 188,112,8
MV 188,116,12
TR 108,2,11
MV 192,112,16
TR 110,3,11
MV 192,116,20
DH 2
MV 196,112,24
MV 196,116,28
MV 200,112,32
s_add_u32 m0, %[hm], 4096
MV 200,116,36
s_add_u32 s40, s64, 266240
s_addc_u32 s41, s65, 0
MV 204,112,40
DH 0
MV 204,116,44
WL 4
TR 112,4,10
MV 228,96,40
TR 114,5,10
MV 228,100,44
MV 224,96,32
MV 224,100,36
TR 116,4,11
MV 220,96,24
TR 118,5,11
MV 220,100,28
MV 216,96,16
MV 216,100,20
MV 212,96,8
MV 212,100,12
MV 208,96,0
MV 208,100,4
WL 4
TR 96,2,12
MV 232,104,0
TR 98,3,12
MV 232,108,4
MV 236,104,8
MV 236,108,12
TR 100,2,13
MV 240,104,16
TR 102,3,13
MV 240,108,20
MV 244,104,24
MV 244,108,28
MV 248,104,32
MV 248,108,36
MV 252,104,40
MV 252,108,44
WL 4
TR 104,4,12
MA 188,112,40
TR 106,5,12
MA 188,116,44
MA 184,112,32
MA 184,116,36
TR 108,4,13
MA 180,112,24
TR 110,5,13
MA 180,116,28
MA 176,112,16
MA 176,116,20
MA 172,112,8
MA 172,116,12
MA 168,112,0
WV 6
MA 168,116,4
WL 4
TR 112,2,14
MV 40,96,48
TR 114,3,14
MV 40,100,52
s_add_u32 s44, s84, 4096
s_addc_u32 s45, s85, 0
MV 44,96,56
LA 0,-4
MV 44,100,60
TR 116,2,15
MV 48,96,64
TR 118,3,15
MV 48,100,68
MV 52,96,72
MV 52,100,76
LA 4,-3
MV 56,96,80
MV 56,100,84
MV 60,96,88
MV 60,100,92
WL 4
TR 96,4,14
MV 84,104,88
TR 98,5,14
MV 84,108,92
LA 8,-2
MV 80,104,80
SB 40,32
SB 44,33
MV 80,108,84
TR 100,4,15
SB 48,34
MV 76,104,72
TR 102,5,15
SB 52,35
MV 76,108,76
SB 56,36
SB 60,37
MV 72,104,64
LA 12,-1
MV 72,108,68
CV 12,40,44
CV 13,48,52
MV 68,104,56
CV 16,56,60
SB 41,32
MV 68,108,60
ds_write_b64 v18, v[12:13] offset:24576
MV 64,104,48
SB 45,33
SB 49,34
MV 64,108,52
WL 5
TR 104,2,16
SB 53,35
MV 88,112,48
TR 106,3,16
SB 57,36
MV 88,116,52
LA 16,0
MV 92,112,56
ds_write_b32 v19, v16 offset:24576
MV 92,116,60
TR 108,2,17
SB 61,37
MV 96,112,64
TR 110,3,17
CV 14,41,45
MV 96,116,68
CV 15,49,53
CV 17,57,61
MV 100,112,72
LA 20,1
MV 100,116,76
ds_write_b64 v18, v[14:15] offset:25344
MV 104,112,80
SB 42,32
SB 46,33
MV 104,116,84
s_add_u32 s44, s44, 6144
s_addc_u32 s45, s45, 0
SB 50,34
MV 108,112,88
ds_write_b32 v19, v17 offset:25344
MV 108,116,92
WL 7
TR 112,4,16
SB 54,35
MA 140,96,88
TR 114,5,16
SB 58,36
MA 140,100,92
LA 24,-4
MA 136,96,80
SB 62,37
CV 12,42,46
MA 136,100,84
TR 116,4,17
CV 13,50,54
MA 132,96,72
TR 118,5,17
CV 16,58,62
MA 132,100,76
ds_write_b64 v18, v[12:13] offset:26112
MA 128,96,64
LA 28,-3
MA 128,100,68
SB 43,32
SB 47,33
MA 124,96,56
SB 51,34
SB 55,35
MA 124,100,60
WL 1
s_barrier
ds_write_b32 v19, v16 offset:26112
MA 120,96,48
s_add_u32 m0, %[hm], 6144
SB 59,36
SB 63,37
MA 120,100,52
TR 96,2,18
CV 14,43,47
MV 112,104,48
TR 98,3,18
CV 15,51,55
MV 112,108,52
LA 32,-2
MV 116,104,56
s_add_u32 s40, s64, 268288
s_addc_u32 s41, s65, 0
CV 17,59,63
MV 116,108,60
TR 100,2,19
SB 64,32
MV 120,104,64
TR 102,3,19
SB 68,33
MV 120,108,68
ds_write_b64 v18, v[14:15] offset:26880
MV 124,104,72
LA 36,-1
MV 124,108,76
ds_write_b32 v19, v17 offset:26880
MV 128,104,80
SB 72,34
SB 76,35
MV 128,108,84
SB 80,36
SB 84,37
MV 132,104,88
CV 12,64,68
CV 13,72,76
MV 132,108,92
.Lk2p13:
s_cbranch_execz .Lk2r2
TR 104,4,18
CV 16,80,84
MV 156,112,88
TR 106,5,18
)"
R"(SB 65,32
MV 156,116,92
LA 40,0
MV 152,112,80
ds_write_b64 v18, v[12:13] offset:36864
MV 152,116,84
TR 108,4,19
SB 69,33
MV 148,112,72
TR 110,5,19
SB 73,34
MV 148,116,76
ds_write_b32 v19, v16 offset:36864
MV 144,112,64
LA 44,1
MV 144,116,68
SB 77,35
SB 81,36
MV 140,112,56
SB 85,37
CV 14,65,69
MV 140,116,60
CV 15,73,77
CV 17,81,85
MV 136,112,48
ds_write_b64 v18, v[14:15] offset:37632
MV 136,116,52
WL 7
TR 112,2,20
SB 66,32
MV 160,96,48
TR 114,3,20
SB 70,33
MV 160,100,52
DH 0
MV 164,96,56
ds_write_b32 v19, v17 offset:37632
MV 164,100,60
TR 116,2,21
SB 74,34
MV 168,96,64
s_add_u32 m0, %[hm], 8192
TR 118,3,21
SB 78,35
MV 168,100,68
s_add_u32 s40, s66, 0
s_addc_u32 s41, s67, 0
SB 82,36
MV 172,96,72
DH 0
MV 172,100,76
SB 86,37
CV 12,66,70
MV 176,96,80
CV 13,74,78
CV 16,82,86
MV 176,100,84
ds_write_b64 v18, v[12:13] offset:38400
MV 180,96,88
SB 67,32
SB 71,33
MV 180,100,92
WL 7
TR 96,4,20
SB 75,34
MA 164,104,88
TR 98,5,20
SB 79,35
MA 164,108,92
DH 2
MA 160,104,80
ds_write_b32 v19, v16 offset:38400
MA 160,108,84
TR 100,4,21
SB 83,36
MA 156,104,72
TR 102,5,21
SB 87,37
MA 156,108,76
s_add_u32 m0, %[hm], 12288
v_xor_b32 v1, 0x300, v1
CV 14,67,71
MA 152,104,64
s_add_u32 s40, s64, 262144
s_addc_u32 s41, s65, 0
CV 15,75,79
MA 152,108,68
DH 0
MA 148,104,56
CV 17,83,87
MA 148,108,60
ds_write_b64 v18, v[14:15] offset:39168
MA 144,104,48
SB 88,32
SB 92,33
MA 144,108,52
WL 6
TR 104,2,22
SB 96,34
MV 184,112,48
TR 106,3,22
SB 100,35
MV 184,116,52
DH 2
MV 188,112,56
ds_write_b32 v19, v17 offset:39168
MV 188,116,60
TR 108,2,23
SB 104,36
MV 192,112,64
s_add_u32 m0, %[hm], 16384
TR 110,3,23
SB 108,37
MV 192,116,68
s_add_u32 s40, s64, 266240
s_addc_u32 s41, s65, 0
CV 12,88,92
MV 196,112,72
DH 0
MV 196,116,76
CV 13,96,100
CV 16,104,108
MV 200,112,80
ds_write_b64 v18, v[12:13] offset:49152
MV 200,116,84
SB 89,32
SB 93,33
MV 204,112,88
ds_write_b32 v19, v16 offset:49152
MV 204,116,92
WL 7
TR 112,4,22
SB 97,34
MV 228,96,88
TR 114,5,22
SB 101,35
MV 228,100,92
MV 224,96,80
SB 105,36
SB 109,37
MV 224,100,84
TR 116,4,23
CV 14,89,93
MV 220,96,72
TR 118,5,23
CV 15,97,101
MV 220,100,76
CV 17,105,109
SB 90,32
MV 216,96,64
SB 94,33
SB 98,34
MV 216,100,68
SB 102,35
SB 106,36
MV 212,96,56
WL 0
SB 110,37
CV 12,90,94
MV 212,100,60
s_barrier
RV 24,20,24576
CV 13,98,102
MV 208,96,48
s_add_u32 m0, %[hm], 18432
ds_write_b64 v18, v[14:15] offset:49920
MV 208,100,52
TR 96,2,24
CV 16,106,110
MV 232,104,48
s_add_u32 s42, s88, 0
TR 98,3,24
SB 91,32
MV 232,108,52
s_add_u32 s40, s64, 268288
s_addc_u32 s41, s65, 0
SB 95,33
MV 236,104,56
DH 0
MV 236,108,60
TR 100,2,25
SB 99,34
MV 240,104,64
TR 102,3,25
SB 103,35
MV 240,108,68
s_bitcmp1_b32 s49, 1
s_cselect_b64 s[66:67], s[62:63], s[60:61]
SB 107,36
MV 244,104,72
s_add_u32 m0, %[hm], 20480
s_add_u32 s42, s42, s72
s_addc_u32 s43, s73, 0
MV 244,108,76
s_add_u32 s40, s66, 262144
s_addc_u32 s41, s67, 0
SB 111,37
MV 248,104,80
DH 0
MV 248,108,84
ds_write_b32 v19, v17 offset:49920
MV 252,104,88
CV 14,91,95
CV 15,99,103
MV 252,108,92
TR 104,4,24
CV 17,107,111
MA 188,112,88
TR 106,5,24
v_add_u32 v21, 0xf000, v18
v_add_u32 v22, 0xf000, v19
MA 188,116,92
WL 5
ST 24,0
AR 6,120
MA 184,112,80
ds_write_b64 v18, v[12:13] offset:50688
AR 7,124
MA 184,116,84
TR 108,4,25
AR 8,128
AR 9,132
MA 180,112,72
TR 110,5,25
AR 10,136
AR 11,140
MA 180,116,76
RV 24,20,25600
SB 6,32
SB 7,33
MA 176,112,64
ds_write_b32 v19, v16 offset:50688
SB 8,34
MA 176,116,68
SB 9,35
SB 10,36
SB 11,37
MA 172,112,56
ds_write_b64 v18, v[14:15] offset:51456
CV 12,6,7
MA 172,116,60
CV 13,8,9
CV 16,10,11
AR 6,121
MA 168,112,48
WV 9
ds_write_b32 v19, v17 offset:51456
AR 7,125
MA 168,116,52
WL 7
TR 112,2,26
AR 8,129
AR 9,133
ZV 40,96,0
TR 114,3,26
AR 10,137
AR 11,141
MV 40,100,4
s_add_u32 s44, s84, 53248
s_addc_u32 s45, s85, 0
SB 6,32
SB 7,33
ZV 44,96,8
LA 48,-4
SB 8,34
MV 44,100,12
TR 116,2,27
SB 9,35
SB 10,36
ZV 48,96,16
TR 118,3,27
SB 11,37
CV 14,6,7
MV 48,100,20
ds_write_b64 v21, v[12:13] offset:0
CV 15,8,9
ZV 52,96,24
CV 17,10,11
AR 6,122
AR 7,126
MV 52,100,28
LA 52,-3
AR 8,130
ZV 56,96,32
ds_write_b32 v22, v16 offset:0
AR 9,134
MV 56,100,36
AR 10,138
AR 11,142
SB 6,32
ZV 60,96,40
ds_write_b64 v21, v[14:15] offset:768
SB 7,33
MV 60,100,44
WL 5
TR 96,4,26
SB 8,34
SB 9,35
ZV 84,104,40
TR 98,5,26
SB 10,36
SB 11,37
MV 84,108,44
LA 56,-2
CV 12,6,7
ZV 80,104,32
ds_write_b32 v22, v17 offset:768
CV 13,8,9
MV 80,108,36
TR 100,4,27
CV 16,10,11
AR 6,123
ZV 76,104,24
TR 102,5,27
AR 7,127
AR 8,131
MV 76,108,28
ds_write_b64 v21, v[12:13] offset:1536
AR 9,135
ZV 72,104,16
LA 60,-1
AR 10,139
MV 72,108,20
ds_write_b32 v22, v16 offset:1536
AR 11,143
ZV 68,104,8
SB 6,32
SB 7,33
SB 8,34
MV 68,108,12
SB 9,35
SB 10,36
SB 11,37
ZV 64,104,0
CV 14,6,7
CV 15,8,9
CV 17,10,11
MV 64,108,4
WL 5
TR 104,2,28
ZV 88,112,0
TR 106,3,28
MV 88,116,4
LA 64,0
ZV 92,112,8
ds_write_b64 v21, v[14:15] offset:2304
MV 92,116,12
TR 108,2,29
ZV 96,112,16
TR 110,3,29
MV 96,116,20
ds_write_b32 v22, v17 offset:2304
ZV 100,112,24
LA 68,1
MV 100,116,28
ZV 104,112,32
MV 104,116,36
s_add_u32 s44, s44, 6144
s_addc_u32 s45, s45, 0
ZV 108,112,40
MV 108,116,44
WL 7
TR 112,4,28
ZA 140,96,40
TR 114,5,28
MA 140,100,44
LA 72,-4
ZA 136,96,32
MA 136,100,36
TR 116,4,29
ZA 132,96,24
TR 118,5,29
MA 132,100,28
ZA 128,96,16
LA 76,-3
MA 128,100,20
ZA 124,96,8
MA 124,100,12
WL 0
s_barrier
ZA 120,96,0
MA 120,100,4
TR 96,2,30
MV 112,104,0
TR 98,3,30
MV 112,108,4
LA 80,-2
MV 116,104,8
MV 116,108,12
TR 100,2,31
MV 120,104,16
TR 102,3,31
MV 120,108,20
MV 124,104,24
LA 84,-1
MV 124,108,28
MV 128,104,32
MV 128,108,36
MV 132,104,40
MV 132,108,44
)"
R"(TR 104,4,30
MV 156,112,40
TR 106,5,30
MV 156,116,44
LA 88,0
MV 152,112,32
v_add_u32 v21, 0xf000, v18
v_add_u32 v22, 0xf000, v19
MV 152,116,36
TR 108,4,31
SB 112,32
MV 148,112,24
TR 110,5,31
SB 116,33
MV 148,116,28
SB 120,34
SB 124,35
MV 144,112,16
LA 92,1
MV 144,116,20
SB 128,36
SB 132,37
MV 140,112,8
CV 12,112,116
CV 13,120,124
MV 140,116,12
CV 16,128,132
SB 113,32
MV 136,112,0
ds_write_b64 v21, v[12:13] offset:12288
MV 136,116,4
WL 5
TR 112,2,32
SB 117,33
MV 160,96,0
TR 114,3,32
SB 121,34
MV 160,100,4
DH 2
MV 164,96,8
ds_write_b32 v22, v16 offset:12288
MV 164,100,12
TR 116,2,33
SB 125,35
MV 168,96,16
TR 118,3,33
SB 129,36
MV 168,100,20
s_add_u32 m0, %[hm], 24576
v_xor_b32 v1, 0x100, v1
SB 133,37
MV 172,96,24
s_add_u32 s40, s64, 262144
s_addc_u32 s41, s65, 0
CV 14,113,117
MV 172,100,28
DH 0
MV 176,96,32
CV 15,121,125
CV 17,129,133
MV 176,100,36
ds_write_b64 v21, v[14:15] offset:13056
MV 180,96,40
SB 114,32
SB 118,33
MV 180,100,44
WL 7
TR 96,4,32
SB 122,34
MA 164,104,40
TR 98,5,32
SB 126,35
MA 164,108,44
DH 2
MA 160,104,32
ds_write_b32 v22, v17 offset:13056
MA 160,108,36
TR 100,4,33
SB 130,36
MA 156,104,24
s_add_u32 m0, %[hm], 28672
TR 102,5,33
SB 134,37
MA 156,108,28
s_add_u32 s40, s64, 266240
s_addc_u32 s41, s65, 0
CV 12,114,118
MA 152,104,16
DH 0
MA 152,108,20
CV 13,122,126
CV 16,130,134
MA 148,104,8
ds_write_b64 v21, v[12:13] offset:13824
MA 148,108,12
SB 115,32
SB 119,33
MA 144,104,0
ds_write_b32 v22, v16 offset:13824
MA 144,108,4
WL 7
TR 104,2,34
SB 123,34
MV 184,112,0
TR 106,3,34
SB 127,35
MV 184,116,4
ST 24,1
MV 188,112,8
SB 131,36
SB 135,37
MV 188,116,12
TR 108,2,35
CV 14,115,119
MV 192,112,16
TR 110,3,35
CV 15,123,127
MV 192,116,20
RV 24,20,26624
CV 17,131,135
MV 196,112,24
ds_write_b64 v21, v[14:15] offset:14592
MV 196,116,28
SB 136,32
SB 140,33
MV 200,112,32
ds_write_b32 v22, v17 offset:14592
MV 200,116,36
SB 144,34
SB 148,35
MV 204,112,40
SB 152,36
SB 156,37
MV 204,116,44
WL 8
TR 112,4,34
CV 12,136,140
MV 228,96,40
TR 114,5,34
CV 13,144,148
MV 228,100,44
WL 4
ST 24,2
MV 224,96,32
CV 16,152,156
AW 192,12
MV 224,100,36
TR 116,4,35
AW 193,13
MV 220,96,24
TR 118,5,35
AW 200,16
MV 220,100,28
SB 137,32
SB 141,33
MV 216,96,16
SB 145,34
SB 149,35
MV 216,100,20
SB 153,36
SB 157,37
MV 212,96,8
WL 0
CV 14,137,141
CV 15,145,149
MV 212,100,12
s_barrier
RV 24,20,36864
CV 17,153,157
MV 208,96,0
s_add_u32 m0, %[hm], 30720
AW 194,14
AW 195,15
MV 208,100,4
TR 96,2,0
AW 201,17
MV 232,104,0
s_add_u32 s42, s88, 98304
TR 98,3,0
SB 138,32
MV 232,108,4
s_add_u32 s40, s64, 268288
s_addc_u32 s41, s65, 0
SB 142,33
MV 236,104,8
.Lk2p14:
s_cbranch_execz .Lk2r2
DH 0
MV 236,108,12
TR 100,2,1
SB 146,34
MV 240,104,16
TR 102,3,1
SB 150,35
MV 240,108,20
s_add_u32 m0, %[hm], 32768
s_add_u32 s42, s42, s72
s_addc_u32 s43, s73, 0
MV 244,104,24
s_add_u32 s40, s66, 262144
s_addc_u32 s41, s67, 0
SB 154,36
MV 244,108,28
DH 0
MV 248,104,32
SB 158,37
CV 12,138,142
MV 248,108,36
CV 13,146,150
CV 16,154,158
MV 252,104,40
AW 196,12
AW 197,13
MV 252,108,44
TR 104,4,0
AW 202,16
MA 188,112,40
TR 106,5,0
SB 139,32
MA 188,116,44
WL 4
ST 24,0
MA 184,112,32
SB 143,33
SB 147,34
MA 184,116,36
TR 108,4,1
SB 151,35
MA 180,112,24
TR 110,5,1
SB 155,36
MA 180,116,28
RV 24,20,37888
SB 159,37
MA 176,112,16
CV 14,139,143
CV 15,147,151
MA 176,116,20
CV 17,155,159
AW 198,14
MA 172,112,8
AW 199,15
AW 203,17
MA 172,116,12
SB 160,32
SB 164,33
MA 168,112,0
WV 9
SB 168,34
SB 172,35
MA 168,116,4
WL 5
TR 112,2,2
SB 176,36
MV 40,96,48
TR 114,3,2
SB 180,37
MV 40,100,52
s_add_u32 s44, s84, 102400
s_addc_u32 s45, s85, 0
CV 12,160,164
MV 44,96,56
LA 0,-4
MV 44,100,60
TR 116,2,3
CV 13,168,172
MV 48,96,64
TR 118,3,3
CV 16,176,180
MV 48,100,68
ds_write_b64 v18, v[12:13] offset:24576
MV 52,96,72
SB 161,32
SB 165,33
MV 52,100,76
LA 4,-3
MV 56,96,80
ds_write_b32 v19, v16 offset:24576
MV 56,100,84
SB 169,34
SB 173,35
MV 60,96,88
SB 177,36
SB 181,37
MV 60,100,92
WL 6
TR 96,4,2
CV 14,161,165
MV 84,104,88
TR 98,5,2
CV 15,169,173
MV 84,108,92
LA 8,-2
MV 80,104,80
CV 17,177,181
SB 162,32
MV 80,108,84
TR 100,4,3
SB 166,33
MV 76,104,72
TR 102,5,3
SB 170,34
MV 76,108,76
ds_write_b64 v18, v[14:15] offset:25344
MV 72,104,64
LA 12,-1
MV 72,108,68
ds_write_b32 v19, v17 offset:25344
MV 68,104,56
SB 174,35
SB 178,36
MV 68,108,60
SB 182,37
CV 12,162,166
MV 64,104,48
CV 13,170,174
CV 16,178,182
MV 64,108,52
WL 6
TR 104,2,4
SB 163,32
MV 88,112,48
TR 106,3,4
SB 167,33
MV 88,116,52
LA 16,0
MV 92,112,56
ds_write_b64 v18, v[12:13] offset:26112
MV 92,116,60
TR 108,2,5
SB 171,34
MV 96,112,64
TR 110,3,5
SB 175,35
MV 96,116,68
ds_write_b32 v19, v16 offset:26112
MV 100,112,72
LA 20,1
MV 100,116,76
SB 179,36
SB 183,37
MV 104,112,80
CV 14,163,167
CV 15,171,175
MV 104,116,84
s_add_u32 s44, s44, 6144
s_addc_u32 s45, s45, 0
CV 17,179,183
MV 108,112,88
ds_write_b64 v18, v[14:15] offset:26880
AR 6,144
MV 108,116,92
WL 7
TR 112,4,4
AR 7,148
AR 8,152
MA 140,96,88
TR 114,5,4
AR 9,156
AR 10,160
MA 140,100,92
LA 24,-4
AR 11,164
MA 136,96,80
ds_write_b32 v19, v17 offset:26880
SB 6,32
MA 136,100,84
TR 116,4,5
SB 7,33
SB 8,34
MA 132,96,72
TR 118,5,5
SB 9,35
SB 10,36
MA 132,100,76
SB 11,37
CV 12,6,7
CV 13,8,9
MA 128,96,64
LA 28,-3
CV 16,10,11
MA 128,100,68
AW 204,12
AW 205,13
AW 212,16
MA 124,96,56
WL 0
AR 6,145
AR 7,149
AR 8,153
MA 124,100,60
s_barrier
AR 9,157
AR 10,161
AR 11,165
MA 120,96,48
SB 6,32
SB 7,33
SB 8,34
MA 120,100,52
)"
R"(TR 96,2,6
SB 9,35
SB 10,36
ZV 112,104,48
TR 98,3,6
SB 11,37
CV 14,6,7
MV 112,108,52
LA 32,-2
CV 15,8,9
ZV 116,104,56
CV 17,10,11
AW 206,14
AW 207,15
MV 116,108,60
TR 100,2,7
AW 213,17
AR 6,146
ZV 120,104,64
TR 102,3,7
AR 7,150
AR 8,154
MV 120,108,68
AR 9,158
AR 10,162
AR 11,166
ZV 124,104,72
LA 36,-1
SB 6,32
MV 124,108,76
SB 7,33
SB 8,34
SB 9,35
ZV 128,104,80
SB 10,36
SB 11,37
CV 12,6,7
MV 128,108,84
CV 13,8,9
CV 16,10,11
AW 208,12
ZV 132,104,88
AW 209,13
AW 214,16
AR 6,147
MV 132,108,92
TR 104,4,6
AR 7,151
AR 8,155
ZV 156,112,88
TR 106,5,6
AR 9,159
AR 10,163
MV 156,116,92
LA 40,0
AR 11,167
ZV 152,112,80
SB 6,32
SB 7,33
SB 8,34
MV 152,116,84
TR 108,4,7
SB 9,35
SB 10,36
ZV 148,112,72
TR 110,5,7
SB 11,37
CV 14,6,7
MV 148,116,76
CV 15,8,9
CV 17,10,11
AW 210,14
ZV 144,112,64
LA 44,1
AW 211,15
MV 144,116,68
AW 215,17
ZV 140,112,56
MV 140,116,60
ZV 136,112,48
MV 136,116,52
WL 4
TR 112,2,8
ZV 160,96,48
TR 114,3,8
MV 160,100,52
DH 2
ZV 164,96,56
MV 164,100,60
TR 116,2,9
ZV 168,96,64
PA 20480
TR 118,3,9
MV 168,100,68
ZV 172,96,72
DF
MV 172,100,76
s_add_u32 m0, %[hm], 0
v_xor_b32 v1, 0x700, v1
ZV 176,96,80
s_add_u32 s40, s64, 262144
s_addc_u32 s41, s65, 0
MV 176,100,84
ZV 180,96,88
MV 180,100,92
WL 4
TR 96,4,8
ZA 164,104,88
TR 98,5,8
MA 164,108,92
DH 0
ZA 160,104,80
MA 160,108,84
TR 100,4,9
ZA 156,104,72
TR 102,5,9
MA 156,108,76
ZA 152,104,64
DH 2
MA 152,108,68
ZA 148,104,56
MA 148,108,60
s_add_u32 m0, %[hm], 4096
ZA 144,104,48
s_add_u32 s40, s64, 266240
s_addc_u32 s41, s65, 0
MA 144,108,52
WL 4
TR 104,2,10
MV 184,112,48
TR 106,3,10
MV 184,116,52
DH 0
MV 188,112,56
MV 188,116,60
TR 108,2,11
MV 192,112,64
TR 110,3,11
MV 192,116,68
MV 196,112,72
ST 24,1
MV 196,116,76
MV 200,112,80
RV 24,20,38912
MV 200,116,84
MV 204,112,88
MV 204,116,92
WL 5
TR 112,4,10
MV 228,96,88
TR 114,5,10
MV 228,100,92
MV 224,96,80
SB 184,32
SB 188,33
MV 224,100,84
TR 116,4,11
SB 192,34
MV 220,96,72
TR 118,5,11
SB 196,35
MV 220,100,76
WL 4
ST 24,2
MV 216,96,64
SB 200,36
SB 204,37
MV 216,100,68
CV 12,184,188
CV 13,192,196
MV 212,96,56
CV 16,200,204
AW 216,12
MV 212,100,60
WL 0
s_barrier
RV 24,20,49152
AW 217,13
MV 208,96,48
s_add_u32 m0, %[hm], 6144
AW 224,16
SB 185,32
MV 208,100,52
TR 96,2,12
SB 189,33
MV 232,104,48
s_add_u32 s42, s89, 0
TR 98,3,12
SB 193,34
MV 232,108,52
s_add_u32 s40, s64, 268288
s_addc_u32 s41, s65, 0
SB 197,35
MV 236,104,56
DH 0
MV 236,108,60
TR 100,2,13
SB 201,36
MV 240,104,64
TR 102,3,13
SB 205,37
MV 240,108,68
s_add_u32 m0, %[hm], 8192
s_add_u32 s42, s42, s72
s_addc_u32 s43, s73, 0
MV 244,104,72
s_add_u32 s40, s66, 262144
s_addc_u32 s41, s67, 0
CV 14,185,189
MV 244,108,76
DH 0
MV 248,104,80
CV 15,193,197
CV 17,201,205
MV 248,108,84
AW 218,14
AW 219,15
MV 252,104,88
AW 225,17
SB 186,32
MV 252,108,92
TR 104,4,12
SB 190,33
MA 188,112,88
TR 106,5,12
SB 194,34
MA 188,116,92
WL 4
ST 24,0
MA 184,112,80
SB 198,35
SB 202,36
MA 184,116,84
TR 108,4,13
SB 206,37
MA 180,112,72
TR 110,5,13
CV 12,186,190
MA 180,116,76
RV 24,20,50176
CV 13,194,198
MA 176,112,64
CV 16,202,206
AW 220,12
MA 176,116,68
AW 221,13
AW 226,16
MA 172,112,56
SB 187,32
SB 191,33
MA 172,116,60
SB 195,34
SB 199,35
MA 168,112,48
WV 10
SB 203,36
SB 207,37
MA 168,116,52
WL 5
TR 112,2,14
CV 14,187,191
MV 40,96,0
TR 114,3,14
CV 15,195,199
MV 40,100,4
s_add_u32 s44, s84, 151552
s_addc_u32 s45, s85, 0
CV 17,203,207
MV 44,96,8
LA 48,-4
MV 44,100,12
TR 116,2,15
AW 222,14
MV 48,96,16
TR 118,3,15
AW 223,15
MV 48,100,20
AW 227,17
MV 52,96,24
MV 52,100,28
LA 52,-3
MV 56,96,32
ds_write_b64 v18, a[192:193] offset:36864
MV 56,100,36
SB 208,32
SB 212,33
MV 60,96,40
ds_write_b32 v19, a200 offset:36864
MV 60,100,44
WL 6
TR 96,4,14
SB 216,34
MV 84,104,40
TR 98,5,14
SB 220,35
MV 84,108,44
LA 56,-2
MV 80,104,32
ds_write_b64 v18, a[194:195] offset:37632
MV 80,108,36
TR 100,4,15
SB 224,36
MV 76,104,24
TR 102,5,15
SB 228,37
MV 76,108,28
ds_write_b32 v19, a201 offset:37632
MV 72,104,16
LA 60,-1
MV 72,108,20
ds_write_b64 v18, a[196:197] offset:38400
MV 68,104,8
CV 12,208,212
CV 13,216,220
MV 68,108,12
ds_write_b32 v19, a202 offset:38400
MV 64,104,0
CV 16,224,228
AW 228,12
MV 64,108,4
WL 9
TR 104,2,16
AW 229,13
MV 88,112,0
TR 106,3,16
AW 236,16
MV 88,116,4
LA 64,0
MV 92,112,8
ds_write_b64 v18, a[198:199] offset:39168
MV 92,116,12
TR 108,2,17
SB 209,32
MV 96,112,16
TR 110,3,17
SB 213,33
MV 96,116,20
ds_write_b32 v19, a203 offset:39168
MV 100,112,24
LA 68,1
MV 100,116,28
SB 217,34
SB 221,35
MV 104,112,32
SB 225,36
SB 229,37
MV 104,116,36
s_add_u32 s44, s44, 6144
s_addc_u32 s45, s45, 0
CV 14,209,213
MV 108,112,40
CV 15,217,221
CV 17,225,229
MV 108,116,44
WL 7
TR 112,4,16
AW 230,14
MA 140,96,40
.Lk2p15:
s_cbranch_execz .Lk2r2
TR 114,5,16
AW 231,15
MA 140,100,44
LA 72,-4
MA 136,96,32
AW 237,17
SB 210,32
MA 136,100,36
TR 116,4,17
SB 214,33
MA 132,96,24
TR 118,5,17
SB 218,34
MA 132,100,28
SB 222,35
SB 226,36
MA 128,96,16
LA 76,-3
MA 128,100,20
SB 230,37
CV 12,210,214
MA 124,96,8
CV 13,218,222
CV 16,226,230
MA 124,100,12
WL 0
s_barrier
AW 232,12
AW 233,13
MA 120,96,0
AW 238,16
SB 211,32
MA 120,100,4
TR 96,2,18
SB 215,33
MV 112,104,0
TR 98,3,18
SB 219,34
MV 112,108,4
LA 80,-2
)"
R"(MV 116,104,8
SB 223,35
SB 227,36
MV 116,108,12
TR 100,2,19
SB 231,37
MV 120,104,16
TR 102,3,19
CV 14,211,215
MV 120,108,20
CV 15,219,223
CV 17,227,231
MV 124,104,24
LA 84,-1
MV 124,108,28
AW 234,14
AW 235,15
MV 128,104,32
AW 239,17
MV 128,108,36
SB 232,32
SB 236,33
MV 132,104,40
SB 240,34
SB 244,35
MV 132,108,44
TR 104,4,18
SB 248,36
MV 156,112,40
TR 106,5,18
SB 252,37
MV 156,116,44
LA 88,0
MV 152,112,32
CV 12,232,236
CV 13,240,244
MV 152,116,36
TR 108,4,19
CV 16,248,252
MV 148,112,24
TR 110,5,19
AW 240,12
MV 148,116,28
AW 241,13
AW 248,16
MV 144,112,16
LA 92,1
MV 144,116,20
SB 233,32
SB 237,33
MV 140,112,8
SB 241,34
SB 245,35
MV 140,116,12
SB 249,36
SB 253,37
MV 136,112,0
CV 14,233,237
CV 15,241,245
MV 136,116,4
WL 4
TR 112,2,20
CV 17,249,253
MV 160,96,0
TR 114,3,20
AW 242,14
MV 160,100,4
DH 2
MV 164,96,8
AW 243,15
AW 249,17
MV 164,100,12
TR 116,2,21
SB 234,32
MV 168,96,16
TR 118,3,21
SB 238,33
MV 168,100,20
s_add_u32 m0, %[hm], 12288
v_xor_b32 v1, 0x100, v1
SB 242,34
MV 172,96,24
s_add_u32 s40, s64, 262144
s_addc_u32 s41, s65, 0
SB 246,35
MV 172,100,28
DH 0
MV 176,96,32
SB 250,36
SB 254,37
MV 176,100,36
CV 12,234,238
CV 13,242,246
MV 180,96,40
CV 16,250,254
AW 244,12
MV 180,100,44
WL 4
TR 96,4,20
AW 245,13
MA 164,104,40
TR 98,5,20
AW 250,16
MA 164,108,44
DH 2
MA 160,104,32
SB 235,32
SB 239,33
MA 160,108,36
TR 100,4,21
SB 243,34
MA 156,104,24
s_add_u32 m0, %[hm], 16384
TR 102,5,21
SB 247,35
MA 156,108,28
s_add_u32 s40, s64, 266240
s_addc_u32 s41, s65, 0
SB 251,36
MA 152,104,16
DH 0
MA 152,108,20
SB 255,37
CV 14,235,239
MA 148,104,8
CV 15,243,247
CV 17,251,255
MA 148,108,12
AW 246,14
AW 247,15
MA 144,104,0
AW 251,17
MA 144,108,4
WL 4
TR 104,2,22
AR 6,168
AR 7,172
ZV 184,112,0
TR 106,3,22
AR 8,176
AR 9,180
MV 184,116,4
ST 24,1
AR 10,184
ZV 188,112,8
AR 11,188
SB 6,32
SB 7,33
MV 188,116,12
TR 108,2,23
SB 8,34
SB 9,35
ZV 192,112,16
TR 110,3,23
SB 10,36
SB 11,37
MV 192,116,20
RV 24,20,51200
CV 12,6,7
CV 13,8,9
ZV 196,112,24
CV 16,10,11
AW 192,12
AW 193,13
MV 196,116,28
AW 200,16
AR 6,169
AR 7,173
ZV 200,112,32
AR 8,177
AR 9,181
AR 10,185
MV 200,116,36
AR 11,189
SB 6,32
SB 7,33
ZV 204,112,40
SB 8,34
SB 9,35
SB 10,36
MV 204,116,44
WL 3
TR 112,4,22
SB 11,37
CV 14,6,7
ZV 228,96,40
TR 114,5,22
CV 15,8,9
CV 17,10,11
MV 228,100,44
WL 2
ST 24,2
AW 194,14
ZV 224,96,32
AW 195,15
AW 201,17
AR 6,170
MV 224,100,36
TR 116,4,23
AR 7,174
AR 8,178
ZV 220,96,24
TR 118,5,23
AR 9,182
AR 10,186
MV 220,100,28
AR 11,190
SB 6,32
SB 7,33
ZV 216,96,16
SB 8,34
SB 9,35
SB 10,36
MV 216,100,20
SB 11,37
CV 12,6,7
CV 13,8,9
ZV 212,96,8
WL 0
CV 16,10,11
AW 196,12
AW 197,13
MV 212,100,12
s_barrier
AW 202,16
AR 6,171
AR 7,175
ZV 208,96,0
s_add_u32 m0, %[hm], 18432
AR 8,179
AR 9,183
AR 10,187
MV 208,100,4
TR 96,2,24
AR 11,191
SB 6,32
ZV 232,104,0
TR 98,3,24
SB 7,33
SB 8,34
MV 232,108,4
s_add_u32 s40, s64, 268288
s_addc_u32 s41, s65, 0
SB 9,35
SB 10,36
ZV 236,104,8
DH 0
SB 11,37
MV 236,108,12
TR 100,2,25
CV 14,6,7
CV 15,8,9
ZV 240,104,16
TR 102,3,25
CV 17,10,11
AW 198,14
MV 240,108,20
s_add_u32 m0, %[hm], 20480
AW 199,15
AW 203,17
ZV 244,104,24
s_add_u32 s40, s66, 262144
s_addc_u32 s41, s67, 0
MV 244,108,28
DH 0
ZV 248,104,32
ds_write_b64 v18, a[204:205] offset:49152
MV 248,108,36
ZV 252,104,40
ds_write_b32 v19, a212 offset:49152
MV 252,108,44
TR 104,4,24
ZA 188,112,40
TR 106,5,24
MA 188,116,44
ZA 184,112,32
ds_write_b64 v18, a[206:207] offset:49920
MA 184,116,36
TR 108,4,25
v_add_u32 v23, 0xf000, v20
ZA 180,112,24
TR 110,5,25
MA 180,116,28
RV 24,23,0
ZA 176,112,16
ds_write_b32 v19, a213 offset:49920
MA 176,116,20
ZA 172,112,8
s_add_u32 s42, s89, 98304
ds_write_b64 v18, a[208:209] offset:50688
MA 172,116,12
s_add_u32 s42, s42, s72
s_addc_u32 s43, s73, 0
ZA 168,112,0
WV 8
ds_write_b32 v19, a214 offset:50688
MA 168,116,4
WL 9
TR 112,2,26
MV 40,96,48
TR 114,3,26
MV 40,100,52
s_add_u32 s44, s84, 200704
s_addc_u32 s45, s85, 0
MV 44,96,56
LA 0,-4
MV 44,100,60
TR 116,2,27
MV 48,96,64
TR 118,3,27
MV 48,100,68
ds_write_b64 v18, a[210:211] offset:51456
MV 52,96,72
MV 52,100,76
LA 4,-3
MV 56,96,80
ds_write_b32 v19, a215 offset:51456
MV 56,100,84
MV 60,96,88
MV 60,100,92
WL 9
TR 96,4,26
MV 84,104,88
TR 98,5,26
MV 84,108,92
LA 8,-2
MV 80,104,80
MV 80,108,84
TR 100,4,27
MV 76,104,72
TR 102,5,27
MV 76,108,76
MV 72,104,64
LA 12,-1
MV 72,108,68
MV 68,104,56
MV 68,108,60
MV 64,104,48
MV 64,108,52
WL 6
TR 104,2,28
MV 88,112,48
TR 106,3,28
MV 88,116,52
LA 16,0
MV 92,112,56
MV 92,116,60
TR 108,2,29
MV 96,112,64
TR 110,3,29
MV 96,116,68
MV 100,112,72
LA 20,1
MV 100,116,76
MV 104,112,80
MV 104,116,84
s_add_u32 s44, s44, 6144
s_addc_u32 s45, s45, 0
MV 108,112,88
MV 108,116,92
WL 4
TR 112,4,28
MA 140,96,88
TR 114,5,28
MA 140,100,92
LA 24,-4
MA 136,96,80
MA 136,100,84
TR 116,4,29
MA 132,96,72
TR 118,5,29
MA 132,100,76
MA 128,96,64
LA 28,-3
MA 128,100,68
MA 124,96,56
MA 124,100,60
WL 0
s_barrier
MA 120,96,48
MA 120,100,52
TR 96,2,30
MV 112,104,48
TR 98,3,30
MV 112,108,52
LA 32,-2
MV 116,104,56
MV 116,108,60
TR 100,2,31
MV 120,104,64
TR 102,3,31
MV 120,108,68
MV 124,104,72
LA 36,-1
MV 124,108,76
MV 128,104,80
MV 128,108,84
MV 132,104,88
MV 132,108,92
TR 104,4,30
)"
R"(MV 156,112,88
TR 106,5,30
MV 156,116,92
LA 40,0
MV 152,112,80
MV 152,116,84
TR 108,4,31
MV 148,112,72
TR 110,5,31
MV 148,116,76
MV 144,112,64
LA 44,1
MV 144,116,68
MV 140,112,56
MV 140,116,60
MV 136,112,48
MV 136,116,52
WL 4
TR 112,2,32
MV 160,96,48
TR 114,3,32
MV 160,100,52
DH 2
MV 164,96,56
MV 164,100,60
TR 116,2,33
MV 168,96,64
TR 118,3,33
MV 168,100,68
s_add_u32 m0, %[hm], 24576
v_xor_b32 v1, 0x300, v1
MV 172,96,72
s_add_u32 s40, s64, 262144
s_addc_u32 s41, s65, 0
MV 172,100,76
DH 0
MV 176,96,80
MV 176,100,84
MV 180,96,88
MV 180,100,92
WL 4
TR 96,4,32
MA 164,104,88
TR 98,5,32
MA 164,108,92
DH 2
MA 160,104,80
MA 160,108,84
TR 100,4,33
MA 156,104,72
s_add_u32 m0, %[hm], 28672
TR 102,5,33
MA 156,108,76
s_add_u32 s40, s64, 266240
s_addc_u32 s41, s65, 0
MA 152,104,64
DH 0
MA 152,108,68
MA 148,104,56
MA 148,108,60
s_add_u32 m0, %[wm], 0
MA 144,104,48
s_add_u32 s40, s88, 196608
MA 144,108,52
WL 4
TR 104,2,34
MV 184,112,48
TR 106,3,34
MV 184,116,52
s_add_u32 s40, s40, s68
s_addc_u32 s41, s69, 0
MV 188,112,56
DX 0
MV 188,116,60
TR 108,2,35
MV 192,112,64
TR 110,3,35
MV 192,116,68
MV 196,112,72
MV 196,116,76
DX 1
MV 200,112,80
MV 200,116,84
MV 204,112,88
MV 204,116,92
WL 4
TR 112,4,34
MV 228,96,88
TR 114,5,34
MV 228,100,92
DX 2
MV 224,96,80
MV 224,100,84
TR 116,4,35
MV 220,96,72
TR 118,5,35
MV 220,100,76
MV 216,96,64
ST 24,0
MV 216,100,68
MV 212,96,56
MV 212,100,60
WL 0
s_barrier
RV 24,23,1024
MV 208,96,48
s_add_u32 m0, %[hm], 30720
MV 208,100,52
TR 96,2,0
MV 232,104,48
TR 98,3,0
MV 232,108,52
s_add_u32 s40, s64, 268288
s_addc_u32 s41, s65, 0
MV 236,104,56
.Lk2p16:
s_cbranch_execz .Lk2r2
DH 0
MV 236,108,60
TR 100,2,1
MV 240,104,64
TR 102,3,1
MV 240,108,68
s_add_u32 m0, %[hm], 32768
MV 244,104,72
s_add_u32 s40, s66, 262144
s_addc_u32 s41, s67, 0
MV 244,108,76
DH 0
MV 248,104,80
MV 248,108,84
MV 252,104,88
MV 252,108,92
TR 104,4,0
MA 188,112,88
TR 106,5,0
MA 188,116,92
WL 6
ST 24,1
MA 184,112,80
MA 184,116,84
TR 108,4,1
MA 180,112,72
TR 110,5,1
MA 180,116,76
MA 176,112,64
MA 176,116,68
MA 172,112,56
MA 172,116,60
MA 168,112,48
WV 11
MA 168,116,52
WL 4
TR 112,2,2
MV 40,96,0
TR 114,3,2
MV 40,100,4
s_add_u32 s44, s84, 249856
s_addc_u32 s45, s85, 0
MV 44,96,8
LA 48,-4
MV 44,100,12
TR 116,2,3
MV 48,96,16
TR 118,3,3
MV 48,100,20
MV 52,96,24
MV 52,100,28
LA 52,-3
MV 56,96,32
MV 56,100,36
MV 60,96,40
MV 60,100,44
WL 4
TR 96,4,2
MV 84,104,40
TR 98,5,2
MV 84,108,44
LA 56,-2
MV 80,104,32
MV 80,108,36
TR 100,4,3
MV 76,104,24
TR 102,5,3
MV 76,108,28
MV 72,104,16
LA 60,-1
MV 72,108,20
MV 68,104,8
MV 68,108,12
MV 64,104,0
MV 64,108,4
WL 4
TR 104,2,4
MV 88,112,0
TR 106,3,4
MV 88,116,4
LA 64,0
MV 92,112,8
MV 92,116,12
TR 108,2,5
MV 96,112,16
TR 110,3,5
MV 96,116,20
RV 24,23,2048
MV 100,112,24
LA 68,1
MV 100,116,28
MV 104,112,32
MV 104,116,36
s_add_u32 s44, s44, 6144
s_addc_u32 s45, s45, 0
MV 108,112,40
MV 108,116,44
WL 5
TR 112,4,4
MA 140,96,40
TR 114,5,4
MA 140,100,44
LA 72,-4
MA 136,96,32
MA 136,100,36
TR 116,4,5
MA 132,96,24
TR 118,5,5
MA 132,100,28
MA 128,96,16
LA 76,-3
MA 128,100,20
MA 124,96,8
MA 124,100,12
WL 0
s_barrier
v_add_u32 v21, 0xf000, v18
v_add_u32 v22, 0xf000, v19
MA 120,96,0
ds_write_b64 v21, a[216:217] offset:0
MA 120,100,4
TR 96,2,6
MV 112,104,0
TR 98,3,6
MV 112,108,4
LA 80,-2
MV 116,104,8
ds_write_b32 v22, a224 offset:0
MV 116,108,12
TR 100,2,7
MV 120,104,16
TR 102,3,7
MV 120,108,20
ds_write_b64 v21, a[218:219] offset:768
MV 124,104,24
LA 84,-1
MV 124,108,28
ds_write_b32 v22, a225 offset:768
MV 128,104,32
MV 128,108,36
ds_write_b64 v21, a[220:221] offset:1536
MV 132,104,40
MV 132,108,44
TR 104,4,6
MV 156,112,40
TR 106,5,6
MV 156,116,44
LA 88,0
MV 152,112,32
ds_write_b32 v22, a226 offset:1536
MV 152,116,36
TR 108,4,7
MV 148,112,24
TR 110,5,7
MV 148,116,28
ds_write_b64 v21, a[222:223] offset:2304
MV 144,112,16
LA 92,1
MV 144,116,20
WL 9
ds_write_b32 v22, a227 offset:2304
MV 140,112,8
MV 140,116,12
MV 136,112,0
MV 136,116,4
TR 112,2,8
MV 160,96,0
TR 114,3,8
MV 160,100,4
DH 2
MV 164,96,8
MV 164,100,12
TR 116,2,9
MV 168,96,16
TR 118,3,9
MV 168,100,20
s_add_u32 m0, %[hm], 0
v_xor_b32 v1, 0x100, v1
MV 172,96,24
s_add_u32 s40, s64, 262144
s_addc_u32 s41, s65, 0
MV 172,100,28
DH 0
MV 176,96,32
MV 176,100,36
MV 180,96,40
MV 180,100,44
WL 6
TR 96,4,8
MA 164,104,40
TR 98,5,8
MA 164,108,44
DH 2
MA 160,104,32
MA 160,108,36
TR 100,4,9
MA 156,104,24
s_add_u32 m0, %[hm], 4096
TR 102,5,9
MA 156,108,28
s_add_u32 s40, s64, 266240
s_addc_u32 s41, s65, 0
MA 152,104,16
DH 0
MA 152,108,20
MA 148,104,8
MA 148,108,12
s_add_u32 m0, %[wm], 12288
MA 144,104,0
s_add_u32 s40, s88, 294912
MA 144,108,4
WL 4
TR 104,2,10
MV 184,112,0
TR 106,3,10
MV 184,116,4
s_add_u32 s40, s40, s68
s_addc_u32 s41, s69, 0
MV 188,112,8
DX 0
MV 188,116,12
TR 108,2,11
MV 192,112,16
TR 110,3,11
MV 192,116,20
MV 196,112,24
MV 196,116,28
DX 1
MV 200,112,32
MV 200,116,36
MV 204,112,40
MV 204,116,44
WL 4
TR 112,4,10
MV 228,96,40
TR 114,5,10
MV 228,100,44
DX 2
MV 224,96,32
MV 224,100,36
TR 116,4,11
MV 220,96,24
TR 118,5,11
MV 220,100,28
MV 216,96,16
ST 24,2
MV 216,100,20
MV 212,96,8
MV 212,100,12
v_add_u32 v23, 0xf000, v20
MV 208,96,0
RV 24,23,12288
MV 208,100,4
WL 5
TR 96,2,12
MV 232,104,0
TR 98,3,12
MV 232,108,4
s_add_u32 s42, s88, 49152
MV 236,104,8
s_add_u32 s42, s42, s72
s_addc_u32 s43, s73, 0
MV 236,108,12
TR 100,2,13
MV 240,104,16
TR 102,3,13
MV 240,108,20
WL 4
ST 24,0
MV 244,104,24
MV 244,108,28
MV 248,104,32
MV 248,108,36
MV 252,104,40
MV 252,108,44
TR 104,4,12
MA 188,112,40
TR 106,5,12
MA 188,116,44
MA 184,112,32
MA 184,116,36
TR 108,4,13
MA 180,112,24
)"
R"(TR 110,5,13
MA 180,116,28
MA 176,112,16
MA 176,116,20
MA 172,112,8
MA 172,116,12
MA 168,112,0
WV 9
MA 168,116,4
WL 4
TR 112,2,14
MV 40,96,48
TR 114,3,14
MV 40,100,52
s_add_u32 s44, s84, 299008
s_addc_u32 s45, s85, 0
MV 44,96,56
LA 0,-4
MV 44,100,60
TR 116,2,15
MV 48,96,64
TR 118,3,15
MV 48,100,68
MV 52,96,72
MV 52,100,76
LA 4,-3
MV 56,96,80
MV 56,100,84
MV 60,96,88
MV 60,100,92
WL 4
TR 96,4,14
MV 84,104,88
TR 98,5,14
MV 84,108,92
LA 8,-2
MV 80,104,80
MV 80,108,84
TR 100,4,15
MV 76,104,72
TR 102,5,15
MV 76,108,76
MV 72,104,64
LA 12,-1
MV 72,108,68
MV 68,104,56
MV 68,108,60
MV 64,104,48
MV 64,108,52
WL 4
TR 104,2,16
MV 88,112,48
TR 106,3,16
MV 88,116,52
LA 16,0
MV 92,112,56
MV 92,116,60
TR 108,2,17
MV 96,112,64
TR 110,3,17
MV 96,116,68
MV 100,112,72
LA 20,1
MV 100,116,76
MV 104,112,80
MV 104,116,84
s_add_u32 s44, s44, 6144
s_addc_u32 s45, s45, 0
MV 108,112,88
MV 108,116,92
WL 4
TR 112,4,16
MA 140,96,88
TR 114,5,16
MA 140,100,92
LA 24,-4
MA 136,96,80
s_mov_b64 exec, 0
s_cmp_eq_u32 %[w], 0
s_cbranch_scc1 .Lk2p17
s_cmp_eq_u32 %[w], 1
s_cbranch_scc1 .Lk2p18
s_cmp_eq_u32 %[w], 2
s_cbranch_scc1 .Lk2p19
s_branch .Lk2p20
.Lk2r3:
s_mov_b64 exec, -1
MA 136,100,84
TR 116,4,17
MA 132,96,72
TR 118,5,17
MA 132,100,76
MA 128,96,64
LA 28,-3
MA 128,100,68
MA 124,96,56
MA 124,100,60
WL 0
s_barrier
ds_read_b64 v[12:13], v18 offset:0
MA 120,96,48
s_add_u32 m0, %[hm], 6144
ds_read_b32 v16, v19 offset:0
MA 120,100,52
TR 96,2,18
MV 112,104,48
TR 98,3,18
MV 112,108,52
LA 32,-2
MV 116,104,56
s_add_u32 s40, s64, 268288
s_addc_u32 s41, s65, 0
MV 116,108,60
TR 100,2,19
MV 120,104,64
TR 102,3,19
MV 120,108,68
ds_read_b64 v[14:15], v18 offset:768
MV 124,104,72
LA 36,-1
MV 124,108,76
ds_read_b32 v17, v19 offset:768
MV 128,104,80
MV 128,108,84
MV 132,104,88
MV 132,108,92
TR 104,4,18
MV 156,112,88
WL 7
TR 106,5,18
UL 6,12
MV 156,116,92
LA 40,0
MV 152,112,80
UH 7,12
UL 8,13
MV 152,116,84
TR 108,4,19
AD 40,6
MV 148,112,72
TR 110,5,19
AD 44,7
MV 148,116,76
RV 24,23,13312
AD 48,8
MV 144,112,64
LA 44,1
MV 144,116,68
UH 6,13
UL 7,16
MV 140,112,56
UH 8,16
MV 140,116,60
ds_read_b64 v[12:13], v18 offset:1536
MV 136,112,48
ds_read_b32 v16, v19 offset:1536
AD 52,6
MV 136,116,52
WL 8
TR 112,2,20
AD 56,7
MV 160,96,48
TR 114,3,20
AD 60,8
MV 160,100,52
DH 0
MV 164,96,56
WL 8
UL 6,14
UH 7,14
MV 164,100,60
TR 116,2,21
UL 8,15
MV 168,96,64
s_add_u32 m0, %[hm], 8192
TR 118,3,21
AD 41,6
MV 168,100,68
s_add_u32 s40, s66, 262144
s_addc_u32 s41, s67, 0
AD 45,7
MV 172,96,72
DH 0
MV 172,100,76
AD 49,8
UH 6,15
MV 176,96,80
UL 7,17
UH 8,17
MV 176,100,84
ds_read_b64 v[14:15], v18 offset:2304
MV 180,96,88
ds_read_b32 v17, v19 offset:2304
AD 53,6
MV 180,100,92
WL 6
TR 96,4,20
AD 57,7
MA 164,104,88
TR 98,5,20
AD 61,8
MA 164,108,92
DH 2
MA 160,104,80
UL 6,12
UH 7,12
MA 160,108,84
TR 100,4,21
UL 8,13
MA 156,104,72
PA 20480
TR 102,5,21
AD 42,6
MA 156,108,76
AD 46,7
AD 50,8
MA 152,104,64
DF
MA 152,108,68
s_bitcmp1_b32 s48, 4
s_cselect_b64 s[64:65], s[58:59], s[56:57]
UH 6,13
MA 148,104,56
s_bitcmp1_b32 s48, 20
s_cselect_b32 s40, 16384, 0
UL 7,16
MA 148,108,60
s_add_u32 s64, s64, s40
s_addc_u32 s65, s65, 0
UH 8,16
MA 144,104,48
s_add_u32 m0, %[hm], 12288
v_xor_b32 v1, 0x700, v1
AD 54,6
MA 144,108,52
WL 3
TR 104,2,22
AD 58,7
MV 184,112,48
TR 106,3,22
AD 62,8
MV 184,116,52
s_add_u32 s40, s64, 1048576
s_addc_u32 s41, s65, 0
UL 6,14
MV 188,112,56
DH 0
MV 188,116,60
TR 108,2,23
UH 7,14
MV 192,112,64
TR 110,3,23
UL 8,15
MV 192,116,68
AD 43,6
AD 47,7
MV 196,112,72
AD 51,8
UH 6,15
MV 196,116,76
DH 2
MV 200,112,80
UL 7,17
UH 8,17
MV 200,116,84
AD 55,6
AD 59,7
MV 204,112,88
s_add_u32 m0, %[hm], 16384
AD 63,8
MV 204,116,92
WL 4
TR 112,4,22
MV 228,96,88
TR 114,5,22
MV 228,100,92
s_add_u32 s40, s64, 1052672
s_addc_u32 s41, s65, 0
MV 224,96,80
.Lk2p17:
s_cbranch_execz .Lk2r3
DH 0
MV 224,100,84
TR 116,4,23
MV 220,96,72
TR 118,5,23
MV 220,100,76
MV 216,96,64
MV 216,100,68
ST 24,1
MV 212,96,56
MV 212,100,60
MV 208,96,48
MV 208,100,52
WL 4
TR 96,2,24
MV 232,104,48
TR 98,3,24
MV 232,108,52
MV 236,104,56
MV 236,108,60
TR 100,2,25
MV 240,104,64
TR 102,3,25
MV 240,108,68
MV 244,104,72
MV 244,108,76
MV 248,104,80
MV 248,108,84
MV 252,104,88
MV 252,108,92
WL 4
TR 104,4,24
MA 188,112,88
TR 106,5,24
MA 188,116,92
MA 184,112,80
MA 184,116,84
TR 108,4,25
MA 180,112,72
TR 110,5,25
MA 180,116,76
MA 176,112,64
MA 176,116,68
MA 172,112,56
MA 172,116,60
MA 168,112,48
WV 8
MA 168,116,52
WL 4
TR 112,2,26
MV 40,96,0
TR 114,3,26
MV 40,100,4
s_add_u32 s44, s84, 348160
s_addc_u32 s45, s85, 0
MV 44,96,8
LA 48,-4
MV 44,100,12
TR 116,2,27
MV 48,96,16
TR 118,3,27
MV 48,100,20
MV 52,96,24
MV 52,100,28
LA 52,-3
MV 56,96,32
MV 56,100,36
RV 24,23,14336
MV 60,96,40
MV 60,100,44
WL 5
TR 96,4,26
MV 84,104,40
TR 98,5,26
MV 84,108,44
LA 56,-2
MV 80,104,32
MV 80,108,36
TR 100,4,27
MV 76,104,24
TR 102,5,27
MV 76,108,28
MV 72,104,16
LA 60,-1
MV 72,108,20
MV 68,104,8
MV 68,108,12
MV 64,104,0
MV 64,108,4
WL 5
TR 104,2,28
MV 88,112,0
TR 106,3,28
MV 88,116,4
LA 64,0
MV 92,112,8
MV 92,116,12
TR 108,2,29
MV 96,112,16
TR 110,3,29
MV 96,116,20
MV 100,112,24
LA 68,1
MV 100,116,28
MV 104,112,32
MV 104,116,36
s_add_u32 s44, s44, 6144
s_addc_u32 s45, s45, 0
MV 108,112,40
MV 108,116,44
WL 4
TR 112,4,28
MA 140,96,40
TR 114,5,28
MA 140,100,44
LA 72,-4
MA 136,96,32
MA 136,100,36
TR 116,4,29
)"
R"(MA 132,96,24
TR 118,5,29
MA 132,100,28
MA 128,96,16
LA 76,-3
MA 128,100,20
MA 124,96,8
MA 124,100,12
WL 0
s_barrier
v_add_u32 v21, 0xf000, v18
v_add_u32 v22, 0xf000, v19
MA 120,96,0
s_add_u32 m0, %[hm], 18432
ds_write_b64 v21, a[228:229] offset:12288
MA 120,100,4
TR 96,2,30
MV 112,104,0
TR 98,3,30
MV 112,108,4
LA 80,-2
MV 116,104,8
s_add_u32 s40, s64, 1054720
s_addc_u32 s41, s65, 0
MV 116,108,12
TR 100,2,31
MV 120,104,16
TR 102,3,31
MV 120,108,20
ds_write_b32 v22, a236 offset:12288
MV 124,104,24
LA 84,-1
MV 124,108,28
ds_write_b64 v21, a[230:231] offset:13056
MV 128,104,32
ds_read_b64 v[12:13], v18 offset:12288
MV 128,108,36
ds_write_b32 v22, a237 offset:13056
MV 132,104,40
ds_read_b32 v16, v19 offset:12288
MV 132,108,44
TR 104,4,30
MV 156,112,40
TR 106,5,30
MV 156,116,44
LA 88,0
MV 152,112,32
ds_write_b64 v21, a[232:233] offset:13824
MV 152,116,36
TR 108,4,31
MV 148,112,24
TR 110,5,31
MV 148,116,28
WL 9
ds_write_b32 v22, a238 offset:13824
MV 144,112,16
LA 92,1
MV 144,116,20
ds_write_b64 v21, a[234:235] offset:14592
MV 140,112,8
ds_read_b64 v[14:15], v18 offset:13056
MV 140,116,12
ds_write_b32 v22, a239 offset:14592
MV 136,112,0
ds_read_b32 v17, v19 offset:13056
MV 136,116,4
TR 112,2,32
MV 160,96,0
WL 9
TR 114,3,32
MV 160,100,4
DH 0
MV 164,96,8
MV 164,100,12
TR 116,2,33
UL 6,12
MV 168,96,16
TR 118,3,33
UH 7,12
MV 168,100,20
s_bitcmp1_b32 s49, 4
s_cselect_b64 s[66:67], s[62:63], s[60:61]
UL 8,13
MV 172,96,24
s_add_u32 m0, %[hm], 20480
AD 64,6
AD 68,7
MV 172,100,28
s_add_u32 s40, s66, 1048576
s_addc_u32 s41, s67, 0
AD 72,8
MV 176,96,32
DH 0
MV 176,100,36
UH 6,13
UL 7,16
MV 180,96,40
UH 8,16
MV 180,100,44
WL 6
TR 96,4,32
MA 164,104,40
TR 98,5,32
MA 164,108,44
DH 2
MA 160,104,32
ds_read_b64 v[12:13], v18 offset:13824
MA 160,108,36
TR 100,4,33
MA 156,104,24
TR 102,5,33
MA 156,108,28
s_add_u32 m0, %[hm], 24576
v_xor_b32 v1, 0x100, v1
MA 152,104,16
s_add_u32 s40, s64, 1048576
s_addc_u32 s41, s65, 0
MA 152,108,20
DH 0
MA 148,104,8
ds_read_b32 v16, v19 offset:13824
AD 76,6
MA 148,108,12
AD 80,7
AD 84,8
MA 144,104,0
WL 6
UL 6,14
UH 7,14
MA 144,108,4
TR 104,2,34
UL 8,15
MV 184,112,0
TR 106,3,34
AD 65,6
MV 184,116,4
DH 2
MV 188,112,8
AD 69,7
AD 73,8
MV 188,116,12
TR 108,2,35
UH 6,15
MV 192,112,16
s_add_u32 m0, %[hm], 28672
TR 110,3,35
UL 7,17
MV 192,116,20
s_add_u32 s40, s64, 1052672
s_addc_u32 s41, s65, 0
UH 8,17
MV 196,112,24
DH 0
MV 196,116,28
ds_read_b64 v[14:15], v18 offset:14592
MV 200,112,32
WL 5
ds_read_b32 v17, v19 offset:14592
AD 77,6
MV 200,116,36
s_add_u32 m0, %[wm], 0
AD 81,7
AD 85,8
MV 204,112,40
s_add_u32 s40, s89, 196608
UL 6,12
UH 7,12
MV 204,116,44
TR 112,4,34
UL 8,13
MV 228,96,40
TR 114,5,34
AD 66,6
MV 228,100,44
s_add_u32 s40, s40, s68
s_addc_u32 s41, s69, 0
AD 70,7
MV 224,96,32
DX 0
MV 224,100,36
TR 116,4,35
AD 74,8
MV 220,96,24
TR 118,5,35
UH 6,13
MV 220,100,28
UL 7,16
UH 8,16
MV 216,96,16
AD 78,6
AD 82,7
MV 216,100,20
DX 1
MV 212,96,8
WL 4
AD 86,8
UL 6,14
MV 212,100,12
UH 7,14
UL 8,15
MV 208,96,0
AD 67,6
AD 71,7
MV 208,100,4
TR 96,2,0
AD 75,8
MV 232,104,0
TR 98,3,0
UH 6,15
MV 232,108,4
DX 2
MV 236,104,8
UL 7,17
UH 8,17
MV 236,108,12
TR 100,2,1
AD 79,6
MV 240,104,16
TR 102,3,1
AD 83,7
MV 240,108,20
AD 87,8
MV 244,104,24
ST 24,2
MV 244,108,28
MV 248,104,32
RV 24,20,24576
MV 248,108,36
MV 252,104,40
MV 252,108,44
WL 5
TR 104,4,0
MA 188,112,40
s_add_u32 s42, s89, 49152
TR 106,5,0
MA 188,116,44
s_add_u32 s42, s42, s72
s_addc_u32 s43, s73, 0
MA 184,112,32
MA 184,116,36
TR 108,4,1
MA 180,112,24
TR 110,5,1
MA 180,116,28
WL 4
ST 24,0
MA 176,112,16
MA 176,116,20
MA 172,112,8
MA 172,116,12
MA 168,112,0
MA 168,116,4
WV 11
TR 112,2,2
MV 40,96,48
TR 114,3,2
MV 40,100,52
s_add_u32 s44, s84, 397312
s_addc_u32 s45, s85, 0
MV 44,96,56
LA 0,-4
MV 44,100,60
TR 116,2,3
MV 48,96,64
TR 118,3,3
MV 48,100,68
MV 52,96,72
MV 52,100,76
LA 4,-3
MV 56,96,80
MV 56,100,84
MV 60,96,88
MV 60,100,92
WL 4
TR 96,4,2
MV 84,104,88
TR 98,5,2
MV 84,108,92
LA 8,-2
MV 80,104,80
MV 80,108,84
TR 100,4,3
MV 76,104,72
TR 102,5,3
MV 76,108,76
MV 72,104,64
LA 12,-1
MV 72,108,68
MV 68,104,56
MV 68,108,60
MV 64,104,48
MV 64,108,52
WL 4
TR 104,2,4
MV 88,112,48
TR 106,3,4
MV 88,116,52
LA 16,0
MV 92,112,56
MV 92,116,60
TR 108,2,5
MV 96,112,64
TR 110,3,5
MV 96,116,68
MV 100,112,72
LA 20,1
MV 100,116,76
MV 104,112,80
MV 104,116,84
s_add_u32 s44, s44, 6144
s_addc_u32 s45, s45, 0
MV 108,112,88
MV 108,116,92
WL 4
TR 112,4,4
MA 140,96,88
TR 114,5,4
MA 140,100,92
LA 24,-4
MA 136,96,80
MA 136,100,84
TR 116,4,5
MA 132,96,72
TR 118,5,5
MA 132,100,76
MA 128,96,64
LA 28,-3
MA 128,100,68
MA 124,96,56
MA 124,100,60
WL 0
s_barrier
RV 24,20,25600
MA 120,96,48
s_add_u32 m0, %[hm], 30720
MA 120,100,52
TR 96,2,6
MV 112,104,48
TR 98,3,6
MV 112,108,52
LA 32,-2
MV 116,104,56
s_add_u32 s40, s64, 1054720
s_addc_u32 s41, s65, 0
MV 116,108,60
TR 100,2,7
MV 120,104,64
TR 102,3,7
MV 120,108,68
MV 124,104,72
LA 36,-1
MV 124,108,76
MV 128,104,80
MV 128,108,84
MV 132,104,88
MV 132,108,92
TR 104,4,6
MV 156,112,88
TR 106,5,6
MV 156,116,92
LA 40,0
MV 152,112,80
MV 152,116,84
TR 108,4,7
MV 148,112,72
TR 110,5,7
MV 148,116,76
MV 144,112,64
LA 44,1
MV 144,116,68
MV 140,112,56
MV 140,116,60
MV 136,112,48
MV 136,116,52
WL 4
TR 112,2,8
MV 160,96,48
TR 114,3,8
MV 160,100,52
DH 0
MV 164,96,56
MV 164,100,60
TR 116,2,9
MV 168,96,64
s_add_u32 m0, %[hm], 32768
TR 118,3,9
MV 168,100,68
s_add_u32 s40, s66, 1048576
s_addc_u32 s41, s67, 0
MV 172,96,72
DH 0
MV 172,100,76
MV 176,96,80
MV 176,100,84
MV 180,96,88
MV 180,100,92
WL 4
TR 96,4,8
MA 164,104,88
TR 98,5,8
)"
R"(MA 164,108,92
DH 2
MA 160,104,80
MA 160,108,84
TR 100,4,9
MA 156,104,72
TR 102,5,9
MA 156,108,76
s_add_u32 m0, %[hm], 0
v_xor_b32 v1, 0x300, v1
MA 152,104,64
s_add_u32 s40, s64, 1048576
s_addc_u32 s41, s65, 0
MA 152,108,68
DH 0
MA 148,104,56
MA 148,108,60
MA 144,104,48
MA 144,108,52
WL 4
TR 104,2,10
MV 184,112,48
TR 106,3,10
MV 184,116,52
DH 2
MV 188,112,56
MV 188,116,60
TR 108,2,11
MV 192,112,64
s_add_u32 m0, %[hm], 4096
TR 110,3,11
MV 192,116,68
s_add_u32 s40, s64, 1052672
s_addc_u32 s41, s65, 0
MV 196,112,72
DH 0
MV 196,116,76
MV 200,112,80
.Lk2p18:
s_cbranch_execz .Lk2r3
MV 200,116,84
s_add_u32 m0, %[wm], 12288
MV 204,112,88
s_add_u32 s40, s89, 294912
MV 204,116,92
WL 4
TR 112,4,10
MV 228,96,88
TR 114,5,10
MV 228,100,92
s_add_u32 s40, s40, s68
s_addc_u32 s41, s69, 0
MV 224,96,80
DX 0
MV 224,100,84
TR 116,4,11
MV 220,96,72
TR 118,5,11
MV 220,100,76
MV 216,96,64
MV 216,100,68
DX 1
MV 212,96,56
MV 212,100,60
MV 208,96,48
MV 208,100,52
WL 4
TR 96,2,12
MV 232,104,48
TR 98,3,12
MV 232,108,52
DX 2
MV 236,104,56
MV 236,108,60
TR 100,2,13
MV 240,104,64
TR 102,3,13
MV 240,108,68
MV 244,104,72
ST 24,1
MV 244,108,76
MV 248,104,80
MV 248,108,84
MV 252,104,88
MV 252,108,92
WL 4
TR 104,4,12
MA 188,112,88
TR 106,5,12
MA 188,116,92
MA 184,112,80
MA 184,116,84
TR 108,4,13
MA 180,112,72
TR 110,5,13
MA 180,116,76
MA 176,112,64
RV 24,20,26624
MA 176,116,68
MA 172,112,56
MA 172,116,60
MA 168,112,48
WV 10
MA 168,116,52
WL 5
TR 112,2,14
MV 40,96,0
TR 114,3,14
MV 40,100,4
s_add_u32 s44, s84, 446464
s_addc_u32 s45, s85, 0
MV 44,96,8
LA 48,-4
MV 44,100,12
TR 116,2,15
MV 48,96,16
TR 118,3,15
MV 48,100,20
MV 52,96,24
MV 52,100,28
LA 52,-3
MV 56,96,32
MV 56,100,36
MV 60,96,40
MV 60,100,44
WL 5
TR 96,4,14
MV 84,104,40
TR 98,5,14
MV 84,108,44
LA 56,-2
MV 80,104,32
MV 80,108,36
TR 100,4,15
MV 76,104,24
TR 102,5,15
MV 76,108,28
MV 72,104,16
LA 60,-1
MV 72,108,20
MV 68,104,8
MV 68,108,12
MV 64,104,0
MV 64,108,4
WL 4
TR 104,2,16
MV 88,112,0
TR 106,3,16
MV 88,116,4
LA 64,0
MV 92,112,8
MV 92,116,12
TR 108,2,17
MV 96,112,16
TR 110,3,17
MV 96,116,20
MV 100,112,24
LA 68,1
MV 100,116,28
MV 104,112,32
MV 104,116,36
s_add_u32 s44, s44, 6144
s_addc_u32 s45, s45, 0
MV 108,112,40
MV 108,116,44
WL 4
TR 112,4,16
MA 140,96,40
TR 114,5,16
MA 140,100,44
LA 72,-4
MA 136,96,32
MA 136,100,36
TR 116,4,17
MA 132,96,24
TR 118,5,17
MA 132,100,28
MA 128,96,16
LA 76,-3
MA 128,100,20
MA 124,96,8
MA 124,100,12
WL 0
s_barrier
MA 120,96,0
s_add_u32 m0, %[hm], 6144
ds_write_b64 v18, a[240:241] offset:24576
MA 120,100,4
TR 96,2,18
MV 112,104,0
TR 98,3,18
MV 112,108,4
LA 80,-2
MV 116,104,8
s_add_u32 s40, s64, 1054720
s_addc_u32 s41, s65, 0
MV 116,108,12
TR 100,2,19
MV 120,104,16
TR 102,3,19
MV 120,108,20
ds_write_b32 v19, a248 offset:24576
MV 124,104,24
LA 84,-1
MV 124,108,28
ds_write_b64 v18, a[242:243] offset:25344
MV 128,104,32
ds_read_b64 v[12:13], v18 offset:0
MV 128,108,36
ds_write_b32 v19, a249 offset:25344
MV 132,104,40
ds_read_b32 v16, v19 offset:0
MV 132,108,44
TR 104,4,18
MV 156,112,40
TR 106,5,18
MV 156,116,44
LA 88,0
MV 152,112,32
ds_write_b64 v18, a[244:245] offset:26112
MV 152,116,36
TR 108,4,19
MV 148,112,24
TR 110,5,19
MV 148,116,28
WL 9
ds_write_b32 v19, a250 offset:26112
MV 144,112,16
LA 92,1
MV 144,116,20
ds_write_b64 v18, a[246:247] offset:26880
MV 140,112,8
ds_read_b64 v[14:15], v18 offset:768
MV 140,116,12
ds_write_b32 v19, a251 offset:26880
MV 136,112,0
ds_read_b32 v17, v19 offset:768
MV 136,116,4
TR 112,2,20
MV 160,96,0
WL 9
TR 114,3,20
MV 160,100,4
DH 0
MV 164,96,8
MV 164,100,12
TR 116,2,21
UL 6,12
MV 168,96,16
s_add_u32 m0, %[hm], 8192
TR 118,3,21
UH 7,12
MV 168,100,20
s_add_u32 s40, s66, 1048576
s_addc_u32 s41, s67, 0
UL 8,13
MV 172,96,24
DH 0
MV 172,100,28
AD 88,6
AD 92,7
MV 176,96,32
AD 96,8
UH 6,13
MV 176,100,36
UL 7,16
UH 8,16
MV 180,96,40
ds_read_b64 v[12:13], v18 offset:1536
MV 180,100,44
WL 8
TR 96,4,20
MA 164,104,40
TR 98,5,20
MA 164,108,44
DH 2
MA 160,104,32
ds_read_b32 v16, v19 offset:1536
AD 100,6
MA 160,108,36
TR 100,4,21
AD 104,7
MA 156,104,24
WL 6
TR 102,5,21
AD 108,8
MA 156,108,28
s_add_u32 m0, %[hm], 12288
v_xor_b32 v1, 0x100, v1
UL 6,14
MA 152,104,16
s_add_u32 s40, s64, 1048576
s_addc_u32 s41, s65, 0
UH 7,14
MA 152,108,20
DH 0
MA 148,104,8
UL 8,15
AD 89,6
MA 148,108,12
AD 93,7
AD 97,8
MA 144,104,0
UH 6,15
UL 7,17
MA 144,108,4
WL 6
TR 104,2,22
UH 8,17
MV 184,112,0
TR 106,3,22
MV 184,116,4
DH 2
MV 188,112,8
ds_read_b64 v[14:15], v18 offset:2304
MV 188,116,12
TR 108,2,23
MV 192,112,16
s_add_u32 m0, %[hm], 16384
TR 110,3,23
MV 192,116,20
s_add_u32 s40, s64, 1052672
s_addc_u32 s41, s65, 0
MV 196,112,24
DH 0
MV 196,116,28
ds_read_b32 v17, v19 offset:2304
AD 101,6
MV 200,112,32
AD 105,7
AD 109,8
MV 200,116,36
WL 6
UL 6,12
UH 7,12
MV 204,112,40
UL 8,13
AD 90,6
MV 204,116,44
TR 112,4,22
AD 94,7
MV 228,96,40
TR 114,5,22
AD 98,8
MV 228,100,44
ST 24,2
MV 224,96,32
UH 6,13
UL 7,16
MV 224,100,36
TR 116,4,23
UH 8,16
MV 220,96,24
TR 118,5,23
AD 102,6
MV 220,100,28
RV 24,20,36864
AD 106,7
MV 216,96,16
WL 5
AD 110,8
UL 6,14
MV 216,100,20
UH 7,14
UL 8,15
MV 212,96,8
s_add_u32 s42, s88, 147456
AD 91,6
AD 95,7
MV 212,100,12
s_add_u32 s42, s42, s72
s_addc_u32 s43, s73, 0
AD 99,8
MV 208,96,0
UH 6,15
UL 7,17
MV 208,100,4
TR 96,2,24
UH 8,17
MV 232,104,0
TR 98,3,24
AD 103,6
MV 232,108,4
WL 2
ST 24,0
MV 236,104,8
AD 107,7
AD 111,8
MV 236,108,12
TR 100,2,25
MV 240,104,16
TR 102,3,25
MV 240,108,20
MV 244,104,24
MV 244,108,28
MV 248,104,32
MV 248,108,36
MV 252,104,40
MV 252,108,44
TR 104,4,24
)"
R"(MA 188,112,40
TR 106,5,24
MA 188,116,44
MA 184,112,32
MA 184,116,36
TR 108,4,25
MA 180,112,24
TR 110,5,25
MA 180,116,28
MA 176,112,16
MA 176,116,20
MA 172,112,8
MA 172,116,12
MA 168,112,0
WV 8
MA 168,116,4
WL 4
TR 112,2,26
MV 40,96,48
TR 114,3,26
MV 40,100,52
s_add_u32 s44, s84, 495616
s_addc_u32 s45, s85, 0
MV 44,96,56
LA 0,-4
MV 44,100,60
TR 116,2,27
MV 48,96,64
TR 118,3,27
MV 48,100,68
MV 52,96,72
MV 52,100,76
LA 4,-3
MV 56,96,80
MV 56,100,84
MV 60,96,88
MV 60,100,92
WL 4
TR 96,4,26
MV 84,104,88
TR 98,5,26
MV 84,108,92
LA 8,-2
MV 80,104,80
MV 80,108,84
TR 100,4,27
MV 76,104,72
TR 102,5,27
MV 76,108,76
MV 72,104,64
LA 12,-1
MV 72,108,68
MV 68,104,56
MV 68,108,60
MV 64,104,48
MV 64,108,52
WL 4
TR 104,2,28
MV 88,112,48
TR 106,3,28
MV 88,116,52
LA 16,0
MV 92,112,56
MV 92,116,60
TR 108,2,29
MV 96,112,64
TR 110,3,29
MV 96,116,68
RV 24,20,37888
MV 100,112,72
LA 20,1
MV 100,116,76
MV 104,112,80
MV 104,116,84
s_add_u32 s44, s44, 6144
s_addc_u32 s45, s45, 0
MV 108,112,88
MV 108,116,92
WL 5
TR 112,4,28
MA 140,96,88
TR 114,5,28
MA 140,100,92
LA 24,-4
MA 136,96,80
MA 136,100,84
TR 116,4,29
MA 132,96,72
TR 118,5,29
MA 132,100,76
MA 128,96,64
LA 28,-3
MA 128,100,68
MA 124,96,56
MA 124,100,60
WL 0
s_barrier
MA 120,96,48
s_add_u32 m0, %[hm], 18432
MA 120,100,52
TR 96,2,30
MV 112,104,48
TR 98,3,30
MV 112,108,52
LA 32,-2
MV 116,104,56
s_add_u32 s40, s64, 1054720
s_addc_u32 s41, s65, 0
MV 116,108,60
TR 100,2,31
MV 120,104,64
TR 102,3,31
MV 120,108,68
ds_read_b64 v[12:13], v18 offset:12288
MV 124,104,72
LA 36,-1
MV 124,108,76
ds_read_b32 v16, v19 offset:12288
MV 128,104,80
ds_read_b64 v[14:15], v18 offset:13056
MV 128,108,84
ds_read_b32 v17, v19 offset:13056
MV 132,104,88
MV 132,108,92
TR 104,4,30
MV 156,112,88
TR 106,5,30
MV 156,116,92
LA 40,0
MV 152,112,80
WL 4
UL 6,12
UH 7,12
MV 152,116,84
TR 108,4,31
UL 8,13
MV 148,112,72
TR 110,5,31
AR 9,120
MV 148,116,76
AR 10,124
AR 11,128
MV 144,112,64
LA 44,1
MV 144,116,68
AD 9,6
AD 10,7
MV 140,112,56
AD 11,8
AW 120,9
MV 140,116,60
AW 124,10
AW 128,11
MV 136,112,48
UH 6,13
UL 7,16
MV 136,116,52
TR 112,2,32
UH 8,16
MV 160,96,48
TR 114,3,32
MV 160,100,52
DH 0
MV 164,96,56
ds_read_b64 v[12:13], v18 offset:13824
MV 164,100,60
TR 116,2,33
MV 168,96,64
s_add_u32 m0, %[hm], 20480
TR 118,3,33
MV 168,100,68
s_add_u32 s40, s66, 1048576
s_addc_u32 s41, s67, 0
MV 172,96,72
DH 0
MV 172,100,76
ds_read_b32 v16, v19 offset:13824
AR 9,132
MV 176,96,80
AR 10,136
AR 11,140
MV 176,100,84
AD 9,6
AD 10,7
MV 180,96,88
AD 11,8
AW 132,9
MV 180,100,92
WL 6
TR 96,4,32
AW 136,10
MA 164,104,88
TR 98,5,32
AW 140,11
MA 164,108,92
DH 2
MA 160,104,80
UL 6,14
UH 7,14
MA 160,108,84
TR 100,4,33
UL 8,15
MA 156,104,72
PA 20480
TR 102,5,33
AR 9,121
MA 156,108,76
.Lk2p19:
s_cbranch_execz .Lk2r3
AR 10,125
AR 11,129
MA 152,104,64
DF
MA 152,108,68
s_add_u32 m0, %[hm], 24576
v_xor_b32 v1, 0x700, v1
AD 9,6
MA 148,104,56
s_add_u32 s40, s64, 1048576
s_addc_u32 s41, s65, 0
AD 10,7
MA 148,108,60
AD 11,8
AW 121,9
MA 144,104,48
AW 125,10
AW 129,11
MA 144,108,52
WL 3
TR 104,2,34
UH 6,15
MV 184,112,48
TR 106,3,34
UL 7,17
MV 184,116,52
DH 0
MV 188,112,56
UH 8,17
MV 188,116,60
TR 108,2,35
MV 192,112,64
TR 110,3,35
MV 192,116,68
ds_read_b64 v[14:15], v18 offset:14592
MV 196,112,72
DH 2
MV 196,116,76
ds_read_b32 v17, v19 offset:14592
AR 9,133
MV 200,112,80
AR 10,137
AR 11,141
MV 200,116,84
s_add_u32 m0, %[hm], 28672
AD 9,6
AD 10,7
MV 204,112,88
s_add_u32 s40, s64, 1052672
s_addc_u32 s41, s65, 0
AD 11,8
MV 204,116,92
WL 6
TR 112,4,34
AW 133,9
MV 228,96,88
TR 114,5,34
AW 137,10
MV 228,100,92
DH 0
MV 224,96,80
AW 141,11
UL 6,12
MV 224,100,84
TR 116,4,35
UH 7,12
MV 220,96,72
s_add_u32 m0, %[wm], 0
TR 118,5,35
UL 8,13
MV 220,100,76
s_add_u32 s40, s88, 245760
AR 9,122
AR 10,126
MV 216,96,64
s_add_u32 s40, s40, s68
s_addc_u32 s41, s69, 0
AR 11,130
MV 216,100,68
DX 0
MV 212,96,56
AD 9,6
AD 10,7
MV 212,100,60
AD 11,8
AW 122,9
MV 208,96,48
AW 126,10
AW 130,11
MV 208,100,52
WL 3
TR 96,2,0
UH 6,13
MV 232,104,48
TR 98,3,0
UL 7,16
MV 232,108,52
DX 1
MV 236,104,56
UH 8,16
AR 9,134
MV 236,108,60
TR 100,2,1
AR 10,138
MV 240,104,64
TR 102,3,1
AR 11,142
MV 240,108,68
AD 9,6
AD 10,7
MV 244,104,72
DX 2
MV 244,108,76
AD 11,8
AW 134,9
MV 248,104,80
AW 138,10
AW 142,11
MV 248,108,84
UL 6,14
UH 7,14
MV 252,104,88
UL 8,15
AR 9,123
MV 252,108,92
WL 4
TR 104,4,0
AR 10,127
MA 188,112,88
TR 106,5,0
AR 11,131
MA 188,116,92
ST 24,1
MA 184,112,80
AD 9,6
AD 10,7
MA 184,116,84
TR 108,4,1
AD 11,8
MA 180,112,72
TR 110,5,1
AW 123,9
MA 180,116,76
RV 24,20,38912
AW 127,10
MA 176,112,64
AW 131,11
UH 6,15
MA 176,116,68
UL 7,17
UH 8,17
MA 172,112,56
AR 9,135
AR 10,139
MA 172,116,60
AR 11,143
AD 9,6
MA 168,112,48
WV 11
AD 10,7
AD 11,8
MA 168,116,52
WL 5
TR 112,2,2
AW 135,9
MV 40,96,0
TR 114,3,2
AW 139,10
MV 40,100,4
s_add_u32 s44, s84, 544768
s_addc_u32 s45, s85, 0
AW 143,11
MV 44,96,8
LA 48,-4
MV 44,100,12
TR 116,2,3
MV 48,96,16
TR 118,3,3
MV 48,100,20
MV 52,96,24
MV 52,100,28
LA 52,-3
MV 56,96,32
MV 56,100,36
MV 60,96,40
MV 60,100,44
WL 4
TR 96,4,2
MV 84,104,40
TR 98,5,2
MV 84,108,44
)"
R"(LA 56,-2
MV 80,104,32
MV 80,108,36
TR 100,4,3
MV 76,104,24
TR 102,5,3
MV 76,108,28
MV 72,104,16
LA 60,-1
MV 72,108,20
MV 68,104,8
MV 68,108,12
MV 64,104,0
MV 64,108,4
WL 4
TR 104,2,4
MV 88,112,0
TR 106,3,4
MV 88,116,4
LA 64,0
MV 92,112,8
MV 92,116,12
TR 108,2,5
MV 96,112,16
TR 110,3,5
MV 96,116,20
MV 100,112,24
LA 68,1
MV 100,116,28
MV 104,112,32
MV 104,116,36
s_add_u32 s44, s44, 6144
s_addc_u32 s45, s45, 0
MV 108,112,40
MV 108,116,44
WL 4
TR 112,4,4
MA 140,96,40
TR 114,5,4
MA 140,100,44
LA 72,-4
MA 136,96,32
MA 136,100,36
TR 116,4,5
MA 132,96,24
TR 118,5,5
MA 132,100,28
MA 128,96,16
LA 76,-3
MA 128,100,20
MA 124,96,8
MA 124,100,12
WL 0
s_barrier
MA 120,96,0
s_add_u32 m0, %[hm], 30720
ds_write_b64 v18, a[192:193] offset:36864
MA 120,100,4
TR 96,2,6
MV 112,104,0
TR 98,3,6
MV 112,108,4
LA 80,-2
MV 116,104,8
s_add_u32 s40, s64, 1054720
s_addc_u32 s41, s65, 0
MV 116,108,12
TR 100,2,7
MV 120,104,16
TR 102,3,7
MV 120,108,20
ds_write_b32 v19, a200 offset:36864
MV 124,104,24
LA 84,-1
MV 124,108,28
ds_write_b64 v18, a[194:195] offset:37632
MV 128,104,32
MV 128,108,36
ds_write_b32 v19, a201 offset:37632
MV 132,104,40
MV 132,108,44
TR 104,4,6
MV 156,112,40
TR 106,5,6
MV 156,116,44
LA 88,0
MV 152,112,32
ds_write_b64 v18, a[196:197] offset:38400
MV 152,116,36
TR 108,4,7
MV 148,112,24
TR 110,5,7
MV 148,116,28
ds_write_b32 v19, a202 offset:38400
MV 144,112,16
LA 92,1
MV 144,116,20
ds_write_b64 v18, a[198:199] offset:39168
MV 140,112,8
MV 140,116,12
WL 9
ds_write_b32 v19, a203 offset:39168
MV 136,112,0
MV 136,116,4
TR 112,2,8
MV 160,96,0
TR 114,3,8
MV 160,100,4
DH 0
MV 164,96,8
MV 164,100,12
TR 116,2,9
MV 168,96,16
s_add_u32 m0, %[hm], 32768
TR 118,3,9
MV 168,100,20
s_add_u32 s40, s66, 1048576
s_addc_u32 s41, s67, 0
MV 172,96,24
DH 0
MV 172,100,28
MV 176,96,32
MV 176,100,36
MV 180,96,40
MV 180,100,44
WL 7
TR 96,4,8
MA 164,104,40
TR 98,5,8
MA 164,108,44
DH 2
MA 160,104,32
MA 160,108,36
TR 100,4,9
MA 156,104,24
TR 102,5,9
MA 156,108,28
s_add_u32 m0, %[hm], 0
v_xor_b32 v1, 0x100, v1
MA 152,104,16
s_add_u32 s40, s64, 1048576
s_addc_u32 s41, s65, 0
MA 152,108,20
DH 0
MA 148,104,8
MA 148,108,12
MA 144,104,0
MA 144,108,4
WL 4
TR 104,2,10
MV 184,112,0
TR 106,3,10
MV 184,116,4
DH 2
MV 188,112,8
MV 188,116,12
TR 108,2,11
MV 192,112,16
s_add_u32 m0, %[hm], 4096
TR 110,3,11
MV 192,116,20
s_add_u32 s40, s64, 1052672
s_addc_u32 s41, s65, 0
MV 196,112,24
DH 0
MV 196,116,28
MV 200,112,32
MV 200,116,36
s_add_u32 m0, %[wm], 12288
MV 204,112,40
s_add_u32 s40, s88, 344064
MV 204,116,44
WL 4
TR 112,4,10
MV 228,96,40
TR 114,5,10
MV 228,100,44
s_add_u32 s40, s40, s68
s_addc_u32 s41, s69, 0
MV 224,96,32
DX 0
MV 224,100,36
TR 116,4,11
MV 220,96,24
TR 118,5,11
MV 220,100,28
MV 216,96,16
MV 216,100,20
DX 1
MV 212,96,8
MV 212,100,12
MV 208,96,0
MV 208,100,4
WL 4
TR 96,2,12
MV 232,104,0
TR 98,3,12
MV 232,108,4
DX 2
MV 236,104,8
MV 236,108,12
TR 100,2,13
MV 240,104,16
TR 102,3,13
MV 240,108,20
MV 244,104,24
ST 24,2
MV 244,108,28
MV 248,104,32
RV 24,20,49152
MV 248,108,36
MV 252,104,40
MV 252,108,44
WL 5
TR 104,4,12
MA 188,112,40
s_add_u32 s42, s89, 147456
TR 106,5,12
MA 188,116,44
s_add_u32 s42, s42, s72
s_addc_u32 s43, s73, 0
MA 184,112,32
MA 184,116,36
TR 108,4,13
MA 180,112,24
TR 110,5,13
MA 180,116,28
WL 4
ST 24,0
MA 176,112,16
MA 176,116,20
MA 172,112,8
MA 172,116,12
MA 168,112,0
MA 168,116,4
WV 11
TR 112,2,14
MV 40,96,48
TR 114,3,14
MV 40,100,52
s_add_u32 s44, s84, 593920
s_addc_u32 s45, s85, 0
MV 44,96,56
LA 0,-4
MV 44,100,60
TR 116,2,15
MV 48,96,64
TR 118,3,15
MV 48,100,68
MV 52,96,72
MV 52,100,76
LA 4,-3
MV 56,96,80
MV 56,100,84
RV 24,20,50176
MV 60,96,88
MV 60,100,92
WL 5
TR 96,4,14
MV 84,104,88
TR 98,5,14
MV 84,108,92
LA 8,-2
MV 80,104,80
MV 80,108,84
TR 100,4,15
MV 76,104,72
TR 102,5,15
MV 76,108,76
MV 72,104,64
LA 12,-1
MV 72,108,68
MV 68,104,56
MV 68,108,60
MV 64,104,48
MV 64,108,52
WL 5
TR 104,2,16
MV 88,112,48
TR 106,3,16
MV 88,116,52
LA 16,0
MV 92,112,56
MV 92,116,60
TR 108,2,17
MV 96,112,64
TR 110,3,17
MV 96,116,68
MV 100,112,72
LA 20,1
MV 100,116,76
MV 104,112,80
MV 104,116,84
s_add_u32 s44, s44, 6144
s_addc_u32 s45, s45, 0
MV 108,112,88
MV 108,116,92
WL 4
TR 112,4,16
MA 140,96,88
TR 114,5,16
MA 140,100,92
LA 24,-4
MA 136,96,80
MA 136,100,84
TR 116,4,17
MA 132,96,72
TR 118,5,17
MA 132,100,76
MA 128,96,64
LA 28,-3
MA 128,100,68
MA 124,96,56
MA 124,100,60
WL 0
s_barrier
MA 120,96,48
s_add_u32 m0, %[hm], 6144
MA 120,100,52
TR 96,2,18
MV 112,104,48
TR 98,3,18
MV 112,108,52
LA 32,-2
MV 116,104,56
s_add_u32 s40, s64, 1054720
s_addc_u32 s41, s65, 0
MV 116,108,60
TR 100,2,19
MV 120,104,64
TR 102,3,19
MV 120,108,68
MV 124,104,72
LA 36,-1
MV 124,108,76
MV 128,104,80
MV 128,108,84
MV 132,104,88
MV 132,108,92
TR 104,4,18
MV 156,112,88
TR 106,5,18
MV 156,116,92
LA 40,0
MV 152,112,80
ds_read_b64 v[12:13], v18 offset:0
MV 152,116,84
TR 108,4,19
MV 148,112,72
TR 110,5,19
MV 148,116,76
ds_read_b32 v16, v19 offset:0
MV 144,112,64
.Lk2p20:
s_cbranch_execz .Lk2r3
LA 44,1
MV 144,116,68
ds_read_b64 v[14:15], v18 offset:768
MV 140,112,56
ds_read_b32 v17, v19 offset:768
MV 140,116,60
MV 136,112,48
MV 136,116,52
WL 8
TR 112,2,20
MV 160,96,48
TR 114,3,20
MV 160,100,52
DH 0
MV 164,96,56
WL 4
UL 6,12
UH 7,12
MV 164,100,60
TR 116,2,21
UL 8,13
MV 168,96,64
s_add_u32 m0, %[hm], 8192
TR 118,3,21
AD 112,6
MV 168,100,68
s_add_u32 s40, s66, 1048576
s_addc_u32 s41, s67, 0
AD 116,7
MV 172,96,72
DH 0
MV 172,100,76
AD 120,8
UH 6,13
MV 176,96,80
UL 7,16
UH 8,16
MV 176,100,84
ds_read_b64 v[12:13], v18 offset:1536
MV 180,96,88
ds_read_b32 v16, v19 offset:1536
AD 124,6
MV 180,100,92
TR 96,4,20
AD 128,7
)"
R"(MA 164,104,88
TR 98,5,20
AD 132,8
MA 164,108,92
DH 2
MA 160,104,80
WL 6
UL 6,14
UH 7,14
MA 160,108,84
TR 100,4,21
UL 8,15
MA 156,104,72
TR 102,5,21
AD 113,6
MA 156,108,76
s_add_u32 m0, %[hm], 12288
v_xor_b32 v1, 0x300, v1
AD 117,7
MA 152,104,64
s_add_u32 s40, s64, 1048576
s_addc_u32 s41, s65, 0
AD 121,8
MA 152,108,68
DH 0
MA 148,104,56
UH 6,15
UL 7,17
MA 148,108,60
UH 8,17
MA 144,104,48
ds_read_b64 v[14:15], v18 offset:2304
MA 144,108,52
WL 6
TR 104,2,22
MV 184,112,48
TR 106,3,22
MV 184,116,52
DH 2
MV 188,112,56
ds_read_b32 v17, v19 offset:2304
AD 125,6
MV 188,116,60
WL 5
TR 108,2,23
AD 129,7
MV 192,112,64
s_add_u32 m0, %[hm], 16384
TR 110,3,23
AD 133,8
MV 192,116,68
s_add_u32 s40, s64, 1052672
s_addc_u32 s41, s65, 0
UL 6,12
MV 196,112,72
DH 0
MV 196,116,76
UH 7,12
UL 8,13
MV 200,112,80
AD 114,6
AD 118,7
MV 200,116,84
AD 122,8
UH 6,13
MV 204,112,88
UL 7,16
UH 8,16
MV 204,116,92
WL 6
TR 112,4,22
AD 126,6
MV 228,96,88
TR 114,5,22
AD 130,7
MV 228,100,92
ST 24,1
MV 224,96,80
WL 4
AD 134,8
UL 6,14
MV 224,100,84
TR 116,4,23
UH 7,14
MV 220,96,72
TR 118,5,23
UL 8,15
MV 220,100,76
RV 24,20,51200
AD 115,6
MV 216,96,64
AD 119,7
AD 123,8
MV 216,100,68
UH 6,15
UL 7,17
MV 212,96,56
UH 8,17
AD 127,6
MV 212,100,60
AD 131,7
AD 135,8
MV 208,96,48
MV 208,100,52
WL 5
TR 96,2,24
MV 232,104,48
TR 98,3,24
MV 232,108,52
WL 2
ST 24,2
MV 236,104,56
MV 236,108,60
TR 100,2,25
MV 240,104,64
TR 102,3,25
MV 240,108,68
MV 244,104,72
MV 244,108,76
MV 248,104,80
MV 248,108,84
MV 252,104,88
MV 252,108,92
TR 104,4,24
MA 188,112,88
TR 106,5,24
MA 188,116,92
MA 184,112,80
MA 184,116,84
TR 108,4,25
MA 180,112,72
TR 110,5,25
MA 180,116,76
MA 176,112,64
MA 176,116,68
MA 172,112,56
MA 172,116,60
MA 168,112,48
WV 8
MA 168,116,52
WL 4
TR 112,2,26
MV 40,96,0
TR 114,3,26
MV 40,100,4
s_add_u32 s44, s84, 643072
s_addc_u32 s45, s85, 0
MV 44,96,8
LA 48,-4
MV 44,100,12
TR 116,2,27
MV 48,96,16
TR 118,3,27
MV 48,100,20
MV 52,96,24
MV 52,100,28
LA 52,-3
MV 56,96,32
MV 56,100,36
MV 60,96,40
MV 60,100,44
WL 4
TR 96,4,26
MV 84,104,40
TR 98,5,26
MV 84,108,44
LA 56,-2
MV 80,104,32
MV 80,108,36
TR 100,4,27
MV 76,104,24
TR 102,5,27
MV 76,108,28
MV 72,104,16
LA 60,-1
MV 72,108,20
MV 68,104,8
MV 68,108,12
MV 64,104,0
MV 64,108,4
WL 4
TR 104,2,28
MV 88,112,0
TR 106,3,28
MV 88,116,4
LA 64,0
MV 92,112,8
MV 92,116,12
TR 108,2,29
MV 96,112,16
TR 110,3,29
MV 96,116,20
MV 100,112,24
LA 68,1
MV 100,116,28
MV 104,112,32
MV 104,116,36
s_add_u32 s44, s44, 6144
s_addc_u32 s45, s45, 0
MV 108,112,40
MV 108,116,44
WL 4
TR 112,4,28
MA 140,96,40
TR 114,5,28
MA 140,100,44
LA 72,-4
MA 136,96,32
s_mov_b64 exec, 0
s_cmp_eq_u32 %[w], 0
s_cbranch_scc1 .Lk2p21
s_cmp_eq_u32 %[w], 1
s_cbranch_scc1 .Lk2p22
s_cmp_eq_u32 %[w], 2
s_cbranch_scc1 .Lk2p23
s_branch .Lk2p24
.Lk2r4:
s_mov_b64 exec, -1
MA 136,100,36
TR 116,4,29
MA 132,96,24
TR 118,5,29
MA 132,100,28
MA 128,96,16
LA 76,-3
MA 128,100,20
MA 124,96,8
MA 124,100,12
WL 0
s_barrier
v_add_u32 v23, 0xf000, v20
MA 120,96,0
s_add_u32 m0, %[hm], 18432
RV 24,23,0
MA 120,100,4
TR 96,2,30
MV 112,104,0
TR 98,3,30
MV 112,108,4
s_add_u32 s42, s90, 0
LA 80,-2
MV 116,104,8
s_add_u32 s40, s64, 1054720
s_addc_u32 s41, s65, 0
MV 116,108,12
TR 100,2,31
MV 120,104,16
TR 102,3,31
MV 120,108,20
s_add_u32 s42, s42, s74
s_addc_u32 s43, s75, 0
MV 124,104,24
LA 84,-1
MV 124,108,28
MV 128,104,32
MV 128,108,36
MV 132,104,40
MV 132,108,44
TR 104,4,30
MV 156,112,40
TR 106,5,30
MV 156,116,44
LA 88,0
MV 152,112,32
MV 152,116,36
TR 108,4,31
MV 148,112,24
TR 110,5,31
MV 148,116,28
MV 144,112,16
LA 92,1
MV 144,116,20
MV 140,112,8
MV 140,116,12
MV 136,112,0
MV 136,116,4
WL 4
TR 112,2,32
MV 160,96,0
TR 114,3,32
MV 160,100,4
DH 0
MV 164,96,8
ds_read_b64 v[12:13], v18 offset:12288
MV 164,100,12
TR 116,2,33
MV 168,96,16
s_add_u32 m0, %[hm], 20480
TR 118,3,33
MV 168,100,20
s_add_u32 s40, s66, 1048576
s_addc_u32 s41, s67, 0
MV 172,96,24
DH 0
MV 172,100,28
ds_read_b32 v16, v19 offset:12288
MV 176,96,32
ds_read_b64 v[14:15], v18 offset:13056
MV 176,100,36
ds_read_b32 v17, v19 offset:13056
MV 180,96,40
MV 180,100,44
WL 8
TR 96,4,32
MA 164,104,40
TR 98,5,32
MA 164,108,44
DH 2
MA 160,104,32
WL 4
UL 6,12
UH 7,12
MA 160,108,36
TR 100,4,33
UL 8,13
MA 156,104,24
TR 102,5,33
AD 136,6
MA 156,108,28
s_add_u32 m0, %[hm], 24576
v_xor_b32 v1, 0x100, v1
AD 140,7
MA 152,104,16
s_add_u32 s40, s64, 1048576
s_addc_u32 s41, s65, 0
AD 144,8
MA 152,108,20
DH 0
MA 148,104,8
UH 6,13
UL 7,16
MA 148,108,12
UH 8,16
MA 144,104,0
ds_read_b64 v[12:13], v18 offset:13824
MA 144,108,4
TR 104,2,34
MV 184,112,0
TR 106,3,34
MV 184,116,4
DH 2
MV 188,112,8
ds_read_b32 v16, v19 offset:13824
AD 148,6
MV 188,116,12
WL 5
TR 108,2,35
AD 152,7
MV 192,112,16
s_add_u32 m0, %[hm], 28672
TR 110,3,35
AD 156,8
MV 192,116,20
s_add_u32 s40, s64, 1052672
s_addc_u32 s41, s65, 0
UL 6,14
MV 196,112,24
DH 0
MV 196,116,28
UH 7,14
UL 8,15
MV 200,112,32
AD 137,6
AD 141,7
MV 200,116,36
s_add_u32 m0, %[wm], 0
AD 145,8
UH 6,15
MV 204,112,40
s_add_u32 s40, s89, 245760
UL 7,17
UH 8,17
MV 204,116,44
WL 5
TR 112,4,34
MV 228,96,40
TR 114,5,34
MV 228,100,44
s_add_u32 s40, s40, s68
s_addc_u32 s41, s69, 0
MV 224,96,32
DX 0
MV 224,100,36
TR 116,4,35
MV 220,96,24
TR 118,5,35
MV 220,100,28
ds_read_b64 v[14:15], v18 offset:14592
MV 216,96,16
ds_read_b32 v17, v19 offset:14592
AD 149,6
MV 216,100,20
DX 1
MV 212,96,8
AD 153,7
AD 157,8
MV 212,100,12
WL 6
UL 6,12
UH 7,12
)"
R"(MV 208,96,0
UL 8,13
AD 138,6
MV 208,100,4
TR 96,2,0
AD 142,7
MV 232,104,0
TR 98,3,0
AD 146,8
MV 232,108,4
DX 2
MV 236,104,8
UH 6,13
UL 7,16
MV 236,108,12
TR 100,2,1
UH 8,16
MV 240,104,16
TR 102,3,1
AD 150,6
MV 240,108,20
AD 154,7
AD 158,8
MV 244,104,24
ST 24,0
MV 244,108,28
WL 4
UL 6,14
UH 7,14
MV 248,104,32
UL 8,15
AD 139,6
MV 248,108,36
AD 143,7
AD 147,8
MV 252,104,40
UH 6,15
UL 7,17
MV 252,108,44
TR 104,4,0
UH 8,17
MA 188,112,40
TR 106,5,0
AD 151,6
MA 188,116,44
AD 155,7
AD 159,8
MA 184,112,32
MA 184,116,36
TR 108,4,1
MA 180,112,24
TR 110,5,1
MA 180,116,28
MA 176,112,16
RV 24,23,1024
MA 176,116,20
MA 172,112,8
MA 172,116,12
MA 168,112,0
WV 10
MA 168,116,4
WL 5
TR 112,2,2
MV 40,96,48
TR 114,3,2
MV 40,100,52
s_add_u32 s44, s84, 692224
s_addc_u32 s45, s85, 0
MV 44,96,56
LA 0,-4
MV 44,100,60
TR 116,2,3
MV 48,96,64
TR 118,3,3
MV 48,100,68
MV 52,96,72
MV 52,100,76
LA 4,-3
MV 56,96,80
MV 56,100,84
MV 60,96,88
MV 60,100,92
WL 5
TR 96,4,2
MV 84,104,88
TR 98,5,2
MV 84,108,92
LA 8,-2
MV 80,104,80
MV 80,108,84
TR 100,4,3
MV 76,104,72
TR 102,5,3
MV 76,108,76
MV 72,104,64
LA 12,-1
MV 72,108,68
MV 68,104,56
MV 68,108,60
MV 64,104,48
MV 64,108,52
WL 4
TR 104,2,4
MV 88,112,48
TR 106,3,4
MV 88,116,52
LA 16,0
MV 92,112,56
MV 92,116,60
TR 108,2,5
MV 96,112,64
TR 110,3,5
MV 96,116,68
MV 100,112,72
LA 20,1
MV 100,116,76
MV 104,112,80
MV 104,116,84
s_add_u32 s44, s44, 6144
s_addc_u32 s45, s45, 0
MV 108,112,88
MV 108,116,92
WL 4
TR 112,4,4
MA 140,96,88
TR 114,5,4
MA 140,100,92
LA 24,-4
MA 136,96,80
MA 136,100,84
TR 116,4,5
MA 132,96,72
TR 118,5,5
MA 132,100,76
MA 128,96,64
LA 28,-3
MA 128,100,68
MA 124,96,56
MA 124,100,60
WL 0
s_barrier
MA 120,96,48
s_add_u32 m0, %[hm], 30720
MA 120,100,52
.Lk2p21:
s_cbranch_execz .Lk2r4
TR 96,2,6
MV 112,104,48
TR 98,3,6
MV 112,108,52
LA 32,-2
MV 116,104,56
s_add_u32 s40, s64, 1054720
s_addc_u32 s41, s65, 0
MV 116,108,60
TR 100,2,7
MV 120,104,64
TR 102,3,7
MV 120,108,68
MV 124,104,72
LA 36,-1
MV 124,108,76
MV 128,104,80
MV 128,108,84
MV 132,104,88
MV 132,108,92
TR 104,4,6
MV 156,112,88
TR 106,5,6
MV 156,116,92
LA 40,0
MV 152,112,80
MV 152,116,84
TR 108,4,7
MV 148,112,72
TR 110,5,7
MV 148,116,76
MV 144,112,64
LA 44,1
MV 144,116,68
MV 140,112,56
MV 140,116,60
MV 136,112,48
MV 136,116,52
WL 4
TR 112,2,8
MV 160,96,48
TR 114,3,8
MV 160,100,52
DH 0
MV 164,96,56
MV 164,100,60
TR 116,2,9
MV 168,96,64
s_add_u32 m0, %[hm], 32768
TR 118,3,9
MV 168,100,68
s_add_u32 s40, s66, 1048576
s_addc_u32 s41, s67, 0
MV 172,96,72
DH 0
MV 172,100,76
MV 176,96,80
MV 176,100,84
MV 180,96,88
MV 180,100,92
WL 4
TR 96,4,8
MA 164,104,88
TR 98,5,8
MA 164,108,92
DH 2
MA 160,104,80
MA 160,108,84
TR 100,4,9
MA 156,104,72
PA 20480
TR 102,5,9
MA 156,108,76
MA 152,104,64
DF
MA 152,108,68
s_bitcmp1_b32 s48, 7
s_cselect_b64 s[64:65], s[58:59], s[56:57]
MA 148,104,56
s_bitcmp1_b32 s48, 23
s_cselect_b32 s40, 16384, 0
MA 148,108,60
s_add_u32 s64, s64, s40
s_addc_u32 s65, s65, 0
MA 144,104,48
s_add_u32 m0, %[hm], 0
v_xor_b32 v1, 0x700, v1
MA 144,108,52
WL 4
TR 104,2,10
MV 184,112,48
TR 106,3,10
MV 184,116,52
s_add_u32 s40, s64, 1835008
s_addc_u32 s41, s65, 0
MV 188,112,56
DH 0
MV 188,116,60
TR 108,2,11
MV 192,112,64
TR 110,3,11
MV 192,116,68
MV 196,112,72
MV 196,116,76
DH 2
MV 200,112,80
MV 200,116,84
MV 204,112,88
s_add_u32 m0, %[hm], 4096
MV 204,116,92
WL 4
TR 112,4,10
MV 228,96,88
TR 114,5,10
MV 228,100,92
s_add_u32 s40, s64, 1839104
s_addc_u32 s41, s65, 0
MV 224,96,80
DH 0
MV 224,100,84
TR 116,4,11
MV 220,96,72
TR 118,5,11
MV 220,100,76
s_add_u32 m0, %[wm], 12288
MV 216,96,64
s_add_u32 s40, s89, 344064
MV 216,100,68
s_add_u32 s40, s40, s68
s_addc_u32 s41, s69, 0
MV 212,96,56
DX 0
MV 212,100,60
MV 208,96,48
MV 208,100,52
WL 4
TR 96,2,12
MV 232,104,48
TR 98,3,12
MV 232,108,52
DX 1
MV 236,104,56
MV 236,108,60
TR 100,2,13
MV 240,104,64
TR 102,3,13
MV 240,108,68
MV 244,104,72
DX 2
MV 244,108,76
MV 248,104,80
MV 248,108,84
MV 252,104,88
MV 252,108,92
WL 4
TR 104,4,12
MA 188,112,88
TR 106,5,12
MA 188,116,92
ST 24,1
MA 184,112,80
MA 184,116,84
TR 108,4,13
MA 180,112,72
TR 110,5,13
MA 180,116,76
RV 24,23,2048
MA 176,112,64
MA 176,116,68
MA 172,112,56
MA 172,116,60
MA 168,112,48
WV 11
MA 168,116,52
WL 5
TR 112,2,14
MV 40,96,0
TR 114,3,14
MV 40,100,4
s_add_u32 s44, s84, 741376
s_addc_u32 s45, s85, 0
MV 44,96,8
LA 48,-4
MV 44,100,12
TR 116,2,15
MV 48,96,16
TR 118,3,15
MV 48,100,20
MV 52,96,24
MV 52,100,28
LA 52,-3
MV 56,96,32
MV 56,100,36
MV 60,96,40
MV 60,100,44
WL 5
TR 96,4,14
MV 84,104,40
TR 98,5,14
MV 84,108,44
LA 56,-2
MV 80,104,32
MV 80,108,36
TR 100,4,15
MV 76,104,24
TR 102,5,15
MV 76,108,28
MV 72,104,16
LA 60,-1
MV 72,108,20
MV 68,104,8
MV 68,108,12
MV 64,104,0
MV 64,108,4
WL 4
TR 104,2,16
MV 88,112,0
TR 106,3,16
MV 88,116,4
LA 64,0
MV 92,112,8
MV 92,116,12
TR 108,2,17
MV 96,112,16
TR 110,3,17
MV 96,116,20
MV 100,112,24
LA 68,1
MV 100,116,28
MV 104,112,32
MV 104,116,36
s_add_u32 s44, s44, 6144
s_addc_u32 s45, s45, 0
MV 108,112,40
MV 108,116,44
WL 4
TR 112,4,16
MA 140,96,40
TR 114,5,16
MA 140,100,44
LA 72,-4
MA 136,96,32
MA 136,100,36
TR 116,4,17
MA 132,96,24
TR 118,5,17
MA 132,100,28
MA 128,96,16
LA 76,-3
MA 128,100,20
MA 124,96,8
MA 124,100,12
WL 0
s_barrier
)"
R"(MA 120,96,0
s_add_u32 m0, %[hm], 6144
MA 120,100,4
TR 96,2,18
MV 112,104,0
TR 98,3,18
MV 112,108,4
LA 80,-2
MV 116,104,8
s_add_u32 s40, s64, 1841152
s_addc_u32 s41, s65, 0
MV 116,108,12
TR 100,2,19
MV 120,104,16
TR 102,3,19
MV 120,108,20
MV 124,104,24
LA 84,-1
MV 124,108,28
MV 128,104,32
MV 128,108,36
MV 132,104,40
MV 132,108,44
TR 104,4,18
MV 156,112,40
TR 106,5,18
MV 156,116,44
LA 88,0
MV 152,112,32
MV 152,116,36
TR 108,4,19
MV 148,112,24
TR 110,5,19
MV 148,116,28
MV 144,112,16
LA 92,1
MV 144,116,20
MV 140,112,8
MV 140,116,12
MV 136,112,0
MV 136,116,4
WL 4
TR 112,2,20
MV 160,96,0
TR 114,3,20
MV 160,100,4
DH 0
MV 164,96,8
MV 164,100,12
TR 116,2,21
MV 168,96,16
TR 118,3,21
MV 168,100,20
s_bitcmp1_b32 s49, 7
s_cselect_b64 s[66:67], s[62:63], s[60:61]
MV 172,96,24
s_add_u32 m0, %[hm], 8192
MV 172,100,28
s_add_u32 s40, s66, 1835008
s_addc_u32 s41, s67, 0
MV 176,96,32
DH 0
MV 176,100,36
MV 180,96,40
MV 180,100,44
WL 4
TR 96,4,20
MA 164,104,40
TR 98,5,20
MA 164,108,44
DH 2
MA 160,104,32
ds_read_b64 v[12:13], v18 offset:0
MA 160,108,36
TR 100,4,21
MA 156,104,24
TR 102,5,21
MA 156,108,28
s_add_u32 m0, %[hm], 12288
v_xor_b32 v1, 0x100, v1
MA 152,104,16
s_add_u32 s40, s64, 1835008
s_addc_u32 s41, s65, 0
MA 152,108,20
DH 0
MA 148,104,8
ds_read_b32 v16, v19 offset:0
MA 148,108,12
ds_read_b64 v[14:15], v18 offset:768
MA 144,104,0
ds_read_b32 v17, v19 offset:768
MA 144,108,4
WL 8
TR 104,2,22
MV 184,112,0
TR 106,3,22
MV 184,116,4
DH 2
MV 188,112,8
MV 188,116,12
WL 4
TR 108,2,23
UL 6,12
MV 192,112,16
s_add_u32 m0, %[hm], 16384
TR 110,3,23
UH 7,12
MV 192,116,20
s_add_u32 s40, s64, 1839104
s_addc_u32 s41, s65, 0
UL 8,13
MV 196,112,24
DH 0
MV 196,116,28
AD 160,6
AD 164,7
MV 200,112,32
AD 168,8
UH 6,13
MV 200,116,36
UL 7,16
UH 8,16
MV 204,112,40
ds_read_b64 v[12:13], v18 offset:1536
MV 204,116,44
TR 112,4,22
MV 228,96,40
TR 114,5,22
MV 228,100,44
ST 24,2
MV 224,96,32
ds_read_b32 v16, v19 offset:1536
AD 172,6
MV 224,100,36
TR 116,4,23
v_add_u32 v23, 0xf000, v20
MV 220,96,24
TR 118,5,23
AD 176,7
MV 220,100,28
RV 24,23,12288
AD 180,8
MV 216,96,16
WL 8
UL 6,14
UH 7,14
MV 216,100,20
UL 8,15
AD 161,6
MV 212,96,8
s_add_u32 s42, s90, 49152
AD 165,7
AD 169,8
MV 212,100,12
s_add_u32 s42, s42, s74
s_addc_u32 s43, s75, 0
UH 6,15
MV 208,96,0
UL 7,17
UH 8,17
MV 208,100,4
WL 7
TR 96,2,24
MV 232,104,0
TR 98,3,24
MV 232,108,4
WL 2
ST 24,0
MV 236,104,8
ds_read_b64 v[14:15], v18 offset:2304
MV 236,108,12
TR 100,2,25
MV 240,104,16
TR 102,3,25
MV 240,108,20
RV 24,23,13312
MV 244,104,24
ds_read_b32 v17, v19 offset:2304
AD 173,6
MV 244,108,28
AD 177,7
AD 181,8
MV 248,104,32
UL 6,12
UH 7,12
MV 248,108,36
UL 8,13
AD 162,6
MV 252,104,40
AD 166,7
AD 170,8
MV 252,108,44
TR 104,4,24
UH 6,13
MA 188,112,40
TR 106,5,24
UL 7,16
MA 188,116,44
UH 8,16
AD 174,6
MA 184,112,32
AD 178,7
AD 182,8
MA 184,116,36
WL 2
TR 108,4,25
UL 6,14
MA 180,112,24
TR 110,5,25
UH 7,14
MA 180,116,28
UL 8,15
AD 163,6
MA 176,112,16
AD 167,7
AD 171,8
MA 176,116,20
UH 6,15
UL 7,17
MA 172,112,8
UH 8,17
AD 175,6
MA 172,116,12
AD 179,7
AD 183,8
MA 168,112,0
MA 168,116,4
WV 8
TR 112,2,26
MV 40,96,48
TR 114,3,26
MV 40,100,52
s_add_u32 s44, s84, 790528
s_addc_u32 s45, s85, 0
MV 44,96,56
LA 0,-4
MV 44,100,60
TR 116,2,27
MV 48,96,64
TR 118,3,27
MV 48,100,68
MV 52,96,72
MV 52,100,76
LA 4,-3
MV 56,96,80
MV 56,100,84
MV 60,96,88
MV 60,100,92
WL 4
TR 96,4,26
MV 84,104,88
TR 98,5,26
MV 84,108,92
LA 8,-2
MV 80,104,80
MV 80,108,84
TR 100,4,27
MV 76,104,72
TR 102,5,27
MV 76,108,76
MV 72,104,64
LA 12,-1
MV 72,108,68
MV 68,104,56
MV 68,108,60
MV 64,104,48
MV 64,108,52
WL 4
TR 104,2,28
MV 88,112,48
TR 106,3,28
MV 88,116,52
LA 16,0
MV 92,112,56
MV 92,116,60
TR 108,2,29
MV 96,112,64
TR 110,3,29
MV 96,116,68
MV 100,112,72
LA 20,1
MV 100,116,76
MV 104,112,80
MV 104,116,84
s_add_u32 s44, s44, 6144
s_addc_u32 s45, s45, 0
MV 108,112,88
MV 108,116,92
WL 4
TR 112,4,28
MA 140,96,88
TR 114,5,28
MA 140,100,92
LA 24,-4
MA 136,96,80
MA 136,100,84
TR 116,4,29
MA 132,96,72
TR 118,5,29
MA 132,100,76
MA 128,96,64
.Lk2p22:
s_cbranch_execz .Lk2r4
LA 28,-3
MA 128,100,68
MA 124,96,56
MA 124,100,60
WL 0
s_barrier
MA 120,96,48
s_add_u32 m0, %[hm], 18432
MA 120,100,52
TR 96,2,30
MV 112,104,48
TR 98,3,30
MV 112,108,52
LA 32,-2
MV 116,104,56
s_add_u32 s40, s64, 1841152
s_addc_u32 s41, s65, 0
MV 116,108,60
TR 100,2,31
MV 120,104,64
TR 102,3,31
MV 120,108,68
MV 124,104,72
LA 36,-1
MV 124,108,76
MV 128,104,80
MV 128,108,84
MV 132,104,88
MV 132,108,92
TR 104,4,30
MV 156,112,88
TR 106,5,30
MV 156,116,92
LA 40,0
MV 152,112,80
MV 152,116,84
TR 108,4,31
MV 148,112,72
TR 110,5,31
MV 148,116,76
MV 144,112,64
LA 44,1
MV 144,116,68
MV 140,112,56
MV 140,116,60
MV 136,112,48
MV 136,116,52
WL 4
TR 112,2,32
MV 160,96,48
TR 114,3,32
MV 160,100,52
DH 0
MV 164,96,56
MV 164,100,60
TR 116,2,33
MV 168,96,64
s_add_u32 m0, %[hm], 20480
TR 118,3,33
MV 168,100,68
s_add_u32 s40, s66, 1835008
s_addc_u32 s41, s67, 0
MV 172,96,72
DH 0
MV 172,100,76
MV 176,96,80
MV 176,100,84
MV 180,96,88
MV 180,100,92
WL 4
TR 96,4,32
MA 164,104,88
TR 98,5,32
MA 164,108,92
DH 2
MA 160,104,80
MA 160,108,84
TR 100,4,33
MA 156,104,72
TR 102,5,33
MA 156,108,76
s_add_u32 m0, %[hm], 24576
v_xor_b32 v1, 0x300, v1
MA 152,104,64
s_add_u32 s40, s64, 1835008
s_addc_u32 s41, s65, 0
MA 152,108,68
DH 0
MA 148,104,56
MA 148,108,60
MA 144,104,48
MA 144,108,52
WL 4
TR 104,2,34
MV 184,112,48
TR 106,3,34
MV 184,116,52
)"
R"(DH 2
MV 188,112,56
ds_read_b64 v[12:13], v18 offset:12288
MV 188,116,60
TR 108,2,35
MV 192,112,64
s_add_u32 m0, %[hm], 28672
TR 110,3,35
MV 192,116,68
s_add_u32 s40, s64, 1839104
s_addc_u32 s41, s65, 0
MV 196,112,72
DH 0
MV 196,116,76
ds_read_b32 v16, v19 offset:12288
MV 200,112,80
ds_read_b64 v[14:15], v18 offset:13056
MV 200,116,84
s_add_u32 m0, %[wm], 0
ds_read_b32 v17, v19 offset:13056
MV 204,112,88
s_add_u32 s40, s90, 196608
MV 204,116,92
WL 8
TR 112,4,34
MV 228,96,88
TR 114,5,34
MV 228,100,92
s_add_u32 s40, s40, s70
s_addc_u32 s41, s71, 0
MV 224,96,80
DX 0
MV 224,100,84
WL 4
TR 116,4,35
UL 6,12
MV 220,96,72
TR 118,5,35
UH 7,12
MV 220,100,76
UL 8,13
AR 9,144
MV 216,96,64
AR 10,148
AR 11,152
MV 216,100,68
DX 1
MV 212,96,56
AD 9,6
AD 10,7
MV 212,100,60
AD 11,8
AW 144,9
MV 208,96,48
AW 148,10
AW 152,11
MV 208,100,52
TR 96,2,0
UH 6,13
MV 232,104,48
TR 98,3,0
UL 7,16
MV 232,108,52
DX 2
MV 236,104,56
UH 8,16
MV 236,108,60
TR 100,2,1
MV 240,104,64
TR 102,3,1
MV 240,108,68
ds_read_b64 v[12:13], v18 offset:13824
MV 244,104,72
ST 24,1
MV 244,108,76
ds_read_b32 v16, v19 offset:13824
AR 9,156
MV 248,104,80
RV 24,23,14336
AR 10,160
MV 248,108,84
AR 11,164
AD 9,6
MV 252,104,88
AD 10,7
AD 11,8
MV 252,108,92
WL 7
TR 104,4,0
AW 156,9
MA 188,112,88
TR 106,5,0
AW 160,10
MA 188,116,92
AW 164,11
UL 6,14
MA 184,112,80
UH 7,14
UL 8,15
MA 184,116,84
TR 108,4,1
AR 9,145
MA 180,112,72
TR 110,5,1
AR 10,149
MA 180,116,76
WL 4
ST 24,2
MA 176,112,64
AR 11,153
AD 9,6
MA 176,116,68
AD 10,7
AD 11,8
MA 172,112,56
AW 145,9
AW 149,10
MA 172,116,60
AW 153,11
UH 6,15
MA 168,112,48
UL 7,17
UH 8,17
MA 168,116,52
WV 11
TR 112,2,2
MV 40,96,0
TR 114,3,2
MV 40,100,4
s_add_u32 s44, s84, 839680
s_addc_u32 s45, s85, 0
MV 44,96,8
LA 48,-4
MV 44,100,12
TR 116,2,3
MV 48,96,16
TR 118,3,3
MV 48,100,20
ds_read_b64 v[14:15], v18 offset:14592
MV 52,96,24
ds_read_b32 v17, v19 offset:14592
AR 9,157
MV 52,100,28
LA 52,-3
MV 56,96,32
AR 10,161
AR 11,165
MV 56,100,36
RV 24,20,24576
AD 9,6
MV 60,96,40
AD 10,7
AD 11,8
MV 60,100,44
WL 7
TR 96,4,2
AW 157,9
MV 84,104,40
s_add_u32 s42, s91, 0
TR 98,5,2
AW 161,10
MV 84,108,44
LA 56,-2
MV 80,104,32
s_add_u32 s42, s42, s74
s_addc_u32 s43, s75, 0
AW 165,11
MV 80,108,36
TR 100,4,3
UL 6,12
MV 76,104,24
TR 102,5,3
UH 7,12
MV 76,108,28
UL 8,13
AR 9,146
MV 72,104,16
LA 60,-1
MV 72,108,20
AR 10,150
AR 11,154
MV 68,104,8
AD 9,6
AD 10,7
MV 68,108,12
AD 11,8
AW 146,9
MV 64,104,0
AW 150,10
AW 154,11
MV 64,108,4
WL 4
TR 104,2,4
UH 6,13
MV 88,112,0
TR 106,3,4
UL 7,16
MV 88,116,4
LA 64,0
MV 92,112,8
UH 8,16
AR 9,158
MV 92,116,12
TR 108,2,5
AR 10,162
MV 96,112,16
TR 110,3,5
AR 11,166
MV 96,116,20
AD 9,6
AD 10,7
MV 100,112,24
LA 68,1
MV 100,116,28
AD 11,8
AW 158,9
MV 104,112,32
AW 162,10
AW 166,11
MV 104,116,36
s_add_u32 s44, s44, 6144
s_addc_u32 s45, s45, 0
UL 6,14
MV 108,112,40
UH 7,14
UL 8,15
MV 108,116,44
WL 4
TR 112,4,4
AR 9,147
MA 140,96,40
TR 114,5,4
AR 10,151
MA 140,100,44
LA 72,-4
MA 136,96,32
AR 11,155
AD 9,6
MA 136,100,36
TR 116,4,5
AD 10,7
MA 132,96,24
TR 118,5,5
AD 11,8
MA 132,100,28
AW 147,9
AW 151,10
MA 128,96,16
LA 76,-3
MA 128,100,20
AW 155,11
UH 6,15
MA 124,96,8
UL 7,17
UH 8,17
MA 124,100,12
WL 0
s_barrier
AR 9,159
AR 10,163
MA 120,96,0
s_add_u32 m0, %[hm], 30720
AR 11,167
AD 9,6
MA 120,100,4
TR 96,2,6
AD 10,7
MV 112,104,0
TR 98,3,6
AD 11,8
MV 112,108,4
LA 80,-2
MV 116,104,8
s_add_u32 s40, s64, 1841152
s_addc_u32 s41, s65, 0
AW 159,9
MV 116,108,12
TR 100,2,7
AW 163,10
MV 120,104,16
TR 102,3,7
AW 167,11
MV 120,108,20
MV 124,104,24
LA 84,-1
MV 124,108,28
MV 128,104,32
MV 128,108,36
MV 132,104,40
MV 132,108,44
TR 104,4,6
MV 156,112,40
TR 106,5,6
MV 156,116,44
LA 88,0
MV 152,112,32
MV 152,116,36
TR 108,4,7
MV 148,112,24
TR 110,5,7
MV 148,116,28
MV 144,112,16
LA 92,1
MV 144,116,20
MV 140,112,8
MV 140,116,12
MV 136,112,0
MV 136,116,4
WL 4
TR 112,2,8
MV 160,96,0
TR 114,3,8
MV 160,100,4
DH 0
MV 164,96,8
MV 164,100,12
TR 116,2,9
MV 168,96,16
s_add_u32 m0, %[hm], 32768
TR 118,3,9
MV 168,100,20
s_add_u32 s40, s66, 1835008
s_addc_u32 s41, s67, 0
MV 172,96,24
DH 0
MV 172,100,28
MV 176,96,32
MV 176,100,36
MV 180,96,40
MV 180,100,44
WL 4
TR 96,4,8
MA 164,104,40
TR 98,5,8
MA 164,108,44
DH 2
MA 160,104,32
MA 160,108,36
TR 100,4,9
MA 156,104,24
TR 102,5,9
MA 156,108,28
s_add_u32 m0, %[hm], 0
v_xor_b32 v1, 0x100, v1
MA 152,104,16
s_add_u32 s40, s64, 1835008
s_addc_u32 s41, s65, 0
MA 152,108,20
DH 0
MA 148,104,8
MA 148,108,12
MA 144,104,0
MA 144,108,4
WL 4
TR 104,2,10
MV 184,112,0
TR 106,3,10
MV 184,116,4
DH 2
MV 188,112,8
MV 188,116,12
TR 108,2,11
MV 192,112,16
s_add_u32 m0, %[hm], 4096
TR 110,3,11
MV 192,116,20
s_add_u32 s40, s64, 1839104
s_addc_u32 s41, s65, 0
MV 196,112,24
DH 0
MV 196,116,28
MV 200,112,32
MV 200,116,36
s_add_u32 m0, %[wm], 12288
MV 204,112,40
s_add_u32 s40, s90, 245760
MV 204,116,44
WL 4
TR 112,4,10
MV 228,96,40
TR 114,5,10
MV 228,100,44
s_add_u32 s40, s40, s70
s_addc_u32 s41, s71, 0
MV 224,96,32
DX 0
MV 224,100,36
TR 116,4,11
MV 220,96,24
TR 118,5,11
MV 220,100,28
MV 216,96,16
MV 216,100,20
DX 1
MV 212,96,8
MV 212,100,12
MV 208,96,0
MV 208,100,4
WL 4
TR 96,2,12
)"
R"(MV 232,104,0
TR 98,3,12
MV 232,108,4
DX 2
MV 236,104,8
MV 236,108,12
TR 100,2,13
MV 240,104,16
TR 102,3,13
MV 240,108,20
MV 244,104,24
ST 24,0
MV 244,108,28
MV 248,104,32
RV 24,20,25600
MV 248,108,36
MV 252,104,40
MV 252,108,44
WL 5
TR 104,4,12
MA 188,112,40
TR 106,5,12
MA 188,116,44
MA 184,112,32
MA 184,116,36
TR 108,4,13
MA 180,112,24
TR 110,5,13
MA 180,116,28
WL 4
ST 24,1
MA 176,112,16
MA 176,116,20
MA 172,112,8
MA 172,116,12
MA 168,112,0
MA 168,116,4
WV 11
TR 112,2,14
MV 40,96,48
TR 114,3,14
MV 40,100,52
s_add_u32 s44, s84, 888832
s_addc_u32 s45, s85, 0
MV 44,96,56
LA 0,-4
MV 44,100,60
TR 116,2,15
MV 48,96,64
TR 118,3,15
MV 48,100,68
MV 52,96,72
MV 52,100,76
LA 4,-3
MV 56,96,80
MV 56,100,84
MV 60,96,88
MV 60,100,92
WL 4
TR 96,4,14
MV 84,104,88
.Lk2p23:
s_cbranch_execz .Lk2r4
TR 98,5,14
MV 84,108,92
LA 8,-2
MV 80,104,80
MV 80,108,84
TR 100,4,15
MV 76,104,72
TR 102,5,15
MV 76,108,76
MV 72,104,64
LA 12,-1
MV 72,108,68
MV 68,104,56
MV 68,108,60
MV 64,104,48
MV 64,108,52
WL 4
TR 104,2,16
MV 88,112,48
TR 106,3,16
MV 88,116,52
LA 16,0
MV 92,112,56
MV 92,116,60
TR 108,2,17
MV 96,112,64
TR 110,3,17
MV 96,116,68
MV 100,112,72
LA 20,1
MV 100,116,76
MV 104,112,80
MV 104,116,84
s_add_u32 s44, s44, 6144
s_addc_u32 s45, s45, 0
MV 108,112,88
MV 108,116,92
WL 4
TR 112,4,16
MA 140,96,88
TR 114,5,16
MA 140,100,92
LA 24,-4
MA 136,96,80
MA 136,100,84
TR 116,4,17
MA 132,96,72
TR 118,5,17
MA 132,100,76
MA 128,96,64
LA 28,-3
MA 128,100,68
MA 124,96,56
MA 124,100,60
WL 0
s_barrier
RV 24,20,26624
MA 120,96,48
s_add_u32 m0, %[hm], 6144
MA 120,100,52
TR 96,2,18
MV 112,104,48
TR 98,3,18
MV 112,108,52
LA 32,-2
MV 116,104,56
s_add_u32 s40, s64, 1841152
s_addc_u32 s41, s65, 0
MV 116,108,60
TR 100,2,19
MV 120,104,64
TR 102,3,19
MV 120,108,68
MV 124,104,72
LA 36,-1
MV 124,108,76
MV 128,104,80
MV 128,108,84
MV 132,104,88
MV 132,108,92
TR 104,4,18
MV 156,112,88
TR 106,5,18
MV 156,116,92
LA 40,0
MV 152,112,80
MV 152,116,84
TR 108,4,19
MV 148,112,72
TR 110,5,19
MV 148,116,76
MV 144,112,64
LA 44,1
MV 144,116,68
MV 140,112,56
MV 140,116,60
MV 136,112,48
MV 136,116,52
WL 4
TR 112,2,20
MV 160,96,48
TR 114,3,20
MV 160,100,52
DH 0
MV 164,96,56
MV 164,100,60
TR 116,2,21
MV 168,96,64
s_add_u32 m0, %[hm], 8192
TR 118,3,21
MV 168,100,68
s_add_u32 s40, s66, 1835008
s_addc_u32 s41, s67, 0
MV 172,96,72
DH 0
MV 172,100,76
MV 176,96,80
MV 176,100,84
MV 180,96,88
MV 180,100,92
WL 4
TR 96,4,20
MA 164,104,88
TR 98,5,20
MA 164,108,92
DH 2
MA 160,104,80
MA 160,108,84
TR 100,4,21
MA 156,104,72
PA 20480
TR 102,5,21
MA 156,108,76
MA 152,104,64
DF
MA 152,108,68
s_add_u32 m0, %[hm], 12288
v_xor_b32 v1, 0x700, v1
MA 148,104,56
s_add_u32 s40, s64, 1835008
s_addc_u32 s41, s65, 0
MA 148,108,60
MA 144,104,48
MA 144,108,52
WL 4
TR 104,2,22
MV 184,112,48
TR 106,3,22
MV 184,116,52
DH 0
MV 188,112,56
MV 188,116,60
TR 108,2,23
MV 192,112,64
TR 110,3,23
MV 192,116,68
MV 196,112,72
DH 2
MV 196,116,76
MV 200,112,80
MV 200,116,84
s_add_u32 m0, %[hm], 16384
MV 204,112,88
s_add_u32 s40, s64, 1839104
s_addc_u32 s41, s65, 0
MV 204,116,92
WL 4
TR 112,4,22
MV 228,96,88
TR 114,5,22
MV 228,100,92
DH 0
MV 224,96,80
ds_read_b64 v[12:13], v18 offset:0
MV 224,100,84
TR 116,4,23
MV 220,96,72
TR 118,5,23
MV 220,100,76
ds_read_b32 v16, v19 offset:0
MV 216,96,64
ST 24,2
MV 216,100,68
ds_read_b64 v[14:15], v18 offset:768
MV 212,96,56
ds_read_b32 v17, v19 offset:768
MV 212,100,60
MV 208,96,48
MV 208,100,52
WL 8
TR 96,2,24
MV 232,104,48
TR 98,3,24
MV 232,108,52
WL 4
UL 6,12
UH 7,12
MV 236,104,56
UL 8,13
AD 184,6
MV 236,108,60
TR 100,2,25
AD 188,7
MV 240,104,64
TR 102,3,25
AD 192,8
MV 240,108,68
MV 244,104,72
UH 6,13
UL 7,16
MV 244,108,76
UH 8,16
MV 248,104,80
ds_read_b64 v[12:13], v18 offset:1536
MV 248,108,84
ds_read_b32 v16, v19 offset:1536
AD 196,6
MV 252,104,88
AD 200,7
AD 204,8
MV 252,108,92
WL 6
TR 104,4,24
UL 6,14
MA 188,112,88
TR 106,5,24
UH 7,14
MA 188,116,92
UL 8,15
AD 185,6
MA 184,112,80
AD 189,7
AD 193,8
MA 184,116,84
TR 108,4,25
UH 6,15
MA 180,112,72
TR 110,5,25
UL 7,17
MA 180,116,76
RV 24,20,36864
UH 8,17
MA 176,112,64
ds_read_b64 v[14:15], v18 offset:2304
MA 176,116,68
WL 6
ds_read_b32 v17, v19 offset:2304
AD 197,6
MA 172,112,56
s_add_u32 s42, s91, 49152
AD 201,7
AD 205,8
MA 172,116,60
s_add_u32 s42, s42, s74
s_addc_u32 s43, s75, 0
UL 6,12
MA 168,112,48
UH 7,12
UL 8,13
MA 168,116,52
WV 8
TR 112,2,26
AD 186,6
MV 40,96,0
TR 114,3,26
AD 190,7
MV 40,100,4
s_add_u32 s44, s84, 937984
s_addc_u32 s45, s85, 0
AD 194,8
MV 44,96,8
LA 48,-4
MV 44,100,12
TR 116,2,27
UH 6,13
MV 48,96,16
TR 118,3,27
UL 7,16
MV 48,100,20
UH 8,16
AD 198,6
MV 52,96,24
AD 202,7
AD 206,8
MV 52,100,28
LA 52,-3
MV 56,96,32
WL 4
UL 6,14
UH 7,14
MV 56,100,36
UL 8,15
AD 187,6
MV 60,96,40
AD 191,7
AD 195,8
MV 60,100,44
TR 96,4,26
UH 6,15
MV 84,104,40
TR 98,5,26
UL 7,17
MV 84,108,44
LA 56,-2
MV 80,104,32
UH 8,17
AD 199,6
MV 80,108,36
TR 100,4,27
AD 203,7
MV 76,104,24
TR 102,5,27
AD 207,8
MV 76,108,28
MV 72,104,16
LA 60,-1
MV 72,108,20
MV 68,104,8
MV 68,108,12
MV 64,104,0
MV 64,108,4
WL 4
TR 104,2,28
MV 88,112,0
TR 106,3,28
MV 88,116,4
LA 64,0
MV 92,112,8
MV 92,116,12
TR 108,2,29
MV 96,112,16
TR 110,3,29
MV 96,116,20
MV 100,112,24
LA 68,1
MV 100,116,28
MV 104,112,32
)"
R"(MV 104,116,36
s_add_u32 s44, s44, 6144
s_addc_u32 s45, s45, 0
MV 108,112,40
MV 108,116,44
WL 4
TR 112,4,28
MA 140,96,40
TR 114,5,28
MA 140,100,44
LA 72,-4
MA 136,96,32
MA 136,100,36
TR 116,4,29
MA 132,96,24
TR 118,5,29
MA 132,100,28
MA 128,96,16
LA 76,-3
MA 128,100,20
MA 124,96,8
MA 124,100,12
WL 0
s_barrier
MA 120,96,0
s_add_u32 m0, %[hm], 18432
MA 120,100,4
TR 96,2,30
MV 112,104,0
TR 98,3,30
MV 112,108,4
LA 80,-2
MV 116,104,8
s_add_u32 s40, s64, 1841152
s_addc_u32 s41, s65, 0
MV 116,108,12
TR 100,2,31
MV 120,104,16
TR 102,3,31
MV 120,108,20
MV 124,104,24
LA 84,-1
MV 124,108,28
MV 128,104,32
MV 128,108,36
MV 132,104,40
MV 132,108,44
TR 104,4,30
MV 156,112,40
TR 106,5,30
MV 156,116,44
LA 88,0
MV 152,112,32
MV 152,116,36
TR 108,4,31
MV 148,112,24
TR 110,5,31
MV 148,116,28
MV 144,112,16
LA 92,1
MV 144,116,20
MV 140,112,8
MV 140,116,12
MV 136,112,0
MV 136,116,4
WL 4
TR 112,2,32
MV 160,96,0
TR 114,3,32
MV 160,100,4
DH 0
MV 164,96,8
MV 164,100,12
TR 116,2,33
MV 168,96,16
s_add_u32 m0, %[hm], 20480
TR 118,3,33
MV 168,100,20
s_add_u32 s40, s66, 1835008
s_addc_u32 s41, s67, 0
MV 172,96,24
DH 0
MV 172,100,28
MV 176,96,32
MV 176,100,36
MV 180,96,40
MV 180,100,44
WL 4
TR 96,4,32
MA 164,104,40
TR 98,5,32
MA 164,108,44
DH 2
MA 160,104,32
MA 160,108,36
TR 100,4,33
MA 156,104,24
TR 102,5,33
MA 156,108,28
s_add_u32 m0, %[hm], 24576
v_xor_b32 v1, 0x100, v1
MA 152,104,16
s_add_u32 s40, s64, 1835008
s_addc_u32 s41, s65, 0
MA 152,108,20
DH 0
MA 148,104,8
MA 148,108,12
MA 144,104,0
MA 144,108,4
WL 4
TR 104,2,34
MV 184,112,0
TR 106,3,34
MV 184,116,4
DH 2
MV 188,112,8
MV 188,116,12
TR 108,2,35
MV 192,112,16
s_add_u32 m0, %[hm], 28672
TR 110,3,35
MV 192,116,20
s_add_u32 s40, s64, 1839104
s_addc_u32 s41, s65, 0
MV 196,112,24
DH 0
MV 196,116,28
MV 200,112,32
MV 200,116,36
s_add_u32 m0, %[wm], 0
MV 204,112,40
s_add_u32 s40, s91, 196608
MV 204,116,44
WL 4
TR 112,4,34
MV 228,96,40
TR 114,5,34
MV 228,100,44
s_add_u32 s40, s40, s70
s_addc_u32 s41, s71, 0
MV 224,96,32
DX 0
MV 224,100,36
TR 116,4,35
MV 220,96,24
TR 118,5,35
MV 220,100,28
MV 216,96,16
MV 216,100,20
DX 1
MV 212,96,8
MV 212,100,12
MV 208,96,0
MV 208,100,4
WL 4
TR 96,2,0
MV 232,104,0
TR 98,3,0
MV 232,108,4
DX 2
MV 236,104,8
ds_read_b64 v[12:13], v18 offset:12288
MV 236,108,12
TR 100,2,1
MV 240,104,16
TR 102,3,1
MV 240,108,20
ds_read_b32 v16, v19 offset:12288
MV 244,104,24
ST 24,0
MV 244,108,28
ds_read_b64 v[14:15], v18 offset:13056
MV 248,104,32
RV 24,20,37888
MV 248,108,36
ds_read_b32 v17, v19 offset:13056
MV 252,104,40
MV 252,108,44
WL 9
TR 104,4,0
MA 188,112,40
TR 106,5,0
MA 188,116,44
MA 184,112,32
WL 5
UL 6,12
UH 7,12
MA 184,116,36
TR 108,4,1
UL 8,13
MA 180,112,24
TR 110,5,1
AD 208,6
MA 180,116,28
WL 5
ST 24,1
MA 176,112,16
AD 212,7
AD 216,8
MA 176,116,20
UH 6,13
UL 7,16
MA 172,112,8
UH 8,16
MA 172,116,12
ds_read_b64 v[12:13], v18 offset:13824
MA 168,112,0
ds_read_b32 v16, v19 offset:13824
AD 220,6
MA 168,116,4
WV 11
TR 112,2,2
AD 224,7
MV 40,96,48
WL 7
TR 114,3,2
AD 228,8
MV 40,100,52
s_add_u32 s44, s84, 987136
s_addc_u32 s45, s85, 0
UL 6,14
MV 44,96,56
.Lk2p24:
s_cbranch_execz .Lk2r4
LA 0,-4
MV 44,100,60
TR 116,2,3
UH 7,14
MV 48,96,64
TR 118,3,3
UL 8,15
MV 48,100,68
AD 209,6
AD 213,7
MV 52,96,72
AD 217,8
UH 6,15
MV 52,100,76
LA 4,-3
MV 56,96,80
UL 7,17
UH 8,17
MV 56,100,84
ds_read_b64 v[14:15], v18 offset:14592
MV 60,96,88
ds_read_b32 v17, v19 offset:14592
AD 221,6
MV 60,100,92
WL 6
TR 96,4,2
AD 225,7
MV 84,104,88
TR 98,5,2
AD 229,8
MV 84,108,92
LA 8,-2
MV 80,104,80
UL 6,12
UH 7,12
MV 80,108,84
TR 100,4,3
UL 8,13
MV 76,104,72
TR 102,5,3
AD 210,6
MV 76,108,76
AD 214,7
AD 218,8
MV 72,104,64
LA 12,-1
MV 72,108,68
UH 6,13
UL 7,16
MV 68,104,56
UH 8,16
AD 222,6
MV 68,108,60
AD 226,7
AD 230,8
MV 64,104,48
WL 4
UL 6,14
UH 7,14
MV 64,108,52
TR 104,2,4
UL 8,15
MV 88,112,48
TR 106,3,4
AD 211,6
MV 88,116,52
LA 16,0
MV 92,112,56
AD 215,7
AD 219,8
MV 92,116,60
TR 108,2,5
UH 6,15
MV 96,112,64
TR 110,3,5
UL 7,17
MV 96,116,68
RV 24,20,38912
UH 8,17
MV 100,112,72
LA 20,1
MV 100,116,76
AD 223,6
AD 227,7
MV 104,112,80
AD 231,8
MV 104,116,84
s_add_u32 s44, s44, 6144
s_addc_u32 s45, s45, 0
MV 108,112,88
MV 108,116,92
WL 5
TR 112,4,4
MA 140,96,88
TR 114,5,4
MA 140,100,92
LA 24,-4
MA 136,96,80
MA 136,100,84
TR 116,4,5
MA 132,96,72
TR 118,5,5
MA 132,100,76
MA 128,96,64
LA 28,-3
MA 128,100,68
MA 124,96,56
MA 124,100,60
WL 0
s_barrier
MA 120,96,48
s_add_u32 m0, %[hm], 30720
MA 120,100,52
TR 96,2,6
MV 112,104,48
TR 98,3,6
MV 112,108,52
LA 32,-2
MV 116,104,56
s_add_u32 s40, s64, 1841152
s_addc_u32 s41, s65, 0
MV 116,108,60
TR 100,2,7
MV 120,104,64
TR 102,3,7
MV 120,108,68
MV 124,104,72
LA 36,-1
MV 124,108,76
MV 128,104,80
MV 128,108,84
MV 132,104,88
MV 132,108,92
TR 104,4,6
MV 156,112,88
TR 106,5,6
MV 156,116,92
LA 40,0
MV 152,112,80
MV 152,116,84
TR 108,4,7
MV 148,112,72
TR 110,5,7
MV 148,116,76
MV 144,112,64
LA 44,1
MV 144,116,68
MV 140,112,56
MV 140,116,60
MV 136,112,48
MV 136,116,52
WL 4
TR 112,2,8
MV 160,96,48
TR 114,3,8
MV 160,100,52
DH 0
MV 164,96,56
MV 164,100,60
TR 116,2,9
MV 168,96,64
s_add_u32 m0, %[hm], 32768
TR 118,3,9
MV 168,100,68
s_add_u32 s40, s66, 1835008
s_addc_u32 s41, s67, 0
MV 172,96,72
DH 0
MV 172,100,76
MV 176,96,80
MV 176,100,84
MV 180,96,88
MV 180,100,92
WL 4
TR 96,4,8
)"
R"(MA 164,104,88
TR 98,5,8
MA 164,108,92
DH 2
MA 160,104,80
MA 160,108,84
TR 100,4,9
MA 156,104,72
TR 102,5,9
MA 156,108,76
s_add_u32 m0, %[hm], 0
v_xor_b32 v1, 0x300, v1
MA 152,104,64
s_add_u32 s40, s64, 1835008
s_addc_u32 s41, s65, 0
MA 152,108,68
DH 0
MA 148,104,56
MA 148,108,60
MA 144,104,48
MA 144,108,52
WL 4
TR 104,2,10
MV 184,112,48
TR 106,3,10
MV 184,116,52
DH 2
MV 188,112,56
MV 188,116,60
TR 108,2,11
MV 192,112,64
s_add_u32 m0, %[hm], 4096
TR 110,3,11
MV 192,116,68
s_add_u32 s40, s64, 1839104
s_addc_u32 s41, s65, 0
MV 196,112,72
DH 0
MV 196,116,76
MV 200,112,80
MV 200,116,84
s_add_u32 m0, %[wm], 12288
MV 204,112,88
s_add_u32 s40, s91, 245760
MV 204,116,92
WL 4
TR 112,4,10
MV 228,96,88
TR 114,5,10
MV 228,100,92
s_add_u32 s40, s40, s70
s_addc_u32 s41, s71, 0
MV 224,96,80
DX 0
MV 224,100,84
TR 116,4,11
MV 220,96,72
TR 118,5,11
MV 220,100,76
MV 216,96,64
MV 216,100,68
DX 1
MV 212,96,56
MV 212,100,60
MV 208,96,48
MV 208,100,52
WL 4
TR 96,2,12
MV 232,104,48
TR 98,3,12
MV 232,108,52
DX 2
MV 236,104,56
MV 236,108,60
TR 100,2,13
MV 240,104,64
TR 102,3,13
MV 240,108,68
MV 244,104,72
ST 24,2
MV 244,108,76
MV 248,104,80
MV 248,108,84
MV 252,104,88
MV 252,108,92
WL 4
TR 104,4,12
MA 188,112,88
TR 106,5,12
MA 188,116,92
MA 184,112,80
MA 184,116,84
TR 108,4,13
MA 180,112,72
TR 110,5,13
MA 180,116,76
MA 176,112,64
MA 176,116,68
MA 172,112,56
MA 172,116,60
MA 168,112,48
WV 10
MA 168,116,52
WL 4
TR 112,2,14
MV 40,96,0
TR 114,3,14
MV 40,100,4
s_add_u32 s44, s84, 1036288
s_addc_u32 s45, s85, 0
MV 44,96,8
LA 48,-4
MV 44,100,12
TR 116,2,15
MV 48,96,16
TR 118,3,15
MV 48,100,20
MV 52,96,24
MV 52,100,28
LA 52,-3
MV 56,96,32
MV 56,100,36
MV 60,96,40
MV 60,100,44
WL 4
TR 96,4,14
MV 84,104,40
TR 98,5,14
MV 84,108,44
LA 56,-2
MV 80,104,32
MV 80,108,36
TR 100,4,15
MV 76,104,24
TR 102,5,15
MV 76,108,28
MV 72,104,16
LA 60,-1
MV 72,108,20
MV 68,104,8
MV 68,108,12
MV 64,104,0
MV 64,108,4
WL 4
TR 104,2,16
MV 88,112,0
TR 106,3,16
MV 88,116,4
LA 64,0
MV 92,112,8
MV 92,116,12
TR 108,2,17
MV 96,112,16
TR 110,3,17
MV 96,116,20
MV 100,112,24
LA 68,1
MV 100,116,28
MV 104,112,32
MV 104,116,36
s_add_u32 s44, s44, 6144
s_addc_u32 s45, s45, 0
MV 108,112,40
MV 108,116,44
WL 4
TR 112,4,16
MA 140,96,40
TR 114,5,16
MA 140,100,44
LA 72,-4
MA 136,96,32
s_mov_b64 exec, 0
s_cmp_eq_u32 %[w], 0
s_cbranch_scc1 .Lk2p25
s_cmp_eq_u32 %[w], 1
s_cbranch_scc1 .Lk2p26
s_cmp_eq_u32 %[w], 2
s_cbranch_scc1 .Lk2p27
s_branch .Lk2p28
.Lk2r5:
s_mov_b64 exec, -1
MA 136,100,36
TR 116,4,17
MA 132,96,24
TR 118,5,17
MA 132,100,28
MA 128,96,16
LA 76,-3
MA 128,100,20
MA 124,96,8
MA 124,100,12
WL 0
s_barrier
ds_read_b64 v[12:13], v18 offset:0
MA 120,96,0
s_add_u32 m0, %[hm], 6144
ds_read_b32 v16, v19 offset:0
MA 120,100,4
TR 96,2,18
MV 112,104,0
TR 98,3,18
MV 112,108,4
LA 80,-2
MV 116,104,8
s_add_u32 s40, s64, 1841152
s_addc_u32 s41, s65, 0
MV 116,108,12
TR 100,2,19
MV 120,104,16
TR 102,3,19
MV 120,108,20
ds_read_b64 v[14:15], v18 offset:768
MV 124,104,24
LA 84,-1
MV 124,108,28
ds_read_b32 v17, v19 offset:768
MV 128,104,32
MV 128,108,36
MV 132,104,40
MV 132,108,44
TR 104,4,18
MV 156,112,40
WL 7
TR 106,5,18
UL 6,12
MV 156,116,44
LA 88,0
MV 152,112,32
UH 7,12
UL 8,13
MV 152,116,36
TR 108,4,19
AD 232,6
MV 148,112,24
TR 110,5,19
AD 236,7
MV 148,116,28
AD 240,8
UH 6,13
MV 144,112,16
LA 92,1
MV 144,116,20
UL 7,16
UH 8,16
MV 140,112,8
ds_read_b64 v[12:13], v18 offset:1536
MV 140,116,12
ds_read_b32 v16, v19 offset:1536
AD 244,6
MV 136,112,0
AD 248,7
AD 252,8
MV 136,116,4
WL 6
TR 112,2,20
UL 6,14
MV 160,96,0
TR 114,3,20
UH 7,14
MV 160,100,4
DH 0
MV 164,96,8
UL 8,15
AD 233,6
MV 164,100,12
TR 116,2,21
AD 237,7
MV 168,96,16
s_add_u32 m0, %[hm], 8192
TR 118,3,21
AD 241,8
MV 168,100,20
s_add_u32 s40, s66, 1835008
s_addc_u32 s41, s67, 0
UH 6,15
MV 172,96,24
DH 0
MV 172,100,28
UL 7,17
UH 8,17
MV 176,96,32
ds_read_b64 v[14:15], v18 offset:2304
MV 176,100,36
ds_read_b32 v17, v19 offset:2304
AD 245,6
MV 180,96,40
AD 249,7
AD 253,8
MV 180,100,44
WL 6
TR 96,4,20
UL 6,12
MA 164,104,40
TR 98,5,20
UH 7,12
MA 164,108,44
DH 2
MA 160,104,32
UL 8,13
AD 234,6
MA 160,108,36
TR 100,4,21
AD 238,7
MA 156,104,24
TR 102,5,21
AD 242,8
MA 156,108,28
s_add_u32 m0, %[hm], 12288
v_xor_b32 v1, 0x100, v1
UH 6,13
MA 152,104,16
s_add_u32 s40, s64, 1835008
s_addc_u32 s41, s65, 0
UL 7,16
MA 152,108,20
DH 0
MA 148,104,8
UH 8,16
AD 246,6
MA 148,108,12
AD 250,7
AD 254,8
MA 144,104,0
WL 4
UL 6,14
UH 7,14
MA 144,108,4
TR 104,2,22
UL 8,15
MV 184,112,0
TR 106,3,22
AD 235,6
MV 184,116,4
DH 2
MV 188,112,8
AD 239,7
AD 243,8
MV 188,116,12
TR 108,2,23
UH 6,15
MV 192,112,16
s_add_u32 m0, %[hm], 16384
TR 110,3,23
UL 7,17
MV 192,116,20
s_add_u32 s40, s64, 1839104
s_addc_u32 s41, s65, 0
UH 8,17
MV 196,112,24
DH 0
MV 196,116,28
AD 247,6
AD 251,7
MV 200,112,32
AD 255,8
MV 200,116,36
MV 204,112,40
MV 204,116,44
WL 4
TR 112,4,22
MV 228,96,40
TR 114,5,22
MV 228,100,44
MV 224,96,32
MV 224,100,36
TR 116,4,23
MV 220,96,24
TR 118,5,23
MV 220,100,28
MV 216,96,16
MV 216,100,20
MV 212,96,8
MV 212,100,12
MV 208,96,0
MV 208,100,4
WL 4
TR 96,2,24
MV 232,104,0
TR 98,3,24
MV 232,108,4
MV 236,104,8
MV 236,108,12
TR 100,2,25
MV 240,104,16
TR 102,3,25
MV 240,108,20
MV 244,104,24
MV 244,108,28
MV 248,104,32
MV 248,108,36
MV 252,104,40
MV 252,108,44
WL 4
TR 104,4,24
MA 188,112,40
TR 106,5,24
MA 188,116,44
MA 184,112,32
MA 184,116,36
TR 108,4,25
)"
R"(MA 180,112,24
TR 110,5,25
MA 180,116,28
.Lk2p25:
s_cbranch_execz .Lk2r5
MA 176,112,16
MA 176,116,20
MA 172,112,8
MA 172,116,12
MA 168,112,0
WV 6
MA 168,116,4
WL 4
TR 112,2,26
MV 40,96,48
TR 114,3,26
MV 40,100,52
s_add_u32 s44, s84, 1085440
s_addc_u32 s45, s85, 0
MV 44,96,56
LA 0,-4
MV 44,100,60
TR 116,2,27
MV 48,96,64
TR 118,3,27
MV 48,100,68
MV 52,96,72
MV 52,100,76
LA 4,-3
MV 56,96,80
MV 56,100,84
MV 60,96,88
MV 60,100,92
WL 4
TR 96,4,26
MV 84,104,88
TR 98,5,26
MV 84,108,92
LA 8,-2
MV 80,104,80
MV 80,108,84
TR 100,4,27
MV 76,104,72
TR 102,5,27
MV 76,108,76
MV 72,104,64
LA 12,-1
MV 72,108,68
MV 68,104,56
MV 68,108,60
MV 64,104,48
MV 64,108,52
WL 4
TR 104,2,28
MV 88,112,48
TR 106,3,28
MV 88,116,52
LA 16,0
MV 92,112,56
MV 92,116,60
TR 108,2,29
MV 96,112,64
TR 110,3,29
MV 96,116,68
MV 100,112,72
LA 20,1
MV 100,116,76
MV 104,112,80
MV 104,116,84
s_add_u32 s44, s44, 6144
s_addc_u32 s45, s45, 0
MV 108,112,88
MV 108,116,92
WL 4
TR 112,4,28
MA 140,96,88
TR 114,5,28
MA 140,100,92
LA 24,-4
MA 136,96,80
MA 136,100,84
TR 116,4,29
MA 132,96,72
TR 118,5,29
MA 132,100,76
MA 128,96,64
LA 28,-3
MA 128,100,68
MA 124,96,56
MA 124,100,60
WL 0
s_barrier
ds_read_b64 v[12:13], v18 offset:12288
ds_read_b32 v16, v19 offset:12288
ds_read_b64 v[14:15], v18 offset:13056
MA 120,96,48
s_add_u32 m0, %[hm], 18432
ds_read_b32 v17, v19 offset:13056
MA 120,100,52
TR 96,2,30
MV 112,104,48
TR 98,3,30
MV 112,108,52
LA 32,-2
MV 116,104,56
s_add_u32 s40, s64, 1841152
s_addc_u32 s41, s65, 0
MV 116,108,60
WL 4
TR 100,2,31
UL 6,12
UH 7,12
MV 120,104,64
TR 102,3,31
UL 8,13
AR 9,168
MV 120,108,68
AR 10,172
AR 11,176
AD 9,6
MV 124,104,72
LA 36,-1
AD 10,7
MV 124,108,76
AD 11,8
AW 168,9
AW 172,10
MV 128,104,80
AW 176,11
UH 6,13
UL 7,16
MV 128,108,84
UH 8,16
ds_read_b64 v[12:13], v18 offset:13824
ds_read_b32 v16, v19 offset:13824
MV 132,104,88
AR 9,180
AR 10,184
AR 11,188
MV 132,108,92
TR 104,4,30
AD 9,6
AD 10,7
MV 156,112,88
TR 106,5,30
AD 11,8
AW 180,9
MV 156,116,92
LA 40,0
AW 184,10
MV 152,112,80
WL 4
AW 188,11
UL 6,14
UH 7,14
MV 152,116,84
TR 108,4,31
UL 8,15
AR 9,169
MV 148,112,72
TR 110,5,31
AR 10,173
AR 11,177
MV 148,116,76
AD 9,6
AD 10,7
AD 11,8
MV 144,112,64
LA 44,1
AW 169,9
MV 144,116,68
AW 173,10
AW 177,11
UH 6,15
MV 140,112,56
UL 7,17
UH 8,17
ds_read_b64 v[14:15], v18 offset:14592
MV 140,116,60
ds_read_b32 v17, v19 offset:14592
AR 9,181
AR 10,185
MV 136,112,48
AR 11,189
AD 9,6
AD 10,7
MV 136,116,52
TR 112,2,32
AD 11,8
AW 181,9
MV 160,96,48
TR 114,3,32
AW 185,10
AW 189,11
MV 160,100,52
WL 5
DH 0
UL 6,12
MV 164,96,56
UH 7,12
UL 8,13
AR 9,170
MV 164,100,60
TR 116,2,33
AR 10,174
AR 11,178
MV 168,96,64
s_add_u32 m0, %[hm], 20480
TR 118,3,33
AD 9,6
AD 10,7
MV 168,100,68
s_add_u32 s40, s66, 1835008
s_addc_u32 s41, s67, 0
AD 11,8
AW 170,9
MV 172,96,72
DH 0
AW 174,10
MV 172,100,76
AW 178,11
UH 6,13
UL 7,16
MV 176,96,80
UH 8,16
AR 9,182
AR 10,186
MV 176,100,84
AR 11,190
AD 9,6
AD 10,7
MV 180,96,88
AD 11,8
AW 182,9
AW 186,10
MV 180,100,92
WL 2
TR 96,4,32
AW 190,11
UL 6,14
MA 164,104,88
TR 98,5,32
UH 7,14
UL 8,15
MA 164,108,92
DH 2
AR 9,171
MA 160,104,80
AR 10,175
AR 11,179
AD 9,6
MA 160,108,84
TR 100,4,33
AD 10,7
AD 11,8
MA 156,104,72
PA 20480
TR 102,5,33
AW 171,9
AW 175,10
MA 156,108,76
AW 179,11
UH 6,15
UL 7,17
MA 152,104,64
DF
UH 8,17
MA 152,108,68
s_bitcmp1_b32 s48, 2
s_cselect_b64 s[64:65], s[58:59], s[56:57]
AR 9,183
AR 10,187
MA 148,104,56
s_bitcmp1_b32 s48, 18
s_cselect_b32 s40, 16384, 0
AR 11,191
AD 9,6
MA 148,108,60
s_add_u32 s64, s64, s40
s_addc_u32 s65, s65, 0
AD 10,7
AD 11,8
MA 144,104,48
s_add_u32 m0, %[hm], 24576
v_xor_b32 v1, 0x700, v1
AW 183,9
AW 187,10
MA 144,108,52
WL 2
TR 104,2,34
AW 191,11
MV 184,112,48
TR 106,3,34
MV 184,116,52
s_add_u32 s40, s64, 524288
s_addc_u32 s41, s65, 0
MV 188,112,56
DH 0
MV 188,116,60
TR 108,2,35
MV 192,112,64
TR 110,3,35
MV 192,116,68
MV 196,112,72
MV 196,116,76
DH 2
MV 200,112,80
MV 200,116,84
MV 204,112,88
s_bitcmp1_b32 s48, 1
s_cselect_b64 s[64:65], s[58:59], s[56:57]
MV 204,116,92
WL 4
TR 112,4,34
MV 228,96,88
TR 114,5,34
MV 228,100,92
s_bitcmp1_b32 s48, 17
s_cselect_b32 s40, 16384, 0
MV 224,96,80
s_add_u32 s64, s64, s40
s_addc_u32 s65, s65, 0
MV 224,100,84
s_add_u32 m0, %[hm], 28672
TR 116,4,35
MV 220,96,72
TR 118,5,35
MV 220,100,76
s_add_u32 s40, s64, 266240
s_addc_u32 s41, s65, 0
MV 216,96,64
DH 0
MV 216,100,68
MV 212,96,56
MV 212,100,60
MV 208,96,48
MV 208,100,52
WL 4
TR 96,2,0
MV 232,104,48
TR 98,3,0
MV 232,108,52
MV 236,104,56
MV 236,108,60
TR 100,2,1
MV 240,104,64
TR 102,3,1
MV 240,108,68
MV 244,104,72
MV 244,108,76
MV 248,104,80
MV 248,108,84
MV 252,104,88
MV 252,108,92
WL 4
TR 104,4,0
MA 188,112,88
TR 106,5,0
MA 188,116,92
MA 184,112,80
MA 184,116,84
TR 108,4,1
MA 180,112,72
TR 110,5,1
MA 180,116,76
MA 176,112,64
MA 176,116,68
MA 172,112,56
MA 172,116,60
MA 168,112,48
WV 7
MA 168,116,52
WL 4
TR 112,2,2
MV 40,96,0
TR 114,3,2
MV 40,100,4
s_add_u32 s44, s84, 1134592
s_addc_u32 s45, s85, 0
MV 44,96,8
LA 48,-4
MV 44,100,12
TR 116,2,3
MV 48,96,16
TR 118,3,3
MV 48,100,20
MV 52,96,24
MV 52,100,28
LA 52,-3
MV 56,96,32
MV 56,100,36
MV 60,96,40
MV 60,100,44
WL 4
TR 96,4,2
MV 84,104,40
TR 98,5,2
MV 84,108,44
LA 56,-2
MV 80,104,32
)"
R"(MV 80,108,36
TR 100,4,3
MV 76,104,24
TR 102,5,3
MV 76,108,28
MV 72,104,16
LA 60,-1
MV 72,108,20
MV 68,104,8
MV 68,108,12
MV 64,104,0
MV 64,108,4
WL 4
TR 104,2,4
MV 88,112,0
TR 106,3,4
MV 88,116,4
LA 64,0
MV 92,112,8
MV 92,116,12
TR 108,2,5
MV 96,112,16
TR 110,3,5
MV 96,116,20
MV 100,112,24
LA 68,1
MV 100,116,28
MV 104,112,32
MV 104,116,36
s_add_u32 s44, s44, 6144
s_addc_u32 s45, s45, 0
MV 108,112,40
MV 108,116,44
WL 4
TR 112,4,4
MA 140,96,40
TR 114,5,4
MA 140,100,44
LA 72,-4
MA 136,96,32
MA 136,100,36
TR 116,4,5
MA 132,96,24
TR 118,5,5
MA 132,100,28
MA 128,96,16
LA 76,-3
MA 128,100,20
MA 124,96,8
MA 124,100,12
WL 0
s_barrier
MA 120,96,0
s_add_u32 m0, %[hm], 30720
MA 120,100,4
TR 96,2,6
MV 112,104,0
TR 98,3,6
MV 112,108,4
LA 80,-2
MV 116,104,8
s_add_u32 s40, s64, 268288
s_addc_u32 s41, s65, 0
MV 116,108,12
TR 100,2,7
MV 120,104,16
TR 102,3,7
MV 120,108,20
MV 124,104,24
LA 84,-1
MV 124,108,28
MV 128,104,32
MV 128,108,36
MV 132,104,40
MV 132,108,44
TR 104,4,6
MV 156,112,40
TR 106,5,6
MV 156,116,44
LA 88,0
MV 152,112,32
MV 152,116,36
TR 108,4,7
MV 148,112,24
TR 110,5,7
MV 148,116,28
MV 144,112,16
LA 92,1
MV 144,116,20
MV 140,112,8
MV 140,116,12
MV 136,112,0
MV 136,116,4
WL 4
TR 112,2,8
MV 160,96,0
TR 114,3,8
MV 160,100,4
DH 0
MV 164,96,8
MV 164,100,12
TR 116,2,9
MV 168,96,16
TR 118,3,9
MV 168,100,20
s_bitcmp1_b32 s49, 1
s_cselect_b64 s[66:67], s[62:63], s[60:61]
MV 172,96,24
s_add_u32 m0, %[hm], 32768
MV 172,100,28
s_add_u32 s40, s66, 262144
s_addc_u32 s41, s67, 0
MV 176,96,32
DH 0
MV 176,100,36
MV 180,96,40
MV 180,100,44
WL 4
TR 96,4,8
MA 164,104,40
TR 98,5,8
MA 164,108,44
DH 2
MA 160,104,32
MA 160,108,36
TR 100,4,9
MA 156,104,24
TR 102,5,9
MA 156,108,28
s_bitcmp1_b32 s48, 2
s_cselect_b64 s[64:65], s[58:59], s[56:57]
MA 152,104,16
s_bitcmp1_b32 s48, 18
s_cselect_b32 s40, 16384, 0
MA 152,108,20
s_add_u32 s64, s64, s40
s_addc_u32 s65, s65, 0
MA 148,104,8
s_add_u32 m0, %[hm], 0
v_xor_b32 v1, 0x100, v1
MA 148,108,12
s_add_u32 s40, s64, 524288
s_addc_u32 s41, s65, 0
MA 144,104,0
DH 0
MA 144,108,4
WL 4
TR 104,2,10
MV 184,112,0
TR 106,3,10
MV 184,116,4
MV 188,112,8
MV 188,116,12
TR 108,2,11
MV 192,112,16
TR 110,3,11
MV 192,116,20
DH 2
MV 196,112,24
MV 196,116,28
MV 200,112,32
s_add_u32 m0, %[hm], 4096
MV 200,116,36
s_add_u32 s40, s64, 528384
s_addc_u32 s41, s65, 0
MV 204,112,40
.Lk2p26:
s_cbranch_execz .Lk2r5
DH 0
MV 204,116,44
WL 4
TR 112,4,10
MV 228,96,40
TR 114,5,10
MV 228,100,44
MV 224,96,32
MV 224,100,36
TR 116,4,11
MV 220,96,24
TR 118,5,11
MV 220,100,28
MV 216,96,16
MV 216,100,20
MV 212,96,8
MV 212,100,12
MV 208,96,0
MV 208,100,4
WL 4
TR 96,2,12
MV 232,104,0
TR 98,3,12
MV 232,108,4
MV 236,104,8
MV 236,108,12
TR 100,2,13
MV 240,104,16
TR 102,3,13
MV 240,108,20
MV 244,104,24
MV 244,108,28
MV 248,104,32
MV 248,108,36
MV 252,104,40
MV 252,108,44
WL 4
TR 104,4,12
MA 188,112,40
TR 106,5,12
MA 188,116,44
MA 184,112,32
MA 184,116,36
TR 108,4,13
MA 180,112,24
TR 110,5,13
MA 180,116,28
MA 176,112,16
MA 176,116,20
MA 172,112,8
MA 172,116,12
MA 168,112,0
WV 6
MA 168,116,4
WL 4
TR 112,2,14
MV 40,96,48
TR 114,3,14
MV 40,100,52
s_add_u32 s44, s84, 4096
s_addc_u32 s45, s85, 0
MV 44,96,56
LA 0,-4
MV 44,100,60
TR 116,2,15
MV 48,96,64
TR 118,3,15
MV 48,100,68
MV 52,96,72
MV 52,100,76
LA 4,-3
MV 56,96,80
MV 56,100,84
MV 60,96,88
MV 60,100,92
WL 4
TR 96,4,14
MV 84,104,88
TR 98,5,14
MV 84,108,92
LA 8,-2
MV 80,104,80
SB 40,32
SB 44,33
MV 80,108,84
TR 100,4,15
SB 48,34
MV 76,104,72
TR 102,5,15
SB 52,35
MV 76,108,76
SB 56,36
SB 60,37
MV 72,104,64
LA 12,-1
MV 72,108,68
CV 12,40,44
CV 13,48,52
MV 68,104,56
CV 16,56,60
SB 41,32
MV 68,108,60
ds_write_b64 v18, v[12:13] offset:24576
MV 64,104,48
SB 45,33
SB 49,34
MV 64,108,52
WL 5
TR 104,2,16
SB 53,35
MV 88,112,48
TR 106,3,16
SB 57,36
MV 88,116,52
LA 16,0
MV 92,112,56
ds_write_b32 v19, v16 offset:24576
MV 92,116,60
TR 108,2,17
SB 61,37
MV 96,112,64
TR 110,3,17
CV 14,41,45
MV 96,116,68
CV 15,49,53
CV 17,57,61
MV 100,112,72
LA 20,1
MV 100,116,76
ds_write_b64 v18, v[14:15] offset:25344
MV 104,112,80
SB 42,32
SB 46,33
MV 104,116,84
s_add_u32 s44, s44, 6144
s_addc_u32 s45, s45, 0
SB 50,34
MV 108,112,88
ds_write_b32 v19, v17 offset:25344
MV 108,116,92
WL 7
TR 112,4,16
SB 54,35
MA 140,96,88
TR 114,5,16
SB 58,36
MA 140,100,92
LA 24,-4
MA 136,96,80
SB 62,37
CV 12,42,46
MA 136,100,84
TR 116,4,17
CV 13,50,54
MA 132,96,72
TR 118,5,17
CV 16,58,62
MA 132,100,76
ds_write_b64 v18, v[12:13] offset:26112
MA 128,96,64
LA 28,-3
MA 128,100,68
SB 43,32
SB 47,33
MA 124,96,56
SB 51,34
SB 55,35
MA 124,100,60
WL 1
s_barrier
ds_write_b32 v19, v16 offset:26112
MA 120,96,48
s_add_u32 m0, %[hm], 6144
SB 59,36
SB 63,37
MA 120,100,52
TR 96,2,18
CV 14,43,47
MV 112,104,48
TR 98,3,18
CV 15,51,55
MV 112,108,52
LA 32,-2
MV 116,104,56
s_add_u32 s40, s64, 530432
s_addc_u32 s41, s65, 0
CV 17,59,63
MV 116,108,60
TR 100,2,19
SB 64,32
MV 120,104,64
TR 102,3,19
SB 68,33
MV 120,108,68
ds_write_b64 v18, v[14:15] offset:26880
MV 124,104,72
LA 36,-1
MV 124,108,76
ds_write_b32 v19, v17 offset:26880
MV 128,104,80
SB 72,34
SB 76,35
MV 128,108,84
SB 80,36
SB 84,37
MV 132,104,88
CV 12,64,68
CV 13,72,76
MV 132,108,92
TR 104,4,18
CV 16,80,84
MV 156,112,88
TR 106,5,18
SB 65,32
MV 156,116,92
LA 40,0
MV 152,112,80
ds_write_b64 v18, v[12:13] offset:36864
MV 152,116,84
TR 108,4,19
SB 69,33
MV 148,112,72
TR 110,5,19
SB 73,34
MV 148,116,76
ds_write_b32 v19, v16 offset:36864
MV 144,112,64
LA 44,1
MV 144,116,68
SB 77,35
SB 81,36
MV 140,112,56
)"
R"(SB 85,37
CV 14,65,69
MV 140,116,60
CV 15,73,77
CV 17,81,85
MV 136,112,48
ds_write_b64 v18, v[14:15] offset:37632
MV 136,116,52
WL 7
TR 112,2,20
SB 66,32
MV 160,96,48
TR 114,3,20
SB 70,33
MV 160,100,52
DH 0
MV 164,96,56
ds_write_b32 v19, v17 offset:37632
MV 164,100,60
TR 116,2,21
SB 74,34
MV 168,96,64
s_add_u32 m0, %[hm], 8192
TR 118,3,21
SB 78,35
MV 168,100,68
s_add_u32 s40, s66, 262144
s_addc_u32 s41, s67, 0
SB 82,36
MV 172,96,72
DH 0
MV 172,100,76
SB 86,37
CV 12,66,70
MV 176,96,80
CV 13,74,78
CV 16,82,86
MV 176,100,84
ds_write_b64 v18, v[12:13] offset:38400
MV 180,96,88
SB 67,32
SB 71,33
MV 180,100,92
WL 7
TR 96,4,20
SB 75,34
MA 164,104,88
TR 98,5,20
SB 79,35
MA 164,108,92
DH 2
MA 160,104,80
ds_write_b32 v19, v16 offset:38400
MA 160,108,84
TR 100,4,21
SB 83,36
MA 156,104,72
TR 102,5,21
SB 87,37
MA 156,108,76
s_add_u32 m0, %[hm], 12288
v_xor_b32 v1, 0x300, v1
CV 14,67,71
MA 152,104,64
s_add_u32 s40, s64, 524288
s_addc_u32 s41, s65, 0
CV 15,75,79
MA 152,108,68
DH 0
MA 148,104,56
CV 17,83,87
MA 148,108,60
ds_write_b64 v18, v[14:15] offset:39168
MA 144,104,48
SB 88,32
SB 92,33
MA 144,108,52
WL 6
TR 104,2,22
SB 96,34
MV 184,112,48
TR 106,3,22
SB 100,35
MV 184,116,52
DH 2
MV 188,112,56
ds_write_b32 v19, v17 offset:39168
MV 188,116,60
TR 108,2,23
SB 104,36
MV 192,112,64
s_add_u32 m0, %[hm], 16384
TR 110,3,23
SB 108,37
MV 192,116,68
s_add_u32 s40, s64, 528384
s_addc_u32 s41, s65, 0
CV 12,88,92
MV 196,112,72
DH 0
MV 196,116,76
CV 13,96,100
CV 16,104,108
MV 200,112,80
ds_write_b64 v18, v[12:13] offset:49152
MV 200,116,84
SB 89,32
SB 93,33
MV 204,112,88
ds_write_b32 v19, v16 offset:49152
MV 204,116,92
WL 7
TR 112,4,22
SB 97,34
MV 228,96,88
TR 114,5,22
SB 101,35
MV 228,100,92
MV 224,96,80
SB 105,36
SB 109,37
MV 224,100,84
TR 116,4,23
CV 14,89,93
MV 220,96,72
TR 118,5,23
CV 15,97,101
MV 220,100,76
CV 17,105,109
SB 90,32
MV 216,96,64
SB 94,33
SB 98,34
MV 216,100,68
SB 102,35
SB 106,36
MV 212,96,56
WL 0
SB 110,37
CV 12,90,94
MV 212,100,60
s_barrier
RV 24,20,24576
CV 13,98,102
MV 208,96,48
s_add_u32 m0, %[hm], 18432
ds_write_b64 v18, v[14:15] offset:49920
MV 208,100,52
TR 96,2,24
CV 16,106,110
MV 232,104,48
s_add_u32 s42, s88, 196608
TR 98,3,24
SB 91,32
MV 232,108,52
s_add_u32 s40, s64, 530432
s_addc_u32 s41, s65, 0
SB 95,33
MV 236,104,56
DH 0
MV 236,108,60
TR 100,2,25
SB 99,34
MV 240,104,64
TR 102,3,25
SB 103,35
MV 240,108,68
s_bitcmp1_b32 s49, 2
s_cselect_b64 s[66:67], s[62:63], s[60:61]
SB 107,36
MV 244,104,72
s_add_u32 m0, %[hm], 20480
s_add_u32 s42, s42, s72
s_addc_u32 s43, s73, 0
MV 244,108,76
s_add_u32 s40, s66, 524288
s_addc_u32 s41, s67, 0
SB 111,37
MV 248,104,80
DH 0
MV 248,108,84
ds_write_b32 v19, v17 offset:49920
MV 252,104,88
CV 14,91,95
CV 15,99,103
MV 252,108,92
TR 104,4,24
CV 17,107,111
MA 188,112,88
TR 106,5,24
v_add_u32 v21, 0xf000, v18
v_add_u32 v22, 0xf000, v19
MA 188,116,92
WL 5
ST 24,0
AR 6,120
MA 184,112,80
ds_write_b64 v18, v[12:13] offset:50688
AR 7,124
MA 184,116,84
TR 108,4,25
AR 8,128
AR 9,132
MA 180,112,72
TR 110,5,25
AR 10,136
AR 11,140
MA 180,116,76
RV 24,20,25600
SB 6,32
SB 7,33
MA 176,112,64
ds_write_b32 v19, v16 offset:50688
SB 8,34
MA 176,116,68
SB 9,35
SB 10,36
SB 11,37
MA 172,112,56
ds_write_b64 v18, v[14:15] offset:51456
CV 12,6,7
MA 172,116,60
CV 13,8,9
CV 16,10,11
AR 6,121
MA 168,112,48
WV 9
ds_write_b32 v19, v17 offset:51456
AR 7,125
MA 168,116,52
WL 7
TR 112,2,26
AR 8,129
AR 9,133
ZV 40,96,0
TR 114,3,26
AR 10,137
AR 11,141
MV 40,100,4
s_add_u32 s44, s84, 53248
s_addc_u32 s45, s85, 0
SB 6,32
SB 7,33
ZV 44,96,8
LA 48,-4
SB 8,34
MV 44,100,12
TR 116,2,27
SB 9,35
SB 10,36
ZV 48,96,16
TR 118,3,27
SB 11,37
CV 14,6,7
MV 48,100,20
ds_write_b64 v21, v[12:13] offset:0
CV 15,8,9
ZV 52,96,24
CV 17,10,11
AR 6,122
AR 7,126
MV 52,100,28
LA 52,-3
AR 8,130
ZV 56,96,32
ds_write_b32 v22, v16 offset:0
AR 9,134
MV 56,100,36
AR 10,138
AR 11,142
SB 6,32
ZV 60,96,40
ds_write_b64 v21, v[14:15] offset:768
SB 7,33
MV 60,100,44
WL 5
TR 96,4,26
SB 8,34
SB 9,35
ZV 84,104,40
TR 98,5,26
SB 10,36
SB 11,37
MV 84,108,44
LA 56,-2
CV 12,6,7
ZV 80,104,32
ds_write_b32 v22, v17 offset:768
CV 13,8,9
MV 80,108,36
TR 100,4,27
CV 16,10,11
AR 6,123
ZV 76,104,24
TR 102,5,27
AR 7,127
AR 8,131
MV 76,108,28
ds_write_b64 v21, v[12:13] offset:1536
AR 9,135
ZV 72,104,16
LA 60,-1
AR 10,139
MV 72,108,20
ds_write_b32 v22, v16 offset:1536
AR 11,143
ZV 68,104,8
SB 6,32
SB 7,33
SB 8,34
MV 68,108,12
SB 9,35
SB 10,36
SB 11,37
ZV 64,104,0
CV 14,6,7
CV 15,8,9
CV 17,10,11
MV 64,108,4
WL 5
TR 104,2,28
ZV 88,112,0
TR 106,3,28
MV 88,116,4
LA 64,0
ZV 92,112,8
ds_write_b64 v21, v[14:15] offset:2304
MV 92,116,12
TR 108,2,29
ZV 96,112,16
TR 110,3,29
MV 96,116,20
ds_write_b32 v22, v17 offset:2304
ZV 100,112,24
LA 68,1
MV 100,116,28
ZV 104,112,32
MV 104,116,36
s_add_u32 s44, s44, 6144
s_addc_u32 s45, s45, 0
ZV 108,112,40
MV 108,116,44
WL 7
TR 112,4,28
ZA 140,96,40
.Lk2p27:
s_cbranch_execz .Lk2r5
TR 114,5,28
MA 140,100,44
LA 72,-4
ZA 136,96,32
MA 136,100,36
TR 116,4,29
ZA 132,96,24
TR 118,5,29
MA 132,100,28
ZA 128,96,16
LA 76,-3
MA 128,100,20
ZA 124,96,8
MA 124,100,12
WL 0
s_barrier
ZA 120,96,0
MA 120,100,4
TR 96,2,30
MV 112,104,0
TR 98,3,30
MV 112,108,4
LA 80,-2
MV 116,104,8
MV 116,108,12
TR 100,2,31
MV 120,104,16
TR 102,3,31
MV 120,108,20
MV 124,104,24
LA 84,-1
MV 124,108,28
MV 128,104,32
MV 128,108,36
MV 132,104,40
MV 132,108,44
TR 104,4,30
MV 156,112,40
TR 106,5,30
MV 156,116,44
LA 88,0
MV 152,112,32
v_add_u32 v21, 0xf000, v18
v_add_u32 v22, 0xf000, v19
MV 152,116,36
TR 108,4,31
SB 112,32
MV 148,112,24
TR 110,5,31
SB 116,33
MV 148,116,28
SB 120,34
SB 124,35
)"
R"(MV 144,112,16
LA 92,1
MV 144,116,20
SB 128,36
SB 132,37
MV 140,112,8
CV 12,112,116
CV 13,120,124
MV 140,116,12
CV 16,128,132
SB 113,32
MV 136,112,0
ds_write_b64 v21, v[12:13] offset:12288
MV 136,116,4
WL 5
TR 112,2,32
SB 117,33
MV 160,96,0
TR 114,3,32
SB 121,34
MV 160,100,4
DH 2
MV 164,96,8
ds_write_b32 v22, v16 offset:12288
MV 164,100,12
TR 116,2,33
SB 125,35
MV 168,96,16
TR 118,3,33
SB 129,36
MV 168,100,20
s_add_u32 m0, %[hm], 24576
v_xor_b32 v1, 0x100, v1
SB 133,37
MV 172,96,24
s_add_u32 s40, s64, 524288
s_addc_u32 s41, s65, 0
CV 14,113,117
MV 172,100,28
DH 0
MV 176,96,32
CV 15,121,125
CV 17,129,133
MV 176,100,36
ds_write_b64 v21, v[14:15] offset:13056
MV 180,96,40
SB 114,32
SB 118,33
MV 180,100,44
WL 7
TR 96,4,32
SB 122,34
MA 164,104,40
TR 98,5,32
SB 126,35
MA 164,108,44
DH 2
MA 160,104,32
ds_write_b32 v22, v17 offset:13056
MA 160,108,36
TR 100,4,33
SB 130,36
MA 156,104,24
s_add_u32 m0, %[hm], 28672
TR 102,5,33
SB 134,37
MA 156,108,28
s_add_u32 s40, s64, 528384
s_addc_u32 s41, s65, 0
CV 12,114,118
MA 152,104,16
DH 0
MA 152,108,20
CV 13,122,126
CV 16,130,134
MA 148,104,8
ds_write_b64 v21, v[12:13] offset:13824
MA 148,108,12
SB 115,32
SB 119,33
MA 144,104,0
ds_write_b32 v22, v16 offset:13824
MA 144,108,4
WL 7
TR 104,2,34
SB 123,34
MV 184,112,0
TR 106,3,34
SB 127,35
MV 184,116,4
ST 24,1
MV 188,112,8
SB 131,36
SB 135,37
MV 188,116,12
TR 108,2,35
CV 14,115,119
MV 192,112,16
TR 110,3,35
CV 15,123,127
MV 192,116,20
RV 24,20,26624
CV 17,131,135
MV 196,112,24
ds_write_b64 v21, v[14:15] offset:14592
MV 196,116,28
SB 136,32
SB 140,33
MV 200,112,32
ds_write_b32 v22, v17 offset:14592
MV 200,116,36
SB 144,34
SB 148,35
MV 204,112,40
SB 152,36
SB 156,37
MV 204,116,44
WL 8
TR 112,4,34
CV 12,136,140
MV 228,96,40
TR 114,5,34
CV 13,144,148
MV 228,100,44
WL 4
ST 24,2
MV 224,96,32
CV 16,152,156
AW 204,12
MV 224,100,36
TR 116,4,35
AW 205,13
MV 220,96,24
TR 118,5,35
AW 212,16
MV 220,100,28
SB 137,32
SB 141,33
MV 216,96,16
SB 145,34
SB 149,35
MV 216,100,20
SB 153,36
SB 157,37
MV 212,96,8
WL 0
CV 14,137,141
CV 15,145,149
MV 212,100,12
s_barrier
RV 24,20,36864
CV 17,153,157
MV 208,96,0
s_add_u32 m0, %[hm], 30720
AW 206,14
AW 207,15
MV 208,100,4
TR 96,2,0
AW 213,17
MV 232,104,0
s_add_u32 s42, s88, 294912
TR 98,3,0
SB 138,32
MV 232,108,4
s_add_u32 s40, s64, 530432
s_addc_u32 s41, s65, 0
SB 142,33
MV 236,104,8
DH 0
MV 236,108,12
TR 100,2,1
SB 146,34
MV 240,104,16
TR 102,3,1
SB 150,35
MV 240,108,20
s_add_u32 m0, %[hm], 32768
s_add_u32 s42, s42, s72
s_addc_u32 s43, s73, 0
MV 244,104,24
s_add_u32 s40, s66, 524288
s_addc_u32 s41, s67, 0
SB 154,36
MV 244,108,28
DH 0
MV 248,104,32
SB 158,37
CV 12,138,142
MV 248,108,36
CV 13,146,150
CV 16,154,158
MV 252,104,40
AW 208,12
AW 209,13
MV 252,108,44
TR 104,4,0
AW 214,16
MA 188,112,40
TR 106,5,0
SB 139,32
MA 188,116,44
WL 4
ST 24,0
MA 184,112,32
SB 143,33
SB 147,34
MA 184,116,36
TR 108,4,1
SB 151,35
MA 180,112,24
TR 110,5,1
SB 155,36
MA 180,116,28
RV 24,20,37888
SB 159,37
MA 176,112,16
CV 14,139,143
CV 15,147,151
MA 176,116,20
CV 17,155,159
AW 210,14
MA 172,112,8
AW 211,15
AW 215,17
MA 172,116,12
SB 160,32
SB 164,33
MA 168,112,0
WV 9
SB 168,34
SB 172,35
MA 168,116,4
WL 5
TR 112,2,2
SB 176,36
MV 40,96,48
TR 114,3,2
SB 180,37
MV 40,100,52
s_add_u32 s44, s84, 102400
s_addc_u32 s45, s85, 0
CV 12,160,164
MV 44,96,56
LA 0,-4
MV 44,100,60
TR 116,2,3
CV 13,168,172
MV 48,96,64
TR 118,3,3
CV 16,176,180
MV 48,100,68
ds_write_b64 v18, v[12:13] offset:24576
MV 52,96,72
SB 161,32
SB 165,33
MV 52,100,76
LA 4,-3
MV 56,96,80
ds_write_b32 v19, v16 offset:24576
MV 56,100,84
SB 169,34
SB 173,35
MV 60,96,88
SB 177,36
SB 181,37
MV 60,100,92
WL 6
TR 96,4,2
CV 14,161,165
MV 84,104,88
TR 98,5,2
CV 15,169,173
MV 84,108,92
LA 8,-2
MV 80,104,80
CV 17,177,181
SB 162,32
MV 80,108,84
TR 100,4,3
SB 166,33
MV 76,104,72
TR 102,5,3
SB 170,34
MV 76,108,76
ds_write_b64 v18, v[14:15] offset:25344
MV 72,104,64
LA 12,-1
MV 72,108,68
ds_write_b32 v19, v17 offset:25344
MV 68,104,56
SB 174,35
SB 178,36
MV 68,108,60
SB 182,37
CV 12,162,166
MV 64,104,48
CV 13,170,174
CV 16,178,182
MV 64,108,52
WL 6
TR 104,2,4
SB 163,32
MV 88,112,48
TR 106,3,4
SB 167,33
MV 88,116,52
LA 16,0
MV 92,112,56
ds_write_b64 v18, v[12:13] offset:26112
MV 92,116,60
TR 108,2,5
SB 171,34
MV 96,112,64
TR 110,3,5
SB 175,35
MV 96,116,68
ds_write_b32 v19, v16 offset:26112
MV 100,112,72
LA 20,1
MV 100,116,76
SB 179,36
SB 183,37
MV 104,112,80
CV 14,163,167
CV 15,171,175
MV 104,116,84
s_add_u32 s44, s44, 6144
s_addc_u32 s45, s45, 0
CV 17,179,183
MV 108,112,88
ds_write_b64 v18, v[14:15] offset:26880
AR 6,144
MV 108,116,92
WL 7
TR 112,4,4
AR 7,148
AR 8,152
MA 140,96,88
TR 114,5,4
AR 9,156
AR 10,160
MA 140,100,92
LA 24,-4
AR 11,164
MA 136,96,80
ds_write_b32 v19, v17 offset:26880
SB 6,32
MA 136,100,84
TR 116,4,5
SB 7,33
SB 8,34
MA 132,96,72
TR 118,5,5
SB 9,35
SB 10,36
MA 132,100,76
SB 11,37
CV 12,6,7
CV 13,8,9
MA 128,96,64
LA 28,-3
CV 16,10,11
MA 128,100,68
AW 216,12
AW 217,13
AW 224,16
MA 124,96,56
WL 0
AR 6,145
AR 7,149
AR 8,153
MA 124,100,60
s_barrier
AR 9,157
AR 10,161
AR 11,165
MA 120,96,48
SB 6,32
SB 7,33
SB 8,34
MA 120,100,52
TR 96,2,6
SB 9,35
SB 10,36
ZV 112,104,48
TR 98,3,6
SB 11,37
CV 14,6,7
MV 112,108,52
LA 32,-2
CV 15,8,9
ZV 116,104,56
CV 17,10,11
AW 218,14
AW 219,15
MV 116,108,60
TR 100,2,7
AW 225,17
AR 6,146
ZV 120,104,64
)"
R"(TR 102,3,7
AR 7,150
AR 8,154
MV 120,108,68
AR 9,158
AR 10,162
AR 11,166
ZV 124,104,72
LA 36,-1
SB 6,32
MV 124,108,76
SB 7,33
SB 8,34
SB 9,35
ZV 128,104,80
SB 10,36
SB 11,37
CV 12,6,7
MV 128,108,84
CV 13,8,9
CV 16,10,11
AW 220,12
ZV 132,104,88
AW 221,13
AW 226,16
AR 6,147
MV 132,108,92
TR 104,4,6
AR 7,151
AR 8,155
ZV 156,112,88
TR 106,5,6
AR 9,159
AR 10,163
MV 156,116,92
LA 40,0
AR 11,167
ZV 152,112,80
SB 6,32
SB 7,33
SB 8,34
MV 152,116,84
TR 108,4,7
SB 9,35
SB 10,36
ZV 148,112,72
TR 110,5,7
SB 11,37
CV 14,6,7
MV 148,116,76
CV 15,8,9
CV 17,10,11
AW 222,14
ZV 144,112,64
LA 44,1
AW 223,15
MV 144,116,68
AW 227,17
ZV 140,112,56
MV 140,116,60
ZV 136,112,48
MV 136,116,52
WL 4
TR 112,2,8
ZV 160,96,48
TR 114,3,8
MV 160,100,52
DH 2
ZV 164,96,56
MV 164,100,60
TR 116,2,9
ZV 168,96,64
PA 20480
TR 118,3,9
MV 168,100,68
ZV 172,96,72
DF
MV 172,100,76
s_add_u32 m0, %[hm], 0
v_xor_b32 v1, 0x700, v1
ZV 176,96,80
s_add_u32 s40, s64, 524288
s_addc_u32 s41, s65, 0
MV 176,100,84
ZV 180,96,88
MV 180,100,92
WL 4
TR 96,4,8
ZA 164,104,88
TR 98,5,8
MA 164,108,92
DH 0
ZA 160,104,80
MA 160,108,84
TR 100,4,9
ZA 156,104,72
TR 102,5,9
MA 156,108,76
ZA 152,104,64
DH 2
MA 152,108,68
ZA 148,104,56
MA 148,108,60
s_add_u32 m0, %[hm], 4096
ZA 144,104,48
s_add_u32 s40, s64, 528384
s_addc_u32 s41, s65, 0
MA 144,108,52
WL 4
TR 104,2,10
MV 184,112,48
TR 106,3,10
MV 184,116,52
DH 0
MV 188,112,56
MV 188,116,60
TR 108,2,11
MV 192,112,64
.Lk2p28:
s_cbranch_execz .Lk2r5
TR 110,3,11
MV 192,116,68
MV 196,112,72
ST 24,1
MV 196,116,76
MV 200,112,80
RV 24,20,38912
MV 200,116,84
MV 204,112,88
MV 204,116,92
WL 5
TR 112,4,10
MV 228,96,88
TR 114,5,10
MV 228,100,92
MV 224,96,80
SB 184,32
SB 188,33
MV 224,100,84
TR 116,4,11
SB 192,34
MV 220,96,72
TR 118,5,11
SB 196,35
MV 220,100,76
WL 4
ST 24,2
MV 216,96,64
SB 200,36
SB 204,37
MV 216,100,68
CV 12,184,188
CV 13,192,196
MV 212,96,56
CV 16,200,204
AW 228,12
MV 212,100,60
WL 0
s_barrier
RV 24,20,49152
AW 229,13
MV 208,96,48
s_add_u32 m0, %[hm], 6144
AW 236,16
SB 185,32
MV 208,100,52
TR 96,2,12
SB 189,33
MV 232,104,48
s_add_u32 s42, s89, 196608
TR 98,3,12
SB 193,34
MV 232,108,52
s_add_u32 s40, s64, 530432
s_addc_u32 s41, s65, 0
SB 197,35
MV 236,104,56
DH 0
MV 236,108,60
TR 100,2,13
SB 201,36
MV 240,104,64
TR 102,3,13
SB 205,37
MV 240,108,68
s_add_u32 m0, %[hm], 8192
s_add_u32 s42, s42, s72
s_addc_u32 s43, s73, 0
MV 244,104,72
s_add_u32 s40, s66, 524288
s_addc_u32 s41, s67, 0
CV 14,185,189
MV 244,108,76
DH 0
MV 248,104,80
CV 15,193,197
CV 17,201,205
MV 248,108,84
AW 230,14
AW 231,15
MV 252,104,88
AW 237,17
SB 186,32
MV 252,108,92
TR 104,4,12
SB 190,33
MA 188,112,88
TR 106,5,12
SB 194,34
MA 188,116,92
WL 4
ST 24,0
MA 184,112,80
SB 198,35
SB 202,36
MA 184,116,84
TR 108,4,13
SB 206,37
MA 180,112,72
TR 110,5,13
CV 12,186,190
MA 180,116,76
RV 24,20,50176
CV 13,194,198
MA 176,112,64
CV 16,202,206
AW 232,12
MA 176,116,68
AW 233,13
AW 238,16
MA 172,112,56
SB 187,32
SB 191,33
MA 172,116,60
SB 195,34
SB 199,35
MA 168,112,48
WV 10
SB 203,36
SB 207,37
MA 168,116,52
WL 5
TR 112,2,14
CV 14,187,191
MV 40,96,0
TR 114,3,14
CV 15,195,199
MV 40,100,4
s_add_u32 s44, s84, 151552
s_addc_u32 s45, s85, 0
CV 17,203,207
MV 44,96,8
LA 48,-4
MV 44,100,12
TR 116,2,15
AW 234,14
MV 48,96,16
TR 118,3,15
AW 235,15
MV 48,100,20
AW 239,17
MV 52,96,24
MV 52,100,28
LA 52,-3
MV 56,96,32
ds_write_b64 v18, a[204:205] offset:36864
MV 56,100,36
SB 208,32
SB 212,33
MV 60,96,40
ds_write_b32 v19, a212 offset:36864
MV 60,100,44
WL 6
TR 96,4,14
SB 216,34
MV 84,104,40
TR 98,5,14
SB 220,35
MV 84,108,44
LA 56,-2
MV 80,104,32
ds_write_b64 v18, a[206:207] offset:37632
MV 80,108,36
TR 100,4,15
SB 224,36
MV 76,104,24
TR 102,5,15
SB 228,37
MV 76,108,28
ds_write_b32 v19, a213 offset:37632
MV 72,104,16
LA 60,-1
MV 72,108,20
ds_write_b64 v18, a[208:209] offset:38400
MV 68,104,8
CV 12,208,212
CV 13,216,220
MV 68,108,12
ds_write_b32 v19, a214 offset:38400
MV 64,104,0
CV 16,224,228
AW 240,12
MV 64,108,4
WL 9
TR 104,2,16
AW 241,13
MV 88,112,0
TR 106,3,16
AW 248,16
MV 88,116,4
LA 64,0
MV 92,112,8
ds_write_b64 v18, a[210:211] offset:39168
MV 92,116,12
TR 108,2,17
SB 209,32
MV 96,112,16
TR 110,3,17
SB 213,33
MV 96,116,20
ds_write_b32 v19, a215 offset:39168
MV 100,112,24
LA 68,1
MV 100,116,28
SB 217,34
SB 221,35
MV 104,112,32
SB 225,36
SB 229,37
MV 104,116,36
s_add_u32 s44, s44, 6144
s_addc_u32 s45, s45, 0
CV 14,209,213
MV 108,112,40
CV 15,217,221
CV 17,225,229
MV 108,116,44
WL 7
TR 112,4,16
AW 242,14
MA 140,96,40
TR 114,5,16
AW 243,15
MA 140,100,44
LA 72,-4
MA 136,96,32
AW 249,17
SB 210,32
MA 136,100,36
TR 116,4,17
SB 214,33
MA 132,96,24
TR 118,5,17
SB 218,34
MA 132,100,28
SB 222,35
SB 226,36
MA 128,96,16
s_mov_b64 exec, 0
s_cmp_eq_u32 %[w], 0
s_cbranch_scc1 .Lk2p29
s_cmp_eq_u32 %[w], 1
s_cbranch_scc1 .Lk2p30
s_cmp_eq_u32 %[w], 2
s_cbranch_scc1 .Lk2p31
s_branch .Lk2p32
.Lk2r6:
s_mov_b64 exec, -1
LA 76,-3
MA 128,100,20
SB 230,37
CV 12,210,214
MA 124,96,8
CV 13,218,222
CV 16,226,230
MA 124,100,12
WL 0
s_barrier
AW 244,12
AW 245,13
MA 120,96,0
AW 250,16
SB 211,32
MA 120,100,4
TR 96,2,18
SB 215,33
MV 112,104,0
TR 98,3,18
SB 219,34
MV 112,108,4
LA 80,-2
MV 116,104,8
SB 223,35
SB 227,36
MV 116,108,12
TR 100,2,19
SB 231,37
MV 120,104,16
TR 102,3,19
CV 14,211,215
)"
R"(MV 120,108,20
CV 15,219,223
CV 17,227,231
MV 124,104,24
LA 84,-1
MV 124,108,28
AW 246,14
AW 247,15
MV 128,104,32
AW 251,17
MV 128,108,36
SB 232,32
SB 236,33
MV 132,104,40
SB 240,34
SB 244,35
MV 132,108,44
TR 104,4,18
SB 248,36
MV 156,112,40
TR 106,5,18
SB 252,37
MV 156,116,44
LA 88,0
MV 152,112,32
CV 12,232,236
CV 13,240,244
MV 152,116,36
TR 108,4,19
CV 16,248,252
MV 148,112,24
TR 110,5,19
AW 192,12
MV 148,116,28
AW 193,13
AW 200,16
MV 144,112,16
LA 92,1
MV 144,116,20
SB 233,32
SB 237,33
MV 140,112,8
SB 241,34
SB 245,35
MV 140,116,12
SB 249,36
SB 253,37
MV 136,112,0
CV 14,233,237
CV 15,241,245
MV 136,116,4
WL 4
TR 112,2,20
CV 17,249,253
MV 160,96,0
TR 114,3,20
AW 194,14
MV 160,100,4
DH 2
MV 164,96,8
AW 195,15
AW 201,17
MV 164,100,12
TR 116,2,21
SB 234,32
MV 168,96,16
TR 118,3,21
SB 238,33
MV 168,100,20
s_add_u32 m0, %[hm], 12288
v_xor_b32 v1, 0x100, v1
SB 242,34
MV 172,96,24
s_add_u32 s40, s64, 524288
s_addc_u32 s41, s65, 0
SB 246,35
MV 172,100,28
DH 0
MV 176,96,32
SB 250,36
SB 254,37
MV 176,100,36
CV 12,234,238
CV 13,242,246
MV 180,96,40
CV 16,250,254
AW 196,12
MV 180,100,44
WL 4
TR 96,4,20
AW 197,13
MA 164,104,40
TR 98,5,20
AW 202,16
MA 164,108,44
DH 2
MA 160,104,32
SB 235,32
SB 239,33
MA 160,108,36
TR 100,4,21
SB 243,34
MA 156,104,24
s_add_u32 m0, %[hm], 16384
TR 102,5,21
SB 247,35
MA 156,108,28
s_add_u32 s40, s64, 528384
s_addc_u32 s41, s65, 0
SB 251,36
MA 152,104,16
DH 0
MA 152,108,20
SB 255,37
CV 14,235,239
MA 148,104,8
CV 15,243,247
CV 17,251,255
MA 148,108,12
AW 198,14
AW 199,15
MA 144,104,0
AW 203,17
MA 144,108,4
WL 4
TR 104,2,22
AR 6,168
AR 7,172
ZV 184,112,0
TR 106,3,22
AR 8,176
AR 9,180
MV 184,116,4
ST 24,1
AR 10,184
ZV 188,112,8
AR 11,188
SB 6,32
SB 7,33
MV 188,116,12
TR 108,2,23
SB 8,34
SB 9,35
ZV 192,112,16
TR 110,3,23
SB 10,36
SB 11,37
MV 192,116,20
RV 24,20,51200
CV 12,6,7
CV 13,8,9
ZV 196,112,24
CV 16,10,11
AW 204,12
AW 205,13
MV 196,116,28
AW 212,16
AR 6,169
AR 7,173
ZV 200,112,32
AR 8,177
AR 9,181
AR 10,185
MV 200,116,36
AR 11,189
SB 6,32
SB 7,33
ZV 204,112,40
SB 8,34
SB 9,35
SB 10,36
MV 204,116,44
WL 3
TR 112,4,22
SB 11,37
CV 14,6,7
ZV 228,96,40
TR 114,5,22
CV 15,8,9
CV 17,10,11
MV 228,100,44
WL 2
ST 24,2
AW 206,14
ZV 224,96,32
AW 207,15
AW 213,17
AR 6,170
MV 224,100,36
TR 116,4,23
AR 7,174
AR 8,178
ZV 220,96,24
TR 118,5,23
AR 9,182
AR 10,186
MV 220,100,28
AR 11,190
SB 6,32
SB 7,33
ZV 216,96,16
SB 8,34
SB 9,35
SB 10,36
MV 216,100,20
SB 11,37
CV 12,6,7
CV 13,8,9
ZV 212,96,8
WL 0
CV 16,10,11
AW 208,12
AW 209,13
MV 212,100,12
s_barrier
AW 214,16
AR 6,171
AR 7,175
ZV 208,96,0
s_add_u32 m0, %[hm], 18432
AR 8,179
AR 9,183
AR 10,187
MV 208,100,4
TR 96,2,24
AR 11,191
SB 6,32
ZV 232,104,0
TR 98,3,24
SB 7,33
SB 8,34
MV 232,108,4
s_add_u32 s40, s64, 530432
s_addc_u32 s41, s65, 0
SB 9,35
SB 10,36
ZV 236,104,8
DH 0
SB 11,37
MV 236,108,12
TR 100,2,25
CV 14,6,7
CV 15,8,9
ZV 240,104,16
TR 102,3,25
CV 17,10,11
AW 210,14
MV 240,108,20
s_add_u32 m0, %[hm], 20480
AW 211,15
AW 215,17
ZV 244,104,24
s_add_u32 s40, s66, 524288
s_addc_u32 s41, s67, 0
MV 244,108,28
DH 0
ZV 248,104,32
ds_write_b64 v18, a[216:217] offset:49152
MV 248,108,36
ZV 252,104,40
ds_write_b32 v19, a224 offset:49152
MV 252,108,44
TR 104,4,24
ZA 188,112,40
TR 106,5,24
MA 188,116,44
ZA 184,112,32
ds_write_b64 v18, a[218:219] offset:49920
MA 184,116,36
TR 108,4,25
v_add_u32 v23, 0xf000, v20
ZA 180,112,24
TR 110,5,25
MA 180,116,28
RV 24,23,0
ZA 176,112,16
ds_write_b32 v19, a225 offset:49920
MA 176,116,20
ZA 172,112,8
s_add_u32 s42, s89, 294912
ds_write_b64 v18, a[220:221] offset:50688
MA 172,116,12
s_add_u32 s42, s42, s72
s_addc_u32 s43, s73, 0
ZA 168,112,0
WV 8
ds_write_b32 v19, a226 offset:50688
MA 168,116,4
WL 9
TR 112,2,26
MV 40,96,48
TR 114,3,26
MV 40,100,52
s_add_u32 s44, s84, 200704
s_addc_u32 s45, s85, 0
MV 44,96,56
LA 0,-4
MV 44,100,60
TR 116,2,27
MV 48,96,64
TR 118,3,27
MV 48,100,68
.Lk2p29:
s_cbranch_execz .Lk2r6
ds_write_b64 v18, a[222:223] offset:51456
MV 52,96,72
MV 52,100,76
LA 4,-3
MV 56,96,80
ds_write_b32 v19, a227 offset:51456
MV 56,100,84
MV 60,96,88
MV 60,100,92
WL 9
TR 96,4,26
MV 84,104,88
TR 98,5,26
MV 84,108,92
LA 8,-2
MV 80,104,80
MV 80,108,84
TR 100,4,27
MV 76,104,72
TR 102,5,27
MV 76,108,76
MV 72,104,64
LA 12,-1
MV 72,108,68
MV 68,104,56
MV 68,108,60
MV 64,104,48
MV 64,108,52
WL 6
TR 104,2,28
MV 88,112,48
TR 106,3,28
MV 88,116,52
LA 16,0
MV 92,112,56
MV 92,116,60
TR 108,2,29
MV 96,112,64
TR 110,3,29
MV 96,116,68
MV 100,112,72
LA 20,1
MV 100,116,76
MV 104,112,80
MV 104,116,84
s_add_u32 s44, s44, 6144
s_addc_u32 s45, s45, 0
MV 108,112,88
MV 108,116,92
WL 4
TR 112,4,28
MA 140,96,88
TR 114,5,28
MA 140,100,92
LA 24,-4
MA 136,96,80
MA 136,100,84
TR 116,4,29
MA 132,96,72
TR 118,5,29
MA 132,100,76
MA 128,96,64
LA 28,-3
MA 128,100,68
MA 124,96,56
MA 124,100,60
WL 0
s_barrier
MA 120,96,48
MA 120,100,52
TR 96,2,30
MV 112,104,48
TR 98,3,30
MV 112,108,52
LA 32,-2
MV 116,104,56
MV 116,108,60
TR 100,2,31
MV 120,104,64
TR 102,3,31
MV 120,108,68
MV 124,104,72
LA 36,-1
MV 124,108,76
MV 128,104,80
MV 128,108,84
MV 132,104,88
MV 132,108,92
TR 104,4,30
MV 156,112,88
TR 106,5,30
MV 156,116,92
LA 40,0
MV 152,112,80
MV 152,116,84
TR 108,4,31
)"
R"(MV 148,112,72
TR 110,5,31
MV 148,116,76
MV 144,112,64
LA 44,1
MV 144,116,68
MV 140,112,56
MV 140,116,60
MV 136,112,48
MV 136,116,52
WL 4
TR 112,2,32
MV 160,96,48
TR 114,3,32
MV 160,100,52
DH 2
MV 164,96,56
MV 164,100,60
TR 116,2,33
MV 168,96,64
TR 118,3,33
MV 168,100,68
s_add_u32 m0, %[hm], 24576
v_xor_b32 v1, 0x300, v1
MV 172,96,72
s_add_u32 s40, s64, 524288
s_addc_u32 s41, s65, 0
MV 172,100,76
DH 0
MV 176,96,80
MV 176,100,84
MV 180,96,88
MV 180,100,92
WL 4
TR 96,4,32
MA 164,104,88
TR 98,5,32
MA 164,108,92
DH 2
MA 160,104,80
MA 160,108,84
TR 100,4,33
MA 156,104,72
s_add_u32 m0, %[hm], 28672
TR 102,5,33
MA 156,108,76
s_add_u32 s40, s64, 528384
s_addc_u32 s41, s65, 0
MA 152,104,64
DH 0
MA 152,108,68
MA 148,104,56
MA 148,108,60
s_add_u32 m0, %[wm], 0
MA 144,104,48
s_add_u32 s40, s88, 393216
MA 144,108,52
WL 4
TR 104,2,34
MV 184,112,48
TR 106,3,34
MV 184,116,52
s_add_u32 s40, s40, s68
s_addc_u32 s41, s69, 0
MV 188,112,56
DX 0
MV 188,116,60
TR 108,2,35
MV 192,112,64
TR 110,3,35
MV 192,116,68
MV 196,112,72
MV 196,116,76
DX 1
MV 200,112,80
MV 200,116,84
MV 204,112,88
MV 204,116,92
WL 4
TR 112,4,34
MV 228,96,88
TR 114,5,34
MV 228,100,92
DX 2
MV 224,96,80
MV 224,100,84
TR 116,4,35
MV 220,96,72
TR 118,5,35
MV 220,100,76
MV 216,96,64
ST 24,0
MV 216,100,68
MV 212,96,56
MV 212,100,60
WL 0
s_barrier
RV 24,23,1024
MV 208,96,48
s_add_u32 m0, %[hm], 30720
MV 208,100,52
TR 96,2,0
MV 232,104,48
TR 98,3,0
MV 232,108,52
s_add_u32 s40, s64, 530432
s_addc_u32 s41, s65, 0
MV 236,104,56
DH 0
MV 236,108,60
TR 100,2,1
MV 240,104,64
TR 102,3,1
MV 240,108,68
s_add_u32 m0, %[hm], 32768
MV 244,104,72
s_add_u32 s40, s66, 524288
s_addc_u32 s41, s67, 0
MV 244,108,76
DH 0
MV 248,104,80
MV 248,108,84
MV 252,104,88
MV 252,108,92
TR 104,4,0
MA 188,112,88
TR 106,5,0
MA 188,116,92
WL 6
ST 24,1
MA 184,112,80
MA 184,116,84
TR 108,4,1
MA 180,112,72
TR 110,5,1
MA 180,116,76
MA 176,112,64
MA 176,116,68
MA 172,112,56
MA 172,116,60
MA 168,112,48
WV 11
MA 168,116,52
WL 4
TR 112,2,2
MV 40,96,0
TR 114,3,2
MV 40,100,4
s_add_u32 s44, s84, 249856
s_addc_u32 s45, s85, 0
MV 44,96,8
LA 48,-4
MV 44,100,12
TR 116,2,3
MV 48,96,16
TR 118,3,3
MV 48,100,20
MV 52,96,24
MV 52,100,28
LA 52,-3
MV 56,96,32
MV 56,100,36
MV 60,96,40
MV 60,100,44
WL 4
TR 96,4,2
MV 84,104,40
TR 98,5,2
MV 84,108,44
LA 56,-2
MV 80,104,32
MV 80,108,36
TR 100,4,3
MV 76,104,24
TR 102,5,3
MV 76,108,28
MV 72,104,16
LA 60,-1
MV 72,108,20
MV 68,104,8
MV 68,108,12
MV 64,104,0
MV 64,108,4
WL 4
TR 104,2,4
MV 88,112,0
TR 106,3,4
MV 88,116,4
LA 64,0
MV 92,112,8
MV 92,116,12
TR 108,2,5
MV 96,112,16
TR 110,3,5
MV 96,116,20
RV 24,23,2048
MV 100,112,24
LA 68,1
MV 100,116,28
MV 104,112,32
MV 104,116,36
s_add_u32 s44, s44, 6144
s_addc_u32 s45, s45, 0
MV 108,112,40
MV 108,116,44
WL 5
TR 112,4,4
MA 140,96,40
TR 114,5,4
MA 140,100,44
LA 72,-4
MA 136,96,32
MA 136,100,36
TR 116,4,5
MA 132,96,24
TR 118,5,5
MA 132,100,28
MA 128,96,16
LA 76,-3
MA 128,100,20
MA 124,96,8
MA 124,100,12
WL 0
s_barrier
v_add_u32 v21, 0xf000, v18
v_add_u32 v22, 0xf000, v19
MA 120,96,0
ds_write_b64 v21, a[228:229] offset:0
MA 120,100,4
TR 96,2,6
MV 112,104,0
TR 98,3,6
MV 112,108,4
LA 80,-2
MV 116,104,8
ds_write_b32 v22, a236 offset:0
MV 116,108,12
TR 100,2,7
MV 120,104,16
TR 102,3,7
MV 120,108,20
ds_write_b64 v21, a[230:231] offset:768
MV 124,104,24
LA 84,-1
MV 124,108,28
ds_write_b32 v22, a237 offset:768
MV 128,104,32
MV 128,108,36
ds_write_b64 v21, a[232:233] offset:1536
MV 132,104,40
MV 132,108,44
TR 104,4,6
MV 156,112,40
TR 106,5,6
MV 156,116,44
LA 88,0
MV 152,112,32
ds_write_b32 v22, a238 offset:1536
MV 152,116,36
TR 108,4,7
MV 148,112,24
TR 110,5,7
MV 148,116,28
ds_write_b64 v21, a[234:235] offset:2304
MV 144,112,16
LA 92,1
MV 144,116,20
WL 9
ds_write_b32 v22, a239 offset:2304
MV 140,112,8
MV 140,116,12
MV 136,112,0
MV 136,116,4
TR 112,2,8
MV 160,96,0
TR 114,3,8
MV 160,100,4
DH 2
MV 164,96,8
MV 164,100,12
TR 116,2,9
MV 168,96,16
TR 118,3,9
MV 168,100,20
s_add_u32 m0, %[hm], 0
v_xor_b32 v1, 0x100, v1
MV 172,96,24
s_add_u32 s40, s64, 524288
s_addc_u32 s41, s65, 0
MV 172,100,28
DH 0
MV 176,96,32
MV 176,100,36
MV 180,96,40
MV 180,100,44
WL 6
TR 96,4,8
MA 164,104,40
TR 98,5,8
MA 164,108,44
DH 2
MA 160,104,32
MA 160,108,36
TR 100,4,9
MA 156,104,24
s_add_u32 m0, %[hm], 4096
TR 102,5,9
MA 156,108,28
s_add_u32 s40, s64, 528384
s_addc_u32 s41, s65, 0
MA 152,104,16
DH 0
MA 152,108,20
MA 148,104,8
MA 148,108,12
s_add_u32 m0, %[wm], 12288
MA 144,104,0
s_add_u32 s40, s88, 491520
MA 144,108,4
WL 4
TR 104,2,10
MV 184,112,0
TR 106,3,10
MV 184,116,4
s_add_u32 s40, s40, s68
s_addc_u32 s41, s69, 0
MV 188,112,8
DX 0
MV 188,116,12
TR 108,2,11
MV 192,112,16
TR 110,3,11
MV 192,116,20
MV 196,112,24
MV 196,116,28
DX 1
MV 200,112,32
MV 200,116,36
MV 204,112,40
MV 204,116,44
WL 4
TR 112,4,10
MV 228,96,40
TR 114,5,10
MV 228,100,44
DX 2
MV 224,96,32
MV 224,100,36
TR 116,4,11
MV 220,96,24
TR 118,5,11
MV 220,100,28
MV 216,96,16
ST 24,2
MV 216,100,20
MV 212,96,8
MV 212,100,12
v_add_u32 v23, 0xf000, v20
MV 208,96,0
RV 24,23,12288
MV 208,100,4
WL 5
TR 96,2,12
MV 232,104,0
TR 98,3,12
MV 232,108,4
s_add_u32 s42, s88, 245760
MV 236,104,8
s_add_u32 s42, s42, s72
s_addc_u32 s43, s73, 0
MV 236,108,12
TR 100,2,13
MV 240,104,16
TR 102,3,13
MV 240,108,20
WL 4
ST 24,0
MV 244,104,24
MV 244,108,28
MV 248,104,32
MV 248,108,36
MV 252,104,40
MV 252,108,44
TR 104,4,12
MA 188,112,40
TR 106,5,12
MA 188,116,44
MA 184,112,32
MA 184,116,36
TR 108,4,13
MA 180,112,24
TR 110,5,13
MA 180,116,28
MA 176,112,16
MA 176,116,20
MA 172,112,8
MA 172,116,12
MA 168,112,0
WV 9
MA 168,116,4
)"
R"(WL 4
TR 112,2,14
MV 40,96,48
TR 114,3,14
MV 40,100,52
s_add_u32 s44, s84, 299008
s_addc_u32 s45, s85, 0
MV 44,96,56
LA 0,-4
MV 44,100,60
TR 116,2,15
MV 48,96,64
TR 118,3,15
MV 48,100,68
MV 52,96,72
MV 52,100,76
LA 4,-3
MV 56,96,80
MV 56,100,84
MV 60,96,88
MV 60,100,92
WL 4
TR 96,4,14
MV 84,104,88
TR 98,5,14
MV 84,108,92
LA 8,-2
MV 80,104,80
MV 80,108,84
TR 100,4,15
MV 76,104,72
TR 102,5,15
MV 76,108,76
MV 72,104,64
LA 12,-1
MV 72,108,68
MV 68,104,56
MV 68,108,60
.Lk2p30:
s_cbranch_execz .Lk2r6
MV 64,104,48
MV 64,108,52
WL 4
TR 104,2,16
MV 88,112,48
TR 106,3,16
MV 88,116,52
LA 16,0
MV 92,112,56
MV 92,116,60
TR 108,2,17
MV 96,112,64
TR 110,3,17
MV 96,116,68
MV 100,112,72
LA 20,1
MV 100,116,76
MV 104,112,80
MV 104,116,84
s_add_u32 s44, s44, 6144
s_addc_u32 s45, s45, 0
MV 108,112,88
MV 108,116,92
WL 4
TR 112,4,16
MA 140,96,88
TR 114,5,16
MA 140,100,92
LA 24,-4
MA 136,96,80
MA 136,100,84
TR 116,4,17
MA 132,96,72
TR 118,5,17
MA 132,100,76
MA 128,96,64
LA 28,-3
MA 128,100,68
MA 124,96,56
MA 124,100,60
WL 0
s_barrier
ds_read_b64 v[12:13], v18 offset:0
MA 120,96,48
s_add_u32 m0, %[hm], 6144
ds_read_b32 v16, v19 offset:0
MA 120,100,52
TR 96,2,18
MV 112,104,48
TR 98,3,18
MV 112,108,52
LA 32,-2
MV 116,104,56
s_add_u32 s40, s64, 530432
s_addc_u32 s41, s65, 0
MV 116,108,60
TR 100,2,19
MV 120,104,64
TR 102,3,19
MV 120,108,68
ds_read_b64 v[14:15], v18 offset:768
MV 124,104,72
LA 36,-1
MV 124,108,76
ds_read_b32 v17, v19 offset:768
MV 128,104,80
MV 128,108,84
MV 132,104,88
MV 132,108,92
TR 104,4,18
MV 156,112,88
WL 7
TR 106,5,18
UL 6,12
MV 156,116,92
LA 40,0
MV 152,112,80
UH 7,12
UL 8,13
MV 152,116,84
TR 108,4,19
AD 40,6
MV 148,112,72
TR 110,5,19
AD 44,7
MV 148,116,76
RV 24,23,13312
AD 48,8
MV 144,112,64
LA 44,1
MV 144,116,68
UH 6,13
UL 7,16
MV 140,112,56
UH 8,16
MV 140,116,60
ds_read_b64 v[12:13], v18 offset:1536
MV 136,112,48
ds_read_b32 v16, v19 offset:1536
AD 52,6
MV 136,116,52
WL 8
TR 112,2,20
AD 56,7
MV 160,96,48
TR 114,3,20
AD 60,8
MV 160,100,52
DH 0
MV 164,96,56
WL 8
UL 6,14
UH 7,14
MV 164,100,60
TR 116,2,21
UL 8,15
MV 168,96,64
s_add_u32 m0, %[hm], 8192
TR 118,3,21
AD 41,6
MV 168,100,68
s_add_u32 s40, s66, 524288
s_addc_u32 s41, s67, 0
AD 45,7
MV 172,96,72
DH 0
MV 172,100,76
AD 49,8
UH 6,15
MV 176,96,80
UL 7,17
UH 8,17
MV 176,100,84
ds_read_b64 v[14:15], v18 offset:2304
MV 180,96,88
ds_read_b32 v17, v19 offset:2304
AD 53,6
MV 180,100,92
WL 6
TR 96,4,20
AD 57,7
MA 164,104,88
TR 98,5,20
AD 61,8
MA 164,108,92
DH 2
MA 160,104,80
UL 6,12
UH 7,12
MA 160,108,84
TR 100,4,21
UL 8,13
MA 156,104,72
PA 20480
TR 102,5,21
AD 42,6
MA 156,108,76
AD 46,7
AD 50,8
MA 152,104,64
DF
MA 152,108,68
s_bitcmp1_b32 s48, 5
s_cselect_b64 s[64:65], s[58:59], s[56:57]
UH 6,13
MA 148,104,56
s_bitcmp1_b32 s48, 21
s_cselect_b32 s40, 16384, 0
UL 7,16
MA 148,108,60
s_add_u32 s64, s64, s40
s_addc_u32 s65, s65, 0
UH 8,16
MA 144,104,48
s_add_u32 m0, %[hm], 12288
v_xor_b32 v1, 0x700, v1
AD 54,6
MA 144,108,52
WL 3
TR 104,2,22
AD 58,7
MV 184,112,48
TR 106,3,22
AD 62,8
MV 184,116,52
s_add_u32 s40, s64, 1310720
s_addc_u32 s41, s65, 0
UL 6,14
MV 188,112,56
DH 0
MV 188,116,60
TR 108,2,23
UH 7,14
MV 192,112,64
TR 110,3,23
UL 8,15
MV 192,116,68
AD 43,6
AD 47,7
MV 196,112,72
AD 51,8
UH 6,15
MV 196,116,76
DH 2
MV 200,112,80
UL 7,17
UH 8,17
MV 200,116,84
AD 55,6
AD 59,7
MV 204,112,88
s_add_u32 m0, %[hm], 16384
AD 63,8
MV 204,116,92
WL 4
TR 112,4,22
MV 228,96,88
TR 114,5,22
MV 228,100,92
s_add_u32 s40, s64, 1314816
s_addc_u32 s41, s65, 0
MV 224,96,80
DH 0
MV 224,100,84
TR 116,4,23
MV 220,96,72
TR 118,5,23
MV 220,100,76
MV 216,96,64
MV 216,100,68
ST 24,1
MV 212,96,56
MV 212,100,60
MV 208,96,48
MV 208,100,52
WL 4
TR 96,2,24
MV 232,104,48
TR 98,3,24
MV 232,108,52
MV 236,104,56
MV 236,108,60
TR 100,2,25
MV 240,104,64
TR 102,3,25
MV 240,108,68
MV 244,104,72
MV 244,108,76
MV 248,104,80
MV 248,108,84
MV 252,104,88
MV 252,108,92
WL 4
TR 104,4,24
MA 188,112,88
TR 106,5,24
MA 188,116,92
MA 184,112,80
MA 184,116,84
TR 108,4,25
MA 180,112,72
TR 110,5,25
MA 180,116,76
MA 176,112,64
MA 176,116,68
MA 172,112,56
MA 172,116,60
MA 168,112,48
WV 8
MA 168,116,52
WL 4
TR 112,2,26
MV 40,96,0
TR 114,3,26
MV 40,100,4
s_add_u32 s44, s84, 348160
s_addc_u32 s45, s85, 0
MV 44,96,8
LA 48,-4
MV 44,100,12
TR 116,2,27
MV 48,96,16
TR 118,3,27
MV 48,100,20
MV 52,96,24
MV 52,100,28
LA 52,-3
MV 56,96,32
MV 56,100,36
RV 24,23,14336
MV 60,96,40
MV 60,100,44
WL 5
TR 96,4,26
MV 84,104,40
TR 98,5,26
MV 84,108,44
LA 56,-2
MV 80,104,32
MV 80,108,36
TR 100,4,27
MV 76,104,24
TR 102,5,27
MV 76,108,28
MV 72,104,16
LA 60,-1
MV 72,108,20
MV 68,104,8
MV 68,108,12
MV 64,104,0
MV 64,108,4
WL 5
TR 104,2,28
MV 88,112,0
TR 106,3,28
MV 88,116,4
LA 64,0
MV 92,112,8
MV 92,116,12
TR 108,2,29
MV 96,112,16
TR 110,3,29
MV 96,116,20
MV 100,112,24
LA 68,1
MV 100,116,28
MV 104,112,32
MV 104,116,36
s_add_u32 s44, s44, 6144
s_addc_u32 s45, s45, 0
MV 108,112,40
MV 108,116,44
WL 4
TR 112,4,28
MA 140,96,40
TR 114,5,28
MA 140,100,44
LA 72,-4
MA 136,96,32
MA 136,100,36
TR 116,4,29
MA 132,96,24
TR 118,5,29
MA 132,100,28
MA 128,96,16
LA 76,-3
MA 128,100,20
MA 124,96,8
MA 124,100,12
WL 0
s_barrier
v_add_u32 v21, 0xf000, v18
v_add_u32 v22, 0xf000, v19
MA 120,96,0
s_add_u32 m0, %[hm], 18432
ds_write_b64 v21, a[240:241] offset:12288
MA 120,100,4
TR 96,2,30
MV 112,104,0
TR 98,3,30
)"
R"(MV 112,108,4
LA 80,-2
MV 116,104,8
s_add_u32 s40, s64, 1316864
s_addc_u32 s41, s65, 0
MV 116,108,12
TR 100,2,31
MV 120,104,16
TR 102,3,31
MV 120,108,20
ds_write_b32 v22, a248 offset:12288
MV 124,104,24
LA 84,-1
MV 124,108,28
ds_write_b64 v21, a[242:243] offset:13056
MV 128,104,32
ds_read_b64 v[12:13], v18 offset:12288
MV 128,108,36
ds_write_b32 v22, a249 offset:13056
MV 132,104,40
ds_read_b32 v16, v19 offset:12288
MV 132,108,44
TR 104,4,30
MV 156,112,40
TR 106,5,30
MV 156,116,44
LA 88,0
MV 152,112,32
ds_write_b64 v21, a[244:245] offset:13824
MV 152,116,36
TR 108,4,31
MV 148,112,24
TR 110,5,31
MV 148,116,28
WL 9
ds_write_b32 v22, a250 offset:13824
MV 144,112,16
LA 92,1
MV 144,116,20
ds_write_b64 v21, a[246:247] offset:14592
MV 140,112,8
ds_read_b64 v[14:15], v18 offset:13056
MV 140,116,12
ds_write_b32 v22, a251 offset:14592
MV 136,112,0
ds_read_b32 v17, v19 offset:13056
MV 136,116,4
TR 112,2,32
MV 160,96,0
WL 9
TR 114,3,32
MV 160,100,4
DH 0
MV 164,96,8
MV 164,100,12
TR 116,2,33
UL 6,12
MV 168,96,16
TR 118,3,33
UH 7,12
MV 168,100,20
s_bitcmp1_b32 s49, 5
s_cselect_b64 s[66:67], s[62:63], s[60:61]
UL 8,13
MV 172,96,24
s_add_u32 m0, %[hm], 20480
AD 64,6
AD 68,7
MV 172,100,28
s_add_u32 s40, s66, 1310720
s_addc_u32 s41, s67, 0
AD 72,8
MV 176,96,32
DH 0
MV 176,100,36
UH 6,13
UL 7,16
MV 180,96,40
UH 8,16
MV 180,100,44
WL 6
TR 96,4,32
MA 164,104,40
TR 98,5,32
MA 164,108,44
DH 2
MA 160,104,32
ds_read_b64 v[12:13], v18 offset:13824
MA 160,108,36
TR 100,4,33
MA 156,104,24
TR 102,5,33
MA 156,108,28
s_add_u32 m0, %[hm], 24576
v_xor_b32 v1, 0x100, v1
MA 152,104,16
s_add_u32 s40, s64, 1310720
s_addc_u32 s41, s65, 0
MA 152,108,20
DH 0
MA 148,104,8
ds_read_b32 v16, v19 offset:13824
AD 76,6
MA 148,108,12
AD 80,7
AD 84,8
MA 144,104,0
WL 6
UL 6,14
UH 7,14
MA 144,108,4
TR 104,2,34
UL 8,15
MV 184,112,0
TR 106,3,34
AD 65,6
MV 184,116,4
DH 2
MV 188,112,8
AD 69,7
AD 73,8
MV 188,116,12
TR 108,2,35
UH 6,15
MV 192,112,16
s_add_u32 m0, %[hm], 28672
TR 110,3,35
UL 7,17
MV 192,116,20
s_add_u32 s40, s64, 1314816
s_addc_u32 s41, s65, 0
UH 8,17
MV 196,112,24
DH 0
MV 196,116,28
ds_read_b64 v[14:15], v18 offset:14592
MV 200,112,32
WL 5
ds_read_b32 v17, v19 offset:14592
AD 77,6
MV 200,116,36
s_add_u32 m0, %[wm], 0
AD 81,7
AD 85,8
MV 204,112,40
s_add_u32 s40, s89, 393216
UL 6,12
UH 7,12
MV 204,116,44
TR 112,4,34
UL 8,13
MV 228,96,40
TR 114,5,34
AD 66,6
MV 228,100,44
s_add_u32 s40, s40, s68
s_addc_u32 s41, s69, 0
AD 70,7
MV 224,96,32
DX 0
MV 224,100,36
TR 116,4,35
AD 74,8
MV 220,96,24
TR 118,5,35
UH 6,13
MV 220,100,28
UL 7,16
UH 8,16
MV 216,96,16
AD 78,6
AD 82,7
MV 216,100,20
DX 1
MV 212,96,8
WL 4
AD 86,8
UL 6,14
MV 212,100,12
UH 7,14
UL 8,15
MV 208,96,0
AD 67,6
AD 71,7
MV 208,100,4
TR 96,2,0
AD 75,8
MV 232,104,0
TR 98,3,0
UH 6,15
MV 232,108,4
DX 2
MV 236,104,8
UL 7,17
UH 8,17
MV 236,108,12
TR 100,2,1
AD 79,6
MV 240,104,16
TR 102,3,1
AD 83,7
MV 240,108,20
AD 87,8
MV 244,104,24
ST 24,2
MV 244,108,28
MV 248,104,32
RV 24,20,24576
MV 248,108,36
MV 252,104,40
MV 252,108,44
WL 5
TR 104,4,0
MA 188,112,40
s_add_u32 s42, s89, 245760
TR 106,5,0
MA 188,116,44
s_add_u32 s42, s42, s72
s_addc_u32 s43, s73, 0
MA 184,112,32
MA 184,116,36
.Lk2p31:
s_cbranch_execz .Lk2r6
TR 108,4,1
MA 180,112,24
TR 110,5,1
MA 180,116,28
WL 4
ST 24,0
MA 176,112,16
MA 176,116,20
MA 172,112,8
MA 172,116,12
MA 168,112,0
MA 168,116,4
WV 11
TR 112,2,2
MV 40,96,48
TR 114,3,2
MV 40,100,52
s_add_u32 s44, s84, 397312
s_addc_u32 s45, s85, 0
MV 44,96,56
LA 0,-4
MV 44,100,60
TR 116,2,3
MV 48,96,64
TR 118,3,3
MV 48,100,68
MV 52,96,72
MV 52,100,76
LA 4,-3
MV 56,96,80
MV 56,100,84
MV 60,96,88
MV 60,100,92
WL 4
TR 96,4,2
MV 84,104,88
TR 98,5,2
MV 84,108,92
LA 8,-2
MV 80,104,80
MV 80,108,84
TR 100,4,3
MV 76,104,72
TR 102,5,3
MV 76,108,76
MV 72,104,64
LA 12,-1
MV 72,108,68
MV 68,104,56
MV 68,108,60
MV 64,104,48
MV 64,108,52
WL 4
TR 104,2,4
MV 88,112,48
TR 106,3,4
MV 88,116,52
LA 16,0
MV 92,112,56
MV 92,116,60
TR 108,2,5
MV 96,112,64
TR 110,3,5
MV 96,116,68
MV 100,112,72
LA 20,1
MV 100,116,76
MV 104,112,80
MV 104,116,84
s_add_u32 s44, s44, 6144
s_addc_u32 s45, s45, 0
MV 108,112,88
MV 108,116,92
WL 4
TR 112,4,4
MA 140,96,88
TR 114,5,4
MA 140,100,92
LA 24,-4
MA 136,96,80
MA 136,100,84
TR 116,4,5
MA 132,96,72
TR 118,5,5
MA 132,100,76
MA 128,96,64
LA 28,-3
MA 128,100,68
MA 124,96,56
MA 124,100,60
WL 0
s_barrier
RV 24,20,25600
MA 120,96,48
s_add_u32 m0, %[hm], 30720
MA 120,100,52
TR 96,2,6
MV 112,104,48
TR 98,3,6
MV 112,108,52
LA 32,-2
MV 116,104,56
s_add_u32 s40, s64, 1316864
s_addc_u32 s41, s65, 0
MV 116,108,60
TR 100,2,7
MV 120,104,64
TR 102,3,7
MV 120,108,68
MV 124,104,72
LA 36,-1
MV 124,108,76
MV 128,104,80
MV 128,108,84
MV 132,104,88
MV 132,108,92
TR 104,4,6
MV 156,112,88
TR 106,5,6
MV 156,116,92
LA 40,0
MV 152,112,80
MV 152,116,84
TR 108,4,7
MV 148,112,72
TR 110,5,7
MV 148,116,76
MV 144,112,64
LA 44,1
MV 144,116,68
MV 140,112,56
MV 140,116,60
MV 136,112,48
MV 136,116,52
WL 4
TR 112,2,8
MV 160,96,48
TR 114,3,8
MV 160,100,52
DH 0
MV 164,96,56
MV 164,100,60
TR 116,2,9
MV 168,96,64
s_add_u32 m0, %[hm], 32768
TR 118,3,9
MV 168,100,68
s_add_u32 s40, s66, 1310720
s_addc_u32 s41, s67, 0
MV 172,96,72
DH 0
MV 172,100,76
MV 176,96,80
MV 176,100,84
MV 180,96,88
MV 180,100,92
WL 4
TR 96,4,8
MA 164,104,88
TR 98,5,8
MA 164,108,92
DH 2
MA 160,104,80
MA 160,108,84
TR 100,4,9
MA 156,104,72
TR 102,5,9
MA 156,108,76
s_add_u32 m0, %[hm], 0
v_xor_b32 v1, 0x300, v1
MA 152,104,64
s_add_u32 s40, s64, 1310720
s_addc_u32 s41, s65, 0
MA 152,108,68
DH 0
MA 148,104,56
MA 148,108,60
)"
R"(MA 144,104,48
MA 144,108,52
WL 4
TR 104,2,10
MV 184,112,48
TR 106,3,10
MV 184,116,52
DH 2
MV 188,112,56
MV 188,116,60
TR 108,2,11
MV 192,112,64
s_add_u32 m0, %[hm], 4096
TR 110,3,11
MV 192,116,68
s_add_u32 s40, s64, 1314816
s_addc_u32 s41, s65, 0
MV 196,112,72
DH 0
MV 196,116,76
MV 200,112,80
MV 200,116,84
s_add_u32 m0, %[wm], 12288
MV 204,112,88
s_add_u32 s40, s89, 491520
MV 204,116,92
WL 4
TR 112,4,10
MV 228,96,88
TR 114,5,10
MV 228,100,92
s_add_u32 s40, s40, s68
s_addc_u32 s41, s69, 0
MV 224,96,80
DX 0
MV 224,100,84
TR 116,4,11
MV 220,96,72
TR 118,5,11
MV 220,100,76
MV 216,96,64
MV 216,100,68
DX 1
MV 212,96,56
MV 212,100,60
MV 208,96,48
MV 208,100,52
WL 4
TR 96,2,12
MV 232,104,48
TR 98,3,12
MV 232,108,52
DX 2
MV 236,104,56
MV 236,108,60
TR 100,2,13
MV 240,104,64
TR 102,3,13
MV 240,108,68
MV 244,104,72
ST 24,1
MV 244,108,76
MV 248,104,80
MV 248,108,84
MV 252,104,88
MV 252,108,92
WL 4
TR 104,4,12
MA 188,112,88
TR 106,5,12
MA 188,116,92
MA 184,112,80
MA 184,116,84
TR 108,4,13
MA 180,112,72
TR 110,5,13
MA 180,116,76
MA 176,112,64
RV 24,20,26624
MA 176,116,68
MA 172,112,56
MA 172,116,60
MA 168,112,48
WV 10
MA 168,116,52
WL 5
TR 112,2,14
MV 40,96,0
TR 114,3,14
MV 40,100,4
s_add_u32 s44, s84, 446464
s_addc_u32 s45, s85, 0
MV 44,96,8
LA 48,-4
MV 44,100,12
TR 116,2,15
MV 48,96,16
TR 118,3,15
MV 48,100,20
MV 52,96,24
MV 52,100,28
LA 52,-3
MV 56,96,32
MV 56,100,36
MV 60,96,40
MV 60,100,44
WL 5
TR 96,4,14
MV 84,104,40
TR 98,5,14
MV 84,108,44
LA 56,-2
MV 80,104,32
MV 80,108,36
TR 100,4,15
MV 76,104,24
TR 102,5,15
MV 76,108,28
MV 72,104,16
LA 60,-1
MV 72,108,20
MV 68,104,8
MV 68,108,12
MV 64,104,0
MV 64,108,4
WL 4
TR 104,2,16
MV 88,112,0
TR 106,3,16
MV 88,116,4
LA 64,0
MV 92,112,8
MV 92,116,12
TR 108,2,17
MV 96,112,16
TR 110,3,17
MV 96,116,20
MV 100,112,24
LA 68,1
MV 100,116,28
MV 104,112,32
MV 104,116,36
s_add_u32 s44, s44, 6144
s_addc_u32 s45, s45, 0
MV 108,112,40
MV 108,116,44
WL 4
TR 112,4,16
MA 140,96,40
TR 114,5,16
MA 140,100,44
LA 72,-4
MA 136,96,32
MA 136,100,36
TR 116,4,17
MA 132,96,24
TR 118,5,17
MA 132,100,28
MA 128,96,16
LA 76,-3
MA 128,100,20
MA 124,96,8
MA 124,100,12
WL 0
s_barrier
MA 120,96,0
s_add_u32 m0, %[hm], 6144
ds_write_b64 v18, a[192:193] offset:24576
MA 120,100,4
TR 96,2,18
MV 112,104,0
TR 98,3,18
MV 112,108,4
LA 80,-2
MV 116,104,8
s_add_u32 s40, s64, 1316864
s_addc_u32 s41, s65, 0
MV 116,108,12
TR 100,2,19
MV 120,104,16
TR 102,3,19
MV 120,108,20
ds_write_b32 v19, a200 offset:24576
MV 124,104,24
LA 84,-1
MV 124,108,28
ds_write_b64 v18, a[194:195] offset:25344
MV 128,104,32
ds_read_b64 v[12:13], v18 offset:0
MV 128,108,36
ds_write_b32 v19, a201 offset:25344
MV 132,104,40
ds_read_b32 v16, v19 offset:0
MV 132,108,44
TR 104,4,18
MV 156,112,40
TR 106,5,18
MV 156,116,44
LA 88,0
MV 152,112,32
ds_write_b64 v18, a[196:197] offset:26112
MV 152,116,36
TR 108,4,19
MV 148,112,24
TR 110,5,19
MV 148,116,28
WL 9
ds_write_b32 v19, a202 offset:26112
MV 144,112,16
LA 92,1
MV 144,116,20
ds_write_b64 v18, a[198:199] offset:26880
MV 140,112,8
ds_read_b64 v[14:15], v18 offset:768
MV 140,116,12
ds_write_b32 v19, a203 offset:26880
MV 136,112,0
ds_read_b32 v17, v19 offset:768
MV 136,116,4
TR 112,2,20
MV 160,96,0
WL 9
TR 114,3,20
MV 160,100,4
DH 0
MV 164,96,8
MV 164,100,12
TR 116,2,21
UL 6,12
MV 168,96,16
s_add_u32 m0, %[hm], 8192
TR 118,3,21
UH 7,12
MV 168,100,20
s_add_u32 s40, s66, 1310720
s_addc_u32 s41, s67, 0
UL 8,13
MV 172,96,24
DH 0
MV 172,100,28
AD 88,6
AD 92,7
MV 176,96,32
AD 96,8
UH 6,13
MV 176,100,36
UL 7,16
UH 8,16
MV 180,96,40
ds_read_b64 v[12:13], v18 offset:1536
MV 180,100,44
WL 8
TR 96,4,20
MA 164,104,40
TR 98,5,20
MA 164,108,44
DH 2
MA 160,104,32
ds_read_b32 v16, v19 offset:1536
AD 100,6
MA 160,108,36
TR 100,4,21
AD 104,7
MA 156,104,24
WL 6
TR 102,5,21
AD 108,8
MA 156,108,28
s_add_u32 m0, %[hm], 12288
v_xor_b32 v1, 0x100, v1
UL 6,14
MA 152,104,16
s_add_u32 s40, s64, 1310720
s_addc_u32 s41, s65, 0
UH 7,14
MA 152,108,20
DH 0
MA 148,104,8
UL 8,15
AD 89,6
MA 148,108,12
AD 93,7
AD 97,8
MA 144,104,0
UH 6,15
UL 7,17
MA 144,108,4
WL 6
TR 104,2,22
UH 8,17
MV 184,112,0
TR 106,3,22
MV 184,116,4
DH 2
MV 188,112,8
ds_read_b64 v[14:15], v18 offset:2304
MV 188,116,12
TR 108,2,23
MV 192,112,16
s_add_u32 m0, %[hm], 16384
TR 110,3,23
MV 192,116,20
s_add_u32 s40, s64, 1314816
s_addc_u32 s41, s65, 0
MV 196,112,24
DH 0
MV 196,116,28
ds_read_b32 v17, v19 offset:2304
AD 101,6
MV 200,112,32
AD 105,7
AD 109,8
MV 200,116,36
WL 6
UL 6,12
UH 7,12
MV 204,112,40
UL 8,13
AD 90,6
MV 204,116,44
TR 112,4,22
AD 94,7
MV 228,96,40
TR 114,5,22
AD 98,8
MV 228,100,44
ST 24,2
MV 224,96,32
UH 6,13
UL 7,16
MV 224,100,36
TR 116,4,23
UH 8,16
MV 220,96,24
TR 118,5,23
AD 102,6
MV 220,100,28
RV 24,20,36864
AD 106,7
MV 216,96,16
WL 5
AD 110,8
UL 6,14
MV 216,100,20
UH 7,14
UL 8,15
MV 212,96,8
s_add_u32 s42, s88, 344064
AD 91,6
AD 95,7
MV 212,100,12
s_add_u32 s42, s42, s72
s_addc_u32 s43, s73, 0
AD 99,8
MV 208,96,0
UH 6,15
UL 7,17
MV 208,100,4
TR 96,2,24
UH 8,17
MV 232,104,0
TR 98,3,24
AD 103,6
MV 232,108,4
WL 2
ST 24,0
MV 236,104,8
AD 107,7
AD 111,8
MV 236,108,12
TR 100,2,25
MV 240,104,16
TR 102,3,25
MV 240,108,20
MV 244,104,24
MV 244,108,28
MV 248,104,32
MV 248,108,36
MV 252,104,40
MV 252,108,44
TR 104,4,24
MA 188,112,40
.Lk2p32:
s_cbranch_execz .Lk2r6
TR 106,5,24
MA 188,116,44
MA 184,112,32
MA 184,116,36
TR 108,4,25
MA 180,112,24
TR 110,5,25
MA 180,116,28
MA 176,112,16
MA 176,116,20
MA 172,112,8
MA 172,116,12
MA 168,112,0
WV 8
MA 168,116,4
WL 4
)"
R"(TR 112,2,26
MV 40,96,48
TR 114,3,26
MV 40,100,52
s_add_u32 s44, s84, 495616
s_addc_u32 s45, s85, 0
MV 44,96,56
LA 0,-4
MV 44,100,60
TR 116,2,27
MV 48,96,64
TR 118,3,27
MV 48,100,68
MV 52,96,72
MV 52,100,76
LA 4,-3
MV 56,96,80
MV 56,100,84
MV 60,96,88
MV 60,100,92
WL 4
TR 96,4,26
MV 84,104,88
TR 98,5,26
MV 84,108,92
LA 8,-2
MV 80,104,80
MV 80,108,84
TR 100,4,27
MV 76,104,72
TR 102,5,27
MV 76,108,76
MV 72,104,64
LA 12,-1
MV 72,108,68
MV 68,104,56
MV 68,108,60
MV 64,104,48
MV 64,108,52
WL 4
TR 104,2,28
MV 88,112,48
TR 106,3,28
MV 88,116,52
LA 16,0
MV 92,112,56
MV 92,116,60
TR 108,2,29
MV 96,112,64
TR 110,3,29
MV 96,116,68
RV 24,20,37888
MV 100,112,72
LA 20,1
MV 100,116,76
MV 104,112,80
MV 104,116,84
s_add_u32 s44, s44, 6144
s_addc_u32 s45, s45, 0
MV 108,112,88
MV 108,116,92
WL 5
TR 112,4,28
MA 140,96,88
TR 114,5,28
MA 140,100,92
LA 24,-4
MA 136,96,80
MA 136,100,84
TR 116,4,29
MA 132,96,72
TR 118,5,29
MA 132,100,76
MA 128,96,64
LA 28,-3
MA 128,100,68
MA 124,96,56
MA 124,100,60
WL 0
s_barrier
MA 120,96,48
s_add_u32 m0, %[hm], 18432
MA 120,100,52
TR 96,2,30
MV 112,104,48
TR 98,3,30
MV 112,108,52
LA 32,-2
MV 116,104,56
s_add_u32 s40, s64, 1316864
s_addc_u32 s41, s65, 0
MV 116,108,60
TR 100,2,31
MV 120,104,64
TR 102,3,31
MV 120,108,68
ds_read_b64 v[12:13], v18 offset:12288
MV 124,104,72
LA 36,-1
MV 124,108,76
ds_read_b32 v16, v19 offset:12288
MV 128,104,80
ds_read_b64 v[14:15], v18 offset:13056
MV 128,108,84
ds_read_b32 v17, v19 offset:13056
MV 132,104,88
MV 132,108,92
TR 104,4,30
MV 156,112,88
TR 106,5,30
MV 156,116,92
LA 40,0
MV 152,112,80
WL 4
UL 6,12
UH 7,12
MV 152,116,84
TR 108,4,31
UL 8,13
MV 148,112,72
TR 110,5,31
AR 9,120
MV 148,116,76
AR 10,124
AR 11,128
MV 144,112,64
LA 44,1
MV 144,116,68
AD 9,6
AD 10,7
MV 140,112,56
AD 11,8
AW 120,9
MV 140,116,60
AW 124,10
AW 128,11
MV 136,112,48
UH 6,13
UL 7,16
MV 136,116,52
TR 112,2,32
UH 8,16
MV 160,96,48
TR 114,3,32
MV 160,100,52
DH 0
MV 164,96,56
ds_read_b64 v[12:13], v18 offset:13824
MV 164,100,60
TR 116,2,33
MV 168,96,64
s_add_u32 m0, %[hm], 20480
TR 118,3,33
MV 168,100,68
s_add_u32 s40, s66, 1310720
s_addc_u32 s41, s67, 0
MV 172,96,72
DH 0
MV 172,100,76
ds_read_b32 v16, v19 offset:13824
AR 9,132
MV 176,96,80
AR 10,136
AR 11,140
MV 176,100,84
AD 9,6
AD 10,7
MV 180,96,88
AD 11,8
AW 132,9
MV 180,100,92
WL 6
TR 96,4,32
AW 136,10
MA 164,104,88
TR 98,5,32
AW 140,11
MA 164,108,92
DH 2
MA 160,104,80
UL 6,14
UH 7,14
MA 160,108,84
TR 100,4,33
UL 8,15
MA 156,104,72
PA 20480
TR 102,5,33
AR 9,121
MA 156,108,76
AR 10,125
AR 11,129
MA 152,104,64
DF
MA 152,108,68
s_add_u32 m0, %[hm], 24576
v_xor_b32 v1, 0x700, v1
AD 9,6
MA 148,104,56
s_add_u32 s40, s64, 1310720
s_addc_u32 s41, s65, 0
AD 10,7
MA 148,108,60
AD 11,8
AW 121,9
MA 144,104,48
AW 125,10
AW 129,11
MA 144,108,52
WL 3
TR 104,2,34
UH 6,15
MV 184,112,48
TR 106,3,34
UL 7,17
MV 184,116,52
DH 0
MV 188,112,56
UH 8,17
MV 188,116,60
TR 108,2,35
MV 192,112,64
TR 110,3,35
MV 192,116,68
ds_read_b64 v[14:15], v18 offset:14592
MV 196,112,72
DH 2
MV 196,116,76
ds_read_b32 v17, v19 offset:14592
AR 9,133
MV 200,112,80
AR 10,137
AR 11,141
MV 200,116,84
s_add_u32 m0, %[hm], 28672
AD 9,6
AD 10,7
MV 204,112,88
s_add_u32 s40, s64, 1314816
s_addc_u32 s41, s65, 0
AD 11,8
MV 204,116,92
WL 6
TR 112,4,34
AW 133,9
MV 228,96,88
TR 114,5,34
AW 137,10
MV 228,100,92
DH 0
MV 224,96,80
AW 141,11
UL 6,12
MV 224,100,84
TR 116,4,35
UH 7,12
MV 220,96,72
s_add_u32 m0, %[wm], 0
TR 118,5,35
UL 8,13
MV 220,100,76
s_add_u32 s40, s88, 442368
AR 9,122
AR 10,126
MV 216,96,64
s_add_u32 s40, s40, s68
s_addc_u32 s41, s69, 0
AR 11,130
MV 216,100,68
DX 0
MV 212,96,56
AD 9,6
AD 10,7
MV 212,100,60
AD 11,8
AW 122,9
MV 208,96,48
AW 126,10
AW 130,11
MV 208,100,52
WL 3
TR 96,2,0
UH 6,13
MV 232,104,48
TR 98,3,0
UL 7,16
MV 232,108,52
DX 1
MV 236,104,56
UH 8,16
AR 9,134
MV 236,108,60
TR 100,2,1
AR 10,138
MV 240,104,64
TR 102,3,1
AR 11,142
MV 240,108,68
AD 9,6
AD 10,7
MV 244,104,72
DX 2
MV 244,108,76
AD 11,8
AW 134,9
MV 248,104,80
AW 138,10
AW 142,11
MV 248,108,84
UL 6,14
UH 7,14
MV 252,104,88
UL 8,15
AR 9,123
MV 252,108,92
WL 4
TR 104,4,0
AR 10,127
MA 188,112,88
TR 106,5,0
AR 11,131
MA 188,116,92
ST 24,1
MA 184,112,80
AD 9,6
AD 10,7
MA 184,116,84
TR 108,4,1
AD 11,8
MA 180,112,72
TR 110,5,1
AW 123,9
MA 180,116,76
RV 24,20,38912
AW 127,10
MA 176,112,64
AW 131,11
UH 6,15
MA 176,116,68
UL 7,17
UH 8,17
MA 172,112,56
AR 9,135
AR 10,139
MA 172,116,60
AR 11,143
AD 9,6
MA 168,112,48
WV 11
AD 10,7
AD 11,8
MA 168,116,52
WL 5
TR 112,2,2
AW 135,9
MV 40,96,0
TR 114,3,2
AW 139,10
MV 40,100,4
s_add_u32 s44, s84, 544768
s_addc_u32 s45, s85, 0
AW 143,11
MV 44,96,8
LA 48,-4
MV 44,100,12
TR 116,2,3
MV 48,96,16
TR 118,3,3
MV 48,100,20
MV 52,96,24
MV 52,100,28
LA 52,-3
MV 56,96,32
MV 56,100,36
MV 60,96,40
MV 60,100,44
WL 4
TR 96,4,2
MV 84,104,40
TR 98,5,2
MV 84,108,44
LA 56,-2
MV 80,104,32
MV 80,108,36
TR 100,4,3
MV 76,104,24
TR 102,5,3
MV 76,108,28
MV 72,104,16
LA 60,-1
MV 72,108,20
MV 68,104,8
MV 68,108,12
MV 64,104,0
MV 64,108,4
WL 4
TR 104,2,4
MV 88,112,0
TR 106,3,4
MV 88,116,4
)"
R"(LA 64,0
MV 92,112,8
MV 92,116,12
TR 108,2,5
MV 96,112,16
TR 110,3,5
MV 96,116,20
MV 100,112,24
LA 68,1
MV 100,116,28
MV 104,112,32
MV 104,116,36
s_add_u32 s44, s44, 6144
s_addc_u32 s45, s45, 0
MV 108,112,40
MV 108,116,44
WL 4
TR 112,4,4
MA 140,96,40
TR 114,5,4
MA 140,100,44
LA 72,-4
MA 136,96,32
s_mov_b64 exec, 0
s_cmp_eq_u32 %[w], 0
s_cbranch_scc1 .Lk2p33
s_cmp_eq_u32 %[w], 1
s_cbranch_scc1 .Lk2p34
s_cmp_eq_u32 %[w], 2
s_cbranch_scc1 .Lk2p35
s_branch .Lk2p36
.Lk2r7:
s_mov_b64 exec, -1
MA 136,100,36
TR 116,4,5
MA 132,96,24
TR 118,5,5
MA 132,100,28
MA 128,96,16
LA 76,-3
MA 128,100,20
MA 124,96,8
MA 124,100,12
WL 0
s_barrier
MA 120,96,0
s_add_u32 m0, %[hm], 30720
ds_write_b64 v18, a[204:205] offset:36864
MA 120,100,4
TR 96,2,6
MV 112,104,0
TR 98,3,6
MV 112,108,4
LA 80,-2
MV 116,104,8
s_add_u32 s40, s64, 1316864
s_addc_u32 s41, s65, 0
MV 116,108,12
TR 100,2,7
MV 120,104,16
TR 102,3,7
MV 120,108,20
ds_write_b32 v19, a212 offset:36864
MV 124,104,24
LA 84,-1
MV 124,108,28
ds_write_b64 v18, a[206:207] offset:37632
MV 128,104,32
MV 128,108,36
ds_write_b32 v19, a213 offset:37632
MV 132,104,40
MV 132,108,44
TR 104,4,6
MV 156,112,40
TR 106,5,6
MV 156,116,44
LA 88,0
MV 152,112,32
ds_write_b64 v18, a[208:209] offset:38400
MV 152,116,36
TR 108,4,7
MV 148,112,24
TR 110,5,7
MV 148,116,28
ds_write_b32 v19, a214 offset:38400
MV 144,112,16
LA 92,1
MV 144,116,20
ds_write_b64 v18, a[210:211] offset:39168
MV 140,112,8
MV 140,116,12
WL 9
ds_write_b32 v19, a215 offset:39168
MV 136,112,0
MV 136,116,4
TR 112,2,8
MV 160,96,0
TR 114,3,8
MV 160,100,4
DH 0
MV 164,96,8
MV 164,100,12
TR 116,2,9
MV 168,96,16
s_add_u32 m0, %[hm], 32768
TR 118,3,9
MV 168,100,20
s_add_u32 s40, s66, 1310720
s_addc_u32 s41, s67, 0
MV 172,96,24
DH 0
MV 172,100,28
MV 176,96,32
MV 176,100,36
MV 180,96,40
MV 180,100,44
WL 7
TR 96,4,8
MA 164,104,40
TR 98,5,8
MA 164,108,44
DH 2
MA 160,104,32
MA 160,108,36
TR 100,4,9
MA 156,104,24
TR 102,5,9
MA 156,108,28
s_add_u32 m0, %[hm], 0
v_xor_b32 v1, 0x100, v1
MA 152,104,16
s_add_u32 s40, s64, 1310720
s_addc_u32 s41, s65, 0
MA 152,108,20
DH 0
MA 148,104,8
MA 148,108,12
MA 144,104,0
MA 144,108,4
WL 4
TR 104,2,10
MV 184,112,0
.Lk2p33:
s_cbranch_execz .Lk2r7
TR 106,3,10
MV 184,116,4
DH 2
MV 188,112,8
MV 188,116,12
TR 108,2,11
MV 192,112,16
s_add_u32 m0, %[hm], 4096
TR 110,3,11
MV 192,116,20
s_add_u32 s40, s64, 1314816
s_addc_u32 s41, s65, 0
MV 196,112,24
DH 0
MV 196,116,28
MV 200,112,32
MV 200,116,36
s_add_u32 m0, %[wm], 12288
MV 204,112,40
s_add_u32 s40, s88, 540672
MV 204,116,44
WL 4
TR 112,4,10
MV 228,96,40
TR 114,5,10
MV 228,100,44
s_add_u32 s40, s40, s68
s_addc_u32 s41, s69, 0
MV 224,96,32
DX 0
MV 224,100,36
TR 116,4,11
MV 220,96,24
TR 118,5,11
MV 220,100,28
MV 216,96,16
MV 216,100,20
DX 1
MV 212,96,8
MV 212,100,12
MV 208,96,0
MV 208,100,4
WL 4
TR 96,2,12
MV 232,104,0
TR 98,3,12
MV 232,108,4
DX 2
MV 236,104,8
MV 236,108,12
TR 100,2,13
MV 240,104,16
TR 102,3,13
MV 240,108,20
MV 244,104,24
ST 24,2
MV 244,108,28
MV 248,104,32
RV 24,20,49152
MV 248,108,36
MV 252,104,40
MV 252,108,44
WL 5
TR 104,4,12
MA 188,112,40
s_add_u32 s42, s89, 344064
TR 106,5,12
MA 188,116,44
s_add_u32 s42, s42, s72
s_addc_u32 s43, s73, 0
MA 184,112,32
MA 184,116,36
TR 108,4,13
MA 180,112,24
TR 110,5,13
MA 180,116,28
WL 4
ST 24,0
MA 176,112,16
MA 176,116,20
MA 172,112,8
MA 172,116,12
MA 168,112,0
MA 168,116,4
WV 11
TR 112,2,14
MV 40,96,48
TR 114,3,14
MV 40,100,52
s_add_u32 s44, s84, 593920
s_addc_u32 s45, s85, 0
MV 44,96,56
LA 0,-4
MV 44,100,60
TR 116,2,15
MV 48,96,64
TR 118,3,15
MV 48,100,68
MV 52,96,72
MV 52,100,76
LA 4,-3
MV 56,96,80
MV 56,100,84
RV 24,20,50176
MV 60,96,88
MV 60,100,92
WL 5
TR 96,4,14
MV 84,104,88
TR 98,5,14
MV 84,108,92
LA 8,-2
MV 80,104,80
MV 80,108,84
TR 100,4,15
MV 76,104,72
TR 102,5,15
MV 76,108,76
MV 72,104,64
LA 12,-1
MV 72,108,68
MV 68,104,56
MV 68,108,60
MV 64,104,48
MV 64,108,52
WL 5
TR 104,2,16
MV 88,112,48
TR 106,3,16
MV 88,116,52
LA 16,0
MV 92,112,56
MV 92,116,60
TR 108,2,17
MV 96,112,64
TR 110,3,17
MV 96,116,68
MV 100,112,72
LA 20,1
MV 100,116,76
MV 104,112,80
MV 104,116,84
s_add_u32 s44, s44, 6144
s_addc_u32 s45, s45, 0
MV 108,112,88
MV 108,116,92
WL 4
TR 112,4,16
MA 140,96,88
TR 114,5,16
MA 140,100,92
LA 24,-4
MA 136,96,80
MA 136,100,84
TR 116,4,17
MA 132,96,72
TR 118,5,17
MA 132,100,76
MA 128,96,64
LA 28,-3
MA 128,100,68
MA 124,96,56
MA 124,100,60
WL 0
s_barrier
MA 120,96,48
s_add_u32 m0, %[hm], 6144
MA 120,100,52
TR 96,2,18
MV 112,104,48
TR 98,3,18
MV 112,108,52
LA 32,-2
MV 116,104,56
s_add_u32 s40, s64, 1316864
s_addc_u32 s41, s65, 0
MV 116,108,60
TR 100,2,19
MV 120,104,64
TR 102,3,19
MV 120,108,68
MV 124,104,72
LA 36,-1
MV 124,108,76
MV 128,104,80
MV 128,108,84
MV 132,104,88
MV 132,108,92
TR 104,4,18
MV 156,112,88
TR 106,5,18
MV 156,116,92
LA 40,0
MV 152,112,80
ds_read_b64 v[12:13], v18 offset:0
MV 152,116,84
TR 108,4,19
MV 148,112,72
TR 110,5,19
MV 148,116,76
ds_read_b32 v16, v19 offset:0
MV 144,112,64
LA 44,1
MV 144,116,68
ds_read_b64 v[14:15], v18 offset:768
MV 140,112,56
ds_read_b32 v17, v19 offset:768
MV 140,116,60
MV 136,112,48
MV 136,116,52
WL 8
TR 112,2,20
MV 160,96,48
TR 114,3,20
MV 160,100,52
DH 0
MV 164,96,56
WL 4
UL 6,12
UH 7,12
MV 164,100,60
TR 116,2,21
UL 8,13
MV 168,96,64
s_add_u32 m0, %[hm], 8192
TR 118,3,21
AD 112,6
MV 168,100,68
s_add_u32 s40, s66, 1310720
s_addc_u32 s41, s67, 0
AD 116,7
MV 172,96,72
DH 0
MV 172,100,76
AD 120,8
UH 6,13
MV 176,96,80
UL 7,16
UH 8,16
MV 176,100,84
ds_read_b64 v[12:13], v18 offset:1536
MV 180,96,88
ds_read_b32 v16, v19 offset:1536
AD 124,6
MV 180,100,92
TR 96,4,20
AD 128,7
MA 164,104,88
TR 98,5,20
AD 132,8
MA 164,108,92
DH 2
MA 160,104,80
WL 6
UL 6,14
UH 7,14
)"
R"(MA 160,108,84
TR 100,4,21
UL 8,15
MA 156,104,72
TR 102,5,21
AD 113,6
MA 156,108,76
s_add_u32 m0, %[hm], 12288
v_xor_b32 v1, 0x300, v1
AD 117,7
MA 152,104,64
s_add_u32 s40, s64, 1310720
s_addc_u32 s41, s65, 0
AD 121,8
MA 152,108,68
DH 0
MA 148,104,56
UH 6,15
UL 7,17
MA 148,108,60
UH 8,17
MA 144,104,48
ds_read_b64 v[14:15], v18 offset:2304
MA 144,108,52
WL 6
TR 104,2,22
MV 184,112,48
TR 106,3,22
MV 184,116,52
DH 2
MV 188,112,56
ds_read_b32 v17, v19 offset:2304
AD 125,6
MV 188,116,60
WL 5
TR 108,2,23
AD 129,7
MV 192,112,64
s_add_u32 m0, %[hm], 16384
TR 110,3,23
AD 133,8
MV 192,116,68
s_add_u32 s40, s64, 1314816
s_addc_u32 s41, s65, 0
UL 6,12
MV 196,112,72
DH 0
MV 196,116,76
UH 7,12
UL 8,13
MV 200,112,80
AD 114,6
AD 118,7
MV 200,116,84
AD 122,8
UH 6,13
MV 204,112,88
UL 7,16
UH 8,16
MV 204,116,92
WL 6
TR 112,4,22
AD 126,6
MV 228,96,88
TR 114,5,22
AD 130,7
MV 228,100,92
ST 24,1
MV 224,96,80
WL 4
AD 134,8
UL 6,14
MV 224,100,84
TR 116,4,23
UH 7,14
MV 220,96,72
TR 118,5,23
UL 8,15
MV 220,100,76
RV 24,20,51200
AD 115,6
MV 216,96,64
AD 119,7
AD 123,8
MV 216,100,68
UH 6,15
UL 7,17
MV 212,96,56
UH 8,17
AD 127,6
MV 212,100,60
AD 131,7
AD 135,8
MV 208,96,48
MV 208,100,52
WL 5
TR 96,2,24
MV 232,104,48
TR 98,3,24
MV 232,108,52
WL 2
ST 24,2
MV 236,104,56
MV 236,108,60
TR 100,2,25
MV 240,104,64
TR 102,3,25
MV 240,108,68
MV 244,104,72
MV 244,108,76
MV 248,104,80
MV 248,108,84
MV 252,104,88
MV 252,108,92
TR 104,4,24
MA 188,112,88
TR 106,5,24
MA 188,116,92
MA 184,112,80
MA 184,116,84
TR 108,4,25
MA 180,112,72
TR 110,5,25
MA 180,116,76
MA 176,112,64
MA 176,116,68
MA 172,112,56
MA 172,116,60
MA 168,112,48
WV 8
MA 168,116,52
WL 4
TR 112,2,26
MV 40,96,0
TR 114,3,26
MV 40,100,4
s_add_u32 s44, s84, 643072
s_addc_u32 s45, s85, 0
MV 44,96,8
LA 48,-4
MV 44,100,12
TR 116,2,27
MV 48,96,16
TR 118,3,27
MV 48,100,20
MV 52,96,24
MV 52,100,28
LA 52,-3
MV 56,96,32
MV 56,100,36
MV 60,96,40
MV 60,100,44
WL 4
TR 96,4,26
MV 84,104,40
TR 98,5,26
MV 84,108,44
LA 56,-2
MV 80,104,32
MV 80,108,36
TR 100,4,27
MV 76,104,24
TR 102,5,27
MV 76,108,28
MV 72,104,16
LA 60,-1
MV 72,108,20
MV 68,104,8
MV 68,108,12
MV 64,104,0
MV 64,108,4
WL 4
TR 104,2,28
MV 88,112,0
TR 106,3,28
MV 88,116,4
LA 64,0
MV 92,112,8
MV 92,116,12
TR 108,2,29
MV 96,112,16
TR 110,3,29
MV 96,116,20
MV 100,112,24
LA 68,1
MV 100,116,28
MV 104,112,32
MV 104,116,36
s_add_u32 s44, s44, 6144
s_addc_u32 s45, s45, 0
MV 108,112,40
MV 108,116,44
WL 4
TR 112,4,28
MA 140,96,40
TR 114,5,28
MA 140,100,44
LA 72,-4
MA 136,96,32
MA 136,100,36
TR 116,4,29
MA 132,96,24
TR 118,5,29
MA 132,100,28
MA 128,96,16
LA 76,-3
MA 128,100,20
MA 124,96,8
MA 124,100,12
WL 0
s_barrier
v_add_u32 v23, 0xf000, v20
MA 120,96,0
s_add_u32 m0, %[hm], 18432
RV 24,23,0
MA 120,100,4
TR 96,2,30
MV 112,104,0
TR 98,3,30
MV 112,108,4
s_add_u32 s42, s90, 196608
LA 80,-2
MV 116,104,8
s_add_u32 s40, s64, 1316864
s_addc_u32 s41, s65, 0
MV 116,108,12
TR 100,2,31
MV 120,104,16
TR 102,3,31
MV 120,108,20
s_add_u32 s42, s42, s74
s_addc_u32 s43, s75, 0
MV 124,104,24
LA 84,-1
MV 124,108,28
MV 128,104,32
MV 128,108,36
MV 132,104,40
MV 132,108,44
TR 104,4,30
MV 156,112,40
TR 106,5,30
MV 156,116,44
LA 88,0
MV 152,112,32
MV 152,116,36
TR 108,4,31
MV 148,112,24
TR 110,5,31
MV 148,116,28
MV 144,112,16
LA 92,1
MV 144,116,20
MV 140,112,8
MV 140,116,12
MV 136,112,0
MV 136,116,4
WL 4
TR 112,2,32
MV 160,96,0
TR 114,3,32
MV 160,100,4
DH 0
MV 164,96,8
ds_read_b64 v[12:13], v18 offset:12288
MV 164,100,12
TR 116,2,33
MV 168,96,16
s_add_u32 m0, %[hm], 20480
TR 118,3,33
MV 168,100,20
s_add_u32 s40, s66, 1310720
s_addc_u32 s41, s67, 0
MV 172,96,24
DH 0
MV 172,100,28
ds_read_b32 v16, v19 offset:12288
MV 176,96,32
ds_read_b64 v[14:15], v18 offset:13056
MV 176,100,36
ds_read_b32 v17, v19 offset:13056
MV 180,96,40
MV 180,100,44
WL 8
TR 96,4,32
MA 164,104,40
TR 98,5,32
MA 164,108,44
DH 2
MA 160,104,32
WL 4
UL 6,12
UH 7,12
MA 160,108,36
TR 100,4,33
UL 8,13
MA 156,104,24
TR 102,5,33
AD 136,6
MA 156,108,28
s_add_u32 m0, %[hm], 24576
v_xor_b32 v1, 0x100, v1
AD 140,7
MA 152,104,16
s_add_u32 s40, s64, 1310720
s_addc_u32 s41, s65, 0
AD 144,8
MA 152,108,20
.Lk2p34:
s_cbranch_execz .Lk2r7
DH 0
MA 148,104,8
UH 6,13
UL 7,16
MA 148,108,12
UH 8,16
MA 144,104,0
ds_read_b64 v[12:13], v18 offset:13824
MA 144,108,4
TR 104,2,34
MV 184,112,0
TR 106,3,34
MV 184,116,4
DH 2
MV 188,112,8
ds_read_b32 v16, v19 offset:13824
AD 148,6
MV 188,116,12
WL 5
TR 108,2,35
AD 152,7
MV 192,112,16
s_add_u32 m0, %[hm], 28672
TR 110,3,35
AD 156,8
MV 192,116,20
s_add_u32 s40, s64, 1314816
s_addc_u32 s41, s65, 0
UL 6,14
MV 196,112,24
DH 0
MV 196,116,28
UH 7,14
UL 8,15
MV 200,112,32
AD 137,6
AD 141,7
MV 200,116,36
s_add_u32 m0, %[wm], 0
AD 145,8
UH 6,15
MV 204,112,40
s_add_u32 s40, s89, 442368
UL 7,17
UH 8,17
MV 204,116,44
WL 5
TR 112,4,34
MV 228,96,40
TR 114,5,34
MV 228,100,44
s_add_u32 s40, s40, s68
s_addc_u32 s41, s69, 0
MV 224,96,32
DX 0
MV 224,100,36
TR 116,4,35
MV 220,96,24
TR 118,5,35
MV 220,100,28
ds_read_b64 v[14:15], v18 offset:14592
MV 216,96,16
ds_read_b32 v17, v19 offset:14592
AD 149,6
MV 216,100,20
DX 1
MV 212,96,8
AD 153,7
AD 157,8
MV 212,100,12
WL 6
UL 6,12
UH 7,12
MV 208,96,0
UL 8,13
AD 138,6
MV 208,100,4
TR 96,2,0
AD 142,7
MV 232,104,0
TR 98,3,0
AD 146,8
MV 232,108,4
DX 2
MV 236,104,8
UH 6,13
UL 7,16
MV 236,108,12
TR 100,2,1
UH 8,16
)"
R"(MV 240,104,16
TR 102,3,1
AD 150,6
MV 240,108,20
AD 154,7
AD 158,8
MV 244,104,24
ST 24,0
MV 244,108,28
WL 4
UL 6,14
UH 7,14
MV 248,104,32
UL 8,15
AD 139,6
MV 248,108,36
AD 143,7
AD 147,8
MV 252,104,40
UH 6,15
UL 7,17
MV 252,108,44
TR 104,4,0
UH 8,17
MA 188,112,40
TR 106,5,0
AD 151,6
MA 188,116,44
AD 155,7
AD 159,8
MA 184,112,32
MA 184,116,36
TR 108,4,1
MA 180,112,24
TR 110,5,1
MA 180,116,28
MA 176,112,16
RV 24,23,1024
MA 176,116,20
MA 172,112,8
MA 172,116,12
MA 168,112,0
WV 10
MA 168,116,4
WL 5
TR 112,2,2
MV 40,96,48
TR 114,3,2
MV 40,100,52
s_add_u32 s44, s84, 692224
s_addc_u32 s45, s85, 0
MV 44,96,56
LA 0,-4
MV 44,100,60
TR 116,2,3
MV 48,96,64
TR 118,3,3
MV 48,100,68
MV 52,96,72
MV 52,100,76
LA 4,-3
MV 56,96,80
MV 56,100,84
MV 60,96,88
MV 60,100,92
WL 5
TR 96,4,2
MV 84,104,88
TR 98,5,2
MV 84,108,92
LA 8,-2
MV 80,104,80
MV 80,108,84
TR 100,4,3
MV 76,104,72
TR 102,5,3
MV 76,108,76
MV 72,104,64
LA 12,-1
MV 72,108,68
MV 68,104,56
MV 68,108,60
MV 64,104,48
MV 64,108,52
WL 4
TR 104,2,4
MV 88,112,48
TR 106,3,4
MV 88,116,52
LA 16,0
MV 92,112,56
MV 92,116,60
TR 108,2,5
MV 96,112,64
TR 110,3,5
MV 96,116,68
MV 100,112,72
LA 20,1
MV 100,116,76
MV 104,112,80
MV 104,116,84
s_add_u32 s44, s44, 6144
s_addc_u32 s45, s45, 0
MV 108,112,88
MV 108,116,92
WL 4
TR 112,4,4
MA 140,96,88
TR 114,5,4
MA 140,100,92
LA 24,-4
MA 136,96,80
MA 136,100,84
TR 116,4,5
MA 132,96,72
TR 118,5,5
MA 132,100,76
MA 128,96,64
LA 28,-3
MA 128,100,68
MA 124,96,56
MA 124,100,60
WL 0
s_barrier
MA 120,96,48
s_add_u32 m0, %[hm], 30720
MA 120,100,52
TR 96,2,6
MV 112,104,48
TR 98,3,6
MV 112,108,52
LA 32,-2
MV 116,104,56
s_add_u32 s40, s64, 1316864
s_addc_u32 s41, s65, 0
MV 116,108,60
TR 100,2,7
MV 120,104,64
TR 102,3,7
MV 120,108,68
MV 124,104,72
LA 36,-1
MV 124,108,76
MV 128,104,80
MV 128,108,84
MV 132,104,88
MV 132,108,92
TR 104,4,6
MV 156,112,88
TR 106,5,6
MV 156,116,92
LA 40,0
MV 152,112,80
MV 152,116,84
TR 108,4,7
MV 148,112,72
TR 110,5,7
MV 148,116,76
MV 144,112,64
LA 44,1
MV 144,116,68
MV 140,112,56
MV 140,116,60
MV 136,112,48
MV 136,116,52
WL 4
TR 112,2,8
MV 160,96,48
TR 114,3,8
MV 160,100,52
DH 0
MV 164,96,56
MV 164,100,60
TR 116,2,9
MV 168,96,64
s_add_u32 m0, %[hm], 32768
TR 118,3,9
MV 168,100,68
s_add_u32 s40, s66, 1310720
s_addc_u32 s41, s67, 0
MV 172,96,72
DH 0
MV 172,100,76
MV 176,96,80
MV 176,100,84
MV 180,96,88
MV 180,100,92
WL 4
TR 96,4,8
MA 164,104,88
TR 98,5,8
MA 164,108,92
DH 2
MA 160,104,80
MA 160,108,84
TR 100,4,9
MA 156,104,72
TR 102,5,9
MA 156,108,76
s_bitcmp1_b32 s48, 8
s_cselect_b64 s[64:65], s[58:59], s[56:57]
MA 152,104,64
s_bitcmp1_b32 s48, 24
s_cselect_b32 s40, 16384, 0
MA 152,108,68
s_add_u32 s64, s64, s40
s_addc_u32 s65, s65, 0
MA 148,104,56
s_add_u32 m0, %[hm], 0
v_xor_b32 v1, 0x700, v1
MA 148,108,60
s_add_u32 s40, s64, 2097152
s_addc_u32 s41, s65, 0
MA 144,104,48
DH 0
MA 144,108,52
WL 4
TR 104,2,10
MV 184,112,48
TR 106,3,10
MV 184,116,52
MV 188,112,56
MV 188,116,60
TR 108,2,11
MV 192,112,64
TR 110,3,11
MV 192,116,68
DH 2
MV 196,112,72
MV 196,116,76
MV 200,112,80
s_add_u32 m0, %[hm], 4096
MV 200,116,84
s_add_u32 s40, s64, 2101248
s_addc_u32 s41, s65, 0
MV 204,112,88
DH 0
MV 204,116,92
WL 4
TR 112,4,10
MV 228,96,88
TR 114,5,10
MV 228,100,92
s_add_u32 m0, %[wm], 12288
MV 224,96,80
s_add_u32 s40, s89, 540672
MV 224,100,84
TR 116,4,11
MV 220,96,72
TR 118,5,11
MV 220,100,76
s_add_u32 s40, s40, s68
s_addc_u32 s41, s69, 0
MV 216,96,64
DX 0
MV 216,100,68
MV 212,96,56
MV 212,100,60
MV 208,96,48
MV 208,100,52
WL 4
TR 96,2,12
MV 232,104,48
TR 98,3,12
MV 232,108,52
DX 1
MV 236,104,56
MV 236,108,60
TR 100,2,13
MV 240,104,64
TR 102,3,13
MV 240,108,68
MV 244,104,72
DX 2
MV 244,108,76
MV 248,104,80
MV 248,108,84
MV 252,104,88
MV 252,108,92
WL 4
TR 104,4,12
MA 188,112,88
TR 106,5,12
MA 188,116,92
ST 24,1
MA 184,112,80
MA 184,116,84
TR 108,4,13
MA 180,112,72
TR 110,5,13
MA 180,116,76
RV 24,23,2048
MA 176,112,64
MA 176,116,68
MA 172,112,56
MA 172,116,60
MA 168,112,48
WV 10
MA 168,116,52
WL 5
TR 112,2,14
MV 40,96,0
TR 114,3,14
MV 40,100,4
s_add_u32 s44, s84, 741376
s_addc_u32 s45, s85, 0
MV 44,96,8
LA 48,-4
MV 44,100,12
TR 116,2,15
MV 48,96,16
TR 118,3,15
MV 48,100,20
MV 52,96,24
MV 52,100,28
LA 52,-3
MV 56,96,32
MV 56,100,36
MV 60,96,40
MV 60,100,44
WL 5
TR 96,4,14
MV 84,104,40
TR 98,5,14
MV 84,108,44
LA 56,-2
MV 80,104,32
MV 80,108,36
TR 100,4,15
MV 76,104,24
TR 102,5,15
MV 76,108,28
MV 72,104,16
LA 60,-1
MV 72,108,20
MV 68,104,8
MV 68,108,12
MV 64,104,0
MV 64,108,4
WL 4
TR 104,2,16
MV 88,112,0
TR 106,3,16
MV 88,116,4
LA 64,0
MV 92,112,8
MV 92,116,12
TR 108,2,17
MV 96,112,16
TR 110,3,17
MV 96,116,20
MV 100,112,24
LA 68,1
MV 100,116,28
MV 104,112,32
MV 104,116,36
s_add_u32 s44, s44, 6144
s_addc_u32 s45, s45, 0
MV 108,112,40
MV 108,116,44
WL 4
TR 112,4,16
MA 140,96,40
TR 114,5,16
MA 140,100,44
LA 72,-4
MA 136,96,32
MA 136,100,36
TR 116,4,17
MA 132,96,24
TR 118,5,17
MA 132,100,28
MA 128,96,16
LA 76,-3
MA 128,100,20
MA 124,96,8
MA 124,100,12
WL 0
s_barrier
MA 120,96,0
s_add_u32 m0, %[hm], 6144
MA 120,100,4
TR 96,2,18
MV 112,104,0
TR 98,3,18
MV 112,108,4
LA 80,-2
MV 116,104,8
s_add_u32 s40, s64, 2103296
s_addc_u32 s41, s65, 0
MV 116,108,12
TR 100,2,19
MV 120,104,16
TR 102,3,19
MV 120,108,20
MV 124,104,24
LA 84,-1
MV 124,108,28
MV 128,104,32
MV 128,108,36
)"
R"(MV 132,104,40
MV 132,108,44
TR 104,4,18
MV 156,112,40
TR 106,5,18
MV 156,116,44
LA 88,0
MV 152,112,32
MV 152,116,36
TR 108,4,19
MV 148,112,24
TR 110,5,19
MV 148,116,28
MV 144,112,16
LA 92,1
MV 144,116,20
MV 140,112,8
MV 140,116,12
MV 136,112,0
MV 136,116,4
WL 4
TR 112,2,20
MV 160,96,0
TR 114,3,20
MV 160,100,4
DH 0
MV 164,96,8
MV 164,100,12
TR 116,2,21
MV 168,96,16
TR 118,3,21
MV 168,100,20
s_bitcmp1_b32 s49, 8
s_cselect_b64 s[66:67], s[62:63], s[60:61]
MV 172,96,24
s_add_u32 m0, %[hm], 8192
MV 172,100,28
s_add_u32 s40, s66, 2097152
s_addc_u32 s41, s67, 0
MV 176,96,32
DH 0
MV 176,100,36
MV 180,96,40
MV 180,100,44
WL 4
TR 96,4,20
MA 164,104,40
TR 98,5,20
MA 164,108,44
DH 2
MA 160,104,32
ds_read_b64 v[12:13], v18 offset:0
MA 160,108,36
TR 100,4,21
MA 156,104,24
TR 102,5,21
MA 156,108,28
s_add_u32 m0, %[hm], 12288
v_xor_b32 v1, 0x100, v1
MA 152,104,16
s_add_u32 s40, s64, 2097152
s_addc_u32 s41, s65, 0
MA 152,108,20
.Lk2p35:
s_cbranch_execz .Lk2r7
DH 0
MA 148,104,8
ds_read_b32 v16, v19 offset:0
MA 148,108,12
ds_read_b64 v[14:15], v18 offset:768
MA 144,104,0
ds_read_b32 v17, v19 offset:768
MA 144,108,4
WL 8
TR 104,2,22
MV 184,112,0
TR 106,3,22
MV 184,116,4
DH 2
MV 188,112,8
MV 188,116,12
WL 4
TR 108,2,23
UL 6,12
MV 192,112,16
s_add_u32 m0, %[hm], 16384
TR 110,3,23
UH 7,12
MV 192,116,20
s_add_u32 s40, s64, 2101248
s_addc_u32 s41, s65, 0
UL 8,13
MV 196,112,24
DH 0
MV 196,116,28
AD 160,6
AD 164,7
MV 200,112,32
AD 168,8
UH 6,13
MV 200,116,36
UL 7,16
UH 8,16
MV 204,112,40
ds_read_b64 v[12:13], v18 offset:1536
MV 204,116,44
TR 112,4,22
MV 228,96,40
TR 114,5,22
MV 228,100,44
ST 24,2
MV 224,96,32
ds_read_b32 v16, v19 offset:1536
AD 172,6
MV 224,100,36
TR 116,4,23
v_add_u32 v23, 0xf000, v20
MV 220,96,24
TR 118,5,23
AD 176,7
MV 220,100,28
RV 24,23,12288
AD 180,8
MV 216,96,16
WL 8
UL 6,14
UH 7,14
MV 216,100,20
UL 8,15
AD 161,6
MV 212,96,8
s_add_u32 s42, s90, 245760
AD 165,7
AD 169,8
MV 212,100,12
s_add_u32 s42, s42, s74
s_addc_u32 s43, s75, 0
UH 6,15
MV 208,96,0
UL 7,17
UH 8,17
MV 208,100,4
WL 7
TR 96,2,24
MV 232,104,0
TR 98,3,24
MV 232,108,4
WL 2
ST 24,0
MV 236,104,8
ds_read_b64 v[14:15], v18 offset:2304
MV 236,108,12
TR 100,2,25
MV 240,104,16
TR 102,3,25
MV 240,108,20
RV 24,23,13312
MV 244,104,24
ds_read_b32 v17, v19 offset:2304
AD 173,6
MV 244,108,28
AD 177,7
AD 181,8
MV 248,104,32
UL 6,12
UH 7,12
MV 248,108,36
UL 8,13
AD 162,6
MV 252,104,40
AD 166,7
AD 170,8
MV 252,108,44
TR 104,4,24
UH 6,13
MA 188,112,40
TR 106,5,24
UL 7,16
MA 188,116,44
UH 8,16
AD 174,6
MA 184,112,32
AD 178,7
AD 182,8
MA 184,116,36
WL 2
TR 108,4,25
UL 6,14
MA 180,112,24
TR 110,5,25
UH 7,14
MA 180,116,28
UL 8,15
AD 163,6
MA 176,112,16
AD 167,7
AD 171,8
MA 176,116,20
UH 6,15
UL 7,17
MA 172,112,8
UH 8,17
AD 175,6
MA 172,116,12
AD 179,7
AD 183,8
MA 168,112,0
MA 168,116,4
WV 8
TR 112,2,26
MV 40,96,48
TR 114,3,26
MV 40,100,52
s_add_u32 s44, s84, 790528
s_addc_u32 s45, s85, 0
MV 44,96,56
LA 0,-4
MV 44,100,60
TR 116,2,27
MV 48,96,64
TR 118,3,27
MV 48,100,68
MV 52,96,72
MV 52,100,76
LA 4,-3
MV 56,96,80
MV 56,100,84
MV 60,96,88
MV 60,100,92
WL 4
TR 96,4,26
MV 84,104,88
TR 98,5,26
MV 84,108,92
LA 8,-2
MV 80,104,80
MV 80,108,84
TR 100,4,27
MV 76,104,72
TR 102,5,27
MV 76,108,76
MV 72,104,64
LA 12,-1
MV 72,108,68
MV 68,104,56
MV 68,108,60
MV 64,104,48
MV 64,108,52
WL 4
TR 104,2,28
MV 88,112,48
TR 106,3,28
MV 88,116,52
LA 16,0
MV 92,112,56
MV 92,116,60
TR 108,2,29
MV 96,112,64
TR 110,3,29
MV 96,116,68
MV 100,112,72
LA 20,1
MV 100,116,76
MV 104,112,80
MV 104,116,84
s_add_u32 s44, s44, 6144
s_addc_u32 s45, s45, 0
MV 108,112,88
MV 108,116,92
WL 4
TR 112,4,28
MA 140,96,88
TR 114,5,28
MA 140,100,92
LA 24,-4
MA 136,96,80
MA 136,100,84
TR 116,4,29
MA 132,96,72
TR 118,5,29
MA 132,100,76
MA 128,96,64
LA 28,-3
MA 128,100,68
MA 124,96,56
MA 124,100,60
WL 0
s_barrier
MA 120,96,48
s_add_u32 m0, %[hm], 18432
MA 120,100,52
TR 96,2,30
MV 112,104,48
TR 98,3,30
MV 112,108,52
LA 32,-2
MV 116,104,56
s_add_u32 s40, s64, 2103296
s_addc_u32 s41, s65, 0
MV 116,108,60
TR 100,2,31
MV 120,104,64
TR 102,3,31
MV 120,108,68
MV 124,104,72
LA 36,-1
MV 124,108,76
MV 128,104,80
MV 128,108,84
MV 132,104,88
MV 132,108,92
TR 104,4,30
MV 156,112,88
TR 106,5,30
MV 156,116,92
LA 40,0
MV 152,112,80
MV 152,116,84
TR 108,4,31
MV 148,112,72
TR 110,5,31
MV 148,116,76
MV 144,112,64
LA 44,1
MV 144,116,68
MV 140,112,56
MV 140,116,60
MV 136,112,48
MV 136,116,52
WL 4
TR 112,2,32
MV 160,96,48
TR 114,3,32
MV 160,100,52
DH 0
MV 164,96,56
MV 164,100,60
TR 116,2,33
MV 168,96,64
s_add_u32 m0, %[hm], 20480
TR 118,3,33
MV 168,100,68
s_add_u32 s40, s66, 2097152
s_addc_u32 s41, s67, 0
MV 172,96,72
DH 0
MV 172,100,76
MV 176,96,80
MV 176,100,84
MV 180,96,88
MV 180,100,92
WL 4
TR 96,4,32
MA 164,104,88
TR 98,5,32
MA 164,108,92
DH 2
MA 160,104,80
MA 160,108,84
TR 100,4,33
MA 156,104,72
TR 102,5,33
MA 156,108,76
s_add_u32 m0, %[hm], 24576
v_xor_b32 v1, 0x300, v1
MA 152,104,64
s_add_u32 s40, s64, 2097152
s_addc_u32 s41, s65, 0
MA 152,108,68
DH 0
MA 148,104,56
MA 148,108,60
MA 144,104,48
MA 144,108,52
WL 4
TR 104,2,34
MV 184,112,48
TR 106,3,34
MV 184,116,52
DH 2
MV 188,112,56
ds_read_b64 v[12:13], v18 offset:12288
MV 188,116,60
TR 108,2,35
MV 192,112,64
s_add_u32 m0, %[hm], 28672
TR 110,3,35
MV 192,116,68
s_add_u32 s40, s64, 2101248
s_addc_u32 s41, s65, 0
MV 196,112,72
DH 0
MV 196,116,76
ds_read_b32 v16, v19 offset:12288
MV 200,112,80
ds_read_b64 v[14:15], v18 offset:13056
MV 200,116,84
s_add_u32 m0, %[wm], 0
ds_read_b32 v17, v19 offset:13056
MV 204,112,88
)"
R"(s_add_u32 s40, s90, 393216
MV 204,116,92
WL 8
TR 112,4,34
MV 228,96,88
TR 114,5,34
MV 228,100,92
s_add_u32 s40, s40, s70
s_addc_u32 s41, s71, 0
MV 224,96,80
DX 0
MV 224,100,84
WL 4
TR 116,4,35
UL 6,12
MV 220,96,72
TR 118,5,35
UH 7,12
MV 220,100,76
UL 8,13
AR 9,144
MV 216,96,64
AR 10,148
AR 11,152
MV 216,100,68
DX 1
MV 212,96,56
AD 9,6
AD 10,7
MV 212,100,60
AD 11,8
AW 144,9
MV 208,96,48
AW 148,10
AW 152,11
MV 208,100,52
TR 96,2,0
UH 6,13
MV 232,104,48
TR 98,3,0
UL 7,16
MV 232,108,52
DX 2
MV 236,104,56
UH 8,16
MV 236,108,60
TR 100,2,1
MV 240,104,64
TR 102,3,1
MV 240,108,68
ds_read_b64 v[12:13], v18 offset:13824
MV 244,104,72
ST 24,1
MV 244,108,76
ds_read_b32 v16, v19 offset:13824
AR 9,156
MV 248,104,80
RV 24,23,14336
AR 10,160
MV 248,108,84
AR 11,164
AD 9,6
MV 252,104,88
AD 10,7
AD 11,8
MV 252,108,92
WL 7
TR 104,4,0
AW 156,9
MA 188,112,88
TR 106,5,0
AW 160,10
MA 188,116,92
AW 164,11
UL 6,14
MA 184,112,80
UH 7,14
UL 8,15
MA 184,116,84
TR 108,4,1
AR 9,145
MA 180,112,72
TR 110,5,1
AR 10,149
MA 180,116,76
WL 4
ST 24,2
MA 176,112,64
AR 11,153
AD 9,6
MA 176,116,68
AD 10,7
AD 11,8
MA 172,112,56
AW 145,9
AW 149,10
MA 172,116,60
AW 153,11
UH 6,15
MA 168,112,48
UL 7,17
UH 8,17
MA 168,116,52
WV 11
TR 112,2,2
MV 40,96,0
TR 114,3,2
MV 40,100,4
s_add_u32 s44, s84, 839680
s_addc_u32 s45, s85, 0
MV 44,96,8
LA 48,-4
MV 44,100,12
TR 116,2,3
MV 48,96,16
TR 118,3,3
MV 48,100,20
ds_read_b64 v[14:15], v18 offset:14592
MV 52,96,24
ds_read_b32 v17, v19 offset:14592
AR 9,157
MV 52,100,28
LA 52,-3
MV 56,96,32
AR 10,161
AR 11,165
MV 56,100,36
RV 24,20,24576
AD 9,6
MV 60,96,40
AD 10,7
AD 11,8
MV 60,100,44
WL 7
TR 96,4,2
AW 157,9
MV 84,104,40
s_add_u32 s42, s91, 196608
TR 98,5,2
AW 161,10
MV 84,108,44
LA 56,-2
MV 80,104,32
s_add_u32 s42, s42, s74
s_addc_u32 s43, s75, 0
AW 165,11
MV 80,108,36
TR 100,4,3
UL 6,12
MV 76,104,24
TR 102,5,3
UH 7,12
MV 76,108,28
UL 8,13
AR 9,146
MV 72,104,16
LA 60,-1
MV 72,108,20
AR 10,150
AR 11,154
MV 68,104,8
AD 9,6
AD 10,7
MV 68,108,12
AD 11,8
AW 146,9
MV 64,104,0
AW 150,10
AW 154,11
MV 64,108,4
WL 4
TR 104,2,4
UH 6,13
MV 88,112,0
TR 106,3,4
UL 7,16
MV 88,116,4
LA 64,0
MV 92,112,8
UH 8,16
AR 9,158
MV 92,116,12
TR 108,2,5
AR 10,162
MV 96,112,16
TR 110,3,5
AR 11,166
MV 96,116,20
AD 9,6
AD 10,7
MV 100,112,24
LA 68,1
MV 100,116,28
AD 11,8
AW 158,9
MV 104,112,32
AW 162,10
AW 166,11
MV 104,116,36
s_add_u32 s44, s44, 6144
s_addc_u32 s45, s45, 0
UL 6,14
MV 108,112,40
UH 7,14
UL 8,15
MV 108,116,44
WL 4
TR 112,4,4
AR 9,147
MA 140,96,40
TR 114,5,4
AR 10,151
MA 140,100,44
LA 72,-4
MA 136,96,32
AR 11,155
AD 9,6
MA 136,100,36
TR 116,4,5
AD 10,7
MA 132,96,24
TR 118,5,5
AD 11,8
MA 132,100,28
AW 147,9
AW 151,10
MA 128,96,16
LA 76,-3
MA 128,100,20
AW 155,11
UH 6,15
MA 124,96,8
UL 7,17
UH 8,17
MA 124,100,12
WL 0
s_barrier
AR 9,159
AR 10,163
MA 120,96,0
s_add_u32 m0, %[hm], 30720
AR 11,167
AD 9,6
MA 120,100,4
.Lk2p36:
s_cbranch_execz .Lk2r7
TR 96,2,6
AD 10,7
MV 112,104,0
TR 98,3,6
AD 11,8
MV 112,108,4
LA 80,-2
MV 116,104,8
s_add_u32 s40, s64, 2103296
s_addc_u32 s41, s65, 0
AW 159,9
MV 116,108,12
TR 100,2,7
AW 163,10
MV 120,104,16
TR 102,3,7
AW 167,11
MV 120,108,20
MV 124,104,24
LA 84,-1
MV 124,108,28
MV 128,104,32
MV 128,108,36
MV 132,104,40
MV 132,108,44
TR 104,4,6
MV 156,112,40
TR 106,5,6
MV 156,116,44
LA 88,0
MV 152,112,32
MV 152,116,36
TR 108,4,7
MV 148,112,24
TR 110,5,7
MV 148,116,28
MV 144,112,16
LA 92,1
MV 144,116,20
MV 140,112,8
MV 140,116,12
MV 136,112,0
MV 136,116,4
WL 4
TR 112,2,8
MV 160,96,0
TR 114,3,8
MV 160,100,4
DH 0
MV 164,96,8
MV 164,100,12
TR 116,2,9
MV 168,96,16
s_add_u32 m0, %[hm], 32768
TR 118,3,9
MV 168,100,20
s_add_u32 s40, s66, 2097152
s_addc_u32 s41, s67, 0
MV 172,96,24
DH 0
MV 172,100,28
MV 176,96,32
MV 176,100,36
MV 180,96,40
MV 180,100,44
WL 4
TR 96,4,8
MA 164,104,40
TR 98,5,8
MA 164,108,44
DH 2
MA 160,104,32
MA 160,108,36
TR 100,4,9
MA 156,104,24
TR 102,5,9
MA 156,108,28
s_add_u32 m0, %[hm], 0
v_xor_b32 v1, 0x100, v1
MA 152,104,16
s_add_u32 s40, s64, 2097152
s_addc_u32 s41, s65, 0
MA 152,108,20
DH 0
MA 148,104,8
MA 148,108,12
MA 144,104,0
MA 144,108,4
WL 4
TR 104,2,10
MV 184,112,0
TR 106,3,10
MV 184,116,4
DH 2
MV 188,112,8
MV 188,116,12
TR 108,2,11
MV 192,112,16
s_add_u32 m0, %[hm], 4096
TR 110,3,11
MV 192,116,20
s_add_u32 s40, s64, 2101248
s_addc_u32 s41, s65, 0
MV 196,112,24
DH 0
MV 196,116,28
MV 200,112,32
MV 200,116,36
s_add_u32 m0, %[wm], 12288
MV 204,112,40
s_add_u32 s40, s90, 442368
MV 204,116,44
WL 4
TR 112,4,10
MV 228,96,40
TR 114,5,10
MV 228,100,44
s_add_u32 s40, s40, s70
s_addc_u32 s41, s71, 0
MV 224,96,32
DX 0
MV 224,100,36
TR 116,4,11
MV 220,96,24
TR 118,5,11
MV 220,100,28
MV 216,96,16
MV 216,100,20
DX 1
MV 212,96,8
MV 212,100,12
MV 208,96,0
MV 208,100,4
WL 4
TR 96,2,12
MV 232,104,0
TR 98,3,12
MV 232,108,4
DX 2
MV 236,104,8
MV 236,108,12
TR 100,2,13
MV 240,104,16
TR 102,3,13
MV 240,108,20
MV 244,104,24
ST 24,0
MV 244,108,28
MV 248,104,32
RV 24,20,25600
MV 248,108,36
MV 252,104,40
MV 252,108,44
WL 5
)"
R"(TR 104,4,12
MA 188,112,40
TR 106,5,12
MA 188,116,44
MA 184,112,32
MA 184,116,36
TR 108,4,13
MA 180,112,24
TR 110,5,13
MA 180,116,28
WL 4
ST 24,1
MA 176,112,16
MA 176,116,20
MA 172,112,8
MA 172,116,12
MA 168,112,0
MA 168,116,4
WV 11
TR 112,2,14
MV 40,96,48
TR 114,3,14
MV 40,100,52
s_add_u32 s44, s84, 888832
s_addc_u32 s45, s85, 0
MV 44,96,56
LA 0,-4
MV 44,100,60
TR 116,2,15
MV 48,96,64
TR 118,3,15
MV 48,100,68
MV 52,96,72
MV 52,100,76
LA 4,-3
MV 56,96,80
MV 56,100,84
MV 60,96,88
MV 60,100,92
WL 4
TR 96,4,14
MV 84,104,88
TR 98,5,14
MV 84,108,92
LA 8,-2
MV 80,104,80
MV 80,108,84
TR 100,4,15
MV 76,104,72
TR 102,5,15
MV 76,108,76
MV 72,104,64
LA 12,-1
MV 72,108,68
MV 68,104,56
MV 68,108,60
MV 64,104,48
MV 64,108,52
WL 4
TR 104,2,16
MV 88,112,48
TR 106,3,16
MV 88,116,52
LA 16,0
MV 92,112,56
MV 92,116,60
TR 108,2,17
MV 96,112,64
TR 110,3,17
MV 96,116,68
MV 100,112,72
LA 20,1
MV 100,116,76
MV 104,112,80
MV 104,116,84
s_add_u32 s44, s44, 6144
s_addc_u32 s45, s45, 0
MV 108,112,88
MV 108,116,92
WL 4
TR 112,4,16
MA 140,96,88
TR 114,5,16
MA 140,100,92
LA 24,-4
MA 136,96,80
s_mov_b64 exec, 0
s_cmp_eq_u32 %[w], 0
s_cbranch_scc1 .Lk2p37
s_cmp_eq_u32 %[w], 1
s_cbranch_scc1 .Lk2p38
s_cmp_eq_u32 %[w], 2
s_cbranch_scc1 .Lk2p39
s_branch .Lk2p40
.Lk2r8:
s_mov_b64 exec, -1
MA 136,100,84
TR 116,4,17
MA 132,96,72
TR 118,5,17
MA 132,100,76
MA 128,96,64
LA 28,-3
MA 128,100,68
MA 124,96,56
MA 124,100,60
WL 0
s_barrier
RV 24,20,26624
MA 120,96,48
s_add_u32 m0, %[hm], 6144
MA 120,100,52
TR 96,2,18
MV 112,104,48
TR 98,3,18
MV 112,108,52
LA 32,-2
MV 116,104,56
s_add_u32 s40, s64, 2103296
s_addc_u32 s41, s65, 0
MV 116,108,60
TR 100,2,19
MV 120,104,64
TR 102,3,19
MV 120,108,68
MV 124,104,72
LA 36,-1
MV 124,108,76
MV 128,104,80
MV 128,108,84
MV 132,104,88
MV 132,108,92
TR 104,4,18
MV 156,112,88
TR 106,5,18
MV 156,116,92
LA 40,0
MV 152,112,80
MV 152,116,84
TR 108,4,19
MV 148,112,72
TR 110,5,19
MV 148,116,76
MV 144,112,64
LA 44,1
MV 144,116,68
MV 140,112,56
MV 140,116,60
MV 136,112,48
MV 136,116,52
WL 4
TR 112,2,20
MV 160,96,48
TR 114,3,20
MV 160,100,52
DH 0
MV 164,96,56
MV 164,100,60
TR 116,2,21
MV 168,96,64
s_add_u32 m0, %[hm], 8192
TR 118,3,21
MV 168,100,68
s_add_u32 s40, s66, 2097152
s_addc_u32 s41, s67, 0
MV 172,96,72
DH 0
MV 172,100,76
MV 176,96,80
MV 176,100,84
MV 180,96,88
MV 180,100,92
WL 4
TR 96,4,20
MA 164,104,88
TR 98,5,20
MA 164,108,92
DH 2
MA 160,104,80
MA 160,108,84
TR 100,4,21
MA 156,104,72
TR 102,5,21
MA 156,108,76
s_add_u32 m0, %[hm], 12288
v_xor_b32 v1, 0x700, v1
MA 152,104,64
s_add_u32 s40, s64, 2097152
s_addc_u32 s41, s65, 0
MA 152,108,68
DH 0
MA 148,104,56
MA 148,108,60
MA 144,104,48
MA 144,108,52
WL 4
TR 104,2,22
MV 184,112,48
TR 106,3,22
MV 184,116,52
DH 2
MV 188,112,56
MV 188,116,60
TR 108,2,23
MV 192,112,64
s_add_u32 m0, %[hm], 16384
TR 110,3,23
MV 192,116,68
s_add_u32 s40, s64, 2101248
s_addc_u32 s41, s65, 0
MV 196,112,72
DH 0
MV 196,116,76
MV 200,112,80
MV 200,116,84
MV 204,112,88
MV 204,116,92
WL 4
TR 112,4,22
MV 228,96,88
TR 114,5,22
MV 228,100,92
ST 24,2
MV 224,96,80
ds_read_b64 v[12:13], v18 offset:0
MV 224,100,84
TR 116,4,23
MV 220,96,72
TR 118,5,23
MV 220,100,76
ds_read_b32 v16, v19 offset:0
MV 216,96,64
ds_read_b64 v[14:15], v18 offset:768
MV 216,100,68
ds_read_b32 v17, v19 offset:768
MV 212,96,56
MV 212,100,60
MV 208,96,48
MV 208,100,52
WL 8
TR 96,2,24
MV 232,104,48
WL 3
TR 98,3,24
UL 6,12
MV 232,108,52
MV 236,104,56
UH 7,12
UL 8,13
MV 236,108,60
TR 100,2,25
AD 184,6
MV 240,104,64
TR 102,3,25
AD 188,7
MV 240,108,68
AD 192,8
UH 6,13
MV 244,104,72
UL 7,16
UH 8,16
MV 244,108,76
ds_read_b64 v[12:13], v18 offset:1536
MV 248,104,80
ds_read_b32 v16, v19 offset:1536
AD 196,6
MV 248,108,84
AD 200,7
AD 204,8
MV 252,104,88
WL 6
UL 6,14
UH 7,14
MV 252,108,92
TR 104,4,24
UL 8,15
MA 188,112,88
TR 106,5,24
AD 185,6
MA 188,116,92
AD 189,7
AD 193,8
MA 184,112,80
UH 6,15
UL 7,17
MA 184,116,84
TR 108,4,25
UH 8,17
MA 180,112,72
TR 110,5,25
MA 180,116,76
RV 24,20,36864
MA 176,112,64
ds_read_b64 v[14:15], v18 offset:2304
MA 176,116,68
WL 6
ds_read_b32 v17, v19 offset:2304
AD 197,6
MA 172,112,56
s_add_u32 s42, s91, 245760
AD 201,7
AD 205,8
MA 172,116,60
s_add_u32 s42, s42, s74
s_addc_u32 s43, s75, 0
UL 6,12
MA 168,112,48
UH 7,12
UL 8,13
MA 168,116,52
WV 7
TR 112,2,26
AD 186,6
MV 40,96,0
TR 114,3,26
AD 190,7
MV 40,100,4
s_add_u32 s44, s84, 937984
s_addc_u32 s45, s85, 0
AD 194,8
MV 44,96,8
LA 48,-4
MV 44,100,12
TR 116,2,27
UH 6,13
MV 48,96,16
TR 118,3,27
UL 7,16
MV 48,100,20
UH 8,16
AD 198,6
MV 52,96,24
AD 202,7
AD 206,8
MV 52,100,28
LA 52,-3
MV 56,96,32
WL 4
UL 6,14
UH 7,14
MV 56,100,36
UL 8,15
AD 187,6
MV 60,96,40
AD 191,7
AD 195,8
MV 60,100,44
TR 96,4,26
UH 6,15
MV 84,104,40
TR 98,5,26
UL 7,17
MV 84,108,44
LA 56,-2
MV 80,104,32
UH 8,17
AD 199,6
MV 80,108,36
TR 100,4,27
AD 203,7
MV 76,104,24
TR 102,5,27
AD 207,8
MV 76,108,28
MV 72,104,16
LA 60,-1
MV 72,108,20
MV 68,104,8
MV 68,108,12
MV 64,104,0
MV 64,108,4
WL 4
TR 104,2,28
MV 88,112,0
TR 106,3,28
MV 88,116,4
LA 64,0
MV 92,112,8
MV 92,116,12
TR 108,2,29
MV 96,112,16
TR 110,3,29
MV 96,116,20
MV 100,112,24
LA 68,1
MV 100,116,28
MV 104,112,32
MV 104,116,36
s_add_u32 s44, s44, 6144
s_addc_u32 s45, s45, 0
MV 108,112,40
MV 108,116,44
WL 4
TR 112,4,28
MA 140,96,40
TR 114,5,28
MA 140,100,44
LA 72,-4
MA 136,96,32
MA 136,100,36
)"
R"(TR 116,4,29
MA 132,96,24
TR 118,5,29
MA 132,100,28
MA 128,96,16
LA 76,-3
MA 128,100,20
MA 124,96,8
MA 124,100,12
WL 0
s_barrier
MA 120,96,0
s_add_u32 m0, %[hm], 18432
MA 120,100,4
.Lk2p37:
s_cbranch_execz .Lk2r8
TR 96,2,30
MV 112,104,0
TR 98,3,30
MV 112,108,4
LA 80,-2
MV 116,104,8
s_add_u32 s40, s64, 2103296
s_addc_u32 s41, s65, 0
MV 116,108,12
TR 100,2,31
MV 120,104,16
TR 102,3,31
MV 120,108,20
MV 124,104,24
LA 84,-1
MV 124,108,28
MV 128,104,32
MV 128,108,36
MV 132,104,40
MV 132,108,44
TR 104,4,30
MV 156,112,40
TR 106,5,30
MV 156,116,44
LA 88,0
MV 152,112,32
MV 152,116,36
TR 108,4,31
MV 148,112,24
TR 110,5,31
MV 148,116,28
MV 144,112,16
LA 92,1
MV 144,116,20
MV 140,112,8
MV 140,116,12
MV 136,112,0
MV 136,116,4
WL 4
TR 112,2,32
MV 160,96,0
TR 114,3,32
MV 160,100,4
DH 0
MV 164,96,8
MV 164,100,12
TR 116,2,33
MV 168,96,16
s_add_u32 m0, %[hm], 20480
TR 118,3,33
MV 168,100,20
s_add_u32 s40, s66, 2097152
s_addc_u32 s41, s67, 0
MV 172,96,24
DH 0
MV 172,100,28
MV 176,96,32
MV 176,100,36
MV 180,96,40
MV 180,100,44
WL 4
TR 96,4,32
MA 164,104,40
TR 98,5,32
MA 164,108,44
DH 2
MA 160,104,32
MA 160,108,36
TR 100,4,33
MA 156,104,24
TR 102,5,33
MA 156,108,28
s_add_u32 m0, %[hm], 24576
v_xor_b32 v1, 0x100, v1
MA 152,104,16
s_add_u32 s40, s64, 2097152
s_addc_u32 s41, s65, 0
MA 152,108,20
DH 0
MA 148,104,8
MA 148,108,12
MA 144,104,0
MA 144,108,4
WL 4
TR 104,2,34
MV 184,112,0
TR 106,3,34
MV 184,116,4
DH 2
MV 188,112,8
MV 188,116,12
TR 108,2,35
MV 192,112,16
s_add_u32 m0, %[hm], 28672
TR 110,3,35
MV 192,116,20
s_add_u32 s40, s64, 2101248
s_addc_u32 s41, s65, 0
MV 196,112,24
DH 0
MV 196,116,28
MV 200,112,32
MV 200,116,36
s_add_u32 m0, %[wm], 0
MV 204,112,40
s_add_u32 s40, s91, 393216
MV 204,116,44
WL 4
TR 112,4,34
MV 228,96,40
TR 114,5,34
MV 228,100,44
s_add_u32 s40, s40, s70
s_addc_u32 s41, s71, 0
MV 224,96,32
DX 0
MV 224,100,36
TR 116,4,35
MV 220,96,24
TR 118,5,35
MV 220,100,28
MV 216,96,16
MV 216,100,20
DX 1
MV 212,96,8
MV 212,100,12
MV 208,96,0
MV 208,100,4
WL 4
TR 96,2,0
MV 232,104,0
TR 98,3,0
MV 232,108,4
DX 2
MV 236,104,8
ds_read_b64 v[12:13], v18 offset:12288
MV 236,108,12
TR 100,2,1
MV 240,104,16
TR 102,3,1
MV 240,108,20
ds_read_b32 v16, v19 offset:12288
MV 244,104,24
ST 24,0
MV 244,108,28
ds_read_b64 v[14:15], v18 offset:13056
MV 248,104,32
RV 24,20,37888
MV 248,108,36
ds_read_b32 v17, v19 offset:13056
MV 252,104,40
MV 252,108,44
WL 9
TR 104,4,0
MA 188,112,40
TR 106,5,0
MA 188,116,44
MA 184,112,32
WL 5
UL 6,12
UH 7,12
MA 184,116,36
TR 108,4,1
UL 8,13
MA 180,112,24
TR 110,5,1
AD 208,6
MA 180,116,28
WL 5
ST 24,1
MA 176,112,16
AD 212,7
AD 216,8
MA 176,116,20
UH 6,13
UL 7,16
MA 172,112,8
UH 8,16
MA 172,116,12
ds_read_b64 v[12:13], v18 offset:13824
MA 168,112,0
ds_read_b32 v16, v19 offset:13824
AD 220,6
MA 168,116,4
WV 11
TR 112,2,2
AD 224,7
MV 40,96,48
WL 7
TR 114,3,2
AD 228,8
MV 40,100,52
s_add_u32 s44, s84, 987136
s_addc_u32 s45, s85, 0
UL 6,14
MV 44,96,56
LA 0,-4
MV 44,100,60
TR 116,2,3
UH 7,14
MV 48,96,64
TR 118,3,3
UL 8,15
MV 48,100,68
AD 209,6
AD 213,7
MV 52,96,72
AD 217,8
UH 6,15
MV 52,100,76
LA 4,-3
MV 56,96,80
UL 7,17
UH 8,17
MV 56,100,84
ds_read_b64 v[14:15], v18 offset:14592
MV 60,96,88
ds_read_b32 v17, v19 offset:14592
AD 221,6
MV 60,100,92
WL 6
TR 96,4,2
AD 225,7
MV 84,104,88
TR 98,5,2
AD 229,8
MV 84,108,92
LA 8,-2
MV 80,104,80
UL 6,12
UH 7,12
MV 80,108,84
TR 100,4,3
UL 8,13
MV 76,104,72
TR 102,5,3
AD 210,6
MV 76,108,76
AD 214,7
AD 218,8
MV 72,104,64
LA 12,-1
MV 72,108,68
UH 6,13
UL 7,16
MV 68,104,56
UH 8,16
AD 222,6
MV 68,108,60
AD 226,7
AD 230,8
MV 64,104,48
WL 4
UL 6,14
UH 7,14
MV 64,108,52
TR 104,2,4
UL 8,15
MV 88,112,48
TR 106,3,4
AD 211,6
MV 88,116,52
LA 16,0
MV 92,112,56
AD 215,7
AD 219,8
MV 92,116,60
TR 108,2,5
UH 6,15
MV 96,112,64
TR 110,3,5
UL 7,17
MV 96,116,68
RV 24,20,38912
UH 8,17
MV 100,112,72
LA 20,1
MV 100,116,76
AD 223,6
AD 227,7
MV 104,112,80
AD 231,8
MV 104,116,84
s_add_u32 s44, s44, 6144
s_addc_u32 s45, s45, 0
MV 108,112,88
MV 108,116,92
WL 5
TR 112,4,4
MA 140,96,88
TR 114,5,4
MA 140,100,92
LA 24,-4
MA 136,96,80
MA 136,100,84
TR 116,4,5
MA 132,96,72
TR 118,5,5
MA 132,100,76
MA 128,96,64
LA 28,-3
MA 128,100,68
MA 124,96,56
MA 124,100,60
WL 0
s_barrier
MA 120,96,48
s_add_u32 m0, %[hm], 30720
MA 120,100,52
TR 96,2,6
MV 112,104,48
TR 98,3,6
MV 112,108,52
LA 32,-2
MV 116,104,56
s_add_u32 s40, s64, 2103296
s_addc_u32 s41, s65, 0
MV 116,108,60
TR 100,2,7
MV 120,104,64
TR 102,3,7
MV 120,108,68
MV 124,104,72
LA 36,-1
MV 124,108,76
MV 128,104,80
MV 128,108,84
MV 132,104,88
MV 132,108,92
TR 104,4,6
MV 156,112,88
TR 106,5,6
MV 156,116,92
LA 40,0
MV 152,112,80
MV 152,116,84
TR 108,4,7
MV 148,112,72
TR 110,5,7
MV 148,116,76
MV 144,112,64
LA 44,1
MV 144,116,68
MV 140,112,56
MV 140,116,60
MV 136,112,48
MV 136,116,52
WL 4
TR 112,2,8
MV 160,96,48
TR 114,3,8
MV 160,100,52
DH 0
MV 164,96,56
MV 164,100,60
TR 116,2,9
MV 168,96,64
s_add_u32 m0, %[hm], 32768
TR 118,3,9
MV 168,100,68
s_add_u32 s40, s66, 2097152
s_addc_u32 s41, s67, 0
MV 172,96,72
DH 0
MV 172,100,76
MV 176,96,80
MV 176,100,84
MV 180,96,88
MV 180,100,92
WL 4
TR 96,4,8
MA 164,104,88
TR 98,5,8
MA 164,108,92
DH 2
MA 160,104,80
MA 160,108,84
TR 100,4,9
MA 156,104,72
TR 102,5,9
MA 156,108,76
s_add_u32 m0, %[hm], 0
v_xor_b32 v1, 0x300, v1
MA 152,104,64
)"
R"(s_add_u32 s40, s64, 2097152
s_addc_u32 s41, s65, 0
MA 152,108,68
DH 0
MA 148,104,56
MA 148,108,60
MA 144,104,48
MA 144,108,52
WL 4
TR 104,2,10
MV 184,112,48
TR 106,3,10
MV 184,116,52
DH 2
MV 188,112,56
MV 188,116,60
TR 108,2,11
MV 192,112,64
s_add_u32 m0, %[hm], 4096
TR 110,3,11
MV 192,116,68
s_add_u32 s40, s64, 2101248
s_addc_u32 s41, s65, 0
MV 196,112,72
DH 0
MV 196,116,76
MV 200,112,80
MV 200,116,84
s_add_u32 m0, %[wm], 12288
MV 204,112,88
s_add_u32 s40, s91, 442368
MV 204,116,92
WL 4
TR 112,4,10
MV 228,96,88
TR 114,5,10
MV 228,100,92
s_add_u32 s40, s40, s70
s_addc_u32 s41, s71, 0
MV 224,96,80
DX 0
MV 224,100,84
TR 116,4,11
MV 220,96,72
TR 118,5,11
MV 220,100,76
MV 216,96,64
MV 216,100,68
DX 1
MV 212,96,56
MV 212,100,60
MV 208,96,48
MV 208,100,52
WL 4
TR 96,2,12
MV 232,104,48
TR 98,3,12
MV 232,108,52
DX 2
MV 236,104,56
MV 236,108,60
TR 100,2,13
MV 240,104,64
TR 102,3,13
MV 240,108,68
MV 244,104,72
ST 24,2
MV 244,108,76
MV 248,104,80
MV 248,108,84
MV 252,104,88
MV 252,108,92
WL 4
TR 104,4,12
MA 188,112,88
TR 106,5,12
MA 188,116,92
MA 184,112,80
MA 184,116,84
TR 108,4,13
MA 180,112,72
TR 110,5,13
MA 180,116,76
MA 176,112,64
MA 176,116,68
MA 172,112,56
MA 172,116,60
MA 168,112,48
WV 10
MA 168,116,52
WL 4
TR 112,2,14
MV 40,96,0
TR 114,3,14
MV 40,100,4
s_add_u32 s44, s84, 1036288
s_addc_u32 s45, s85, 0
MV 44,96,8
LA 48,-4
MV 44,100,12
TR 116,2,15
MV 48,96,16
TR 118,3,15
MV 48,100,20
MV 52,96,24
MV 52,100,28
LA 52,-3
MV 56,96,32
MV 56,100,36
MV 60,96,40
MV 60,100,44
WL 4
TR 96,4,14
MV 84,104,40
TR 98,5,14
MV 84,108,44
LA 56,-2
MV 80,104,32
MV 80,108,36
TR 100,4,15
MV 76,104,24
TR 102,5,15
MV 76,108,28
MV 72,104,16
LA 60,-1
MV 72,108,20
MV 68,104,8
MV 68,108,12
MV 64,104,0
MV 64,108,4
WL 4
TR 104,2,16
MV 88,112,0
TR 106,3,16
MV 88,116,4
LA 64,0
MV 92,112,8
MV 92,116,12
TR 108,2,17
MV 96,112,16
TR 110,3,17
MV 96,116,20
MV 100,112,24
LA 68,1
MV 100,116,28
MV 104,112,32
MV 104,116,36
s_add_u32 s44, s44, 6144
s_addc_u32 s45, s45, 0
MV 108,112,40
MV 108,116,44
WL 4
TR 112,4,16
MA 140,96,40
TR 114,5,16
MA 140,100,44
LA 72,-4
MA 136,96,32
MA 136,100,36
TR 116,4,17
MA 132,96,24
TR 118,5,17
MA 132,100,28
MA 128,96,16
LA 76,-3
MA 128,100,20
MA 124,96,8
MA 124,100,12
WL 0
s_barrier
ds_read_b64 v[12:13], v18 offset:0
MA 120,96,0
s_add_u32 m0, %[hm], 6144
ds_read_b32 v16, v19 offset:0
MA 120,100,4
.Lk2p38:
s_cbranch_execz .Lk2r8
TR 96,2,18
MV 112,104,0
TR 98,3,18
MV 112,108,4
LA 80,-2
MV 116,104,8
s_add_u32 s40, s64, 2103296
s_addc_u32 s41, s65, 0
MV 116,108,12
TR 100,2,19
MV 120,104,16
TR 102,3,19
MV 120,108,20
ds_read_b64 v[14:15], v18 offset:768
MV 124,104,24
LA 84,-1
MV 124,108,28
ds_read_b32 v17, v19 offset:768
MV 128,104,32
MV 128,108,36
MV 132,104,40
MV 132,108,44
TR 104,4,18
MV 156,112,40
WL 7
TR 106,5,18
UL 6,12
MV 156,116,44
LA 88,0
MV 152,112,32
UH 7,12
UL 8,13
MV 152,116,36
TR 108,4,19
AD 232,6
MV 148,112,24
TR 110,5,19
AD 236,7
MV 148,116,28
AD 240,8
UH 6,13
MV 144,112,16
LA 92,1
MV 144,116,20
UL 7,16
UH 8,16
MV 140,112,8
ds_read_b64 v[12:13], v18 offset:1536
MV 140,116,12
ds_read_b32 v16, v19 offset:1536
AD 244,6
MV 136,112,0
AD 248,7
AD 252,8
MV 136,116,4
WL 6
TR 112,2,20
UL 6,14
MV 160,96,0
TR 114,3,20
UH 7,14
MV 160,100,4
DH 0
MV 164,96,8
UL 8,15
AD 233,6
MV 164,100,12
TR 116,2,21
AD 237,7
MV 168,96,16
s_add_u32 m0, %[hm], 8192
TR 118,3,21
AD 241,8
MV 168,100,20
s_add_u32 s40, s66, 2097152
s_addc_u32 s41, s67, 0
UH 6,15
MV 172,96,24
DH 0
MV 172,100,28
UL 7,17
UH 8,17
MV 176,96,32
ds_read_b64 v[14:15], v18 offset:2304
MV 176,100,36
ds_read_b32 v17, v19 offset:2304
AD 245,6
MV 180,96,40
AD 249,7
AD 253,8
MV 180,100,44
WL 6
TR 96,4,20
UL 6,12
MA 164,104,40
TR 98,5,20
UH 7,12
MA 164,108,44
DH 2
MA 160,104,32
UL 8,13
AD 234,6
MA 160,108,36
TR 100,4,21
AD 238,7
MA 156,104,24
TR 102,5,21
AD 242,8
MA 156,108,28
s_add_u32 m0, %[hm], 12288
v_xor_b32 v1, 0x100, v1
UH 6,13
MA 152,104,16
s_add_u32 s40, s64, 2097152
s_addc_u32 s41, s65, 0
UL 7,16
MA 152,108,20
DH 0
MA 148,104,8
UH 8,16
AD 246,6
MA 148,108,12
AD 250,7
AD 254,8
MA 144,104,0
WL 4
UL 6,14
UH 7,14
MA 144,108,4
TR 104,2,22
UL 8,15
MV 184,112,0
TR 106,3,22
AD 235,6
MV 184,116,4
DH 2
MV 188,112,8
AD 239,7
AD 243,8
MV 188,116,12
TR 108,2,23
UH 6,15
MV 192,112,16
s_add_u32 m0, %[hm], 16384
TR 110,3,23
UL 7,17
MV 192,116,20
s_add_u32 s40, s64, 2101248
s_addc_u32 s41, s65, 0
UH 8,17
MV 196,112,24
DH 0
MV 196,116,28
AD 247,6
AD 251,7
MV 200,112,32
AD 255,8
MV 200,116,36
MV 204,112,40
MV 204,116,44
WL 4
TR 112,4,22
MV 228,96,40
TR 114,5,22
MV 228,100,44
MV 224,96,32
MV 224,100,36
TR 116,4,23
MV 220,96,24
TR 118,5,23
MV 220,100,28
MV 216,96,16
MV 216,100,20
MV 212,96,8
MV 212,100,12
MV 208,96,0
MV 208,100,4
WL 4
TR 96,2,24
MV 232,104,0
TR 98,3,24
MV 232,108,4
MV 236,104,8
MV 236,108,12
TR 100,2,25
MV 240,104,16
TR 102,3,25
MV 240,108,20
MV 244,104,24
MV 244,108,28
MV 248,104,32
MV 248,108,36
MV 252,104,40
MV 252,108,44
WL 4
TR 104,4,24
MA 188,112,40
TR 106,5,24
MA 188,116,44
MA 184,112,32
MA 184,116,36
TR 108,4,25
MA 180,112,24
TR 110,5,25
MA 180,116,28
MA 176,112,16
MA 176,116,20
MA 172,112,8
MA 172,116,12
MA 168,112,0
WV 6
MA 168,116,4
WL 4
TR 112,2,26
MV 40,96,48
TR 114,3,26
MV 40,100,52
s_add_u32 s44, s84, 1085440
s_addc_u32 s45, s85, 0
MV 44,96,56
LA 0,-4
MV 44,100,60
TR 116,2,27
)"
R"(MV 48,96,64
TR 118,3,27
MV 48,100,68
MV 52,96,72
MV 52,100,76
LA 4,-3
MV 56,96,80
MV 56,100,84
MV 60,96,88
MV 60,100,92
WL 4
TR 96,4,26
MV 84,104,88
TR 98,5,26
MV 84,108,92
LA 8,-2
MV 80,104,80
MV 80,108,84
TR 100,4,27
MV 76,104,72
TR 102,5,27
MV 76,108,76
MV 72,104,64
LA 12,-1
MV 72,108,68
MV 68,104,56
MV 68,108,60
MV 64,104,48
MV 64,108,52
WL 4
TR 104,2,28
MV 88,112,48
TR 106,3,28
MV 88,116,52
LA 16,0
MV 92,112,56
MV 92,116,60
TR 108,2,29
MV 96,112,64
TR 110,3,29
MV 96,116,68
MV 100,112,72
LA 20,1
MV 100,116,76
MV 104,112,80
MV 104,116,84
s_add_u32 s44, s44, 6144
s_addc_u32 s45, s45, 0
MV 108,112,88
MV 108,116,92
WL 4
TR 112,4,28
MA 140,96,88
TR 114,5,28
MA 140,100,92
LA 24,-4
MA 136,96,80
MA 136,100,84
TR 116,4,29
MA 132,96,72
TR 118,5,29
MA 132,100,76
MA 128,96,64
LA 28,-3
MA 128,100,68
MA 124,96,56
MA 124,100,60
WL 0
s_barrier
ds_read_b64 v[12:13], v18 offset:12288
ds_read_b32 v16, v19 offset:12288
ds_read_b64 v[14:15], v18 offset:13056
MA 120,96,48
s_add_u32 m0, %[hm], 18432
ds_read_b32 v17, v19 offset:13056
MA 120,100,52
TR 96,2,30
MV 112,104,48
TR 98,3,30
MV 112,108,52
LA 32,-2
MV 116,104,56
s_add_u32 s40, s64, 2103296
s_addc_u32 s41, s65, 0
MV 116,108,60
WL 4
TR 100,2,31
UL 6,12
UH 7,12
MV 120,104,64
TR 102,3,31
UL 8,13
AR 9,168
MV 120,108,68
AR 10,172
AR 11,176
AD 9,6
MV 124,104,72
LA 36,-1
AD 10,7
MV 124,108,76
AD 11,8
AW 168,9
AW 172,10
MV 128,104,80
AW 176,11
UH 6,13
UL 7,16
MV 128,108,84
UH 8,16
ds_read_b64 v[12:13], v18 offset:13824
ds_read_b32 v16, v19 offset:13824
MV 132,104,88
AR 9,180
AR 10,184
AR 11,188
MV 132,108,92
TR 104,4,30
AD 9,6
AD 10,7
MV 156,112,88
TR 106,5,30
AD 11,8
AW 180,9
MV 156,116,92
LA 40,0
AW 184,10
MV 152,112,80
WL 4
AW 188,11
UL 6,14
UH 7,14
MV 152,116,84
TR 108,4,31
UL 8,15
AR 9,169
MV 148,112,72
TR 110,5,31
AR 10,173
AR 11,177
MV 148,116,76
AD 9,6
AD 10,7
AD 11,8
MV 144,112,64
LA 44,1
AW 169,9
MV 144,116,68
AW 173,10
AW 177,11
UH 6,15
MV 140,112,56
UL 7,17
UH 8,17
ds_read_b64 v[14:15], v18 offset:14592
MV 140,116,60
ds_read_b32 v17, v19 offset:14592
AR 9,181
AR 10,185
MV 136,112,48
AR 11,189
AD 9,6
AD 10,7
MV 136,116,52
TR 112,2,32
AD 11,8
AW 181,9
MV 160,96,48
TR 114,3,32
AW 185,10
AW 189,11
MV 160,100,52
WL 5
DH 0
UL 6,12
MV 164,96,56
UH 7,12
UL 8,13
AR 9,170
MV 164,100,60
TR 116,2,33
AR 10,174
AR 11,178
MV 168,96,64
s_add_u32 m0, %[hm], 20480
TR 118,3,33
AD 9,6
AD 10,7
MV 168,100,68
s_add_u32 s40, s66, 2097152
s_addc_u32 s41, s67, 0
AD 11,8
AW 170,9
MV 172,96,72
DH 0
AW 174,10
MV 172,100,76
AW 178,11
UH 6,13
UL 7,16
MV 176,96,80
UH 8,16
AR 9,182
AR 10,186
MV 176,100,84
AR 11,190
AD 9,6
AD 10,7
MV 180,96,88
AD 11,8
AW 182,9
AW 186,10
MV 180,100,92
WL 2
TR 96,4,32
AW 190,11
UL 6,14
MA 164,104,88
TR 98,5,32
UH 7,14
UL 8,15
MA 164,108,92
DH 2
AR 9,171
MA 160,104,80
AR 10,175
AR 11,179
AD 9,6
MA 160,108,84
TR 100,4,33
AD 10,7
AD 11,8
MA 156,104,72
TR 102,5,33
AW 171,9
AW 175,10
MA 156,108,76
s_bitcmp1_b32 s48, 2
s_cselect_b64 s[64:65], s[58:59], s[56:57]
AW 179,11
UH 6,15
MA 152,104,64
s_bitcmp1_b32 s48, 18
s_cselect_b32 s40, 16384, 0
UL 7,17
UH 8,17
MA 152,108,68
s_add_u32 s64, s64, s40
s_addc_u32 s65, s65, 0
AR 9,183
AR 10,187
MA 148,104,56
s_add_u32 m0, %[hm], 24576
v_xor_b32 v1, 0x700, v1
AR 11,191
AD 9,6
MA 148,108,60
s_add_u32 s40, s64, 528384
s_addc_u32 s41, s65, 0
AD 10,7
AD 11,8
MA 144,104,48
DH 0
AW 183,9
MA 144,108,52
WL 2
TR 104,2,34
AW 187,10
AW 191,11
MV 184,112,48
TR 106,3,34
MV 184,116,52
MV 188,112,56
MV 188,116,60
TR 108,2,35
MV 192,112,64
TR 110,3,35
MV 192,116,68
DH 2
MV 196,112,72
MV 196,116,76
MV 200,112,80
s_bitcmp1_b32 s49, 2
s_cselect_b64 s[66:67], s[62:63], s[60:61]
MV 200,116,84
s_add_u32 m0, %[hm], 28672
MV 204,112,88
s_add_u32 s40, s66, 524288
s_addc_u32 s41, s67, 0
MV 204,116,92
WL 4
TR 112,4,34
MV 228,96,88
TR 114,5,34
MV 228,100,92
DH 0
MV 224,96,80
SB 184,32
SB 185,32
MV 224,100,84
TR 116,4,35
SB 186,32
MV 220,96,72
TR 118,5,35
SB 187,32
MV 220,100,76
SB 188,33
SB 189,33
MV 216,96,64
MV 216,100,68
SB 190,33
SB 191,33
MV 212,96,56
SB 192,34
SB 193,34
MV 212,100,60
SB 194,34
SB 195,34
MV 208,96,48
SB 196,35
SB 197,35
MV 208,100,52
WL 4
TR 96,2,0
SB 198,35
MV 232,104,48
TR 98,3,0
SB 199,35
MV 232,108,52
SB 200,36
SB 201,36
MV 236,104,56
SB 202,36
SB 203,36
MV 236,108,60
TR 100,2,1
SB 204,37
MV 240,104,64
TR 102,3,1
SB 205,37
MV 240,108,68
MV 244,104,72
SB 206,37
SB 207,37
MV 244,108,76
SB 208,32
SB 209,32
MV 248,104,80
SB 210,32
SB 211,32
MV 248,108,84
SB 212,33
SB 213,33
MV 252,104,88
SB 214,33
SB 215,33
MV 252,108,92
WL 4
TR 104,4,0
SB 216,34
MA 188,112,88
TR 106,5,0
SB 217,34
MA 188,116,92
SB 218,34
SB 219,34
MA 184,112,80
SB 220,35
SB 221,35
MA 184,116,84
TR 108,4,1
SB 222,35
MA 180,112,72
.Lk2p39:
s_cbranch_execz .Lk2r8
TR 110,5,1
SB 223,35
MA 180,116,76
SB 224,36
SB 225,36
MA 176,112,64
SB 226,36
SB 227,36
MA 176,116,68
SB 228,37
SB 229,37
MA 172,112,56
SB 230,37
SB 231,37
MA 172,116,60
SB 232,32
SB 233,32
MA 168,112,48
WV 6
SB 234,32
SB 235,32
MA 168,116,52
WL 4
TR 112,2,2
SB 236,33
)"
R"(MV 40,96,0
TR 114,3,2
SB 237,33
MV 40,100,4
s_add_u32 s44, s84, 1134592
s_addc_u32 s45, s85, 0
SB 238,33
MV 44,96,8
LA 48,-4
MV 44,100,12
TR 116,2,3
SB 239,33
MV 48,96,16
TR 118,3,3
SB 240,34
MV 48,100,20
SB 241,34
SB 242,34
MV 52,96,24
SB 243,34
SB 244,35
MV 52,100,28
LA 52,-3
MV 56,96,32
SB 245,35
SB 246,35
MV 56,100,36
SB 247,35
SB 248,36
MV 60,96,40
SB 249,36
SB 250,36
MV 60,100,44
WL 4
TR 96,4,2
SB 251,36
MV 84,104,40
TR 98,5,2
SB 252,37
MV 84,108,44
LA 56,-2
MV 80,104,32
SB 253,37
SB 254,37
MV 80,108,36
TR 100,4,3
SB 255,37
MV 76,104,24
TR 102,5,3
SB 40,32
MV 76,108,28
SB 41,32
SB 42,32
MV 72,104,16
LA 60,-1
MV 72,108,20
SB 43,32
SB 44,33
MV 68,104,8
SB 45,33
SB 46,33
MV 68,108,12
SB 47,33
SB 48,34
MV 64,104,0
SB 49,34
SB 50,34
MV 64,108,4
WL 4
TR 104,2,4
SB 51,34
MV 88,112,0
TR 106,3,4
SB 52,35
MV 88,116,4
LA 64,0
MV 92,112,8
SB 53,35
SB 54,35
MV 92,116,12
TR 108,2,5
SB 55,35
MV 96,112,16
TR 110,3,5
SB 56,36
MV 96,116,20
SB 57,36
SB 58,36
MV 100,112,24
LA 68,1
MV 100,116,28
SB 59,36
SB 60,37
MV 104,112,32
SB 61,37
SB 62,37
MV 104,116,36
s_add_u32 s44, s44, 6144
s_addc_u32 s45, s45, 0
SB 63,37
MV 108,112,40
SB 64,32
SB 65,32
MV 108,116,44
WL 4
TR 112,4,4
SB 66,32
MA 140,96,40
TR 114,5,4
SB 67,32
MA 140,100,44
LA 72,-4
MA 136,96,32
SB 68,33
SB 69,33
MA 136,100,36
TR 116,4,5
SB 70,33
MA 132,96,24
TR 118,5,5
SB 71,33
MA 132,100,28
SB 72,34
SB 73,34
MA 128,96,16
LA 76,-3
MA 128,100,20
SB 74,34
SB 75,34
MA 124,96,8
SB 76,35
SB 77,35
MA 124,100,12
WL 0
s_barrier
SB 78,35
SB 79,35
MA 120,96,0
s_add_u32 m0, %[hm], 30720
SB 80,36
SB 81,36
MA 120,100,4
TR 96,2,6
SB 82,36
MV 112,104,0
TR 98,3,6
SB 83,36
MV 112,108,4
LA 80,-2
MV 116,104,8
s_add_u32 s40, s66, 526336
s_addc_u32 s41, s67, 0
SB 84,37
MV 116,108,12
TR 100,2,7
SB 85,37
MV 120,104,16
TR 102,3,7
SB 86,37
MV 120,108,20
SB 87,37
MV 124,104,24
LA 84,-1
MV 124,108,28
SB 88,32
SB 89,32
MV 128,104,32
SB 90,32
SB 91,32
MV 128,108,36
SB 92,33
SB 93,33
MV 132,104,40
SB 94,33
SB 95,33
MV 132,108,44
TR 104,4,6
SB 96,34
MV 156,112,40
TR 106,5,6
SB 97,34
MV 156,116,44
LA 88,0
MV 152,112,32
SB 98,34
SB 99,34
MV 152,116,36
TR 108,4,7
SB 100,35
MV 148,112,24
TR 110,5,7
SB 101,35
MV 148,116,28
SB 102,35
SB 103,35
MV 144,112,16
LA 92,1
MV 144,116,20
SB 104,36
SB 105,36
MV 140,112,8
SB 106,36
SB 107,36
MV 140,116,12
SB 108,37
SB 109,37
MV 136,112,0
SB 110,37
SB 111,37
MV 136,116,4
WL 4
TR 112,2,8
SB 112,32
MV 160,96,0
TR 114,3,8
SB 113,32
MV 160,100,4
DH 0
MV 164,96,8
SB 114,32
SB 115,32
MV 164,100,12
TR 116,2,9
SB 116,33
MV 168,96,16
s_add_u32 m0, %[hm], 32768
TR 118,3,9
v_xor_b32 v1, 0x100, v1
MV 168,100,20
s_add_u32 s40, s66, 524288
s_addc_u32 s41, s67, 0
SB 117,33
MV 172,96,24
DH 0
MV 172,100,28
SB 118,33
SB 119,33
MV 176,96,32
SB 120,34
SB 121,34
MV 176,100,36
SB 122,34
SB 123,34
MV 180,96,40
SB 124,35
SB 125,35
MV 180,100,44
WL 4
TR 96,4,8
SB 126,35
MA 164,104,40
TR 98,5,8
SB 127,35
MA 164,108,44
DH 2
MA 160,104,32
SB 128,36
SB 129,36
MA 160,108,36
TR 100,4,9
SB 130,36
MA 156,104,24
TR 102,5,9
SB 131,36
MA 156,108,28
SB 132,37
SB 133,37
MA 152,104,16
MA 152,108,20
SB 134,37
SB 135,37
MA 148,104,8
SB 136,32
SB 137,32
MA 148,108,12
SB 138,32
SB 139,32
MA 144,104,0
SB 140,33
SB 141,33
MA 144,108,4
WL 4
TR 104,2,10
SB 142,33
MV 184,112,0
TR 106,3,10
SB 143,33
MV 184,116,4
SB 144,34
SB 145,34
MV 188,112,8
SB 146,34
SB 147,34
MV 188,116,12
TR 108,2,11
SB 148,35
MV 192,112,16
TR 110,3,11
SB 149,35
MV 192,116,20
MV 196,112,24
SB 150,35
SB 151,35
MV 196,116,28
SB 152,36
SB 153,36
MV 200,112,32
SB 154,36
SB 155,36
MV 200,116,36
SB 156,37
SB 157,37
MV 204,112,40
SB 158,37
SB 159,37
MV 204,116,44
WL 4
TR 112,4,10
SB 160,32
MV 228,96,40
TR 114,5,10
SB 161,32
MV 228,100,44
SB 162,32
SB 163,32
MV 224,96,32
SB 164,33
SB 165,33
MV 224,100,36
TR 116,4,11
SB 166,33
MV 220,96,24
TR 118,5,11
SB 167,33
MV 220,100,28
SB 168,34
SB 169,34
MV 216,96,16
SB 170,34
SB 171,34
MV 216,100,20
SB 172,35
SB 173,35
MV 212,96,8
SB 174,35
SB 175,35
MV 212,100,12
SB 176,36
SB 177,36
MV 208,96,0
SB 178,36
SB 179,36
MV 208,100,4
WL 4
TR 96,2,12
SB 180,37
MV 232,104,0
TR 98,3,12
SB 181,37
MV 232,108,4
SB 182,37
SB 183,37
MV 236,104,8
MV 236,108,12
TR 100,2,13
MV 240,104,16
TR 102,3,13
MV 240,108,20
MV 244,104,24
MV 244,108,28
MV 248,104,32
MV 248,108,36
MV 252,104,40
MV 252,108,44
WL 4
TR 104,4,12
MA 188,112,40
TR 106,5,12
MA 188,116,44
MA 184,112,32
MA 184,116,36
TR 108,4,13
MA 180,112,24
TR 110,5,13
MA 180,116,28
MA 176,112,16
MA 176,116,20
MA 172,112,8
MA 172,116,12
MA 168,112,0
WV 3
MA 168,116,4
WL 4
TR 112,2,14
MV 40,96,48
TR 114,3,14
MV 40,100,52
s_add_u32 s44, s84, 4096
s_addc_u32 s45, s85, 0
LA 0,-4
)"
R"(MV 44,96,56
MV 44,100,60
TR 116,2,15
MV 48,96,64
TR 118,3,15
MV 48,100,68
MV 52,96,72
LA 4,-3
MV 52,100,76
MV 56,96,80
MV 56,100,84
MV 60,96,88
MV 60,100,92
WL 4
TR 96,4,14
LA 8,-2
MV 84,104,88
TR 98,5,14
MV 84,108,92
MV 80,104,80
CV 12,40,44
CV 13,48,52
MV 80,108,84
TR 100,4,15
CV 16,56,60
MV 76,104,72
TR 102,5,15
LA 12,-1
MV 76,108,76
ds_write_b64 v18, v[12:13] offset:49152
MV 72,104,64
CV 14,41,45
CV 15,49,53
MV 72,108,68
CV 17,57,61
CV 12,42,46
MV 68,104,56
ds_write_b32 v19, v16 offset:49152
MV 68,108,60
LA 16,0
CV 13,50,54
MV 64,104,48
ds_write_b64 v18, v[14:15] offset:49920
MV 64,108,52
WL 4
TR 104,2,16
CV 16,58,62
MV 88,112,48
TR 106,3,16
CV 14,43,47
MV 88,116,52
ds_write_b32 v19, v17 offset:49920
MV 92,112,56
LA 20,1
CV 15,51,55
MV 92,116,60
TR 108,2,17
CV 17,59,63
MV 96,112,64
TR 110,3,17
ds_write_b64 v18, v[12:13] offset:50688
MV 96,116,68
s_add_u32 s44, s44, 6144
s_addc_u32 s45, s45, 0
LA 24,-4
MV 100,112,72
v_add_u32 v21, 0xf000, v18
v_add_u32 v22, 0xf000, v19
MV 100,116,76
ds_write_b32 v19, v16 offset:50688
MV 104,112,80
CV 12,64,68
CV 13,72,76
MV 104,116,84
ds_write_b64 v18, v[14:15] offset:51456
MV 108,112,88
LA 28,-3
CV 16,80,84
MV 108,116,92
WL 5
TR 112,4,16
CV 14,65,69
MA 140,96,88
TR 114,5,16
ds_write_b32 v19, v17 offset:51456
MA 140,100,92
ds_write_b64 v21, v[12:13] offset:0
MA 136,96,80
ds_write_b32 v22, v16 offset:0
MA 136,100,84
TR 116,4,17
LA 32,-2
MA 132,96,72
TR 118,5,17
CV 15,73,77
MA 132,100,76
CV 17,81,85
CV 12,66,70
MA 128,96,64
CV 13,74,78
CV 16,82,86
MA 128,100,68
MA 124,96,56
WL 0
LA 36,-1
ds_write_b64 v21, v[14:15] offset:768
MA 124,100,60
s_barrier
RV 24,20,49152
CV 14,67,71
MA 120,96,48
ds_write_b32 v22, v17 offset:768
MA 120,100,52
TR 96,2,18
CV 15,75,79
MV 112,104,48
s_add_u32 s42, s88, 393216
TR 98,3,18
CV 17,83,87
MV 112,108,52
WL 3
LA 40,0
ds_write_b64 v21, v[12:13] offset:1536
MV 116,104,56
s_add_u32 s42, s42, s72
s_addc_u32 s43, s73, 0
ST 24,0
MV 116,108,60
TR 100,2,19
MV 120,104,64
TR 102,3,19
ds_write_b32 v22, v16 offset:1536
MV 120,108,68
ds_write_b64 v21, v[14:15] offset:2304
MV 124,104,72
LA 44,1
v_add_u32 v21, 0xf000, v18
MV 124,108,76
MV 128,104,80
ds_write_b32 v22, v17 offset:2304
MV 128,108,84
WL 0
s_barrier
RV 28,20,50176
v_add_u32 v22, 0xf000, v19
MV 132,104,88
CV 12,88,92
CV 13,96,100
MV 132,108,92
TR 104,4,18
CV 16,104,108
MV 156,112,88
TR 106,5,18
CV 14,89,93
MV 156,116,92
RV 24,20,51200
CV 15,97,101
MV 152,112,80
ds_write_b64 v21, v[12:13] offset:12288
MV 152,116,84
WL 4
TR 108,4,19
ST 28,1
MV 148,112,72
TR 110,5,19
CV 17,105,109
MV 148,116,76
v_add_u32 v23, 0xf000, v20
ds_write_b32 v22, v16 offset:12288
MV 144,112,64
CV 12,90,94
CV 13,98,102
MV 144,116,68
WL 4
ST 24,2
CV 16,106,110
MV 140,112,56
ds_write_b64 v21, v[14:15] offset:13056
MV 140,116,60
s_barrier
RV 28,23,0
CV 14,91,95
MV 136,112,48
ds_write_b32 v22, v17 offset:13056
MV 136,116,52
TR 112,2,20
CV 15,99,103
MV 160,96,48
s_add_u32 s42, s88, 491520
TR 114,3,20
CV 17,107,111
MV 160,100,52
s_add_u32 s42, s42, s72
s_addc_u32 s43, s73, 0
WL 3
ST 28,0
ds_write_b64 v21, v[12:13] offset:13824
MV 164,96,56
.Lk2p40:
s_cbranch_execz .Lk2r8
RV 24,23,1024
AR 6,120
MV 164,100,60
TR 116,2,21
AR 7,124
MV 168,96,64
TR 118,3,21
ds_write_b32 v22, v16 offset:13824
MV 168,100,68
ds_write_b64 v21, v[14:15] offset:14592
MV 172,96,72
AR 8,128
AR 9,132
MV 172,100,76
AR 10,136
AR 11,140
MV 176,96,80
WL 4
ST 24,1
ds_write_b32 v22, v17 offset:14592
MV 176,100,84
WL 0
s_barrier
RV 28,23,2048
SB 6,32
MV 180,96,88
SB 7,33
SB 8,34
MV 180,100,92
TR 96,4,20
SB 9,35
MA 164,104,88
TR 98,5,20
SB 10,36
MA 164,108,92
v_add_u32 v23, 0xf000, v20
SB 11,37
MA 160,104,80
RV 24,23,12288
CV 12,6,7
MA 160,108,84
WL 3
TR 100,4,21
ST 28,2
MA 156,104,72
TR 102,5,21
CV 13,8,9
MA 156,108,76
s_add_u32 s42, s89, 393216
CV 16,10,11
AR 6,121
MA 152,104,64
s_add_u32 s42, s42, s72
s_addc_u32 s43, s73, 0
WL 2
ST 24,0
MA 152,108,68
AR 7,125
AR 8,129
MA 148,104,56
ds_write_b64 v18, v[12:13] offset:24576
MA 148,108,60
s_barrier
RV 28,23,13312
AR 9,133
MA 144,104,48
ds_write_b32 v19, v16 offset:24576
MA 144,108,52
TR 104,2,22
AR 10,137
MV 184,112,48
TR 106,3,22
AR 11,141
MV 184,116,52
RV 24,23,14336
SB 6,32
MV 188,112,56
SB 7,33
SB 8,34
MV 188,116,60
WL 4
TR 108,2,23
ST 28,1
MV 192,112,64
TR 110,3,23
SB 9,35
MV 192,116,68
SB 10,36
SB 11,37
MV 196,112,72
CV 14,6,7
CV 15,8,9
MV 196,116,76
WL 2
ST 24,2
CV 17,10,11
MV 200,112,80
AR 6,122
AR 7,126
MV 200,116,84
s_barrier
ds_write_b64 v18, v[14:15] offset:25344
MV 204,112,88
ds_write_b32 v19, v17 offset:25344
MV 204,116,92
TR 112,4,22
AR 8,130
MV 228,96,88
TR 114,5,22
AR 9,134
MV 228,100,92
AR 10,138
AR 11,142
MV 224,96,80
SB 6,32
SB 7,33
MV 224,100,84
TR 116,4,23
SB 8,34
MV 220,96,72
TR 118,5,23
SB 9,35
MV 220,100,76
SB 10,36
SB 11,37
MV 216,96,64
CV 12,6,7
CV 13,8,9
MV 216,100,68
CV 16,10,11
AR 6,123
MV 212,96,56
WL 0
AR 7,127
AR 8,131
MV 212,100,60
s_barrier
ds_write_b64 v18, v[12:13] offset:26112
MV 208,96,48
ds_write_b32 v19, v16 offset:26112
MV 208,100,52
TR 96,2,24
AR 9,135
MV 232,104,48
TR 98,3,24
AR 10,139
MV 232,108,52
AR 11,143
SB 6,32
MV 236,104,56
SB 7,33
SB 8,34
MV 236,108,60
TR 100,2,25
SB 9,35
MV 240,104,64
TR 102,3,25
SB 10,36
MV 240,108,68
SB 11,37
CV 14,6,7
MV 244,104,72
CV 15,8,9
CV 17,10,11
MV 244,108,76
MV 248,104,80
MV 248,108,84
ds_write_b64 v18, v[14:15] offset:26880
MV 252,104,88
ds_write_b32 v19, v17 offset:26880
MV 252,108,92
TR 104,4,24
MA 188,112,88
TR 106,5,24
MA 188,116,92
s_mov_b64 exec, 0
s_cmp_eq_u32 %[w], 0
s_cbranch_scc1 .Lk2p41
s_cmp_eq_u32 %[w], 1
s_cbranch_scc1 .Lk2r9
s_cmp_eq_u32 %[w], 2
s_cbranch_scc1 .Lk2r9
s_branch .Lk2r9
.Lk2r9:
s_mov_b64 exec, -1
MA 184,112,80
MA 184,116,84
TR 108,4,25
MA 180,112,72
TR 110,5,25
MA 180,116,76
MA 176,112,64
MA 176,116,68
MA 172,112,56
WL 4
MA 172,116,60
s_barrier
RV 28,20,24576
MA 168,112,48
MA 168,116,52
)"
R"(WV 8
TR 112,2,26
MV 112,96,0
s_add_u32 s42, s89, 491520
TR 114,3,26
MV 112,100,4
s_add_u32 s44, s84, 53248
s_addc_u32 s45, s85, 0
LA 48,-4
MV 116,96,8
s_add_u32 s42, s42, s72
s_addc_u32 s43, s73, 0
WL 2
ST 28,0
MV 116,100,12
TR 116,2,27
LA 52,-3
MV 120,96,16
TR 118,3,27
MV 120,100,20
LA 56,-2
MV 124,96,24
MV 124,100,28
LA 60,-1
MV 128,96,32
MV 128,100,36
LA 64,0
MV 132,96,40
RV 24,20,25600
MV 132,100,44
TR 96,4,26
LA 68,1
MV 156,104,40
TR 98,5,26
MV 156,108,44
s_add_u32 s44, s44, 6144
s_addc_u32 s45, s45, 0
LA 72,-4
MV 152,104,32
RV 28,20,26624
CV 12,112,116
MV 152,108,36
TR 100,4,27
LA 76,-3
MV 148,104,24
WL 4
TR 102,5,27
ST 24,1
MV 148,108,28
LA 80,-2
CV 13,120,124
MV 144,104,16
CV 16,128,132
CV 14,113,117
MV 144,108,20
LA 84,-1
CV 15,121,125
MV 140,104,8
WL 2
ST 28,2
CV 17,129,133
MV 140,108,12
s_barrier
LA 88,0
ds_write_b64 v18, v[12:13] offset:36864
MV 136,104,0
ds_write_b32 v19, v16 offset:36864
MV 136,108,4
TR 104,2,28
LA 92,1
MV 160,112,0
TR 106,3,28
CV 12,114,118
MV 160,116,4
ds_write_b64 v18, v[14:15] offset:37632
MV 164,112,8
ds_write_b32 v19, v17 offset:37632
MV 164,116,12
TR 108,2,29
CV 13,122,126
MV 168,112,16
TR 110,3,29
CV 16,130,134
MV 168,116,20
ds_write_b64 v18, v[12:13] offset:38400
MV 172,112,24
CV 14,115,119
CV 15,123,127
MV 172,116,28
CV 17,131,135
MV 176,112,32
ds_write_b32 v19, v16 offset:38400
MV 176,116,36
ds_write_b64 v18, v[14:15] offset:39168
MV 180,112,40
ds_write_b32 v19, v17 offset:39168
MV 180,116,44
WL 8
TR 112,4,28
CV 12,136,140
MA 164,96,40
TR 114,5,28
CV 13,144,148
MA 164,100,44
CV 16,152,156
CV 14,137,141
MA 160,96,32
ds_write_b64 v18, v[12:13] offset:0
MA 160,100,36
TR 116,4,29
CV 15,145,149
MA 156,96,24
TR 118,5,29
CV 17,153,157
MA 156,100,28
ds_write_b32 v19, v16 offset:0
MA 152,96,16
CV 12,138,142
CV 13,146,150
MA 152,100,20
CV 16,154,158
MA 148,96,8
WL 1
ds_write_b64 v18, v[14:15] offset:768
MA 148,100,12
s_barrier
RV 24,20,36864
CV 14,139,143
MA 144,96,0
ds_write_b32 v19, v17 offset:768
MA 144,100,4
TR 96,2,30
CV 15,147,151
MV 184,104,0
s_add_u32 s42, s88, 442368
TR 98,3,30
CV 17,155,159
MV 184,108,4
s_add_u32 s42, s42, s72
s_addc_u32 s43, s73, 0
WL 3
ST 24,0
ds_write_b64 v18, v[12:13] offset:1536
MV 188,104,8
RV 28,20,37888
CV 12,160,164
MV 188,108,12
TR 100,2,31
CV 13,168,172
MV 192,104,16
TR 102,3,31
ds_write_b32 v19, v16 offset:1536
MV 192,108,20
ds_write_b64 v18, v[14:15] offset:2304
MV 196,104,24
CV 16,176,180
CV 14,161,165
MV 196,108,28
CV 15,169,173
MV 200,104,32
WL 4
ST 28,1
ds_write_b32 v19, v17 offset:2304
MV 200,108,36
WL 0
s_barrier
RV 24,20,38912
CV 17,177,181
MV 204,104,40
ds_write_b64 v18, v[12:13] offset:12288
MV 204,108,44
TR 104,4,30
CV 12,162,166
MV 228,112,40
TR 106,5,30
CV 13,170,174
MV 228,116,44
RV 28,20,0
ds_write_b32 v19, v16 offset:12288
MV 224,112,32
ds_write_b64 v18, v[14:15] offset:13056
MV 224,116,36
WL 6
TR 108,4,31
ST 24,2
MV 220,112,24
s_add_u32 s42, s88, 540672
TR 110,5,31
CV 16,178,182
MV 220,116,28
s_add_u32 s42, s42, s72
s_addc_u32 s43, s73, 0
WL 4
ST 28,0
ds_write_b32 v19, v17 offset:13056
MV 216,112,16
CV 14,163,167
CV 15,171,175
MV 216,116,20
CV 17,179,183
MV 212,112,8
ds_write_b64 v18, v[12:13] offset:13824
MV 212,116,12
s_barrier
RV 24,20,1024
AR 6,144
MV 208,112,0
ds_write_b32 v19, v16 offset:13824
MV 208,116,4
TR 112,2,32
AR 7,148
MV 232,96,0
TR 114,3,32
AR 8,152
MV 232,100,4
RV 28,20,2048
ds_write_b64 v18, v[14:15] offset:14592
MV 236,96,8
ds_write_b32 v19, v17 offset:14592
MV 236,100,12
WL 6
TR 116,2,33
ST 24,1
MV 240,96,16
TR 118,3,33
AR 9,156
MV 240,100,20
AR 10,160
AR 11,164
MV 244,96,24
SB 6,32
SB 7,33
MV 244,100,28
WL 4
ST 28,2
SB 8,34
MV 248,96,32
WL 2
SB 9,35
SB 10,36
MV 248,100,36
s_barrier
RV 24,20,12288
SB 11,37
MV 252,96,40
CV 12,6,7
CV 13,8,9
MV 252,100,44
TR 96,4,32
CV 16,10,11
MA 188,104,40
s_add_u32 s42, s89, 442368
TR 98,5,32
AR 6,145
MA 188,108,44
s_add_u32 s42, s42, s72
s_addc_u32 s43, s73, 0
WL 2
ST 24,0
MA 184,104,32
RV 28,20,13312
ds_write_b64 v18, v[12:13] offset:49152
MA 184,108,36
TR 100,4,33
AR 7,149
MA 180,104,24
TR 102,5,33
AR 8,153
MA 180,108,28
ds_write_b32 v19, v16 offset:49152
MA 176,104,16
AR 9,157
AR 10,161
MA 176,108,20
AR 11,165
SB 6,32
MA 172,104,8
WL 4
ST 28,1
SB 7,33
MA 172,108,12
RV 24,20,14336
SB 8,34
MA 168,104,0
SB 9,35
SB 10,36
MA 168,108,4
WV 8
TR 104,2,34
SB 11,37
MV 184,112,48
TR 106,3,34
CV 14,6,7
MV 184,116,52
CV 15,8,9
CV 17,10,11
MV 188,112,56
ds_write_b64 v18, v[14:15] offset:49920
MV 188,116,60
WL 3
TR 108,2,35
ST 24,2
MV 192,112,64
TR 110,3,35
AR 6,146
MV 192,116,68
ds_write_b32 v19, v17 offset:49920
MV 196,112,72
AR 7,150
AR 8,154
MV 196,116,76
AR 9,158
AR 10,162
MV 200,112,80
AR 11,166
SB 6,32
MV 200,116,84
SB 7,33
SB 8,34
MV 204,112,88
SB 9,35
SB 10,36
MV 204,116,92
TR 112,4,34
SB 11,37
MV 228,96,88
TR 114,5,34
CV 12,6,7
MV 228,100,92
CV 13,8,9
CV 16,10,11
MV 224,96,80
ds_write_b64 v18, v[12:13] offset:50688
MV 224,100,84
TR 116,4,35
AR 6,147
MV 220,96,72
TR 118,5,35
AR 7,151
MV 220,100,76
ds_write_b32 v19, v16 offset:50688
MV 216,96,64
AR 8,155
AR 9,159
MV 216,100,68
AR 10,163
AR 11,167
MV 212,96,56
SB 6,32
SB 7,33
MV 212,100,60
SB 8,34
SB 9,35
MV 208,96,48
SB 10,36
SB 11,37
MV 208,100,52
WL 6
CV 14,6,7
CV 15,8,9
MV 232,104,48
.Lk2p41:
s_cbranch_execz .Lk2r9
CV 17,10,11
MV 232,108,52
ds_write_b64 v18, v[14:15] offset:51456
MV 236,104,56
ds_write_b32 v19, v17 offset:51456
MV 236,108,60
v_add_u32 v21, 0xf000, v18
v_add_u32 v22, 0xf000, v19
MV 240,104,64
CV 12,184,188
CV 13,192,196
MV 240,108,68
CV 16,200,204
CV 14,185,189
MV 244,104,72
CV 15,193,197
CV 17,201,205
MV 244,108,76
MV 248,104,80
WL 0
ds_write_b64 v21, v[12:13] offset:0
MV 248,108,84
s_barrier
RV 28,20,49152
CV 12,186,190
MV 252,104,88
ds_write_b32 v22, v16 offset:0
MV 252,108,92
RV 24,20,50176
CV 13,194,198
MA 188,112,88
s_add_u32 s42, s89, 540672
ds_write_b64 v21, v[14:15] offset:768
MA 188,116,92
s_add_u32 s42, s42, s72
s_addc_u32 s43, s73, 0
WL 3
)"
R"(ST 28,0
MA 184,112,80
ds_write_b32 v22, v17 offset:768
MA 184,116,84
RV 28,20,51200
CV 16,202,206
MA 180,112,72
ds_write_b64 v21, v[12:13] offset:1536
MA 180,116,76
WL 4
ST 24,1
CV 14,187,191
MA 176,112,64
CV 15,195,199
CV 17,203,207
MA 176,116,68
ds_write_b32 v22, v16 offset:1536
MA 172,112,56
MA 172,116,60
WL 2
ST 28,2
MA 168,112,48
ds_write_b64 v21, v[14:15] offset:2304
MA 168,116,52
ds_write_b32 v22, v17 offset:2304
v_add_u32 v21, 0xf000, v18
s_nop 0
v_add_u32 v22, 0xf000, v19
CV 12,208,212
CV 13,216,220
CV 16,224,228
CV 14,209,213
ds_write_b64 v21, v[12:13] offset:12288
ds_write_b32 v22, v16 offset:12288
CV 15,217,221
CV 17,225,229
WL 2
s_barrier
v_add_u32 v23, 0xf000, v20
RV 24,23,0
ds_write_b64 v21, v[14:15] offset:13056
ds_write_b32 v22, v17 offset:13056
RV 28,23,1024
CV 12,210,214
s_add_u32 s42, s90, 393216
CV 13,218,222
CV 16,226,230
s_add_u32 s42, s42, s74
s_addc_u32 s43, s75, 0
WL 3
ST 24,0
ds_write_b64 v21, v[12:13] offset:13824
ds_write_b32 v22, v16 offset:13824
RV 24,23,2048
CV 14,211,215
CV 15,219,223
CV 17,227,231
WL 3
ST 28,1
ds_write_b64 v21, v[14:15] offset:14592
ds_write_b32 v22, v17 offset:14592
CV 12,232,236
CV 13,240,244
CV 16,248,252
CV 14,233,237
WL 2
ST 24,2
ds_write_b64 v18, v[12:13] offset:24576
ds_write_b32 v19, v16 offset:24576
WL 2
s_barrier
v_add_u32 v23, 0xf000, v20
RV 28,23,12288
CV 15,241,245
CV 17,249,253
RV 24,23,13312
ds_write_b64 v18, v[14:15] offset:25344
s_add_u32 s42, s90, 442368
ds_write_b32 v19, v17 offset:25344
s_add_u32 s42, s42, s74
s_addc_u32 s43, s75, 0
WL 3
ST 28,0
CV 12,234,238
CV 13,242,246
RV 28,23,14336
CV 16,250,254
ds_write_b64 v18, v[12:13] offset:26112
WL 4
ST 24,1
ds_write_b32 v19, v16 offset:26112
CV 14,235,239
CV 15,243,247
CV 17,251,255
ds_write_b64 v18, v[14:15] offset:26880
WL 3
ST 28,2
ds_write_b32 v19, v17 offset:26880
AR 6,168
AR 7,172
WL 0
s_barrier
RV 24,20,24576
AR 8,176
AR 9,180
AR 10,184
RV 28,20,25600
AR 11,188
s_add_u32 s42, s91, 393216
SB 6,32
SB 7,33
s_add_u32 s42, s42, s74
s_addc_u32 s43, s75, 0
WL 1
ST 24,0
SB 8,34
SB 9,35
RV 24,20,26624
SB 10,36
SB 11,37
CV 12,6,7
WL 1
ST 28,1
CV 13,8,9
CV 16,10,11
AR 6,169
ds_write_b64 v18, v[12:13] offset:36864
ds_write_b32 v19, v16 offset:36864
WL 2
ST 24,2
AR 7,173
AR 8,177
AR 9,181
AR 10,185
AR 11,189
SB 6,32
SB 7,33
SB 8,34
SB 9,35
SB 10,36
SB 11,37
CV 14,6,7
CV 15,8,9
CV 17,10,11
AR 6,170
ds_write_b64 v18, v[14:15] offset:37632
ds_write_b32 v19, v17 offset:37632
AR 7,174
AR 8,178
AR 9,182
AR 10,186
AR 11,190
SB 6,32
SB 7,33
SB 8,34
SB 9,35
SB 10,36
SB 11,37
CV 12,6,7
CV 13,8,9
CV 16,10,11
ds_write_b64 v18, v[12:13] offset:38400
ds_write_b32 v19, v16 offset:38400
AR 6,171
AR 7,175
AR 8,179
AR 9,183
AR 10,187
AR 11,191
SB 6,32
SB 7,33
SB 8,34
SB 9,35
SB 10,36
SB 11,37
CV 14,6,7
CV 15,8,9
CV 17,10,11
ds_write_b64 v18, v[14:15] offset:39168
ds_write_b32 v19, v17 offset:39168
WL 0
s_barrier
RV 28,20,36864
RV 24,20,37888
s_add_u32 s42, s91, 442368
s_add_u32 s42, s42, s74
s_addc_u32 s43, s75, 0
WL 1
ST 28,0
s_nop 1
RV 28,20,38912
WL 1
ST 24,1
WL 0
ST 28,2
s_waitcnt vmcnt(0) lgkmcnt(0)
.purgem FL
.purgem MA
.purgem MV
.purgem ZA
.purgem ZV
.purgem SB
.purgem TR
.purgem LA
.purgem RV
.purgem PB
.purgem DH
.purgem DG
.purgem DX
.purgem ST
.purgem QO
.purgem AN
.purgem AW
.purgem AR
.purgem UL
.purgem UH
.purgem AD
.purgem PA
.purgem DF
.purgem CV
.purgem PM
.purgem GA
.purgem GW
.purgem GE
.purgem GP
.purgem FS
.purgem FB
.purgem FG
.purgem FX
.purgem SX
.purgem SZ
.purgem SY
.purgem WL
.purgem WV
)"
K2_END
      : [flg] "=&s"(flg) : K2_OPS : K2_CLOB);
  if (flg) {
    slow_bar((unsigned*)(g_buf + G_BAR), w);
    const bool hbf = __hip_atomic_load((const unsigned*)(g_buf + G_FLAG) + 96, __ATOMIC_RELAXED, __HIP_MEMORY_SCOPE_AGENT) != 0;
    const u16* const volatile __attribute__((address_space(4)))* ka = (const u16* const volatile __attribute__((address_space(4)))*)__builtin_amdgcn_kernarg_segment_ptr();
    if (hbf) {
      slow_fc1(ka[0], ka[3], (char*)ka[1], (float*)smem + w * HID, w);
      slow_bar((unsigned*)(g_buf + G_BAR + 64), w);
    }
    slow_fc2(ka[0], (const char*)ka[1], (u16*)ka[6], ka[7], ka[5], smem, hbf, w);
  }
  TS(15);
  MK_END(4);
}

extern "C" void kernel_launch(void* const* d_in, const int* in_sizes, int n_in,
                              void* d_out, int out_size, void* d_ws, size_t ws_size,
                              hipStream_t stream) {
  const u16* x = (const u16*)d_in[0];
  const u16* dw = (const u16*)d_in[1];
  const u16* w1 = (const u16*)d_in[2];
  const u16* w2 = (const u16*)d_in[3];
  const u16* grn_g = (const u16*)d_in[4];
  const u16* grn_b = (const u16*)d_in[5];
  char* ws = (char*)d_ws;
  if (ws_size < H_BYTES) return;
  (void)hipFuncSetAttribute((const void*)k_block, hipFuncAttributeMaxDynamicSharedMemorySize, K1_LDS);
  k_block<<<NWG, 256, K1_LDS, stream>>>(x, ws, w1, dw, grn_b, w2, (const u16*)d_out, grn_g);
}
```
